```python
import torch

def _softmax(x, axis=-1):
    return torch.softmax(x, dim=axis)
def _gelu(x):
    return torch.nn.functional.gelu(x, approximate="tanh")
def _silu(x):
    return torch.nn.functional.silu(x)
def _sigmoid(x):
    return torch.sigmoid(x)
def _layernorm(x, axis=-1, eps=1e-5):
    m = x.mean(dim=axis, keepdim=True)
    v = x.var(dim=axis, keepdim=True, unbiased=False)
    return (x - m) / torch.sqrt(v + eps)
def _scatter_add(tgt, idx, src, axis=0):
    return tgt.clone().index_add_(axis, idx, src)
def _segment_sum(src, seg, n):
    out = torch.zeros((n,) + src.shape[1:], dtype=src.dtype)
    return out.index_add_(0, seg, src)
def _pad(x, pw):
    flat = tuple(v for pair in reversed(pw) for v in pair)
    return torch.nn.functional.pad(x, flat)

kernel_name = 'recurrent_scan__compressed_kv_cla__dense_activation_mish__prompt'

def setup_inputs():

    _w = (lambda a: (lambda t: t.to(torch.bfloat16) if t.is_floating_point() else t)(torch.as_tensor(a)))
    def _A_setup():
        B, H, Sq, Sk, D, M = 1, 32, 2048, 2048, 128, 640
        Dc = 4 * D
        return {
            "x": _w((lambda *s: torch.randn(*s, dtype=torch.bfloat16) * 0.05)(B, Sq, M)),
            "c_kv": _w((lambda *s: torch.randn(*s, dtype=torch.bfloat16) * 0.8)(B, Sk, Dc)),
            "w_q": _w((lambda *s: torch.randn(*s, dtype=torch.bfloat16) * 1.6)(M, H, D) * (M ** -0.5)),
            "w_uk": _w((lambda *s: torch.randn(*s, dtype=torch.bfloat16) * 1.6)(Dc, H, D) * (Dc ** -0.5)),
            "w_uv": _w((lambda *s: torch.randn(*s, dtype=torch.bfloat16) * 0.8)(Dc, H, D) * (Dc ** -0.5)),
            "w_o": _w((lambda *s: torch.randn(*s, dtype=torch.bfloat16) * 0.8)(H, D, M) * ((H * D) ** -0.5)),
        }
    _A_in = _A_setup()

    def _B_setup():
        B, S, D, F = 1, 2048, 640, 1728
        return {
            "x": _w((lambda *s: torch.randn(*s, dtype=torch.bfloat16) * 0.05)(B, S, D)),
            "w1": _w((lambda *s: torch.randn(*s, dtype=torch.bfloat16) * 0.8)(D, F) * (D ** -0.5)),
            "b1": _w((lambda *s: torch.randn(*s, dtype=torch.bfloat16) * 0.8)(F)),
            "w2": _w((lambda *s: torch.randn(*s, dtype=torch.bfloat16) * 0.2)(F, D) * (F ** -0.5)),
        }
    _B_in = _B_setup()

    return {
        "A_x": _A_in["x"],
        "A_c_kv": _A_in["c_kv"],
        "A_w_q": _A_in["w_q"],
        "A_w_uk": _A_in["w_uk"],
        "A_w_uv": _A_in["w_uv"],
        "A_w_o": _A_in["w_o"],
        "B_x": _B_in["x"],
        "B_w1": _B_in["w1"],
        "B_b1": _B_in["b1"],
        "B_w2": _B_in["w2"],
    }

def reference(A_x, A_c_kv, A_w_q, A_w_uk, A_w_uv, A_w_o, B_x, B_w1, B_b1, B_w2):
    def _A_ref(x, c_kv, w_q, w_uk, w_uv, w_o):
        B, Sq, M = x.shape[0], x.shape[1], x.shape[2]
        Sk = c_kv.shape[1]
        H, D = w_q.shape[1], w_q.shape[2]
        q = torch.einsum("bqm,mhd->bhqd", x, w_q)
        k = torch.einsum("bkc,chd->bhkd", c_kv, w_uk)
        v = torch.einsum("bkc,chd->bhkd", c_kv, w_uv)
        scores = torch.einsum("bhqd,bhkd->bhqk", q, k) * (D ** -0.5)
        off = Sk - Sq
        qi = torch.arange(Sq)[:, None]
        kj = torch.arange(Sk)[None, :]
        cmask = kj <= (qi + off)
        scores = torch.where(cmask[None, None], scores, -1e9)
        p = _softmax(scores, axis=-1)
        ctx = torch.einsum("bhqk,bhkd->bhqd", p, v)
        return torch.einsum("bhqd,hdm->bqm", ctx, w_o)

    def _B_ref(x, w1, b1, w2):
        h = torch.einsum("bsd,df->bsf", x, w1) + b1
        sp = torch.log1p(torch.exp(-torch.abs(h))) + torch.where(h > 0, h, torch.zeros_like(h))
        act = h * torch.tanh(sp)
        return torch.einsum("bsf,fd->bsd", act, w2)

    x = A_x
    xn = _layernorm(x, axis=-1)
    s = _A_ref(xn, A_c_kv, A_w_q, A_w_uk, A_w_uv, A_w_o)
    g = _silu(_B_ref(xn, B_w1, B_b1, B_w2))
    return x + s * g
```

```cpp
#include <hip/hip_runtime.h>
#include <hip/hip_bf16.h>
#include <stdint.h>

typedef __bf16 bf16x8 __attribute__((ext_vector_type(8)));
typedef __bf16 bf16x2 __attribute__((ext_vector_type(2)));
typedef short s16x4 __attribute__((ext_vector_type(4)));
typedef short s16x8 __attribute__((ext_vector_type(8)));
typedef short v4i16_t __attribute__((ext_vector_type(4)));
typedef float f32x16 __attribute__((ext_vector_type(16)));
typedef float f32x4 __attribute__((ext_vector_type(4)));
typedef float f32x2 __attribute__((ext_vector_type(2)));
typedef unsigned u32x4 __attribute__((ext_vector_type(4)));
typedef unsigned u32x2 __attribute__((ext_vector_type(2)));
typedef __attribute__((address_space(3))) const char* lds_cptr;
typedef __attribute__((address_space(3))) char* lds_ptr;

#define DI __device__ __forceinline__
#define SBAR() __builtin_amdgcn_sched_barrier(0)
#define MFMA(a, b, c) __builtin_amdgcn_mfma_f32_16x16x32_bf16((a), (b), (c), 0, 0, 0)
#define PIN(x) asm volatile("" : "+v"(x))
#define PINR(x) asm volatile("" ::"v"(x))

constexpr int S_ = 2048, NH = 32, HD = 128, DM = 640, DC = 512, FF = 1728;
constexpr int NKS_X = DM / 32;
constexpr int NKS_C = (NH * HD) / 32;
constexpr int NKS_H = FF / 32;
constexpr int NKS_CH = NKS_C + NKS_H;
constexpr size_t WS_XF = 0;
constexpr size_t WS_KF = WS_XF + (size_t)128 * NKS_X * 1024;
constexpr size_t WS_VF = WS_KF + (size_t)NH * 128 * 4 * 1024;
constexpr size_t WS_CH = WS_VF + (size_t)NH * 64 * 8 * 1024;
constexpr size_t WS_QH = WS_CH + (size_t)128 * NKS_CH * 1024;
constexpr size_t WS_PP = WS_QH + (size_t)256 * 32768;
constexpr size_t PP_BYTES = (size_t)64 * 5 * 8 * 10 * 1024;
constexpr size_t WS_FL = WS_PP + PP_BYTES;
constexpr size_t WS_END = WS_FL + (size_t)256 * 128;

DI unsigned rfl(unsigned x) { return __builtin_amdgcn_readfirstlane(x); }
#define STAMP(i)
#define TICK(acc)
DI const char* uptr(const char* p) { const unsigned long long v = (unsigned long long)p; const unsigned lo = rfl((unsigned)v), hi = rfl((unsigned)(v >> 32)); return (const char*)(((unsigned long long)hi << 32) | lo); }
DI unsigned cvtpk(float lo, float hi) { f32x2 v = {lo, hi}; bf16x2 b = __builtin_convertvector(v, bf16x2); return __builtin_bit_cast(unsigned, b); }
DI bf16x8 lds_frag(lds_cptr p) { return *(const __attribute__((address_space(3))) bf16x8*)p; }
DI s16x4 lds_tr(lds_cptr p) { return __builtin_bit_cast(s16x4, __builtin_amdgcn_ds_read_tr16_b64_v4i16((__attribute__((address_space(3))) v4i16_t*)p)); }
DI bf16x8 tr_frag(lds_cptr p0, lds_cptr p1) { s16x4 lo = lds_tr(p0), hi = lds_tr(p1); s16x8 v = __builtin_shufflevector(lo, hi, 0, 1, 2, 3, 4, 5, 6, 7); return __builtin_bit_cast(bf16x8, v); }
template <int OFF> DI void dma1(unsigned m0v, const void* sbase, unsigned voff) {
    asm volatile("s_mov_b32 m0, %0\n\ts_nop 0\n\tglobal_load_lds_dwordx4 %1, %2 offset:%3" ::"s"(m0v), "v"(voff), "s"(sbase), "i"(OFF) : "memory");
}
DI u32x4 pack44(const f32x4& a, const f32x4& b) { u32x4 w; w[0] = cvtpk(a[0], a[1]); w[1] = cvtpk(a[2], a[3]); w[2] = cvtpk(b[0], b[1]); w[3] = cvtpk(b[2], b[3]); return w; }
DI u32x4 pack44s(const f32x4& a, const f32x4& b, float sc) { u32x4 w; w[0] = cvtpk(a[0] * sc, a[1] * sc); w[1] = cvtpk(a[2] * sc, a[3] * sc); w[2] = cvtpk(b[0] * sc, b[1] * sc); w[3] = cvtpk(b[2] * sc, b[3] * sc); return w; }
DI u32x4 pack8(const f32x16& a, int b, float sc) {
    u32x4 w; w[0] = cvtpk(a[b + 0] * sc, a[b + 1] * sc); w[1] = cvtpk(a[b + 2] * sc, a[b + 3] * sc);
    w[2] = cvtpk(a[b + 4] * sc, a[b + 5] * sc); w[3] = cvtpk(a[b + 6] * sc, a[b + 7] * sc); return w;
}
DI u32x4 pack8n(const f32x16& a, int b) {
    u32x4 w; w[0] = cvtpk(a[b + 0], a[b + 1]); w[1] = cvtpk(a[b + 2], a[b + 3]); w[2] = cvtpk(a[b + 4], a[b + 5]); w[3] = cvtpk(a[b + 6], a[b + 7]); return w;
}
DI float bf2f(unsigned short u) { return __uint_as_float((unsigned)u << 16); }
DI float bflo(unsigned w) { return __uint_as_float(w << 16); }
DI float bfhi(unsigned w) { return __uint_as_float(w & 0xffff0000u); }
DI float xhalf_sum(float v) { auto r = __builtin_amdgcn_permlane32_swap(__float_as_uint(v), __float_as_uint(v), false, false); return __uint_as_float(r[0]) + __uint_as_float(r[1]); }
DI float xhalf_max(float v) { auto r = __builtin_amdgcn_permlane32_swap(__float_as_uint(v), __float_as_uint(v), false, false); return __builtin_fmaxf(__uint_as_float(r[0]), __uint_as_float(r[1])); }

template <bool VT, class F>
DI void k1_mma(f32x4 (&acc)[8][4], int half, const bf16x8 (&c)[4], const bf16x8 (&w)[4], F&& filler) {
#pragma unroll
    for (int rt = 0; rt < 4; ++rt)
#pragma unroll
        for (int nt = 0; nt < 4; ++nt) {
            if (half == 0) acc[rt][nt] = VT ? MFMA(c[rt], w[nt], acc[rt][nt]) : MFMA(w[nt], c[rt], acc[rt][nt]);
            else acc[4 + rt][nt] = VT ? MFMA(c[rt], w[nt], acc[4 + rt][nt]) : MFMA(w[nt], c[rt], acc[4 + rt][nt]);
            filler(rt * 4 + nt);
        }
}

template <bool VT>
DI void k1_tile(const char* __restrict__ Abase, const char* __restrict__ Wbase, char* __restrict__ outp, char* lds, const unsigned (&avo)[4], const unsigned (&wvo)[4],
                int hp, int rb, char* stamp_base) {
    const unsigned tid = threadIdx.x, lane = tid & 63, c = lane & 15, g = lane >> 4;
    const unsigned wid = rfl(tid >> 6), wm = wid >> 2, wn = wid & 3;
    const unsigned lds0 = (unsigned)(uintptr_t)lds;
    unsigned aaddr[2], baddr[4];
    {
        const unsigned x = (c >> 1) & 7;
#pragma unroll
        for (int s = 0; s < 2; ++s) aaddr[s] = (128 * wm + c) * 128 + (((4 * s + g) ^ x) << 4);
        const unsigned q = (lane & 15) >> 2, p = lane & 3, f = q | ((g & 1) << 2);
#pragma unroll
        for (int nt = 0; nt < 4; ++nt) baddr[nt] = 32768 + (8 * g + q) * 512 + (((8 * wn + 2 * nt + (p >> 1)) ^ (f << 1)) << 4) + ((p & 1) << 3);
    }
    f32x4 acc[8][4];
#pragma unroll
    for (int i = 0; i < 8; ++i)
#pragma unroll
        for (int j = 0; j < 4; ++j) acc[i][j] = f32x4{};
    bf16x8 cf[2][4], wf[2][4];
    lds_cptr L = (lds_cptr)lds;
#define K1_RC(buf, stg, s, half) do { _Pragma("unroll") for (int rt = 0; rt < 4; ++rt) cf[buf][rt] = lds_frag(L + (stg) + aaddr[s] + ((half) * 4 + rt) * 2048); } while (0)
#define K1_RW(buf, stg, s) do { _Pragma("unroll") for (int nt = 0; nt < 4; ++nt) wf[buf][nt] = tr_frag(L + (stg) + baddr[nt] + (s) * 16384, L + (stg) + baddr[nt] + (s) * 16384 + 2048); } while (0)
    const char* Aw = Abase + (size_t)wid * 32 * 1024;
    const char* Ww = Wbase + (size_t)wid * 8 * 8192;
    const unsigned m0a = lds0 + wid * 4096, m0w = lds0 + 32768 + wid * 4096;
#define K1_DMA(kt, stg)                                                                   \
    do {                                                                                  \
        const char* a_ = Aw + (kt) * 128; const char* w_ = Ww + (size_t)(kt) * 64 * 8192; \
        dma1<0>(m0a + (stg), a_, avo[0]); dma1<1024>(m0a + (stg), a_, avo[1]); dma1<2048>(m0a + (stg), a_, avo[2]); dma1<3072>(m0a + (stg), a_, avo[3]); \
        dma1<0>(m0w + (stg), w_, wvo[0]); dma1<1024>(m0w + (stg), w_, wvo[1]); dma1<2048>(m0w + (stg), w_, wvo[2]); dma1<3072>(m0w + (stg), w_, wvo[3]); \
    } while (0)
    STAMP(1);
    asm volatile("s_waitcnt vmcnt(10)\n\ts_barrier" ::: "memory");
    STAMP(2);
    K1_RW(0, 0, 0); K1_RC(0, 0, 0, 0);
#define K1_RC1(buf, stg, s, half, rt) cf[buf][rt] = lds_frag(L + (stg) + aaddr[s] + ((half) * 4 + (rt)) * 2048)
#define K1_RW1(buf, stg, s, nt) wf[buf][nt] = tr_frag(L + (stg) + baddr[nt] + (s) * 16384, L + (stg) + baddr[nt] + (s) * 16384 + 2048)
#pragma unroll
    for (int kt = 0; kt < 8; ++kt) {
        const unsigned stg = (kt & 1) * 65536, nstg = stg ^ 65536;
        SBAR();
        k1_mma<VT>(acc, 0, cf[0], wf[0], [&](int m) { if (m < 8 && (m & 1) == 0) { K1_RC1(1, stg, 0, 1, m >> 1); SBAR(); } });
        SBAR();
        k1_mma<VT>(acc, 1, cf[1], wf[0], [&](int m) { if (m < 4) { K1_RC1(0, stg, 1, 0, m); SBAR(); } else if (m < 12 && (m & 1) == 0) { K1_RW1(1, stg, 1, (m - 4) >> 1); SBAR(); } });
        SBAR();
        k1_mma<VT>(acc, 0, cf[0], wf[1], [&](int m) { if (m < 8 && (m & 1) == 0) { K1_RC1(1, stg, 1, 1, m >> 1); SBAR(); } });
        SBAR();
        asm volatile("s_waitcnt vmcnt(0) lgkmcnt(0)\n\ts_barrier" ::: "memory");
        const char* a_ = Aw + (kt + 2) * 128; const char* w_ = Ww + (size_t)(kt + 2) * 64 * 8192;
        k1_mma<VT>(acc, 1, cf[1], wf[1], [&](int m) {
            if (kt + 1 < 8) { if (m < 4) { K1_RW1(0, nstg, 0, m); SBAR(); } else if (m < 8) { K1_RC1(0, nstg, 0, 0, m - 4); SBAR(); } }
            if (kt + 2 < 8) {
                if (m == 1) { dma1<0>(m0a + stg, a_, avo[0]); SBAR(); } if (m == 3) { dma1<1024>(m0a + stg, a_, avo[1]); SBAR(); }
                if (m == 5) { dma1<2048>(m0a + stg, a_, avo[2]); SBAR(); } if (m == 7) { dma1<3072>(m0a + stg, a_, avo[3]); SBAR(); }
                if (m == 9) { dma1<0>(m0w + stg, w_, wvo[0]); SBAR(); } if (m == 11) { dma1<1024>(m0w + stg, w_, wvo[1]); SBAR(); }
                if (m == 13) { dma1<2048>(m0w + stg, w_, wvo[2]); SBAR(); } if (m == 15) { dma1<3072>(m0w + stg, w_, wvo[3]); SBAR(); }
            }
        });
        SBAR();
    }
    STAMP(3);
    const int head = 2 * hp + (wn >> 1), d0 = (wn & 1) * 64;
    const int key0 = 256 * rb + 128 * wm;
    if (VT) {
#pragma unroll
        for (int u = 0; u < 4; ++u)
#pragma unroll
            for (int nt = 0; nt < 4; ++nt) {
                const size_t off = ((size_t)(head * 64 + (key0 >> 5) + u) * 8 + (d0 >> 4) + nt) * 1024;
                *(u32x4*)(outp + off + lane * 16) = pack44(acc[2 * u][nt], acc[2 * u + 1][nt]);
            }
    } else {
#pragma unroll
        for (int kt = 0; kt < 8; ++kt)
#pragma unroll
            for (int v = 0; v < 2; ++v) {
                const size_t off = ((size_t)(head * 128 + (key0 >> 4) + kt) * 4 + (d0 >> 5) + v) * 1024;
                *(u32x4*)(outp + off + lane * 16) = pack44(acc[kt][2 * v], acc[kt][2 * v + 1]);
            }
    }
    STAMP(4);
    STAMP(5);
#undef K1_RC
#undef K1_RW
#undef K1_DMA
}

__global__ __launch_bounds__(512, 2) void k1_kvproj(const char* __restrict__ x, const char* __restrict__ ckv, const char* __restrict__ wuk,
                                                    const char* __restrict__ wuv, char* __restrict__ ws) {
    extern __shared__ __attribute__((aligned(16))) char lds[];
    const unsigned tid = threadIdx.x, lane = tid & 63;
    const unsigned wid = rfl(tid >> 6);
    char* stamp_base = ws; (void)stamp_base;
    STAMP(0);
    const int b = blockIdx.x, xcd = b & 7, idx = b >> 3;
    if (tid == 0) *(unsigned*)(ws + WS_FL + (size_t)b * 128) = 0u;
    const int vt = idx & 1, hp = 2 * xcd + ((idx >> 1) & 1), rb = idx >> 2;
    const char* Abase = ckv + (size_t)rb * 256 * 1024;
    const char* Wbase = (vt ? wuv : wuk) + hp * 512;
    const int row = 8 * b + wid;
    const unsigned l40 = lane < 40 ? lane : 39;
    u32x4 x0, x1;
    {
        const char* xr = uptr(x + (size_t)row * 1280);
        const unsigned vo = l40 * 32;
        asm volatile("global_load_dwordx4 %0, %2, %3\n\tglobal_load_dwordx4 %1, %2, %3 offset:16" : "=&v"(x0), "=&v"(x1) : "v"(vo), "s"(xr) : "memory");
    }
    unsigned avo[4], wvo[4];
    {
        const unsigned lds0 = (unsigned)(uintptr_t)lds;
        const char* Aw = Abase + (size_t)wid * 32 * 1024;
        const char* Ww = Wbase + (size_t)wid * 8 * 8192;
        const unsigned m0a = lds0 + wid * 4096, m0w = lds0 + 32768 + wid * 4096;
#pragma unroll
        for (int p = 0; p < 4; ++p) {
            const unsigned rw = 8 * p + (lane >> 3);
            avo[p] = rw * 1024 + (((lane & 7) ^ ((rw >> 1) & 7)) << 4) - 1024 * p;
            const unsigned k = 2 * p + (lane >> 5);
            const unsigned f = (k & 3) | ((wid & 1) << 2);
            wvo[p] = k * 8192 + (((lane & 31) ^ (f << 1)) << 4) - 1024 * p;
        }
#pragma unroll
        for (int kt = 0; kt < 2; ++kt) {
            const char* a_ = Aw + kt * 128; const char* w_ = Ww + (size_t)kt * 64 * 8192; const unsigned stg = kt * 65536;
            dma1<0>(m0a + stg, a_, avo[0]); dma1<1024>(m0a + stg, a_, avo[1]); dma1<2048>(m0a + stg, a_, avo[2]); dma1<3072>(m0a + stg, a_, avo[3]);
            dma1<0>(m0w + stg, w_, wvo[0]); dma1<1024>(m0w + stg, w_, wvo[1]); dma1<2048>(m0w + stg, w_, wvo[2]); dma1<3072>(m0w + stg, w_, wvo[3]);
        }
    }
    {
        asm volatile("s_waitcnt vmcnt(16)" : "+v"(x0), "+v"(x1)::"memory");
        float v[16];
#pragma unroll
        for (int i = 0; i < 4; ++i) { v[2 * i] = bflo(x0[i]); v[2 * i + 1] = bfhi(x0[i]); v[8 + 2 * i] = bflo(x1[i]); v[8 + 2 * i + 1] = bfhi(x1[i]); }
        float s = 0.f;
#pragma unroll
        for (int i = 0; i < 16; ++i) s += v[i];
        if (lane >= 40) s = 0.f;
#pragma unroll
        for (int o = 32; o >= 1; o >>= 1) s += __shfl_xor(s, o);
        const float mean = s * (1.0f / DM);
        float q = 0.f;
#pragma unroll
        for (int i = 0; i < 16; ++i) { v[i] -= mean; q += v[i] * v[i]; }
        if (lane >= 40) q = 0.f;
#pragma unroll
        for (int o = 32; o >= 1; o >>= 1) q += __shfl_xor(q, o);
        const float rstd = 1.0f / sqrtf(q * (1.0f / DM) + 1e-5f);
        u32x4 o0, o1;
#pragma unroll
        for (int i = 0; i < 4; ++i) { o0[i] = cvtpk(v[2 * i] * rstd, v[2 * i + 1] * rstd); o1[i] = cvtpk(v[8 + 2 * i] * rstd, v[8 + 2 * i + 1] * rstd); }
        char* xf = ws + WS_XF + ((size_t)(row >> 4) * NKS_X + (l40 >> 1)) * 1024 + ((row & 15) + 32 * (l40 & 1)) * 16;
        if (lane < 40) { *(u32x4*)xf = o0; *(u32x4*)(xf + 256) = o1; }
    }
    if (vt) k1_tile<true>(Abase, Wbase, ws + WS_VF, lds, avo, wvo, hp, rb, ws);
    else    k1_tile<false>(Abase, Wbase, ws + WS_KF, lds, avo, wvo, hp, rb, ws);
}

constexpr float QSCALE = 0.08838834764831845f * 1.4426950408889634f;
constexpr float THR = 8.0f;
constexpr int WQ_PIECE = 1040, W1_PIECE = 1056, W1_OFF = 16 * WQ_PIECE, PSLOT = W1_OFF + 8 * W1_PIECE;

DI float x4_max(float x) {
    auto s = __builtin_amdgcn_permlane16_swap(__float_as_uint(x), __float_as_uint(x), false, false);
    x = __builtin_fmaxf(__uint_as_float(s[0]), __uint_as_float(s[1]));
    auto t = __builtin_amdgcn_permlane32_swap(__float_as_uint(x), __float_as_uint(x), false, false);
    return __builtin_fmaxf(__uint_as_float(t[0]), __uint_as_float(t[1]));
}
DI float x4_sum(float x) {
    auto s = __builtin_amdgcn_permlane16_swap(__float_as_uint(x), __float_as_uint(x), false, false);
    x = __uint_as_float(s[0]) + __uint_as_float(s[1]);
    auto t = __builtin_amdgcn_permlane32_swap(__float_as_uint(x), __float_as_uint(x), false, false);
    return __uint_as_float(t[0]) + __uint_as_float(t[1]);
}
DI float mish_f(float h) {
    const float e = __builtin_amdgcn_exp2f(__builtin_fminf(h, 30.0f) * 1.4426950408889634f);
    const float n = e * (e + 2.0f);
    return h * n * __builtin_amdgcn_rcpf(n + 2.0f);
}

template <int NFT>
DI void proj_phase(const char* __restrict__ wq, const char* __restrict__ w1, const char* __restrict__ b1, char* __restrict__ ws, char* lds,
                   int head, int f0, int q16_0, bf16x8 (&qf)[2][4]) {
    const unsigned tid = threadIdx.x, lane = tid & 63, g = lane >> 4;
    const unsigned wid = rfl(tid >> 6);
    const unsigned lds0 = (unsigned)(uintptr_t)lds;
    lds_cptr L = (lds_cptr)lds;
    const unsigned wqvo = ((lane >> 2) & 3) * 8192 + (lane >> 4) * 64 + (lane & 3) * 16;
    const unsigned w1vo = ((lane >> 2) & 7) * 3456 + (NFT == 4 ? (lane >> 5) * 64 : 0) + (lane & 3) * 16;
    const char* wqs = wq + (size_t)wid * 8 * 8192 + head * 256;
    const char* w1s = w1 + (size_t)wid * 8 * 3456 + f0 * 2;
#define PJ_DMA(c, slot)                                                                                         \
    do {                                                                                                        \
        dma1<0>(lds0 + (slot) + (2 * wid) * WQ_PIECE, wqs + (size_t)(c) * 64 * 8192, wqvo);                     \
        dma1<0>(lds0 + (slot) + (2 * wid + 1) * WQ_PIECE, wqs + (size_t)(c) * 64 * 8192 + 4 * 8192, wqvo);      \
        dma1<0>(lds0 + (slot) + W1_OFF + wid * W1_PIECE, w1s + (size_t)(c) * 64 * 3456, w1vo);                  \
    } while (0)
    const char* xs = ws + WS_XF + (size_t)q16_0 * NKS_X * 1024;
    const unsigned xv0 = lane * 16, xv1 = lane * 16 + NKS_X * 1024;
    u32x4 xa[4], xb[4];
#define PJ_XLOAD(X, c)                                                                                          \
    do {                                                                                                        \
        const char* p_ = xs + (c) * 2048;                                                                       \
        asm volatile("global_load_dwordx4 %0, %4, %6\n\tglobal_load_dwordx4 %1, %5, %6\n\tglobal_load_dwordx4 %2, %4, %6 offset:1024\n\tglobal_load_dwordx4 %3, %5, %6 offset:1024" \
                     : "=&v"(X[0]), "=&v"(X[1]), "=&v"(X[2]), "=&v"(X[3]) : "v"(xv0), "v"(xv1), "s"(p_) : "memory"); \
    } while (0)
    const unsigned q = (lane & 15) >> 2, p = lane & 3;
    const unsigned wqb = 2 * g * WQ_PIECE + q * 64 + p * 8;
    const unsigned w1b = W1_OFF + g * W1_PIECE + q * 64 + p * 8;
    f32x4 aq[8][2], ah[NFT][2];
#pragma unroll
    for (int i = 0; i < 8; ++i) { aq[i][0] = f32x4{}; aq[i][1] = f32x4{}; }
#pragma unroll
    for (int i = 0; i < NFT; ++i) { ah[i][0] = f32x4{}; ah[i][1] = f32x4{}; }
    constexpr int NT = 8 + NFT;
    bf16x8 wf[4];
#define PJ_RD(slot, s, n)                                                                                       \
    (((n) < 8) ? tr_frag(L + (slot) + wqb + (8 * (s)) * WQ_PIECE + ((n) >> 1) * 256 + ((n) & 1) * 32, L + (slot) + wqb + (8 * (s) + 1) * WQ_PIECE + ((n) >> 1) * 256 + ((n) & 1) * 32) \
               : tr_frag(L + (slot) + w1b + (4 * (s)) * W1_PIECE + (((n) - 8) >> 1) * 512 + ((n) & 1) * 32, L + (slot) + w1b + (4 * (s)) * W1_PIECE + 256 + (((n) - 8) >> 1) * 512 + ((n) & 1) * 32))
    PJ_XLOAD(xa, 0);
    PJ_DMA(0, 0); PJ_DMA(1, PSLOT);
    asm volatile("s_waitcnt vmcnt(3)\n\ts_barrier" : "+v"(xa[0]), "+v"(xa[1]), "+v"(xa[2]), "+v"(xa[3])::"memory");
#pragma unroll
    for (int n = 0; n < 3; ++n) wf[n] = PJ_RD(0, 0, n);
#define PJ_CHUNK(c, XC, XN)                                                                                     \
    do {                                                                                                        \
        constexpr int slot_ = ((c) % 3) * PSLOT, nslot_ = (((c) + 1) % 3) * PSLOT, dslot_ = (((c) + 2) % 3) * PSLOT; \
        if ((c) + 1 < 10) PJ_XLOAD(XN, (c) + 1);                                                                \
        if ((c) + 2 < 10) PJ_DMA((c) + 2, dslot_);                                                              \
        SBAR();                                                                                                 \
        _Pragma("unroll") for (int m = 0; m < 2 * NT; ++m) {                                                    \
            const int s = m / NT, n = m % NT;                                                                   \
            if (m == 2 * NT - 3) {       \
                if ((c) + 2 < 10) asm volatile("s_waitcnt vmcnt(3) lgkmcnt(0)\n\ts_barrier" : "+v"(XN[0]), "+v"(XN[1]), "+v"(XN[2]), "+v"(XN[3])::"memory"); \
                else if ((c) + 1 < 10) asm volatile("s_waitcnt vmcnt(0) lgkmcnt(0)\n\ts_barrier" : "+v"(XN[0]), "+v"(XN[1]), "+v"(XN[2]), "+v"(XN[3])::"memory"); \
            }                                                                                                   \
            const int m3 = m + 3;                                                                               \
            if (m3 < 2 * NT) wf[m3 & 3] = PJ_RD(slot_, m3 / NT, m3 % NT);                                       \
            else if ((c) + 1 < 10) wf[m3 & 3] = PJ_RD(nslot_, 0, m3 - 2 * NT);                                  \
            const bf16x8 x0 = __builtin_bit_cast(bf16x8, XC[2 * s]), x1 = __builtin_bit_cast(bf16x8, XC[2 * s + 1]); \
            if (n < 8) { aq[n & 7][0] = MFMA(wf[m & 3], x0, aq[n & 7][0]); aq[n & 7][1] = MFMA(wf[m & 3], x1, aq[n & 7][1]); } \
            else { ah[(n - 8) % NFT][0] = MFMA(wf[m & 3], x0, ah[(n - 8) % NFT][0]); ah[(n - 8) % NFT][1] = MFMA(wf[m & 3], x1, ah[(n - 8) % NFT][1]); } \
            SBAR();                                                                                             \
        }                                                                                                       \
    } while (0)
    static_assert((2 * NT) % 4 == 0, "fragment window");
    PJ_CHUNK(0, xa, xb); PJ_CHUNK(1, xb, xa); PJ_CHUNK(2, xa, xb); PJ_CHUNK(3, xb, xa); PJ_CHUNK(4, xa, xb);
    PJ_CHUNK(5, xb, xa); PJ_CHUNK(6, xa, xb); PJ_CHUNK(7, xb, xa); PJ_CHUNK(8, xa, xb); PJ_CHUNK(9, xb, xa);
#undef PJ_CHUNK
#undef PJ_RD
#undef PJ_XLOAD
#undef PJ_DMA
#pragma unroll
    for (int a = 0; a < 2; ++a)
#pragma unroll
        for (int v = 0; v < 4; ++v) qf[a][v] = __builtin_bit_cast(bf16x8, pack44s(aq[2 * v][a], aq[2 * v + 1][a], QSCALE));
#pragma unroll
    for (int v = 0; v < NFT / 2; ++v) {
        float bias[8];
#pragma unroll
        for (int t = 0; t < 2; ++t) {
            const u32x2 bb = *(const u32x2*)(b1 + (f0 + 32 * v + 16 * t + 4 * g) * 2);
            bias[4 * t + 0] = bflo(bb[0]); bias[4 * t + 1] = bfhi(bb[0]); bias[4 * t + 2] = bflo(bb[1]); bias[4 * t + 3] = bfhi(bb[1]);
        }
#pragma unroll
        for (int a = 0; a < 2; ++a) {
            f32x4 h0, h1;
#pragma unroll
            for (int i = 0; i < 4; ++i) { h0[i] = mish_f(ah[2 * v][a][i] + bias[i]); h1[i] = mish_f(ah[2 * v + 1][a][i] + bias[4 + i]); }
            *(u32x4*)(ws + WS_CH + ((size_t)(q16_0 + a) * NKS_CH + NKS_C + (f0 >> 5) + v) * 1024 + lane * 16) = pack44(h0, h1);
        }
    }
}

DI void mfma_acc(f32x4& acc, const bf16x8& a, const bf16x8& b) { asm volatile("v_mfma_f32_16x16x32_bf16 %0, %1, %2, %0" : "+v"(acc) : "v"(a), "v"(b)); }
DI void mfma_ini(f32x4& d, const bf16x8& a, const bf16x8& b, const f32x4& c) { asm volatile("v_mfma_f32_16x16x32_bf16 %0, %1, %2, %3" : "=&v"(d) : "v"(a), "v"(b), "v"(c)); }
DI float v_exp(float x) { asm volatile("v_exp_f32 %0, %0" : "+v"(x)); return x; }
DI float v_max3(float a, float b, float c) { float r; asm volatile("v_max3_f32 %0, %1, %2, %3" : "=v"(r) : "v"(a), "v"(b), "v"(c)); return r; }
DI unsigned v_cvt(float lo, float hi) { unsigned r; asm volatile("v_cvt_pk_bf16_f32 %0, %1, %2" : "=v"(r) : "v"(lo), "v"(hi)); return r; }
DI f32x2 v_pkadd(f32x2 a, f32x2 b) { asm volatile("v_pk_add_f32 %0, %0, %1" : "+v"(a) : "v"(b)); return a; }
DI float v_add(float a, float b) { asm volatile("v_add_f32 %0, %0, %1" : "+v"(a) : "v"(b)); return a; }
DI void set_m0(unsigned v) { asm volatile("s_mov_b32 m0, %0" ::"s"(v) : "memory"); }
template <int OFF> DI void dma_nom0(const void* sbase, unsigned voff) { asm volatile("global_load_lds_dwordx4 %0, %1 offset:%2" ::"v"(voff), "s"(sbase), "i"(OFF) : "memory"); }

#define PKADD 1

__global__ __launch_bounds__(512, 2) void k2_attn(const char* __restrict__ wq, const char* __restrict__ w1, const char* __restrict__ b1, char* __restrict__ ws) {
    extern __shared__ __attribute__((aligned(16))) char lds[];
    const unsigned tid = threadIdx.x, lane = tid & 63, c = lane & 15, g = lane >> 4;
    const unsigned wid = rfl(tid >> 6), grp = wid >> 2, w4 = wid & 3;
    char* stamp_base = ws; (void)stamp_base;
    STAMP(6);
    const int b = blockIdx.x, xcd = b & 7, idx = b >> 3;
    const int head = 4 * xcd + (idx & 3), pi = idx >> 2;
    const int HB = 15 - pi, LB = pi;
    const int myblk = grp ? LB : HB;
    bf16x8 qf[2][4];
    {
        const int q16_0 = 8 * myblk + 2 * w4;
        if (head < 22) proj_phase<4>(wq, w1, b1, ws, lds, head, 64 * head, q16_0, qf);
        else           proj_phase<2>(wq, w1, b1, ws, lds, head, 1408 + 32 * (head - 22), q16_0, qf);
    }
    STAMP(7);
    if (grp == 0) {
        char* qh = ws + WS_QH + (size_t)b * 32768 + w4 * 8192 + lane * 16;
#pragma unroll
        for (int a = 0; a < 2; ++a)
#pragma unroll
            for (int v = 0; v < 4; ++v) *(bf16x8*)(qh + (a * 4 + v) * 1024) = qf[a][v];
    }
    const unsigned lds0 = (unsigned)(uintptr_t)lds;
    const unsigned gb = grp * 81920;
    lds_cptr Lk = (lds_cptr)lds + gb + lane * 16;
    lds_cptr Lv = (lds_cptr)lds + gb + 49152 + lane * 16;
    const unsigned m0k = lds0 + gb + w4 * 4096, m0v = m0k + 49152;
    const unsigned dvo = lane * 16;
    const int nL = 2 * pi + 2;
    const int nsw = grp ? nL : 1000;
    const size_t jump = (size_t)(17 - nL) * 16384;
    const char* kptr = uptr(ws + WS_KF + (size_t)head * 524288 + w4 * 4096);
    const char* vptr = uptr(ws + WS_VF + (size_t)head * 524288 + w4 * 4096);
    int q0 = 128 * myblk + 32 * w4;
    int tile = 0;
    asm volatile("s_waitcnt lgkmcnt(0)\n\ts_barrier" ::: "memory");
    set_m0(m0k);
    asm volatile("s_nop 0");
    dma_nom0<0>(kptr, dvo); dma_nom0<1024>(kptr, dvo); dma_nom0<2048>(kptr, dvo); dma_nom0<3072>(kptr, dvo);
    kptr += 16384;
    set_m0(m0k + 16384);
    asm volatile("s_nop 0");
    dma_nom0<0>(kptr, dvo); dma_nom0<1024>(kptr, dvo); dma_nom0<2048>(kptr, dvo); dma_nom0<3072>(kptr, dvo);
    kptr += 16384; if (nsw == 2) kptr += jump;
    f32x4 o[8][2], negm[2];
    float mref[2] = {0.f, 0.f};
    f32x2 lsum[2] = {f32x2{0.f, 0.f}, f32x2{0.f, 0.f}};
    float fct[2] = {1.f, 1.f};
    bf16x8 pf[2][2], kf[6], vf[6];
#pragma unroll
    for (int i = 0; i < 8; ++i) { o[i][0] = f32x4{}; o[i][1] = f32x4{}; }
    negm[0] = f32x4{}; negm[1] = f32x4{};
    float lfin[2] = {1.f, 1.f};
    f32x4 SA[4][2], SB[4][2];
    unsigned ks_cur = 0, ks_nxt = 16384, ks_dma = 32768;
    asm volatile("s_waitcnt vmcnt(4)\n\ts_barrier" ::: "memory");
#define KIDX(n) (((n) & 3) * 4 + ((n) >> 2))
#pragma unroll
    for (int n = 0; n < 4; ++n) kf[n] = lds_frag(Lk + KIDX(n) * 1024);

#define A_FILL0(SP, n) do { const int e_ = 2 * (n), a_ = e_ >> 4, k_ = (e_ >> 2) & 3;                                                \
        if (PKADD) { lsum[a_] += f32x2{SP[k_][a_][e_ & 3], SP[k_][a_][(e_ & 3) + 1]}; }                                               \
        else { float x_ = lsum[a_][0] + SP[k_][a_][e_ & 3], y_ = lsum[a_][1] + SP[k_][a_][(e_ & 3) + 1]; PINR(x_); PINR(y_); lsum[a_] = f32x2{x_, y_}; } SBAR(); } while (0)
#define A_FILL1(SP, n) do { const int a_ = (n) >> 3, u_ = ((n) >> 2) & 1, w_ = (n) & 3;                                                \
        const unsigned cv_ = cvtpk(SP[2 * u_ + (w_ >> 1)][a_][2 * (w_ & 1)], SP[2 * u_ + (w_ >> 1)][a_][2 * (w_ & 1) + 1]); PINR(cv_); \
        if (PKADD) PINR(lsum[(n) >> 3]);                                                                                              \
        u32x4 pw_ = __builtin_bit_cast(u32x4, pf[a_][u_]); pw_[w_] = cv_; pf[a_][u_] = __builtin_bit_cast(bf16x8, pw_); SBAR(); } while (0)
#define MX3(a, b, c) __builtin_fmaxf(__builtin_fmaxf((a), (b)), (c))
#define PHASE_A(SC, SP, HASP, DK)                                                                                                     \
    do {                                                                                                                              \
        TICK(tO);                                                                                                                     \
        lds_cptr kp_ = Lk + ks_cur;                                                                                                   \
        if (DK) set_m0(m0k + ks_dma);                                                                                                 \
        _Pragma("unroll") for (int p = 0; p < 8; ++p) {                                                                               \
            if (p + 2 < 8) { kf[(2 * p + 4) % 6] = lds_frag(kp_ + KIDX(2 * p + 4) * 1024); kf[(2 * p + 5) % 6] = lds_frag(kp_ + KIDX(2 * p + 5) * 1024); } \
            SBAR();                                                                                                                   \
            _Pragma("unroll") for (int z = 0; z < 2; ++z) {                                                                           \
                const int n = 2 * p + 1 - z, m = 2 * p + z, v = n >> 2, kt = n & 3;                                                   \
                if (v == 0) mfma_ini(SC[kt][0], kf[n % 6], qf[0][v], negm[0]); else mfma_acc(SC[kt][0], kf[n % 6], qf[0][v]);         \
                SBAR();                                                                                                               \
                if (HASP) A_FILL0(SP, m);                                                                                             \
                if (v == 0) mfma_ini(SC[kt][1], kf[n % 6], qf[1][v], negm[1]); else mfma_acc(SC[kt][1], kf[n % 6], qf[1][v]);         \
                SBAR();                                                                                                               \
                if (HASP) A_FILL1(SP, m);                                                                                             \
            }                                                                                                                         \
            if (DK) { if (p == 0) dma_nom0<0>(kptr, dvo); if (p == 2) dma_nom0<1024>(kptr, dvo); if (p == 4) dma_nom0<2048>(kptr, dvo); if (p == 6) dma_nom0<3072>(kptr, dvo); } \
            SBAR();                                                                                                                   \
        }                                                                                                                             \
    } while (0)

#define SEL(SC, e) SC[((e) >> 2) & 3][(e) >> 4][(e) & 3]
#define B_FILL(SC, j)                                                               \
    do { const int lo_ = (32 * ((j) - 8)) / 24, hi_ = (32 * ((j) - 7)) / 24;                                                          \
        if (lo_ < hi_) SEL(SC, lo_) = v_exp(SEL(SC, lo_));                                                                            \
        if (lo_ + 1 < hi_) SEL(SC, lo_ + 1) = v_exp(SEL(SC, lo_ + 1)); } while (0)
#define PHASE_B(SC, VS, HASP, FIRST, DK, KN)                                                                                          \
    do {                                                                                                                              \
        TICK(tA);                                                                                                                     \
        if (DK) asm volatile("s_waitcnt vmcnt(4) lgkmcnt(0)\n\ts_barrier" ::: "memory");                                              \
        else asm volatile("s_waitcnt vmcnt(0) lgkmcnt(0)\n\ts_barrier" ::: "memory");                                                 \
        TICK(tX);                                                                                                                     \
        lds_cptr vp_ = Lv + (VS) * 16384;                                                                                  \
        if (HASP) { _Pragma("unroll") for (int n = 0; n < 4; ++n) vf[n] = lds_frag(vp_ + n * 1024); }                                 \
        set_m0(m0v + (1 - (VS)) * 16384);                                                                                             \
        const bool first_ = (FIRST);                                                                                                  \
        if (64 * tile + 63 > q0) {                                                                     \
            asm volatile("s_nop 7");                                                                                                  \
            _Pragma("unroll") for (int a = 0; a < 2; ++a) {                                                                           \
                const int dq = q0 - 64 * tile + 16 * a + (int)c - 4 * (int)g;                                                         \
                _Pragma("unroll") for (int kt = 0; kt < 4; ++kt) _Pragma("unroll") for (int i = 0; i < 4; ++i)                        \
                    if (16 * kt + i > dq) SC[kt][a][i] = -__builtin_inff();                                                            \
            }                                                                                                                         \
            asm volatile("s_nop 1");                                                                                                  \
        }                                                                                                                             \
        float rm0_ = 0.f, rm1_ = 0.f;                                                                                                 \
        bool pend_ = false;                                                                                                           \
        lds_cptr kn_ = Lk + ks_nxt;                                                                                                   \
        _Pragma("unroll") for (int p = 0; p < 8; ++p) {                                                                               \
            if (p == 2) {                                                                                                \
                if (__builtin_expect(first_ || __any(rm0_ > THR || rm1_ > THR), 0)) {                                                 \
                    float r_[2] = {x4_max(rm0_), x4_max(rm1_)};                                                                       \
                    _Pragma("unroll") for (int a = 0; a < 2; ++a) {                                                                   \
                        const float dl = first_ ? (r_[a] == -__builtin_inff() ? 0.f : r_[a]) : __builtin_fmaxf(r_[a], 0.f);           \
                        mref[a] += dl;                                                                                                \
                        const float f = first_ ? 1.f : __builtin_amdgcn_exp2f(-dl);                                                   \
                        fct[a] = f; lsum[a] *= f;                                                                                     \
                        _Pragma("unroll") for (int kt = 0; kt < 4; ++kt) _Pragma("unroll") for (int i = 0; i < 4; ++i) SC[kt][a][i] -= dl; \
                        const float nm = -mref[a]; negm[a] = f32x4{nm, nm, nm, nm};                                                   \
                    }                                                                                                                 \
                    pend_ = !first_;                                                                                                  \
                    asm volatile("s_nop 1");                                                                                          \
                }                                                                                                                     \
            }                                                                                                                         \
            if (HASP && p + 2 < 8) { vf[(2 * p + 4) % 6] = lds_frag(vp_ + (2 * p + 4) * 1024); vf[(2 * p + 5) % 6] = lds_frag(vp_ + (2 * p + 5) * 1024); } \
            if ((KN) && p >= 6) { kf[2 * (p - 6)] = lds_frag(kn_ + KIDX(2 * (p - 6)) * 1024); kf[2 * (p - 6) + 1] = lds_frag(kn_ + KIDX(2 * (p - 6) + 1) * 1024); } \
            SBAR();                                                                                                                   \
            _Pragma("unroll") for (int z = 0; z < 2; ++z) {                                                                           \
                const int n = 2 * p + 1 - z, m = 2 * p + z, u = n >> 3, dt = n & 7;                                                   \
                if (HASP) mfma_acc(o[dt][0], vf[n % 6], pf[0][u]);                                                                    \
                SBAR();                                                                                                               \
                if (m == 0) { rm0_ = MX3(SC[0][0][0], SC[0][0][1], SC[0][0][2]); rm1_ = MX3(SC[0][1][0], SC[0][1][1], SC[0][1][2]); PINR(rm0_); PINR(rm1_); } \
                else if (m < 4) { rm0_ = MX3(rm0_, SC[m][0][1], SC[m][0][2]); rm1_ = MX3(rm1_, SC[m][1][1], SC[m][1][2]); PINR(rm0_); PINR(rm1_); } \
                else B_FILL(SC, 2 * m);                                                                                               \
                SBAR();                                                                                                               \
                if (HASP) mfma_acc(o[dt][1], vf[n % 6], pf[1][u]);                                                                    \
                SBAR();                                                                                                               \
                if (m < 4) { rm0_ = MX3(rm0_, SC[m][0][3], SC[m][0][0]); rm1_ = MX3(rm1_, SC[m][1][3], SC[m][1][0]); PINR(rm0_); PINR(rm1_); } \
                else B_FILL(SC, 2 * m + 1);                                                                                           \
                SBAR();                                                                                                               \
            }                                                                                                                         \
            if (p == 0) dma_nom0<0>(vptr, dvo); if (p == 1) dma_nom0<1024>(vptr, dvo); if (p == 2) dma_nom0<2048>(vptr, dvo); if (p == 3) dma_nom0<3072>(vptr, dvo); \
            SBAR();                                                                                                                   \
        }                                                                                                                             \
        TICK(tB);                                                                                                                     \
        if (__builtin_expect(pend_, 0)) {                                                                                             \
            asm volatile("s_nop 7");                                                                                                  \
            _Pragma("unroll") for (int i = 0; i < 8; ++i) { o[i][0] *= fct[0]; o[i][1] *= fct[1]; }                                   \
            asm volatile("s_nop 1");                                                                                                  \
        }                                                                                                                             \
    } while (0)
#define ADVANCE(t)                                                                                                                    \
    do {                                                                                                                              \
        const unsigned r_ = ks_cur; ks_cur = ks_nxt; ks_nxt = ks_dma; ks_dma = r_;                                                    \
        kptr += 16384 + ((t) + 3 == nsw ? jump : 0);                                                                                  \
        vptr += 16384 + ((t) + 1 == nsw ? jump : 0);                                                                                  \
        tile += 1 + ((t) + 1 == nsw ? 17 - nL : 0);                                                                                   \
    } while (0)
#define STORE_CTX(blk, L0, L1)                                                                                                        \
    do {                                                                                                                              \
        const float i0_ = __builtin_amdgcn_rcpf(L0), i1_ = __builtin_amdgcn_rcpf(L1);                                                 \
        char* cp_ = ws + WS_CH + ((size_t)(8 * (blk) + 2 * w4) * NKS_CH + 4 * head) * 1024 + lane * 16;                               \
        _Pragma("unroll") for (int v = 0; v < 4; ++v) {                                                                               \
            *(u32x4*)(cp_ + v * 1024) = pack44s(o[2 * v][0], o[2 * v + 1][0], i0_);                                                   \
            *(u32x4*)(cp_ + (size_t)NKS_CH * 1024 + v * 1024) = pack44s(o[2 * v][1], o[2 * v + 1][1], i1_);                           \
        }                                                                                                                             \
    } while (0)
#define STEP2(t, DK1, DK2, KN2)                                                                                                       \
    do {                                                                                                                              \
        PHASE_A(SB, SA, true, DK1);                                                                                                   \
        const bool sw = ((t) + 1 == nsw);                                                                                             \
        if (sw) {                          \
            const char* qs_ = uptr(ws + WS_QH + (size_t)b * 32768 + w4 * 8192);                                                       \
            asm volatile("global_load_dwordx4 %0, %8, %9\n\tglobal_load_dwordx4 %1, %8, %9 offset:1024\n\tglobal_load_dwordx4 %2, %8, %9 offset:2048\n\tglobal_load_dwordx4 %3, %8, %9 offset:3072" \
                         : "+v"(qf[0][0]), "+v"(qf[0][1]), "+v"(qf[0][2]), "+v"(qf[0][3]), "+v"(qf[1][0]), "+v"(qf[1][1]), "+v"(qf[1][2]), "+v"(qf[1][3]) \
                         : "v"(dvo), "s"(qs_) : "memory");                                                                            \
            asm volatile("global_load_dwordx4 %4, %8, %9\n\tglobal_load_dwordx4 %5, %8, %9 offset:1024\n\tglobal_load_dwordx4 %6, %8, %9 offset:2048\n\tglobal_load_dwordx4 %7, %8, %9 offset:3072" \
                         : "+v"(qf[0][0]), "+v"(qf[0][1]), "+v"(qf[0][2]), "+v"(qf[0][3]), "+v"(qf[1][0]), "+v"(qf[1][1]), "+v"(qf[1][2]), "+v"(qf[1][3]) \
                         : "v"(dvo), "s"(qs_ + 4096) : "memory");                                                                     \
        }                                                                                                                             \
        PHASE_B(SB, 0, true, false, DK1, true);                                                                                       \
        ADVANCE(t);                                                                                                                   \
        if (sw) {                                                                                                                     \
            negm[0] = f32x4{}; negm[1] = f32x4{}; mref[0] = mref[1] = 0.f;                                                            \
            asm volatile("s_waitcnt vmcnt(4)" : "+v"(qf[0][0]), "+v"(qf[0][1]), "+v"(qf[0][2]), "+v"(qf[0][3]), "+v"(qf[1][0]), "+v"(qf[1][1]), "+v"(qf[1][2]), "+v"(qf[1][3]), "+v"(negm[0]), "+v"(negm[1])::"memory"); \
            asm volatile("s_nop 1");                                                                                                  \
        }                                                                                                                             \
        PHASE_A(SA, SB, true, DK2);                                                                                                   \
        if (sw) {                                                                                                                     \
            lfin[0] = x4_sum(lsum[0][0] + lsum[0][1]); lfin[1] = x4_sum(lsum[1][0] + lsum[1][1]);                                     \
            lsum[0] = f32x2{0.f, 0.f}; lsum[1] = f32x2{0.f, 0.f};                                                                     \
            q0 = 128 * HB + 32 * w4;                                                                                                  \
        }                                                                                                                             \
        PHASE_B(SA, 1, true, sw, DK2, KN2);                                                                                           \
        ADVANCE((t) + 1);                                                                                                             \
        if (sw) {                                                                                                                     \
            asm volatile("s_nop 7");                                                                                                  \
            STORE_CTX(LB, lfin[0], lfin[1]);                                                                                          \
            _Pragma("unroll") for (int i = 0; i < 8; ++i) { o[i][0] = f32x4{}; o[i][1] = f32x4{}; }                                   \
            asm volatile("s_nop 1" : "+v"(o[0][0]), "+v"(o[0][1]));                                                                   \
        }                                                                                                                             \
    } while (0)

    PHASE_A(SA, SB, false, true);
    PHASE_B(SA, 1, false, true, true, true);
    ADVANCE(0);
    for (int t = 1; t < 15; t += 2) STEP2(t, true, true, true);
    STEP2(15, false, false, false);
    {
        asm volatile("s_nop 7");
#pragma unroll
        for (int e = 0; e < 32; e += 2) lsum[e >> 4] += f32x2{SEL(SA, e), SEL(SA, e + 1)};
#pragma unroll
        for (int a = 0; a < 2; ++a)
#pragma unroll
            for (int u = 0; u < 2; ++u) pf[a][u] = __builtin_bit_cast(bf16x8, pack44(SA[2 * u][a], SA[2 * u + 1][a]));
        asm volatile("s_waitcnt vmcnt(0) lgkmcnt(0)\n\ts_barrier" : "+v"(pf[0][0]), "+v"(pf[0][1]), "+v"(pf[1][0]), "+v"(pf[1][1])::"memory");
        lds_cptr vp_ = Lv;
#pragma unroll
        for (int hb = 0; hb < 2; ++hb) {
            bf16x8 vr[8];
#pragma unroll
            for (int n = 0; n < 8; ++n) vr[n] = lds_frag(vp_ + (8 * hb + n) * 1024);
            SBAR();
#pragma unroll
            for (int n = 0; n < 8; ++n) { mfma_acc(o[n][0], vr[n], pf[0][hb]); mfma_acc(o[n][1], vr[n], pf[1][hb]); }
        }
        asm volatile("s_nop 7");
    }
    STAMP(8);
    {
        float ls[2] = {x4_sum(lsum[0][0] + lsum[0][1]), x4_sum(lsum[1][0] + lsum[1][1])};
        float mm[2] = {ls[0] > 0.f ? mref[0] : -__builtin_inff(), ls[1] > 0.f ? mref[1] : -__builtin_inff()};
        asm volatile("s_waitcnt lgkmcnt(0)\n\ts_barrier" ::: "memory");
        lds_ptr X = (lds_ptr)lds + wid * 8192 + lane * 16;
        if (grp == 0) {
#pragma unroll
            for (int i = 0; i < 4; ++i) { *(__attribute__((address_space(3))) f32x4*)(X + (2 * i) * 1024) = o[4 + i][0]; *(__attribute__((address_space(3))) f32x4*)(X + (2 * i + 1) * 1024) = o[4 + i][1]; }
        } else {
#pragma unroll
            for (int i = 0; i < 4; ++i) { *(__attribute__((address_space(3))) f32x4*)(X + (2 * i) * 1024) = o[i][0]; *(__attribute__((address_space(3))) f32x4*)(X + (2 * i + 1) * 1024) = o[i][1]; }
        }
        lds_ptr Y = (lds_ptr)lds + 65536 + wid * 1024 + lane * 16;
        *(__attribute__((address_space(3))) f32x4*)Y = f32x4{mm[0], mm[1], ls[0], ls[1]};
        asm volatile("s_waitcnt lgkmcnt(0)\n\ts_barrier" ::: "memory");
        const unsigned ow = wid ^ 4;
        lds_cptr Xo = (lds_cptr)lds + ow * 8192 + lane * 16;
        const f32x4 oml = *(const __attribute__((address_space(3))) f32x4*)((lds_cptr)lds + 65536 + ow * 1024 + lane * 16);
        float fs[2], fo[2];
#pragma unroll
        for (int a = 0; a < 2; ++a) {
            const float mx = __builtin_fmaxf(mm[a], oml[a]);
            const float e0 = __builtin_amdgcn_exp2f(mm[a] - mx), e1 = __builtin_amdgcn_exp2f(oml[a] - mx);
            const float inv = __builtin_amdgcn_rcpf(ls[a] * e0 + oml[2 + a] * e1);
            fs[a] = e0 * inv; fo[a] = e1 * inv;
        }
        char* cp = ws + WS_CH + ((size_t)(8 * HB + 2 * w4) * NKS_CH + 4 * head + 2 * grp) * 1024 + lane * 16;
#pragma unroll
        for (int v = 0; v < 2; ++v)
#pragma unroll
            for (int a = 0; a < 2; ++a) {
                const f32x4 p0 = *(const __attribute__((address_space(3))) f32x4*)(Xo + (2 * (2 * v) + a) * 1024);
                const f32x4 p1 = *(const __attribute__((address_space(3))) f32x4*)(Xo + (2 * (2 * v + 1) + a) * 1024);
                const f32x4 s0 = grp ? o[4 + 2 * v][a] : o[2 * v][a];
                const f32x4 s1 = grp ? o[4 + 2 * v + 1][a] : o[2 * v + 1][a];
                *(u32x4*)(cp + (size_t)a * NKS_CH * 1024 + v * 1024) = pack44(s0 * fs[a] + p0 * fo[a], s1 * fs[a] + p1 * fo[a]);
            }
    }
    STAMP(9);
#undef KIDX
#undef A_FILL0
#undef A_FILL1
#undef MX3
#undef PHASE_A
#undef PHASE_B
#undef SEL
#undef B_FILL
#undef ADVANCE
#undef STORE_CTX
#undef STEP2
}

typedef __attribute__((address_space(1))) unsigned gu32;
constexpr int K3_SLOT = 36864;
DI float silu_f(float v) { return v * __builtin_amdgcn_rcpf(1.0f + __builtin_amdgcn_exp2f(-1.4426950408889634f * v)); }

template <int KS>
DI void k3_finish(const f32x4 (&res)[10], const f32x4 (&res2)[10], const char* __restrict__ x, char* __restrict__ out, char* __restrict__ ws,
                  int grp64, int qt, int mt) {
    const unsigned tid = threadIdx.x, lane = tid & 63, c = lane & 15, g = lane >> 4;
    const unsigned wid = rfl(tid >> 6), grp = wid >> 2, w4 = wid & 3, qh = w4 >> 1, mh = w4 & 1;
    const __amdgpu_buffer_rsrc_t rs = __builtin_amdgcn_make_buffer_rsrc(ws + WS_PP + (size_t)grp64 * (5 * 8 * 10240), (short)0, 5 * 8 * 10240, 0x00020000);
    const unsigned vo = wid * 10240 + lane * 16;
#pragma unroll
    for (int i = 0; i < 10; ++i)
        if ((i & 3) != KS) {
            __builtin_amdgcn_raw_buffer_store_b128(__builtin_bit_cast(u32x4, res[i]), rs, vo + i * 1024, (KS < 2 ? KS : KS + 1) * 81920, 16);
            if (KS == 2) __builtin_amdgcn_raw_buffer_store_b128(__builtin_bit_cast(u32x4, res2[i]), rs, vo + i * 1024, 2 * 81920, 16);
        }
    constexpr int NO = KS < 2 ? 3 : 2;
    u32x2 xv[NO];
    unsigned oo[NO];
#pragma unroll
    for (int j = 0; j < NO; ++j) {
        const int i = KS + 4 * j, a = i / 5, n = i % 5;
        oo[j] = (16 * (8 * qt + 4 * qh + 2 * grp + a) + c) * (DM * 2) + (16 * (10 * mt + 5 * mh + n) + 4 * g) * 2;
        xv[j] = *(const u32x2*)(x + oo[j]);
    }
    asm volatile("s_waitcnt vmcnt(0)\n\ts_barrier" ::: "memory");
    gu32* fl = (gu32*)(ws + WS_FL + (size_t)grp64 * 4 * 128);
    if (tid == 0) __hip_atomic_store(fl + KS * 32, 1u, __ATOMIC_RELAXED, __HIP_MEMORY_SCOPE_AGENT);
    for (unsigned spins = 0; spins < (1u << 22); ++spins) {
        unsigned v = 1u;
        if (lane < 4 && lane != KS) v = __hip_atomic_load(fl + lane * 32, __ATOMIC_RELAXED, __HIP_MEMORY_SCOPE_AGENT);
        if (__all(v != 0u)) break;
        __builtin_amdgcn_s_sleep(1);
    }
    asm volatile("" ::: "memory");
    f32x4 p[NO][5];
#pragma unroll
    for (int j = 0; j < NO; ++j)
#pragma unroll
        for (int st = 0; st < 5; ++st) {
            const int i = KS + 4 * j;
            const bool own = (st < 3 && st == KS) || (st == 3 && KS == 2) || (st == 4 && KS == 3);
            if (own) p[j][st] = (KS == 2 && st == 2) ? res2[i] : res[i];
            else p[j][st] = __builtin_bit_cast(f32x4, __builtin_amdgcn_raw_buffer_load_b128(rs, vo + i * 1024, st * 81920, 16));
        }
#pragma unroll
    for (int j = 0; j < NO; ++j) {
        const f32x4 sv = p[j][0] + p[j][1] + p[j][2], bv = p[j][3] + p[j][4];
        const float o0 = bflo(xv[j][0]) + sv[0] * silu_f(bv[0]), o1 = bfhi(xv[j][0]) + sv[1] * silu_f(bv[1]);
        const float o2 = bflo(xv[j][1]) + sv[2] * silu_f(bv[2]), o3 = bfhi(xv[j][1]) + sv[3] * silu_f(bv[3]);
        u32x2 w; w[0] = cvtpk(o0, o1); w[1] = cvtpk(o2, o3);
        *(u32x2*)(out + oo[j]) = w;
    }
}

__global__ __launch_bounds__(512, 2) void k3_out(const char* __restrict__ x, const char* __restrict__ wo, const char* __restrict__ w2, char* __restrict__ ws,
                                                 char* __restrict__ out) {
    extern __shared__ __attribute__((aligned(16))) char lds[];
    const unsigned tid = threadIdx.x, lane = tid & 63, g = lane >> 4;
    const unsigned wid = rfl(tid >> 6), grp = wid >> 2, w4 = wid & 3, qh = w4 >> 1, mh = w4 & 1;
    char* stamp_base = ws; (void)stamp_base;
    STAMP(10);
    const int b = blockIdx.x, xcd = b & 7, idx = b >> 3;
    const int ks = xcd >> 1, qt = 8 * (xcd & 1) + (idx >> 2), mt = idx & 3;
    const int st0 = ks == 0 ? 0 : ks == 1 ? 23 : ks == 2 ? 46 : 68, nst = ks == 2 ? 22 : 23;
    const int sw = ks == 2 ? 18 : 1000;
    const unsigned lds0 = (unsigned)(uintptr_t)lds;
    lds_cptr L = (lds_cptr)lds;
    const char* aptr = uptr(ws + WS_CH + ((size_t)(8 * qt + wid) * NKS_CH + 2 * st0) * 1024);
    const char* w2b = uptr(w2 + mt * 320);
    const char* wptr = st0 < 64 ? uptr(wo + (size_t)st0 * 64 * 1280 + mt * 320) : w2b + (size_t)(st0 - 64) * 64 * 1280;
    int sdma = st0;
    const unsigned avo = lane * 16;
    unsigned wvo[3];
    const unsigned p0 = grp ? 12 + 2 * w4 : 3 * w4;
#pragma unroll
    for (int i = 0; i < 3; ++i) {
        const unsigned o = (p0 + i) * 1024 + lane * 16, row = (o / 320) & 63, c16 = (o % 320) >> 4;
        wvo[i] = row * 1280 + ((c16 ^ (((row >> 2) & 1) << 1)) << 4) - 1024 * i + (i == 2 && grp ? 2048 : 0);
    }
    const unsigned m0a = lds0 + wid * 2048, m0w = lds0 + 16384 + p0 * 1024;
#define K3_ADV() do { ++sdma; aptr += 2048; wptr = sdma == 64 ? w2b : wptr + 81920; } while (0)
#define K3_DMA(slot) do { set_m0(m0a + (slot)); asm volatile("s_nop 0"); dma_nom0<0>(aptr, avo); dma_nom0<1024>(aptr, avo); \
        set_m0(m0w + (slot)); asm volatile("s_nop 0"); dma_nom0<0>(wptr, wvo[0]); dma_nom0<1024>(wptr, wvo[1]); if (grp == 0) dma_nom0<2048>(wptr, wvo[2]); K3_ADV(); } while (0)
    K3_DMA(0); K3_DMA(K3_SLOT); K3_DMA(2 * K3_SLOT); K3_DMA(3 * K3_SLOT);
    const unsigned ard = (8 * qh + grp) * 1024 + lane * 16;
    const unsigned q = (lane & 15) >> 2, p = lane & 3;
    const unsigned wrow = 16384 + (32 * grp + 4 * g + q) * 320 + 160 * mh + 8 * p;
    const unsigned sgn = ((g & 1) ? 32u : 0u);
    const unsigned wrdE = (mh & 1) ? wrow - sgn : wrow + sgn;
    const unsigned wrdO = (mh & 1) ? wrow + sgn : wrow - sgn;
    f32x4 acc[5][4], acc2[5][4];
#pragma unroll
    for (int n = 0; n < 5; ++n)
#pragma unroll
        for (int a = 0; a < 4; ++a) { acc[n][a] = f32x4{}; acc2[n][a] = f32x4{}; }
    bf16x8 af[2][4], wf[4], w4f[2];
#define K3_RW(slot, n) tr_frag(L + (slot) + (((n) & 1) ? wrdO : wrdE) + 32 * (n), L + (slot) + (((n) & 1) ? wrdO : wrdE) + 32 * (n) + 16 * 320)
#define K3_RA(slot, a) lds_frag(L + (slot) + ard + (a) * 2048)
    if (grp == 0) asm volatile("s_waitcnt vmcnt(15)\n\ts_barrier" ::: "memory"); else asm volatile("s_waitcnt vmcnt(12)\n\ts_barrier" ::: "memory");
    STAMP(11);
#pragma unroll
    for (int a = 0; a < 4; ++a) af[0][a] = K3_RA(0, a);
#pragma unroll
    for (int n = 0; n < 4; ++n) wf[n] = K3_RW(0, n);
    w4f[0] = K3_RW(0, 4);
    unsigned cur = 0;
#define K3_ITER(P, s)                                                                                                   \
    do {                                                                                                                \
        const int rem_ = nst - (s);                                                  \
        if (rem_ >= 4) { if (grp == 0) asm volatile("s_waitcnt vmcnt(10) lgkmcnt(0)\n\ts_barrier" ::: "memory"); else asm volatile("s_waitcnt vmcnt(8) lgkmcnt(0)\n\ts_barrier" ::: "memory"); } \
        else if (rem_ == 3) { if (grp == 0) asm volatile("s_waitcnt vmcnt(5) lgkmcnt(0)\n\ts_barrier" ::: "memory"); else asm volatile("s_waitcnt vmcnt(4) lgkmcnt(0)\n\ts_barrier" ::: "memory"); } \
        else asm volatile("s_waitcnt vmcnt(0) lgkmcnt(0)\n\ts_barrier" ::: "memory");                                   \
        const unsigned nxt_ = cur == 3 * K3_SLOT ? 0u : cur + K3_SLOT;                                                  \
        const bool rd_ = rem_ > 1, dm_ = rem_ > 4;                                                                      \
        _Pragma("unroll") for (int n = 0; n < 5; ++n) {                                                                 \
            _Pragma("unroll") for (int a = 0; a < 4; ++a) {                                                             \
                mfma_acc(acc[n][a], n < 4 ? wf[n & 3] : w4f[P], af[P][a]);                                              \
                SBAR();                                                                                                 \
                if (rd_) {                                                                                              \
                    if (n == 0 && a == 0) w4f[1 - (P)] = K3_RW(nxt_, 4);                                                \
                    if (n < 4 && a == 1) af[1 - (P)][n & 3] = K3_RA(nxt_, n & 3);                                       \
                    if (n >= 1 && a == 0) wf[(n - 1) & 3] = K3_RW(nxt_, n - 1);                                         \
                }                                                                                                       \
                if (dm_) {                                                                                              \
                    if (n == 0 && a == 2) { set_m0(m0a + cur); }                                                        \
                    if (n == 0 && a == 3) dma_nom0<0>(aptr, avo);                                                       \
                    if (n == 1 && a == 2) dma_nom0<1024>(aptr, avo);                                                    \
                    if (n == 1 && a == 3) set_m0(m0w + cur);                                                            \
                    if (n == 2 && a == 2) dma_nom0<0>(wptr, wvo[0]);                                                    \
                    if (n == 3 && a == 2) dma_nom0<1024>(wptr, wvo[1]);                                                 \
                    if (n == 4 && a == 1) { if (grp == 0) dma_nom0<2048>(wptr, wvo[2]); }                               \
                }                                                                                                       \
                SBAR();                                                                                                 \
            }                                                                                                           \
        }                                                                                                               \
        if (dm_) K3_ADV();                                                                                              \
        cur = nxt_;                                                                                                     \
    } while (0)
    int s = 0;
    for (; s + 1 < nst; s += 2) {
        if (s == sw) {
            asm volatile("s_nop 7\n\ts_nop 7");
#pragma unroll
            for (int n = 0; n < 5; ++n)
#pragma unroll
                for (int a = 0; a < 4; ++a) { acc2[n][a] = acc[n][a]; acc[n][a] = f32x4{}; PIN(acc2[n][a]); PIN(acc[n][a]); }
            asm volatile("s_nop 1");
        }
        K3_ITER(0, s);
        K3_ITER(1, s + 1);
    }
    if (s < nst) K3_ITER(0, s);
    asm volatile("s_nop 7\n\ts_nop 7");
    STAMP(12);
    f32x4 res[10], res2[10];
    {
        lds_ptr X = (lds_ptr)lds + wid * 10240 + lane * 16;
        lds_cptr Y = (lds_cptr)lds + (wid ^ 4) * 10240 + lane * 16;
#pragma unroll
        for (int a = 0; a < 2; ++a)
#pragma unroll
            for (int n = 0; n < 5; ++n) {
                *(__attribute__((address_space(3))) f32x4*)(X + (a * 5 + n) * 1024) = grp ? acc[n][a] : acc[n][2 + a];
                if (ks == 2) *(__attribute__((address_space(3))) f32x4*)(X + 81920 + (a * 5 + n) * 1024) = grp ? acc2[n][a] : acc2[n][2 + a];
            }
        asm volatile("s_waitcnt lgkmcnt(0)\n\ts_barrier" ::: "memory");
#pragma unroll
        for (int a = 0; a < 2; ++a)
#pragma unroll
            for (int n = 0; n < 5; ++n) {
                res[a * 5 + n] = (grp ? acc[n][2 + a] : acc[n][a]) + *(const __attribute__((address_space(3))) f32x4*)(Y + (a * 5 + n) * 1024);
                res2[a * 5 + n] = f32x4{};
                if (ks == 2) res2[a * 5 + n] = (grp ? acc2[n][2 + a] : acc2[n][a]) + *(const __attribute__((address_space(3))) f32x4*)(Y + 81920 + (a * 5 + n) * 1024);
            }
    }
    const int grp64 = qt * 4 + mt;
    if (ks == 0) k3_finish<0>(res, res2, x, out, ws, grp64, qt, mt);
    else if (ks == 1) k3_finish<1>(res, res2, x, out, ws, grp64, qt, mt);
    else if (ks == 2) k3_finish<2>(res, res2, x, out, ws, grp64, qt, mt);
    else k3_finish<3>(res, res2, x, out, ws, grp64, qt, mt);
    STAMP(13);
#undef K3_ADV
#undef K3_DMA
#undef K3_RW
#undef K3_RA
#undef K3_ITER
}

extern "C" void kernel_launch(void* const* d_in, const int* in_sizes, int n_in, void* d_out, int out_size, void* d_ws, size_t ws_size,
                              hipStream_t stream) {
    const char* x = (const char*)d_in[0];
    const char* ckv = (const char*)d_in[1];
    const char* wuk = (const char*)d_in[3];
    const char* wuv = (const char*)d_in[4];
    char* ws = (char*)d_ws;
    (void)hipFuncSetAttribute((const void*)k1_kvproj, hipFuncAttributeMaxDynamicSharedMemorySize, 131072);
    k1_kvproj<<<256, 512, 131072, stream>>>(x, ckv, wuk, wuv, ws);
    (void)hipFuncSetAttribute((const void*)k2_attn, hipFuncAttributeMaxDynamicSharedMemorySize, 163840);
    k2_attn<<<256, 512, 163840, stream>>>((const char*)d_in[2], (const char*)d_in[7], (const char*)d_in[8], ws);
    (void)hipFuncSetAttribute((const void*)k3_out, hipFuncAttributeMaxDynamicSharedMemorySize, 163840);
    k3_out<<<256, 512, 163840, stream>>>(x, (const char*)d_in[5], (const char*)d_in[9], ws, (char*)d_out);
}
```

```cpp
#include <hip/hip_runtime.h>
#include <hip/hip_bf16.h>
#include <stdint.h>

typedef __bf16 bf16x8 __attribute__((ext_vector_type(8)));
typedef __bf16 bf16x2 __attribute__((ext_vector_type(2)));
typedef short s16x4 __attribute__((ext_vector_type(4)));
typedef short s16x8 __attribute__((ext_vector_type(8)));
typedef short v4i16_t __attribute__((ext_vector_type(4)));
typedef float f32x16 __attribute__((ext_vector_type(16)));
typedef float f32x4 __attribute__((ext_vector_type(4)));
typedef float f32x2 __attribute__((ext_vector_type(2)));
typedef unsigned u32x4 __attribute__((ext_vector_type(4)));
typedef unsigned u32x2 __attribute__((ext_vector_type(2)));
typedef __attribute__((address_space(3))) const char* lds_cptr;
typedef __attribute__((address_space(3))) char* lds_ptr;

#define DI __device__ __forceinline__
#define SBAR() __builtin_amdgcn_sched_barrier(0)
#define MFMA(a, b, c) __builtin_amdgcn_mfma_f32_16x16x32_bf16((a), (b), (c), 0, 0, 0)
#define PIN(x) asm volatile("" : "+v"(x))
#define PINR(x) asm volatile("" ::"v"(x))

constexpr int S_ = 2048, NH = 32, HD = 128, DM = 640, DC = 512, FF = 1728;
constexpr int NKS_X = DM / 32;
constexpr int NKS_C = (NH * HD) / 32;
constexpr int NKS_H = FF / 32;
constexpr int NKS_CH = NKS_C + NKS_H;
constexpr size_t WS_XF = 0;
constexpr size_t WS_KF = WS_XF + (size_t)128 * NKS_X * 1024;
constexpr size_t WS_VF = WS_KF + (size_t)NH * 128 * 4 * 1024;
constexpr size_t WS_CH = WS_VF + (size_t)NH * 64 * 8 * 1024;
constexpr size_t WS_QH = WS_CH + (size_t)128 * NKS_CH * 1024;
constexpr size_t WS_PP = WS_QH + (size_t)256 * 32768;
constexpr size_t PP_BYTES = (size_t)64 * 5 * 8 * 5 * 1024;
constexpr size_t WS_FL = WS_PP + PP_BYTES;
constexpr size_t WS_END = WS_FL + (size_t)256 * 128;

DI unsigned rfl(unsigned x) { return __builtin_amdgcn_readfirstlane(x); }
#define STAMP(i)
#define TICK(acc)
DI const char* uptr(const char* p) { const unsigned long long v = (unsigned long long)p; const unsigned lo = rfl((unsigned)v), hi = rfl((unsigned)(v >> 32)); return (const char*)(((unsigned long long)hi << 32) | lo); }
DI unsigned cvtpk(float lo, float hi) { f32x2 v = {lo, hi}; bf16x2 b = __builtin_convertvector(v, bf16x2); return __builtin_bit_cast(unsigned, b); }
DI bf16x8 lds_frag(lds_cptr p) { return *(const __attribute__((address_space(3))) bf16x8*)p; }
DI s16x4 lds_tr(lds_cptr p) { return __builtin_bit_cast(s16x4, __builtin_amdgcn_ds_read_tr16_b64_v4i16((__attribute__((address_space(3))) v4i16_t*)p)); }
DI bf16x8 tr_frag(lds_cptr p0, lds_cptr p1) { s16x4 lo = lds_tr(p0), hi = lds_tr(p1); s16x8 v = __builtin_shufflevector(lo, hi, 0, 1, 2, 3, 4, 5, 6, 7); return __builtin_bit_cast(bf16x8, v); }
template <int OFF> DI void dma1(unsigned m0v, const void* sbase, unsigned voff) {
    asm volatile("s_mov_b32 m0, %0\n\ts_nop 0\n\tglobal_load_lds_dwordx4 %1, %2 offset:%3" ::"s"(m0v), "v"(voff), "s"(sbase), "i"(OFF) : "memory");
}
DI u32x4 pack44(const f32x4& a, const f32x4& b) { u32x4 w; w[0] = cvtpk(a[0], a[1]); w[1] = cvtpk(a[2], a[3]); w[2] = cvtpk(b[0], b[1]); w[3] = cvtpk(b[2], b[3]); return w; }
DI u32x4 pack44s(const f32x4& a, const f32x4& b, float sc) { u32x4 w; w[0] = cvtpk(a[0] * sc, a[1] * sc); w[1] = cvtpk(a[2] * sc, a[3] * sc); w[2] = cvtpk(b[0] * sc, b[1] * sc); w[3] = cvtpk(b[2] * sc, b[3] * sc); return w; }
DI u32x4 pack8(const f32x16& a, int b, float sc) {
    u32x4 w; w[0] = cvtpk(a[b + 0] * sc, a[b + 1] * sc); w[1] = cvtpk(a[b + 2] * sc, a[b + 3] * sc);
    w[2] = cvtpk(a[b + 4] * sc, a[b + 5] * sc); w[3] = cvtpk(a[b + 6] * sc, a[b + 7] * sc); return w;
}
DI u32x4 pack8n(const f32x16& a, int b) {
    u32x4 w; w[0] = cvtpk(a[b + 0], a[b + 1]); w[1] = cvtpk(a[b + 2], a[b + 3]); w[2] = cvtpk(a[b + 4], a[b + 5]); w[3] = cvtpk(a[b + 6], a[b + 7]); return w;
}
DI float bf2f(unsigned short u) { return __uint_as_float((unsigned)u << 16); }
DI float bflo(unsigned w) { return __uint_as_float(w << 16); }
DI float bfhi(unsigned w) { return __uint_as_float(w & 0xffff0000u); }
DI float xhalf_sum(float v) { auto r = __builtin_amdgcn_permlane32_swap(__float_as_uint(v), __float_as_uint(v), false, false); return __uint_as_float(r[0]) + __uint_as_float(r[1]); }
DI float xhalf_max(float v) { auto r = __builtin_amdgcn_permlane32_swap(__float_as_uint(v), __float_as_uint(v), false, false); return __builtin_fmaxf(__uint_as_float(r[0]), __uint_as_float(r[1])); }

DI const char* warm_src(const char* wa, const char* wb, unsigned id) {
    const unsigned j = id - 5120u;
    return uptr(id < 5120u ? wa + (size_t)id * 1024 : wb + (size_t)(j < 2159u ? j : 2159u) * 1024);
}
DI void warm_load(u32x4& d, const char* p, unsigned voff) { asm volatile("s_nop 4\n\tglobal_load_dwordx4 %0, %1, %2" : "+v"(d) : "v"(voff), "s"(p) : "memory"); }

#define PREFETCH 1
template <bool VT, class F>
DI void k1_mma(f32x4 (&acc)[8][4], int half, const bf16x8 (&c)[4], const bf16x8 (&w)[4], F&& filler) {
#pragma unroll
    for (int rt = 0; rt < 4; ++rt)
#pragma unroll
        for (int nt = 0; nt < 4; ++nt) {
            if (half == 0) acc[rt][nt] = VT ? MFMA(c[rt], w[nt], acc[rt][nt]) : MFMA(w[nt], c[rt], acc[rt][nt]);
            else acc[4 + rt][nt] = VT ? MFMA(c[rt], w[nt], acc[4 + rt][nt]) : MFMA(w[nt], c[rt], acc[4 + rt][nt]);
            filler(rt * 4 + nt);
        }
}

template <bool VT>
DI void k1_tile(const char* __restrict__ Abase, const char* __restrict__ Wbase, char* __restrict__ outp, char* lds, const unsigned (&avo)[4], const unsigned (&wvo)[4],
                int hp, int rb, char* stamp_base, const char* __restrict__ wq, const char* __restrict__ w1) {
    const unsigned tid = threadIdx.x, lane = tid & 63, c = lane & 15, g = lane >> 4;
    const unsigned wid = rfl(tid >> 6), wm = wid >> 2, wn = wid & 3;
    const unsigned lds0 = (unsigned)(uintptr_t)lds;
    unsigned aaddr[2], baddr[4];
    {
        const unsigned x = (c >> 1) & 7;
#pragma unroll
        for (int s = 0; s < 2; ++s) aaddr[s] = (128 * wm + c) * 128 + (((4 * s + g) ^ x) << 4);
        const unsigned q = (lane & 15) >> 2, p = lane & 3, f = q | ((g & 1) << 2);
#pragma unroll
        for (int nt = 0; nt < 4; ++nt) baddr[nt] = 32768 + (8 * g + q) * 512 + (((8 * wn + 2 * nt + (p >> 1)) ^ (f << 1)) << 4) + ((p & 1) << 3);
    }
    f32x4 acc[8][4];
#pragma unroll
    for (int i = 0; i < 8; ++i)
#pragma unroll
        for (int j = 0; j < 4; ++j) acc[i][j] = f32x4{};
    bf16x8 cf[2][4], wf[2][4];
    lds_cptr L = (lds_cptr)lds;
#define K1_RC(buf, stg, s, half) do { _Pragma("unroll") for (int rt = 0; rt < 4; ++rt) cf[buf][rt] = lds_frag(L + (stg) + aaddr[s] + ((half) * 4 + rt) * 2048); } while (0)
#define K1_RW(buf, stg, s) do { _Pragma("unroll") for (int nt = 0; nt < 4; ++nt) wf[buf][nt] = tr_frag(L + (stg) + baddr[nt] + (s) * 16384, L + (stg) + baddr[nt] + (s) * 16384 + 2048); } while (0)
    const char* Aw = Abase + (size_t)wid * 32 * 1024;
    const char* Ww = Wbase + (size_t)wid * 8 * 8192;
    const unsigned m0a = lds0 + wid * 4096, m0w = lds0 + 32768 + wid * 4096;
#define K1_DMA(kt, stg)                                                                   \
    do {                                                                                  \
        const char* a_ = Aw + (kt) * 128; const char* w_ = Ww + (size_t)(kt) * 64 * 8192; \
        dma1<0>(m0a + (stg), a_, avo[0]); dma1<1024>(m0a + (stg), a_, avo[1]); dma1<2048>(m0a + (stg), a_, avo[2]); dma1<3072>(m0a + (stg), a_, avo[3]); \
        dma1<0>(m0w + (stg), w_, wvo[0]); dma1<1024>(m0w + (stg), w_, wvo[1]); dma1<2048>(m0w + (stg), w_, wvo[2]); dma1<3072>(m0w + (stg), w_, wvo[3]); \
    } while (0)
    u32x4 pfd = u32x4{};
    const unsigned pfid = blockIdx.x * 8 + wid;
    STAMP(1);
    asm volatile("s_waitcnt vmcnt(10)\n\ts_barrier" ::: "memory");
    STAMP(2);
    K1_RW(0, 0, 0); K1_RC(0, 0, 0, 0);
#define K1_RC1(buf, stg, s, half, rt) cf[buf][rt] = lds_frag(L + (stg) + aaddr[s] + ((half) * 4 + (rt)) * 2048)
#define K1_RW1(buf, stg, s, nt) wf[buf][nt] = tr_frag(L + (stg) + baddr[nt] + (s) * 16384, L + (stg) + baddr[nt] + (s) * 16384 + 2048)
#pragma unroll
    for (int kt = 0; kt < 8; ++kt) {
        const unsigned stg = (kt & 1) * 65536, nstg = stg ^ 65536;
        SBAR();
        k1_mma<VT>(acc, 0, cf[0], wf[0], [&](int m) { if (m < 8 && (m & 1) == 0) { K1_RC1(1, stg, 0, 1, m >> 1); SBAR(); } });
        SBAR();
        k1_mma<VT>(acc, 1, cf[1], wf[0], [&](int m) { if (m < 4) { K1_RC1(0, stg, 1, 0, m); SBAR(); } else if (m < 12 && (m & 1) == 0) { K1_RW1(1, stg, 1, (m - 4) >> 1); SBAR(); } });
        SBAR();
        k1_mma<VT>(acc, 0, cf[0], wf[1], [&](int m) { if (m < 8 && (m & 1) == 0) { K1_RC1(1, stg, 1, 1, m >> 1); SBAR(); } });
        SBAR();
        if (PREFETCH && kt == 7) asm volatile("s_waitcnt lgkmcnt(0)\n\ts_barrier" ::: "memory");
        else asm volatile("s_waitcnt vmcnt(0) lgkmcnt(0)\n\ts_barrier" ::: "memory");
        const char* a_ = Aw + (kt + 2) * 128; const char* w_ = Ww + (size_t)(kt + 2) * 64 * 8192;
        k1_mma<VT>(acc, 1, cf[1], wf[1], [&](int m) {
            if (kt + 1 < 8) { if (m < 4) { K1_RW1(0, nstg, 0, m); SBAR(); } else if (m < 8) { K1_RC1(0, nstg, 0, 0, m - 4); SBAR(); } }
            if (kt + 2 < 8) {
                if (m == 1) { dma1<0>(m0a + stg, a_, avo[0]); SBAR(); } if (m == 3) { dma1<1024>(m0a + stg, a_, avo[1]); SBAR(); }
                if (m == 5) { dma1<2048>(m0a + stg, a_, avo[2]); SBAR(); } if (m == 7) { dma1<3072>(m0a + stg, a_, avo[3]); SBAR(); }
                if (m == 9) { dma1<0>(m0w + stg, w_, wvo[0]); SBAR(); } if (m == 11) { dma1<1024>(m0w + stg, w_, wvo[1]); SBAR(); }
                if (m == 13) { dma1<2048>(m0w + stg, w_, wvo[2]); SBAR(); } if (m == 15) { dma1<3072>(m0w + stg, w_, wvo[3]); SBAR(); }
            }
            if (PREFETCH && kt == 6 && (m & 3) == 1) {
                warm_load(pfd, warm_src(wq, w1, pfid + (m >> 2) * 2048), lane * 16);
                SBAR();
            }
        });
        SBAR();
    }
    STAMP(3);
    const int head = 2 * hp + (wn >> 1), d0 = (wn & 1) * 64;
    const int key0 = 256 * rb + 128 * wm;
    if (VT) {
        const unsigned lo = (g >> 1) * 1024 + (c + 32 * (g & 1)) * 16;
#pragma unroll
        for (int u = 0; u < 4; ++u)
#pragma unroll
            for (int nt = 0; nt < 4; ++nt) {
                const size_t off = ((size_t)(head * 64 + (key0 >> 5) + u) * 4 + (d0 >> 5) + (nt >> 1)) * 2048 + (nt & 1) * 256;
                *(u32x4*)(outp + off + lo) = pack44(acc[2 * u][nt], acc[2 * u + 1][nt]);
            }
    } else {
        const unsigned lo = (g >> 1) * 1024 + ((c & 7) + 16 * (c >> 3) + 32 * (g & 1)) * 16;
#pragma unroll
        for (int kt = 0; kt < 8; ++kt)
#pragma unroll
            for (int v = 0; v < 2; ++v) {
                const size_t off = ((size_t)(head * 64 + (key0 >> 5) + (kt >> 1)) * 4 + (d0 >> 5) + v) * 2048 + (kt & 1) * 128;
                *(u32x4*)(outp + off + lo) = pack44(acc[kt][2 * v], acc[kt][2 * v + 1]);
            }
    }
    STAMP(4);
    asm volatile("s_waitcnt vmcnt(0)" : "+v"(pfd)::"memory");
    STAMP(5);
#undef K1_RC
#undef K1_RW
#undef K1_DMA
}

__global__ __launch_bounds__(512, 2) void k1_kvproj(const char* __restrict__ x, const char* __restrict__ ckv, const char* __restrict__ wuk,
                                                    const char* __restrict__ wuv, char* __restrict__ ws, const char* __restrict__ wq, const char* __restrict__ w1) {
    extern __shared__ __attribute__((aligned(16))) char lds[];
    const unsigned tid = threadIdx.x, lane = tid & 63;
    const unsigned wid = rfl(tid >> 6);
    char* stamp_base = ws; (void)stamp_base;
    STAMP(0);
    const int b = blockIdx.x, xcd = b & 7, idx = b >> 3;
    if (tid == 0) *(unsigned*)(ws + WS_FL + (size_t)b * 128) = 0u;
    const int vt = idx & 1, hp = 2 * xcd + ((idx >> 1) & 1), rb = idx >> 2;
    const char* Abase = ckv + (size_t)rb * 256 * 1024;
    const char* Wbase = (vt ? wuv : wuk) + hp * 512;
    const int row = 8 * b + wid;
    const unsigned l40 = lane < 40 ? lane : 39;
    u32x4 x0, x1;
    {
        const char* xr = uptr(x + (size_t)row * 1280);
        const unsigned vo = l40 * 32;
        asm volatile("global_load_dwordx4 %0, %2, %3\n\tglobal_load_dwordx4 %1, %2, %3 offset:16" : "=&v"(x0), "=&v"(x1) : "v"(vo), "s"(xr) : "memory");
    }
    unsigned avo[4], wvo[4];
    {
        const unsigned lds0 = (unsigned)(uintptr_t)lds;
        const char* Aw = Abase + (size_t)wid * 32 * 1024;
        const char* Ww = Wbase + (size_t)wid * 8 * 8192;
        const unsigned m0a = lds0 + wid * 4096, m0w = lds0 + 32768 + wid * 4096;
#pragma unroll
        for (int p = 0; p < 4; ++p) {
            const unsigned rw = 8 * p + (lane >> 3);
            avo[p] = rw * 1024 + (((lane & 7) ^ ((rw >> 1) & 7)) << 4) - 1024 * p;
            const unsigned k = 2 * p + (lane >> 5);
            const unsigned f = (k & 3) | ((wid & 1) << 2);
            wvo[p] = k * 8192 + (((lane & 31) ^ (f << 1)) << 4) - 1024 * p;
        }
#pragma unroll
        for (int kt = 0; kt < 2; ++kt) {
            const char* a_ = Aw + kt * 128; const char* w_ = Ww + (size_t)kt * 64 * 8192; const unsigned stg = kt * 65536;
            dma1<0>(m0a + stg, a_, avo[0]); dma1<1024>(m0a + stg, a_, avo[1]); dma1<2048>(m0a + stg, a_, avo[2]); dma1<3072>(m0a + stg, a_, avo[3]);
            dma1<0>(m0w + stg, w_, wvo[0]); dma1<1024>(m0w + stg, w_, wvo[1]); dma1<2048>(m0w + stg, w_, wvo[2]); dma1<3072>(m0w + stg, w_, wvo[3]);
        }
    }
    {
        asm volatile("s_waitcnt vmcnt(16)" : "+v"(x0), "+v"(x1)::"memory");
        float v[16];
#pragma unroll
        for (int i = 0; i < 4; ++i) { v[2 * i] = bflo(x0[i]); v[2 * i + 1] = bfhi(x0[i]); v[8 + 2 * i] = bflo(x1[i]); v[8 + 2 * i + 1] = bfhi(x1[i]); }
        float s = 0.f;
#pragma unroll
        for (int i = 0; i < 16; ++i) s += v[i];
        if (lane >= 40) s = 0.f;
#pragma unroll
        for (int o = 32; o >= 1; o >>= 1) s += __shfl_xor(s, o);
        const float mean = s * (1.0f / DM);
        float q = 0.f;
#pragma unroll
        for (int i = 0; i < 16; ++i) { v[i] -= mean; q += v[i] * v[i]; }
        if (lane >= 40) q = 0.f;
#pragma unroll
        for (int o = 32; o >= 1; o >>= 1) q += __shfl_xor(q, o);
        const float rstd = 1.0f / sqrtf(q * (1.0f / DM) + 1e-5f);
        u32x4 o0, o1;
#pragma unroll
        for (int i = 0; i < 4; ++i) { o0[i] = cvtpk(v[2 * i] * rstd, v[2 * i + 1] * rstd); o1[i] = cvtpk(v[8 + 2 * i] * rstd, v[8 + 2 * i + 1] * rstd); }
        char* xf = ws + WS_XF + ((size_t)(row >> 4) * NKS_X + (l40 >> 1)) * 1024 + ((row & 15) + 32 * (l40 & 1)) * 16;
        if (lane < 40) { *(u32x4*)xf = o0; *(u32x4*)(xf + 256) = o1; }
    }
    if (vt) k1_tile<true>(Abase, Wbase, ws + WS_VF, lds, avo, wvo, hp, rb, ws, wq, w1);
    else    k1_tile<false>(Abase, Wbase, ws + WS_KF, lds, avo, wvo, hp, rb, ws, wq, w1);
}

constexpr float QSCALE = 0.08838834764831845f * 1.4426950408889634f;
constexpr float THR = 8.0f;
constexpr int WQ_PIECE = 1040, W1_PIECE = 1056, W1_OFF = 16 * WQ_PIECE, PSLOT = W1_OFF + 8 * W1_PIECE;

DI float x4_max(float x) {
    auto s = __builtin_amdgcn_permlane16_swap(__float_as_uint(x), __float_as_uint(x), false, false);
    x = __builtin_fmaxf(__uint_as_float(s[0]), __uint_as_float(s[1]));
    auto t = __builtin_amdgcn_permlane32_swap(__float_as_uint(x), __float_as_uint(x), false, false);
    return __builtin_fmaxf(__uint_as_float(t[0]), __uint_as_float(t[1]));
}
DI float x4_sum(float x) {
    auto s = __builtin_amdgcn_permlane16_swap(__float_as_uint(x), __float_as_uint(x), false, false);
    x = __uint_as_float(s[0]) + __uint_as_float(s[1]);
    auto t = __builtin_amdgcn_permlane32_swap(__float_as_uint(x), __float_as_uint(x), false, false);
    return __uint_as_float(t[0]) + __uint_as_float(t[1]);
}
DI float mish_f(float h) {
    const float e = __builtin_amdgcn_exp2f(__builtin_fminf(h, 30.0f) * 1.4426950408889634f);
    const float n = e * (e + 2.0f);
    return h * n * __builtin_amdgcn_rcpf(n + 2.0f);
}

template <int NFT>
DI void proj_phase(const char* __restrict__ wq, const char* __restrict__ w1, const char* __restrict__ b1, char* __restrict__ ws, char* lds,
                   int head, int f0, int q16_0, bf16x8 (&qf)[2][4], const char* __restrict__ wo, const char* __restrict__ w2, u32x4& pfd) {
    const unsigned tid = threadIdx.x, lane = tid & 63, g = lane >> 4;
    const unsigned wid = rfl(tid >> 6);
    const unsigned lds0 = (unsigned)(uintptr_t)lds;
    lds_cptr L = (lds_cptr)lds;
    const unsigned wqvo = ((lane >> 2) & 3) * 8192 + (lane >> 4) * 64 + (lane & 3) * 16;
    const unsigned w1vo = ((lane >> 2) & 7) * 3456 + (NFT == 4 ? (lane >> 5) * 64 : 0) + (lane & 3) * 16;
    const char* wqs = wq + (size_t)wid * 8 * 8192 + head * 256;
    const char* w1s = w1 + (size_t)wid * 8 * 3456 + f0 * 2;
#define PJ_DMA(c, slot)                                                                                         \
    do {                                                                                                        \
        dma1<0>(lds0 + (slot) + (2 * wid) * WQ_PIECE, wqs + (size_t)(c) * 64 * 8192, wqvo);                     \
        dma1<0>(lds0 + (slot) + (2 * wid + 1) * WQ_PIECE, wqs + (size_t)(c) * 64 * 8192 + 4 * 8192, wqvo);      \
        dma1<0>(lds0 + (slot) + W1_OFF + wid * W1_PIECE, w1s + (size_t)(c) * 64 * 3456, w1vo);                  \
    } while (0)
    const char* xs = ws + WS_XF + (size_t)q16_0 * NKS_X * 1024;
    const unsigned xv0 = lane * 16, xv1 = lane * 16 + NKS_X * 1024;
    u32x4 xa[4], xb[4];
#define PJ_XLOAD(X, c)                                                                                          \
    do {                                                                                                        \
        const char* p_ = xs + (c) * 2048;                                                                       \
        asm volatile("global_load_dwordx4 %0, %4, %6\n\tglobal_load_dwordx4 %1, %5, %6\n\tglobal_load_dwordx4 %2, %4, %6 offset:1024\n\tglobal_load_dwordx4 %3, %5, %6 offset:1024" \
                     : "=&v"(X[0]), "=&v"(X[1]), "=&v"(X[2]), "=&v"(X[3]) : "v"(xv0), "v"(xv1), "s"(p_) : "memory"); \
    } while (0)
    const unsigned q = (lane & 15) >> 2, p = lane & 3;
    const unsigned wqb = 2 * g * WQ_PIECE + q * 64 + p * 8;
    const unsigned w1b = W1_OFF + g * W1_PIECE + q * 64 + p * 8;
    f32x4 aq[8][2], ah[NFT][2];
#pragma unroll
    for (int i = 0; i < 8; ++i) { aq[i][0] = f32x4{}; aq[i][1] = f32x4{}; }
#pragma unroll
    for (int i = 0; i < NFT; ++i) { ah[i][0] = f32x4{}; ah[i][1] = f32x4{}; }
    constexpr int NT = 8 + NFT;
    bf16x8 wf[4];
#define PJ_RD(slot, s, n)                                                                                       \
    (((n) < 8) ? tr_frag(L + (slot) + wqb + (8 * (s)) * WQ_PIECE + ((n) >> 1) * 256 + ((n) & 1) * 32, L + (slot) + wqb + (8 * (s) + 1) * WQ_PIECE + ((n) >> 1) * 256 + ((n) & 1) * 32) \
               : tr_frag(L + (slot) + w1b + (4 * (s)) * W1_PIECE + (((n) - 8) >> 1) * 512 + ((n) & 1) * 32, L + (slot) + w1b + (4 * (s)) * W1_PIECE + 256 + (((n) - 8) >> 1) * 512 + ((n) & 1) * 32))
    u32x2 bb[4];
    {
        const char* bp = uptr(b1 + f0 * 2);
        const unsigned bo = g * 8;
        asm volatile("s_nop 4\n\tglobal_load_dwordx2 %0, %4, %5\n\tglobal_load_dwordx2 %1, %4, %5 offset:32\n\tglobal_load_dwordx2 %2, %4, %5 offset:%6\n\tglobal_load_dwordx2 %3, %4, %5 offset:%7"
                     : "=&v"(bb[0]), "=&v"(bb[1]), "=&v"(bb[2]), "=&v"(bb[3]) : "v"(bo), "s"(bp), "i"(NFT == 4 ? 64 : 0), "i"(NFT == 4 ? 96 : 32) : "memory");
    }
    PJ_XLOAD(xa, 0);
    PJ_DMA(0, 0); PJ_DMA(1, PSLOT);
    asm volatile("s_waitcnt vmcnt(3)\n\ts_barrier" : "+v"(xa[0]), "+v"(xa[1]), "+v"(xa[2]), "+v"(xa[3]), "+v"(bb[0]), "+v"(bb[1]), "+v"(bb[2]), "+v"(bb[3])::"memory");
#pragma unroll
    for (int n = 0; n < 3; ++n) wf[n] = PJ_RD(0, 0, n);
#define PJ_CHUNK(c, XC, XN)                                                                                     \
    do {                                                                                                        \
        constexpr int slot_ = ((c) % 3) * PSLOT, nslot_ = (((c) + 1) % 3) * PSLOT, dslot_ = (((c) + 2) % 3) * PSLOT; \
        if ((c) + 1 < 10) PJ_XLOAD(XN, (c) + 1);                                                                \
        if ((c) + 2 < 10) PJ_DMA((c) + 2, dslot_);                                                              \
        if (PREFETCH && (c) == 9) {       \
            _Pragma("unroll") for (int i = 0; i < 4; ++i) warm_load(pfd, warm_src(wo, w2, blockIdx.x * 8 + wid + i * 2048), lane * 16); \
        }                                                                                                       \
        SBAR();                                                                                                 \
        _Pragma("unroll") for (int m = 0; m < 2 * NT; ++m) {                                                    \
            const int s = m / NT, n = m % NT;                                                                   \
            if (m == 2 * NT - 3) {       \
                if ((c) + 2 < 10) asm volatile("s_waitcnt vmcnt(3) lgkmcnt(0)\n\ts_barrier" : "+v"(XN[0]), "+v"(XN[1]), "+v"(XN[2]), "+v"(XN[3])::"memory"); \
                else if ((c) + 1 < 10) asm volatile("s_waitcnt vmcnt(0) lgkmcnt(0)\n\ts_barrier" : "+v"(XN[0]), "+v"(XN[1]), "+v"(XN[2]), "+v"(XN[3])::"memory"); \
            }                                                                                                   \
            const int m3 = m + 3;                                                                               \
            if (m3 < 2 * NT) wf[m3 & 3] = PJ_RD(slot_, m3 / NT, m3 % NT);                                       \
            else if ((c) + 1 < 10) wf[m3 & 3] = PJ_RD(nslot_, 0, m3 - 2 * NT);                                  \
            const bf16x8 x0 = __builtin_bit_cast(bf16x8, XC[2 * s]), x1 = __builtin_bit_cast(bf16x8, XC[2 * s + 1]); \
            if (n < 8) { aq[n & 7][0] = MFMA(wf[m & 3], x0, aq[n & 7][0]); aq[n & 7][1] = MFMA(wf[m & 3], x1, aq[n & 7][1]); } \
            else { ah[(n - 8) % NFT][0] = MFMA(wf[m & 3], x0, ah[(n - 8) % NFT][0]); ah[(n - 8) % NFT][1] = MFMA(wf[m & 3], x1, ah[(n - 8) % NFT][1]); } \
            SBAR();                                                                                             \
        }                                                                                                       \
    } while (0)
    static_assert((2 * NT) % 4 == 0, "fragment window");
    PJ_CHUNK(0, xa, xb); PJ_CHUNK(1, xb, xa); PJ_CHUNK(2, xa, xb); PJ_CHUNK(3, xb, xa); PJ_CHUNK(4, xa, xb);
    PJ_CHUNK(5, xb, xa); PJ_CHUNK(6, xa, xb); PJ_CHUNK(7, xb, xa); PJ_CHUNK(8, xa, xb); PJ_CHUNK(9, xb, xa);
#undef PJ_CHUNK
#undef PJ_RD
#undef PJ_XLOAD
#undef PJ_DMA
#pragma unroll
    for (int a = 0; a < 2; ++a)
#pragma unroll
        for (int v = 0; v < 4; ++v) qf[a][v] = __builtin_bit_cast(bf16x8, pack44s(aq[2 * v][a], aq[2 * v + 1][a], QSCALE));
#pragma unroll
    for (int v = 0; v < NFT / 2; ++v) {
        float bias[8];
#pragma unroll
        for (int t = 0; t < 2; ++t) {
            const u32x2 q2 = bb[2 * v + t];
            bias[4 * t + 0] = bflo(q2[0]); bias[4 * t + 1] = bfhi(q2[0]); bias[4 * t + 2] = bflo(q2[1]); bias[4 * t + 3] = bfhi(q2[1]);
        }
#pragma unroll
        for (int a = 0; a < 2; ++a) {
            f32x4 h0, h1;
#pragma unroll
            for (int i = 0; i < 4; ++i) { h0[i] = mish_f(ah[2 * v][a][i] + bias[i]); h1[i] = mish_f(ah[2 * v + 1][a][i] + bias[4 + i]); }
            *(u32x4*)(ws + WS_CH + ((size_t)(q16_0 + a) * NKS_CH + NKS_C + (f0 >> 5) + v) * 1024 + lane * 16) = pack44(h0, h1);
        }
    }
}

DI void mfma_acc(f32x4& acc, const bf16x8& a, const bf16x8& b) { asm volatile("v_mfma_f32_16x16x32_bf16 %0, %1, %2, %0" : "+v"(acc) : "v"(a), "v"(b)); }
DI void mfma_acc_s(f32x4& acc, const bf16x8& a, const bf16x8& b) { asm volatile("s_nop 1\n\tv_mfma_f32_16x16x32_bf16 %0, %1, %2, %0" : "+v"(acc) : "v"(a), "v"(b)); }
DI void mfma32_acc(f32x16& acc, const bf16x8& a, const bf16x8& b) { asm volatile("v_mfma_f32_32x32x16_bf16 %0, %1, %2, %0" : "+v"(acc) : "v"(a), "v"(b)); }
DI void mfma32_ini(f32x16& d, const bf16x8& a, const bf16x8& b, const f32x16& c) { asm volatile("v_mfma_f32_32x32x16_bf16 %0, %1, %2, %3" : "=&v"(d) : "v"(a), "v"(b), "v"(c)); }
DI float v_exp(float x) { float r; asm volatile("v_exp_f32 %0, %1" : "=v"(r) : "v"(x)); return r; }
DI float v_add(float a, float b) { float r; asm volatile("v_add_f32 %0, %1, %2" : "=v"(r) : "v"(a), "v"(b)); return r; }
DI unsigned v_cvt(float lo, float hi) { unsigned r; asm volatile("v_cvt_pk_bf16_f32 %0, %1, %2" : "=v"(r) : "v"(lo), "v"(hi)); return r; }
DI void set_m0(unsigned v) { asm volatile("s_mov_b32 m0, %0" ::"s"(v) : "memory"); }
template <int OFF> DI void dma_nom0(const void* sbase, unsigned voff) { asm volatile("global_load_lds_dwordx4 %0, %1 offset:%2" ::"v"(voff), "s"(sbase), "i"(OFF) : "memory"); }
DI void dma4(unsigned m0v, const void* sbase, unsigned voff) {
    asm volatile("s_mov_b32 m0, %0\n\ts_nop 0\n\tglobal_load_lds_dwordx4 %1, %2\n\tglobal_load_lds_dwordx4 %1, %2 offset:1024\n\tglobal_load_lds_dwordx4 %1, %2 offset:2048\n\tglobal_load_lds_dwordx4 %1, %2 offset:3072"
                 ::"s"(m0v), "v"(voff), "s"(sbase) : "memory");
}

#define NOP_SO(S, o) asm volatile("s_nop 7\n\ts_nop 3" : "+v"(S[0]), "+v"(S[1]), "+v"(o[0]), "+v"(o[1]), "+v"(o[2]), "+v"(o[3]))
#define NOP_A(A) asm volatile("s_nop 7\n\ts_nop 1" : "+v"(A[0][0]), "+v"(A[0][1]), "+v"(A[0][2]), "+v"(A[0][3]), "+v"(A[1][0]), "+v"(A[1][1]), "+v"(A[1][2]), "+v"(A[1][3]), \
    "+v"(A[2][0]), "+v"(A[2][1]), "+v"(A[2][2]), "+v"(A[2][3]), "+v"(A[3][0]), "+v"(A[3][1]), "+v"(A[3][2]), "+v"(A[3][3]), "+v"(A[4][0]), "+v"(A[4][1]), "+v"(A[4][2]), "+v"(A[4][3]))
DI float xh_sum(float v) { auto r = __builtin_amdgcn_permlane32_swap(__float_as_uint(v), __float_as_uint(v), false, false); return __uint_as_float(r[0]) + __uint_as_float(r[1]); }
DI float xh_max(float v) { auto r = __builtin_amdgcn_permlane32_swap(__float_as_uint(v), __float_as_uint(v), false, false); return __builtin_fmaxf(__uint_as_float(r[0]), __uint_as_float(r[1])); }
DI float mul_s(float a, float b) { float r; asm("v_mul_f32 %0, %1, %2" : "=v"(r) : "v"(a), "v"(b)); return r; }
DI float sub_s(float a, float b) { float r; asm("v_sub_f32 %0, %1, %2" : "=v"(r) : "v"(a), "v"(b)); return r; }
DI u32x4 pack_o(const f32x16& a, int y, float sc) {
    u32x4 w; w[0] = cvtpk(mul_s(a[4 * y + 0], sc), mul_s(a[4 * y + 1], sc)); w[1] = cvtpk(mul_s(a[4 * y + 2], sc), mul_s(a[4 * y + 3], sc));
    w[2] = cvtpk(mul_s(a[8 + 4 * y + 0], sc), mul_s(a[8 + 4 * y + 1], sc)); w[3] = cvtpk(mul_s(a[8 + 4 * y + 2], sc), mul_s(a[8 + 4 * y + 3], sc)); return w;
}
#define ABL_NODMA 0
#define ABL_NOEXP 0
#define ABL_NOMFMA 0
#define DMAPOS 2
DI void mask_diag(f32x16& s, int dq) {
    const float ninf = -__builtin_inff();
#pragma unroll
    for (int r0 = 0; r0 < 16; r0 += 4) {
        unsigned long long m0, m1, m2, m3;
        const int kc = 16 * ((r0 >> 2) & 1) + 8 * (r0 >> 3);
        float a = s[r0], b = s[r0 + 1], c = s[r0 + 2], d = s[r0 + 3];
        asm("v_cmp_gt_i32_e64 %4, %9, %8\n\tv_cmp_gt_i32_e64 %5, %10, %8\n\tv_cmp_gt_i32_e64 %6, %11, %8\n\tv_cmp_gt_i32_e64 %7, %12, %8\n\t"
            "v_cndmask_b32_e64 %0, %0, %13, %4\n\tv_cndmask_b32_e64 %1, %1, %13, %5\n\tv_cndmask_b32_e64 %2, %2, %13, %6\n\tv_cndmask_b32_e64 %3, %3, %13, %7"
            : "+v"(a), "+v"(b), "+v"(c), "+v"(d), "=&s"(m0), "=&s"(m1), "=&s"(m2), "=&s"(m3)
            : "v"(dq), "i"(kc), "i"(kc + 1), "i"(kc + 2), "i"(kc + 3), "v"(ninf));
        s[r0] = a; s[r0 + 1] = b; s[r0 + 2] = c; s[r0 + 3] = d;
    }
}
#define FRW 8
#define FRA 6
#define PADN -1
#define MPRIO 2

__global__ __launch_bounds__(512, 2) void k2_attn(const char* __restrict__ wq, const char* __restrict__ w1, const char* __restrict__ b1, char* __restrict__ ws,
                                                  const char* __restrict__ wo, const char* __restrict__ w2) {
    extern __shared__ __attribute__((aligned(16))) char lds[];
    const unsigned tid = threadIdx.x, lane = tid & 63;
    const unsigned wid = rfl(tid >> 6), grp = wid >> 2, w4 = wid & 3;
    char* stamp_base = ws; (void)stamp_base;
    STAMP(6);
    const int b = blockIdx.x, xcd = b & 7, idx = b >> 3;
    const int head = 4 * xcd + (idx & 3), pi = idx >> 2;
    const int HB = 15 - pi, LB = pi;
    const int myblk = grp ? LB : HB;
    u32x4 pfd = u32x4{};
    bf16x8 qf[8];
    {
        bf16x8 q16[2][4];
        const int q16_0 = 8 * myblk + 2 * w4;
        if (head < 22) proj_phase<4>(wq, w1, b1, ws, lds, head, 64 * head, q16_0, q16, wo, w2, pfd);
        else           proj_phase<2>(wq, w1, b1, ws, lds, head, 1408 + 32 * (head - 22), q16_0, q16, wo, w2, pfd);
#pragma unroll
        for (int v = 0; v < 4; ++v) {
            const u32x4 r0 = __builtin_bit_cast(u32x4, q16[0][v]), r1 = __builtin_bit_cast(u32x4, q16[1][v]);
            u32x4 z0, z1;
#pragma unroll
            for (int w = 0; w < 4; ++w) {
                const auto s1 = __builtin_amdgcn_permlane16_swap(r0[w], r1[w], false, false);
                const auto s2 = __builtin_amdgcn_permlane32_swap(s1[0], s1[1], false, false);
                z0[w] = s2[0]; z1[w] = s2[1];
            }
            qf[2 * v] = __builtin_bit_cast(bf16x8, z0); qf[2 * v + 1] = __builtin_bit_cast(bf16x8, z1);
        }
    }
    STAMP(7);
    if (grp == 0) {
        char* qh = ws + WS_QH + (size_t)b * 32768 + w4 * 8192 + lane * 16;
#pragma unroll
        for (int s = 0; s < 8; ++s) *(bf16x8*)(qh + s * 1024) = qf[s];
    }
    const unsigned lds0 = (unsigned)(uintptr_t)lds;
    const unsigned gb = grp * 81920;
    lds_cptr Lk = (lds_cptr)lds + gb + lane * 16;
    lds_cptr Lv = (lds_cptr)lds + gb + 49152 + lane * 16;
    const unsigned m0k = lds0 + gb + w4 * 4096, m0v = m0k + 49152;
    const unsigned dvo = lane * 16;
    const int nL = 2 * pi + 2;
    const int nsw = grp ? nL : 1000;
    const size_t jump = (size_t)(17 - nL) * 16384;
    const char* kptr = uptr(ws + WS_KF + (size_t)head * 524288 + w4 * 4096);
    const char* vptr = uptr(ws + WS_VF + (size_t)head * 524288 + w4 * 4096);
    int q0 = 128 * myblk + 32 * w4;
    int tile = 0;
    asm volatile("s_waitcnt lgkmcnt(0)\n\ts_barrier" ::: "memory");
    dma4(m0k, kptr, dvo); kptr += 16384;
    dma4(m0k + 16384, kptr, dvo); kptr += 16384; if (nsw == 2) kptr += jump;
    dma4(m0v, vptr, dvo); vptr += 16384;
    dma4(m0k + 32768, kptr, dvo); kptr += 16384;
    f32x16 o[4], S[2], negm;
    float mref = 0.f, lsum = 0.f, lfin = 1.f;
    bf16x8 pf[4], fr[FRW];
#pragma unroll
    for (int i = 0; i < 4; ++i) o[i] = f32x16{};
    negm = f32x16{}; S[0] = f32x16{}; S[1] = f32x16{};
    asm volatile("s_nop 1" : "+v"(negm));
    NOP_SO(S, o);
    unsigned ks_cur = 0, ks_nxt = 16384, ks_nn = 32768;
    asm volatile("s_waitcnt vmcnt(%1)\n\ts_barrier" : "+v"(pfd) : "i"(DMAPOS == 2 ? 12 : 8) : "memory");
#define KOFF(i) (((i) & 1) * 8192 + ((i) >> 1) * 1024)
#define VOFF(j) (((j) >> 3) * 8192 + ((j) & 3) * 2048 + (((j) >> 2) & 1) * 1024)
#define EARLY_K(slot) do { _Pragma("unroll") for (int i = 0; i < FRA; ++i) fr[i] = lds_frag(Lk + (slot) + KOFF(i)); } while (0)
    EARLY_K(0);
    if (grp) asm volatile("s_barrier" ::: "memory");
#define PHASE_M(t, QK, PV, VS, DV, DK, WN)      \
    do {                                                                                                                              \
        if (MPRIO == 1) __builtin_amdgcn_s_setprio(1);                                                                                \
        if (MPRIO >= 2) __builtin_amdgcn_s_setprio(0);                                                                                \
        lds_cptr kp_ = Lk + ks_cur; lds_cptr vp_ = Lv + (VS) * 16384;                                                                 \
        if (!(QK)) { _Pragma("unroll") for (int i = 16; i < 16 + FRA; ++i) fr[i % FRW] = lds_frag(vp_ + VOFF(i - 16)); }               \
        _Pragma("unroll") for (int i = (QK) ? 0 : 16; i < ((PV) ? 32 : 16); ++i) {                                                    \
            const int n = i + FRA;                                                                                                    \
            if (n < 16) fr[n % FRW] = lds_frag(kp_ + KOFF(n)); else if ((PV) && n < 32) fr[n % FRW] = lds_frag(vp_ + VOFF(n - 16));    \
            SBAR();                                                                                                                   \
            if (ABL_NOMFMA && (i & (ABL_NOMFMA - 1)) != 0) { PINR(fr[i % FRW]); }                                                       \
            else if (i < 2) mfma32_ini(S[i & 1], fr[i % FRW], qf[i >> 1], negm);                                                        \
            else if (i < 16) mfma32_acc(S[i & 1], fr[i % FRW], qf[i >> 1]);                                                             \
            else mfma32_acc(o[i & 3], fr[i % FRW], pf[(i - 16) >> 2]);                                                                  \
            SBAR();                                                                                                                   \
            if (PADN >= 0) {     \
                const bool dm_ = ((DV) && i >= 1 && i <= 7 && (i & 1)) || ((DK) && i >= 9 && i <= 15 && (i & 1));                      \
                if (!dm_) asm volatile("s_nop %0" ::"i"(PADN >= 0 ? PADN : 0)); else if (PADN >= 1) asm volatile("s_nop %0" ::"i"(PADN >= 1 ? PADN - 1 : 0)); \
            }                                                                                                                         \
            if ((QK) && i == 16 && (t) + 1 == nsw) {        \
                const char* qs_ = uptr(ws + WS_QH + (size_t)b * 32768 + w4 * 8192);                                                   \
                asm volatile("s_nop 4\n\tglobal_load_dwordx4 %0, %8, %9\n\tglobal_load_dwordx4 %1, %8, %9 offset:1024\n\tglobal_load_dwordx4 %2, %8, %9 offset:2048\n\tglobal_load_dwordx4 %3, %8, %9 offset:3072" \
                             : "+v"(qf[0]), "+v"(qf[1]), "+v"(qf[2]), "+v"(qf[3]), "+v"(qf[4]), "+v"(qf[5]), "+v"(qf[6]), "+v"(qf[7]) : "v"(dvo), "s"(qs_) : "memory"); \
                asm volatile("global_load_dwordx4 %4, %8, %9\n\tglobal_load_dwordx4 %5, %8, %9 offset:1024\n\tglobal_load_dwordx4 %6, %8, %9 offset:2048\n\tglobal_load_dwordx4 %7, %8, %9 offset:3072" \
                             : "+v"(qf[0]), "+v"(qf[1]), "+v"(qf[2]), "+v"(qf[3]), "+v"(qf[4]), "+v"(qf[5]), "+v"(qf[6]), "+v"(qf[7]) : "v"(dvo), "s"(qs_ + 4096) : "memory"); \
            }                                                                                                                         \
            if ((DV) && !ABL_NODMA && DMAPOS != 2) {                                                              \
                if (i == 0) set_m0(m0v + (1 - (VS)) * 16384);                                                                         \
                if (i == 1) dma_nom0<0>(vptr, dvo); if (i == 3) dma_nom0<1024>(vptr, dvo);                                            \
                if (i == 5) dma_nom0<2048>(vptr, dvo); if (i == 7) dma_nom0<3072>(vptr, dvo);                                         \
            }                                                                                                                         \
            if ((DK) && !ABL_NODMA && DMAPOS == 0) {                                                          \
                if (i == 8) set_m0(m0k + ks_nn);                                                                                      \
                if (i == 9) dma_nom0<0>(kptr, dvo); if (i == 11) dma_nom0<1024>(kptr, dvo);                                           \
                if (i == 13) dma_nom0<2048>(kptr, dvo); if (i == 15) dma_nom0<3072>(kptr, dvo);                                       \
            }                                                                                                                         \
            SBAR();                                                                                                                   \
        }                                                                                                                             \
        if ((DV) && DMAPOS != 2) vptr += 16384 + ((t) + 1 == nsw ? jump : 0);                                                         \
        if ((DK) && DMAPOS == 0) kptr += 16384 + ((t) + 3 == nsw ? jump : 0);                                                         \
        if (MPRIO == 1) __builtin_amdgcn_s_setprio(0);                                                                                \
        if (MPRIO >= 2) __builtin_amdgcn_s_setprio(3);                                                                                \
        TICK(tA);                                                                                                                     \
        if ((WN) >= 0) asm volatile("s_waitcnt vmcnt(%0)\n\ts_barrier" ::"i"((WN) >= 0 ? (WN) : 0) : "memory");                       \
        else asm volatile("s_barrier" ::: "memory");                                                                                  \
        TICK(tX);                                                                                                                     \
    } while (0)
#define EXP_PACK()                    \
    do {                                                                                                                              \
        float e_[32], a_[2];                                                                                                          \
        unsigned c_[16];                                                                                                              \
        _Pragma("unroll") for (int k = 0; k < 18; ++k) {                                                                              \
            if (k < 16) { e_[2 * k] = v_exp(S[k >> 3][2 * (k & 7)]); e_[2 * k + 1] = v_exp(S[k >> 3][2 * (k & 7) + 1]); }             \
            if (k == 1) { a_[0] = e_[0]; a_[1] = e_[1]; }                                                                             \
            if (k >= 2 && k < 17) { a_[0] = v_add(a_[0], e_[2 * k - 2]); a_[1] = v_add(a_[1], e_[2 * k - 1]); }                        \
            if (k >= 2) c_[k - 2] = v_cvt(e_[2 * k - 4], e_[2 * k - 3]);                                                              \
        }                                                                                                                             \
        _Pragma("unroll") for (int k = 0; k < 4; ++k) pf[k] = __builtin_bit_cast(bf16x8, u32x4{c_[4 * k], c_[4 * k + 1], c_[4 * k + 2], c_[4 * k + 3]}); \
        ts_ = v_add(a_[0], a_[1]);                                                                                                    \
    } while (0)
#define REBASE(FIRST)                                                          \
    do {                                                                                                                              \
        float rm = S[0][0];                                                                                                           \
        _Pragma("unroll") for (int e = 1; e < 32; ++e) rm = __builtin_fmaxf(rm, S[e >> 4][e & 15]);                                   \
        rm = xh_max(rm);                                                                                                              \
        const float dl = (FIRST) ? (rm == -__builtin_inff() ? 0.f : rm) : __builtin_fmaxf(rm, 0.f);                                   \
        mref += dl;                                                                                                                   \
        if (!(FIRST)) { float f = __builtin_amdgcn_exp2f(-dl); asm volatile("s_nop 1" : "+v"(f)); lsum *= f;               \
            _Pragma("unroll") for (int i = 0; i < 4; ++i) _Pragma("unroll") for (int r = 0; r < 16; ++r) o[i][r] = mul_s(o[i][r], f); } \
        _Pragma("unroll") for (int r = 0; r < 16; ++r) { S[0][r] = sub_s(S[0][r], dl); S[1][r] = sub_s(S[1][r], dl); }                \
        const float nm = -mref;                                                                                                       \
        _Pragma("unroll") for (int r = 0; r < 16; ++r) negm[r] = nm;                                                                  \
    } while (0)
#define PHASE_V(t, WN, LASTV, VS, DK, DV)                                                                                             \
    do {                                                                                                                              \
        const bool swn = ((t) + 1 == nsw), sw0 = ((t) == nsw);                                                                        \
        const bool diag = 64 * tile + 63 > q0 || sw0, first_ = (t) == 0 || sw0;                                                       \
        if (DMAPOS == 2 && !ABL_NODMA) {                                                                                              \
            if (DV) { dma4(m0v + (VS) * 16384, vptr, dvo); vptr += 16384 + ((t) + 2 == nsw ? jump : 0); }                 \
            if (DK) { dma4(m0k + ks_cur, kptr, dvo); kptr += 16384 + ((t) + 4 == nsw ? jump : 0); }                       \
        }                                                                                                                             \
        NOP_SO(S, o);                                                                                                                 \
        float ts_;                                                                                                                    \
        if (__builtin_expect(diag || first_, 0)) {                                                                                    \
            if (sw0) {                                                                                \
                float il_ = __builtin_amdgcn_rcpf(xh_sum(lsum));                                                                      \
                asm volatile("s_nop 1" : "+v"(il_));                                                                                  \
                char* cp_ = ws + WS_CH + ((size_t)(8 * LB + 2 * w4 + ((lane >> 4) & 1)) * NKS_CH + 4 * head) * 1024 + ((lane & 15) + 16 * (lane >> 5)) * 16; \
                _Pragma("unroll") for (int dt = 0; dt < 4; ++dt) _Pragma("unroll") for (int y = 0; y < 2; ++y)                        \
                    *(u32x4*)(cp_ + dt * 1024 + y * 512) = pack_o(o[dt], y, il_);                                                     \
                _Pragma("unroll") for (int i = 0; i < 4; ++i) o[i] = f32x16{};                                                        \
                lsum = 0.f; mref = 0.f; q0 = 128 * HB + 32 * w4;                                                                      \
            }                                                                                                                         \
            if (64 * tile + 63 > q0) {         \
                const int dq = (int)(lane & 31) - 4 * (int)(lane >> 5);                                                               \
                _Pragma("unroll") for (int u = 0; u < 2; ++u) {                                                                       \
                    const int kb = 64 * tile + 32 * u;                                                                                \
                    if (kb > q0) { _Pragma("unroll") for (int r = 0; r < 16; ++r) S[u][r] = -__builtin_inff(); }                       \
                    else if (kb == q0) mask_diag(S[u], dq);                                                                           \
                }                                                                                                                     \
            }                                                                                                                         \
            if (first_) REBASE(true);                                                                                                 \
        }                                                                                                                             \
        EXP_PACK();                                                                                                                   \
        if (__builtin_expect(!(ABL_NOMFMA || ABL_NODMA) && __any(ts_ > PLIM), 0)) { REBASE(false); EXP_PACK(); }     \
        lsum += ts_;                                                                                                                  \
        if (!(LASTV)) EARLY_K(ks_nxt);                                                                                                \
        if (__builtin_expect(swn, 0)) {                                                                                               \
            negm = f32x16{};                                                                                                          \
            asm volatile("s_waitcnt vmcnt(%9)" : "+v"(qf[0]), "+v"(qf[1]), "+v"(qf[2]), "+v"(qf[3]), "+v"(qf[4]), "+v"(qf[5]), "+v"(qf[6]), "+v"(qf[7]), "+v"(negm) : "i"(DMAPOS == 2 ? (WN) : 0) : "memory"); \
            tile += 17 - nL;                                                                                                          \
        }                                                                                                                             \
        TICK(tB);                                                                                                                     \
        asm volatile("s_waitcnt vmcnt(%5)\n\ts_barrier" : "+v"(pf[0]), "+v"(pf[1]), "+v"(pf[2]), "+v"(pf[3]), "+v"(negm) : "i"(WN) : "memory"); \
        TICK(tO);                                                                                                                     \
        { const unsigned r_ = ks_cur; ks_cur = ks_nxt; ks_nxt = ks_nn; ks_nn = r_; }                                                  \
        tile += 1;                                                                                                                    \
    } while (0)
    constexpr float PLIM = 256.0f;
    PHASE_M(0, true, false, 1, false, false, 8);
    PHASE_V(0, 8, false, 1, true, true);
    for (int t = 1; t < 13; t += 2) {
        PHASE_M(t, true, true, 0, false, false, -1); PHASE_V(t, 8, false, 0, true, true);
        PHASE_M(t + 1, true, true, 1, false, false, -1); PHASE_V(t + 1, 8, false, 1, true, true);
    }
    PHASE_M(13, true, true, 0, false, false, -1); PHASE_V(13, 8, false, 0, true, true);
    PHASE_M(14, true, true, 1, false, false, -1); PHASE_V(14, 4, false, 1, false, true);
    PHASE_M(15, true, true, 0, false, false, -1); PHASE_V(15, 4, false, 0, false, true);
    PHASE_M(16, true, true, 1, false, false, -1); PHASE_V(16, 0, true, 1, false, false);
    PHASE_M(17, false, true, 0, false, false, -1);
    if (!grp) asm volatile("s_barrier" ::: "memory");
    NOP_SO(S, o);
    STAMP(8);
    {
        const float ls = xh_sum(lsum);
        const float mm = ls > 0.f ? mref : -__builtin_inff();
        lds_ptr X = (lds_ptr)lds + wid * 8192 + lane * 16;
        f32x16 keep[2];
        if (grp) {
#pragma unroll
            for (int i = 0; i < 2; ++i) {
                keep[i] = o[2 + i];
#pragma unroll
                for (int y = 0; y < 4; ++y) *(__attribute__((address_space(3))) f32x4*)(X + (4 * i + y) * 1024) = f32x4{o[i][4 * y], o[i][4 * y + 1], o[i][4 * y + 2], o[i][4 * y + 3]};
            }
        } else {
#pragma unroll
            for (int i = 0; i < 2; ++i) {
                keep[i] = o[i];
#pragma unroll
                for (int y = 0; y < 4; ++y) *(__attribute__((address_space(3))) f32x4*)(X + (4 * i + y) * 1024) = f32x4{o[2 + i][4 * y], o[2 + i][4 * y + 1], o[2 + i][4 * y + 2], o[2 + i][4 * y + 3]};
            }
        }
        lds_ptr Y = (lds_ptr)lds + 65536 + wid * 512 + lane * 8;
        *(__attribute__((address_space(3))) f32x2*)Y = f32x2{mm, ls};
        asm volatile("s_waitcnt lgkmcnt(0)\n\ts_barrier" ::: "memory");
        const unsigned ow = wid ^ 4;
        lds_cptr Xo = (lds_cptr)lds + ow * 8192 + lane * 16;
        const f32x2 oml = *(const __attribute__((address_space(3))) f32x2*)((lds_cptr)lds + 65536 + ow * 512 + lane * 8);
        const float mx = __builtin_fmaxf(mm, oml[0]);
        const float e0 = __builtin_amdgcn_exp2f(mm - mx), e1 = __builtin_amdgcn_exp2f(oml[0] - mx);
        const float inv = __builtin_amdgcn_rcpf(ls * e0 + oml[1] * e1);
        const float fs = e0 * inv, fo = e1 * inv;
        char* cp = ws + WS_CH + ((size_t)(8 * HB + 2 * w4 + ((lane >> 4) & 1)) * NKS_CH + 4 * head + 2 * grp) * 1024 + ((lane & 15) + 16 * (lane >> 5)) * 16;
#pragma unroll
        for (int i = 0; i < 2; ++i) {
            f32x16 m;
#pragma unroll
            for (int y = 0; y < 4; ++y) {
                const f32x4 pv = *(const __attribute__((address_space(3))) f32x4*)(Xo + (4 * i + y) * 1024);
#pragma unroll
                for (int r = 0; r < 4; ++r) m[4 * y + r] = keep[i][4 * y + r] * fs + pv[r] * fo;
            }
#pragma unroll
            for (int y = 0; y < 2; ++y) *(u32x4*)(cp + i * 1024 + y * 512) = pack_o(m, y, 1.f);
        }
    }
    STAMP(9);
#undef KOFF
#undef VOFF
#undef EARLY_K
#undef PHASE_M
#undef PHASE_V
#undef EXP_PACK
#undef REBASE
}

typedef __attribute__((address_space(1))) unsigned gu32;
#define K3_NOMFMA 0
#define K3_NODMA 0
#define K3_NORD 0
constexpr int K3_SLOT = 36864;
constexpr int PP_SET = 8 * 5 * 1024;
DI float silu_f(float v) { return v * __builtin_amdgcn_rcpf(1.0f + __builtin_amdgcn_exp2f(-1.4426950408889634f * v)); }
DI f32x4 unpk_lo(const u32x4& w) { return f32x4{bflo(w[0]), bfhi(w[0]), bflo(w[1]), bfhi(w[1])}; }
DI f32x4 unpk_hi(const u32x4& w) { return f32x4{bflo(w[2]), bfhi(w[2]), bflo(w[3]), bfhi(w[3])}; }

template <int KS>
DI void k3_finish(const f32x4 (&res)[10], const f32x4 (&res2)[10], const char* __restrict__ x, char* __restrict__ out, char* __restrict__ ws,
                  int grp64, int qt, int mt) {
    const unsigned tid = threadIdx.x, lane = tid & 63, c = lane & 15, g = lane >> 4;
    const unsigned wid = rfl(tid >> 6), grp = wid >> 2, w4 = wid & 3, qh = w4 >> 1, mh = w4 & 1;
    char* stamp_base = ws; (void)stamp_base;
    const __amdgpu_buffer_rsrc_t rs = __builtin_amdgcn_make_buffer_rsrc(ws + WS_PP + (size_t)grp64 * (5 * PP_SET), (short)0, 5 * PP_SET, 0x00020000);
    const unsigned vo = wid * 5120 + lane * 16;
    const unsigned r = (KS - wid) & 3;
    f32x4 oa[2][2], ob[2][2];
#pragma unroll
    for (int j = 0; j < 5; ++j) {
        if ((j & 3) == r) {
            oa[j >> 2][0] = res[2 * j]; oa[j >> 2][1] = res[2 * j + 1];
            if (KS == 2) { ob[j >> 2][0] = res2[2 * j]; ob[j >> 2][1] = res2[2 * j + 1]; }
        } else {
            __builtin_amdgcn_raw_buffer_store_b128(pack44(res[2 * j], res[2 * j + 1]), rs, vo + j * 1024 + (KS < 2 ? KS : KS + 1) * PP_SET, 0, 16);
            if (KS == 2) __builtin_amdgcn_raw_buffer_store_b128(pack44(res2[2 * j], res2[2 * j + 1]), rs, vo + j * 1024 + 2 * PP_SET, 0, 16);
        }
    }
    const int nown = r == 0 ? 2 : 1;
    u32x2 xv[2][2];
    unsigned oo[2][2];
#pragma unroll
    for (int sl = 0; sl < 2; ++sl)
#pragma unroll
        for (int t = 0; t < 2; ++t) {
            const unsigned i = 2 * (sl ? 4 : r) + t, a = i >= 5 ? 1 : 0, n = i - 5 * a;
            oo[sl][t] = (16 * (8 * qt + 4 * qh + 2 * grp + a) + c) * (DM * 2) + (16 * (10 * mt + 5 * mh + n) + 4 * g) * 2;
            xv[sl][t] = *(const u32x2*)(x + oo[sl][t]);
        }
    STAMP(14);
    asm volatile("s_waitcnt vmcnt(0)\n\ts_barrier" ::: "memory");
    STAMP(15);
    gu32* fl = (gu32*)(ws + WS_FL + (size_t)grp64 * 4 * 128);
    if (tid == 0) __hip_atomic_store(fl + KS * 32, 1u, __ATOMIC_RELAXED, __HIP_MEMORY_SCOPE_AGENT);
    for (unsigned spins = 0; spins < (1u << 22); ++spins) {
        unsigned v = 1u;
        if (lane < 4 && lane != KS) v = __hip_atomic_load(fl + lane * 32, __ATOMIC_RELAXED, __HIP_MEMORY_SCOPE_AGENT);
        if (__all(v != 0u)) break;
        __builtin_amdgcn_s_sleep(1);
    }
    asm volatile("" ::: "memory");
    STAMP(16);
    u32x4 p[2][5];
#pragma unroll
    for (int sl = 0; sl < 2; ++sl)
        if (sl < nown) {
            const unsigned j = sl ? 4 : r;
#pragma unroll
            for (int st = 0; st < 5; ++st) {
                const bool own = (st < 3 && st == KS) || (st == 3 && KS == 2) || (st == 4 && KS == 3);
                if (!own) p[sl][st] = __builtin_amdgcn_raw_buffer_load_b128(rs, vo + j * 1024, st * PP_SET, 16);
            }
        }
#pragma unroll
    for (int sl = 0; sl < 2; ++sl)
        if (sl < nown) {
#pragma unroll
            for (int t = 0; t < 2; ++t) {
                f32x4 sv = f32x4{}, bv = f32x4{};
#pragma unroll
                for (int st = 0; st < 5; ++st) {
                    const bool own = (st < 3 && st == KS) || (st == 3 && KS == 2) || (st == 4 && KS == 3);
                    const f32x4 v = own ? ((KS == 2 && st == 2) ? ob[sl][t] : oa[sl][t]) : (t ? unpk_hi(p[sl][st]) : unpk_lo(p[sl][st]));
                    if (st < 3) sv += v; else bv += v;
                }
                const float o0 = bflo(xv[sl][t][0]) + sv[0] * silu_f(bv[0]), o1 = bfhi(xv[sl][t][0]) + sv[1] * silu_f(bv[1]);
                const float o2 = bflo(xv[sl][t][1]) + sv[2] * silu_f(bv[2]), o3 = bfhi(xv[sl][t][1]) + sv[3] * silu_f(bv[3]);
                u32x2 w; w[0] = cvtpk(o0, o1); w[1] = cvtpk(o2, o3);
                *(u32x2*)(out + oo[sl][t]) = w;
            }
        }
}

template <int GRP>
DI void k3_body(const char* __restrict__ x, const char* __restrict__ wo, const char* __restrict__ w2, char* __restrict__ ws, char* __restrict__ out, char* lds) {
    constexpr int P = GRP ? 4 : 5;
    const unsigned tid = threadIdx.x, lane = tid & 63, g = lane >> 4;
    const unsigned wid = rfl(tid >> 6), w4 = wid & 3, qh = w4 >> 1, mh = w4 & 1;
    char* stamp_base = ws; (void)stamp_base;
    const int b = blockIdx.x, xcd = b & 7, idx = b >> 3;
    const int ks = xcd >> 1, qt = 8 * (xcd & 1) + (idx >> 2), mt = idx & 3;
    const int st0 = ks == 0 ? 0 : ks == 1 ? 23 : ks == 2 ? 46 : 68;
    const unsigned lds0 = (unsigned)(uintptr_t)lds;
    lds_cptr L = (lds_cptr)lds;
    const char* aptr = uptr(ws + WS_CH + ((size_t)(8 * qt + wid) * NKS_CH + 2 * st0) * 1024);
    const char* w2b = uptr(w2 + mt * 320);
    const char* wptr = st0 < 64 ? uptr(wo + (size_t)st0 * 64 * 1280 + mt * 320) : w2b + (size_t)(st0 - 64) * 64 * 1280;
    int sdma = st0;
    const unsigned avo = lane * 16;
    unsigned wvo[3];
    const unsigned p0 = GRP ? 12 + 2 * w4 : 3 * w4;
#pragma unroll
    for (int i = 0; i < P - 2; ++i) {
        const unsigned o = (p0 + i) * 1024 + lane * 16, row = o / 320, c16 = (o % 320) >> 4;
        wvo[i] = row * 1280 + ((c16 ^ (((row >> 2) & 1) << 1)) << 4) - 1024 * i;
    }
    const unsigned m0a = lds0 + wid * 2048, m0w = lds0 + 16384 + p0 * 1024;
#define K3_ADV() do { ++sdma; aptr += 2048; wptr = sdma == 64 ? w2b : wptr + 81920; } while (0)
#define K3_DMA(slot) do { set_m0(m0a + (slot)); asm volatile("s_nop 0"); dma_nom0<0>(aptr, avo); dma_nom0<1024>(aptr, avo); \
        set_m0(m0w + (slot)); asm volatile("s_nop 0"); dma_nom0<0>(wptr, wvo[0]); dma_nom0<1024>(wptr, wvo[1]); if (!GRP) dma_nom0<2048>(wptr, wvo[2]); K3_ADV(); } while (0)
    K3_DMA(0); K3_DMA(K3_SLOT); K3_DMA(2 * K3_SLOT); K3_DMA(3 * K3_SLOT);
    const unsigned ard = (8 * qh + GRP) * 1024 + lane * 16;
    const unsigned q = (lane & 15) >> 2, p = lane & 3;
    const unsigned wrow = 16384 + (32 * GRP + 4 * g + q) * 320 + 160 * mh + 8 * p;
    const unsigned sgn = ((g & 1) ? 32u : 0u);
    const unsigned wrdE = (mh & 1) ? wrow - sgn : wrow + sgn;
    const unsigned wrdO = (mh & 1) ? wrow + sgn : wrow - sgn;
    f32x4 acc[5][4], acc2[5][4];
#pragma unroll
    for (int n = 0; n < 5; ++n)
#pragma unroll
        for (int a = 0; a < 4; ++a) { acc[n][a] = f32x4{}; acc2[n][a] = f32x4{}; }
    NOP_A(acc);
    bf16x8 af[2][4], wf[4], w4f[2];
#define K3_RW(slot, n) tr_frag(L + (slot) + (((n) & 1) ? wrdO : wrdE) + 32 * (n), L + (slot) + (((n) & 1) ? wrdO : wrdE) + 32 * (n) + 16 * 320)
#define K3_RA(slot, a) lds_frag(L + (slot) + ard + (a) * 2048)
    asm volatile("s_waitcnt vmcnt(%0)\n\ts_barrier" ::"i"(3 * P) : "memory");
    STAMP(11);
    unsigned cur = 0;
#define K3_ITER(PAR, WN, RD, DM) K3_ITER_(PAR, WN, RD, DM, false)
#define K3_ITER_(PAR, WN, RD, DM, SAFE)                                                                                 \
    do {                                                                                                                \
        asm volatile("s_waitcnt vmcnt(%0) lgkmcnt(0)\n\ts_barrier" ::"i"(WN) : "memory");                               \
        const unsigned nxt_ = cur == 3 * K3_SLOT ? 0u : cur + K3_SLOT;                                                  \
        _Pragma("unroll") for (int n = 0; n < 5; ++n) {                                                                 \
            _Pragma("unroll") for (int a = 0; a < 4; ++a) {                                                             \
                if (SAFE) mfma_acc_s(acc[n][a], n < 4 ? wf[n & 3] : w4f[PAR], af[PAR][a]);                              \
                else if (!K3_NOMFMA) mfma_acc(acc[n][a], n < 4 ? wf[n & 3] : w4f[PAR], af[PAR][a]);                     \
                else { PINR(n < 4 ? wf[n & 3] : w4f[PAR]); PINR(af[PAR][a]); }                                          \
                SBAR();                                                                                                 \
                if (RD && !K3_NORD) {                                                                                               \
                    if (n == 0 && a == 0) w4f[1 - (PAR)] = K3_RW(nxt_, 4);                                              \
                    if (n < 4 && a == 1) af[1 - (PAR)][n & 3] = K3_RA(nxt_, n & 3);                                     \
                    if (n >= 1 && a == 0) wf[(n - 1) & 3] = K3_RW(nxt_, n - 1);                                         \
                }                                                                                                       \
                if (DM && !K3_NODMA) {                                                                                  \
                    if (n == 0 && a == 2) set_m0(m0a + cur);                                                            \
                    if (n == 0 && a == 3) dma_nom0<0>(aptr, avo);                                                       \
                    if (n == 1 && a == 2) dma_nom0<1024>(aptr, avo);                                                    \
                    if (n == 1 && a == 3) set_m0(m0w + cur);                                                            \
                    if (n == 2 && a == 2) dma_nom0<0>(wptr, wvo[0]);                                                    \
                    if (n == 3 && a == 2) dma_nom0<1024>(wptr, wvo[1]);                                                 \
                    if (n == 4 && a == 1 && !GRP) dma_nom0<2048>(wptr, wvo[2]);                                         \
                }                                                                                                       \
                SBAR();                                                                                                 \
            }                                                                                                           \
        }                                                                                                               \
        if (DM) K3_ADV();                                                                                               \
        cur = nxt_;                                                                                                     \
    } while (0)
#define K3_FIRST(PAR) do { _Pragma("unroll") for (int a = 0; a < 4; ++a) af[PAR][a] = K3_RA(0, a);                      \
        _Pragma("unroll") for (int n = 0; n < 4; ++n) wf[n] = K3_RW(0, n);                                              \
        w4f[PAR] = K3_RW(0, 4); } while (0)
    if (ks != 2) { K3_FIRST(1); K3_ITER_(1, 2 * P, true, true, true); }
    else K3_FIRST(0);
    for (int it = 0; it < 9; ++it) { K3_ITER(0, 2 * P, true, true); K3_ITER(1, 2 * P, true, true); }
    if (ks == 2) {
        NOP_A(acc);
#pragma unroll
        for (int n = 0; n < 5; ++n)
#pragma unroll
            for (int a = 0; a < 4; ++a) { acc2[n][a] = acc[n][a]; acc[n][a] = f32x4{}; PIN(acc2[n][a]); }
        NOP_A(acc);
    }
    K3_ITER(0, 2 * P, true, false); K3_ITER(1, P, true, false); K3_ITER(0, 0, true, false); K3_ITER(1, 0, false, false);
    NOP_A(acc);
    STAMP(12);
    f32x4 res[10], res2[10];
    {
        lds_ptr X = (lds_ptr)lds + wid * 10240 + lane * 16;
        lds_cptr Y = (lds_cptr)lds + (wid ^ 4) * 10240 + lane * 16;
#pragma unroll
        for (int a = 0; a < 2; ++a)
#pragma unroll
            for (int n = 0; n < 5; ++n) *(__attribute__((address_space(3))) f32x4*)(X + (a * 5 + n) * 1024) = acc[n][GRP ? a : 2 + a];
        if (ks == 2) {
#pragma unroll
            for (int a = 0; a < 2; ++a)
#pragma unroll
                for (int n = 0; n < 5; ++n) *(__attribute__((address_space(3))) f32x4*)(X + 81920 + (a * 5 + n) * 1024) = acc2[n][GRP ? a : 2 + a];
        }
        asm volatile("s_waitcnt lgkmcnt(0)\n\ts_barrier" ::: "memory");
#pragma unroll
        for (int a = 0; a < 2; ++a)
#pragma unroll
            for (int n = 0; n < 5; ++n) {
                res[a * 5 + n] = acc[n][GRP ? 2 + a : a] + *(const __attribute__((address_space(3))) f32x4*)(Y + (a * 5 + n) * 1024);
                res2[a * 5 + n] = f32x4{};
            }
        if (ks == 2) {
#pragma unroll
            for (int a = 0; a < 2; ++a)
#pragma unroll
                for (int n = 0; n < 5; ++n) res2[a * 5 + n] = acc2[n][GRP ? 2 + a : a] + *(const __attribute__((address_space(3))) f32x4*)(Y + 81920 + (a * 5 + n) * 1024);
        }
    }
    STAMP(17);
    const int grp64 = qt * 4 + mt;
    if (ks == 0) k3_finish<0>(res, res2, x, out, ws, grp64, qt, mt);
    else if (ks == 1) k3_finish<1>(res, res2, x, out, ws, grp64, qt, mt);
    else if (ks == 2) k3_finish<2>(res, res2, x, out, ws, grp64, qt, mt);
    else k3_finish<3>(res, res2, x, out, ws, grp64, qt, mt);
    STAMP(13);
#undef K3_ADV
#undef K3_DMA
#undef K3_RW
#undef K3_RA
#undef K3_ITER
#undef K3_ITER_
#undef K3_FIRST
}

__global__ __launch_bounds__(512, 2) void k3_out(const char* __restrict__ x, const char* __restrict__ wo, const char* __restrict__ w2, char* __restrict__ ws,
                                                 char* __restrict__ out) {
    extern __shared__ __attribute__((aligned(16))) char lds[];
    char* stamp_base = ws; (void)stamp_base;
    STAMP(10);
    if (rfl(threadIdx.x >> 8) == 0) k3_body<0>(x, wo, w2, ws, out, lds);
    else k3_body<1>(x, wo, w2, ws, out, lds);
}

extern "C" void kernel_launch(void* const* d_in, const int* in_sizes, int n_in, void* d_out, int out_size, void* d_ws, size_t ws_size,
                              hipStream_t stream) {
    const char* x = (const char*)d_in[0];
    const char* ckv = (const char*)d_in[1];
    const char* wuk = (const char*)d_in[3];
    const char* wuv = (const char*)d_in[4];
    char* ws = (char*)d_ws;
    (void)hipFuncSetAttribute((const void*)k1_kvproj, hipFuncAttributeMaxDynamicSharedMemorySize, 131072);
    k1_kvproj<<<256, 512, 131072, stream>>>(x, ckv, wuk, wuv, ws, (const char*)d_in[2], (const char*)d_in[7]);
    (void)hipFuncSetAttribute((const void*)k2_attn, hipFuncAttributeMaxDynamicSharedMemorySize, 163840);
    k2_attn<<<256, 512, 163840, stream>>>((const char*)d_in[2], (const char*)d_in[7], (const char*)d_in[8], ws, (const char*)d_in[5], (const char*)d_in[9]);
    (void)hipFuncSetAttribute((const void*)k3_out, hipFuncAttributeMaxDynamicSharedMemorySize, 163840);
    k3_out<<<256, 512, 163840, stream>>>(x, (const char*)d_in[5], (const char*)d_in[9], ws, (char*)d_out);
}
```

```cpp
#include <hip/hip_runtime.h>
#include <hip/hip_bf16.h>
#include <stdint.h>

typedef __bf16 bf16x8 __attribute__((ext_vector_type(8)));
typedef __bf16 bf16x2 __attribute__((ext_vector_type(2)));
typedef short s16x4 __attribute__((ext_vector_type(4)));
typedef short s16x8 __attribute__((ext_vector_type(8)));
typedef short v4i16_t __attribute__((ext_vector_type(4)));
typedef float f32x16 __attribute__((ext_vector_type(16)));
typedef float f32x4 __attribute__((ext_vector_type(4)));
typedef float f32x2 __attribute__((ext_vector_type(2)));
typedef unsigned u32x4 __attribute__((ext_vector_type(4)));
typedef unsigned u32x2 __attribute__((ext_vector_type(2)));
typedef __attribute__((address_space(3))) const char* lds_cptr;
typedef __attribute__((address_space(3))) char* lds_ptr;

#define DI __device__ __forceinline__
#define SBAR() __builtin_amdgcn_sched_barrier(0)
#define MFMA(a, b, c) __builtin_amdgcn_mfma_f32_16x16x32_bf16((a), (b), (c), 0, 0, 0)
#define PIN(x) asm volatile("" : "+v"(x))
#define PINR(x) asm volatile("" ::"v"(x))

constexpr int S_ = 2048, NH = 32, HD = 128, DM = 640, DC = 512, FF = 1728;
constexpr int NKS_X = DM / 32;
constexpr int NKS_C = (NH * HD) / 32;
constexpr int NKS_H = FF / 32;
constexpr int NKS_CH = NKS_C + NKS_H;
constexpr size_t WS_XF = 0;
constexpr size_t WS_KF = WS_XF + (size_t)128 * NKS_X * 1024;
constexpr size_t WS_VF = WS_KF + (size_t)NH * 128 * 4 * 1024;
constexpr size_t WS_CH = WS_VF + (size_t)NH * 64 * 8 * 1024;
constexpr size_t WS_QH = WS_CH + (size_t)128 * NKS_CH * 1024;
constexpr size_t WS_PP = WS_QH + (size_t)256 * 32768;
constexpr size_t PP_BYTES = (size_t)64 * 5 * 8 * 5 * 1024;
constexpr size_t WS_FL = WS_PP + PP_BYTES;
constexpr size_t FL_EPOCH = 0;
constexpr size_t FL_X = 256;
constexpr size_t FL_KV = FL_X + 2048;
constexpr size_t FL_P = 8192;
constexpr size_t WS_END = WS_FL + FL_P + (size_t)256 * 128;

DI unsigned rfl(unsigned x) { return __builtin_amdgcn_readfirstlane(x); }
DI unsigned flane() { unsigned r; asm volatile("v_mbcnt_lo_u32_b32 %0, -1, 0\n\tv_mbcnt_hi_u32_b32 %0, -1, %0" : "=v"(r)); return r; }
#define STAMP(i)
#define TICK(acc)
DI const char* uptr(const char* p) { const unsigned long long v = (unsigned long long)p; const unsigned lo = rfl((unsigned)v), hi = rfl((unsigned)(v >> 32)); return (const char*)(((unsigned long long)hi << 32) | lo); }
DI unsigned cvtpk(float lo, float hi) { f32x2 v = {lo, hi}; bf16x2 b = __builtin_convertvector(v, bf16x2); return __builtin_bit_cast(unsigned, b); }
DI bf16x8 lds_frag(lds_cptr p) { return *(const __attribute__((address_space(3))) bf16x8*)p; }
DI s16x4 lds_tr(lds_cptr p) { return __builtin_bit_cast(s16x4, __builtin_amdgcn_ds_read_tr16_b64_v4i16((__attribute__((address_space(3))) v4i16_t*)p)); }
DI bf16x8 tr_frag(lds_cptr p0, lds_cptr p1) { s16x4 lo = lds_tr(p0), hi = lds_tr(p1); s16x8 v = __builtin_shufflevector(lo, hi, 0, 1, 2, 3, 4, 5, 6, 7); return __builtin_bit_cast(bf16x8, v); }
template <int OFF> DI void dma1(unsigned m0v, const void* sbase, unsigned voff) {
    asm volatile("s_mov_b32 m0, %0\n\ts_nop 0\n\tglobal_load_lds_dwordx4 %1, %2 offset:%3" ::"s"(m0v), "v"(voff), "s"(sbase), "i"(OFF) : "memory");
}
DI u32x4 pack44(const f32x4& a, const f32x4& b) { u32x4 w; w[0] = cvtpk(a[0], a[1]); w[1] = cvtpk(a[2], a[3]); w[2] = cvtpk(b[0], b[1]); w[3] = cvtpk(b[2], b[3]); return w; }
DI u32x4 pack44s(const f32x4& a, const f32x4& b, float sc) { u32x4 w; w[0] = cvtpk(a[0] * sc, a[1] * sc); w[1] = cvtpk(a[2] * sc, a[3] * sc); w[2] = cvtpk(b[0] * sc, b[1] * sc); w[3] = cvtpk(b[2] * sc, b[3] * sc); return w; }
DI u32x4 pack8(const f32x16& a, int b, float sc) {
    u32x4 w; w[0] = cvtpk(a[b + 0] * sc, a[b + 1] * sc); w[1] = cvtpk(a[b + 2] * sc, a[b + 3] * sc);
    w[2] = cvtpk(a[b + 4] * sc, a[b + 5] * sc); w[3] = cvtpk(a[b + 6] * sc, a[b + 7] * sc); return w;
}
DI u32x4 pack8n(const f32x16& a, int b) {
    u32x4 w; w[0] = cvtpk(a[b + 0], a[b + 1]); w[1] = cvtpk(a[b + 2], a[b + 3]); w[2] = cvtpk(a[b + 4], a[b + 5]); w[3] = cvtpk(a[b + 6], a[b + 7]); return w;
}
DI float bf2f(unsigned short u) { return __uint_as_float((unsigned)u << 16); }
DI float bflo(unsigned w) { return __uint_as_float(w << 16); }
DI float bfhi(unsigned w) { return __uint_as_float(w & 0xffff0000u); }
DI float xhalf_sum(float v) { auto r = __builtin_amdgcn_permlane32_swap(__float_as_uint(v), __float_as_uint(v), false, false); return __uint_as_float(r[0]) + __uint_as_float(r[1]); }
DI float xhalf_max(float v) { auto r = __builtin_amdgcn_permlane32_swap(__float_as_uint(v), __float_as_uint(v), false, false); return __builtin_fmaxf(__uint_as_float(r[0]), __uint_as_float(r[1])); }

DI void warm_load(unsigned& d, const char* wa, const char* wb, unsigned id) {
    const unsigned j = id - 1280u;
    const char* p = uptr(id < 1280u ? wa + (size_t)id * 4096 : wb + (size_t)(j < 539u ? j : 539u) * 4096);
    const unsigned voff = flane() * 64;
    asm volatile("s_nop 4\n\tglobal_load_dword %0, %1, %2" : "=&v"(d) : "v"(voff), "s"(p) : "memory");
}
DI void warm_lds(unsigned m0v, const char* wa, const char* wb, unsigned id) {
    const unsigned j = id - 1280u;
    const char* p = uptr(id < 1280u ? wa + (size_t)id * 4096 : wb + (size_t)(j < 539u ? j : 539u) * 4096);
    const unsigned voff = flane() * 64;
    asm volatile("s_mov_b32 m0, %0\n\ts_nop 4\n\tglobal_load_lds_dword %1, %2" ::"s"(m0v), "v"(voff), "s"(p) : "memory");
}

DI void mfma_acc(f32x4& acc, const bf16x8& a, const bf16x8& b) { asm volatile("v_mfma_f32_16x16x32_bf16 %0, %1, %2, %0" : "+v"(acc) : "v"(a), "v"(b)); }
DI void mfma_acc_s(f32x4& acc, const bf16x8& a, const bf16x8& b) { asm volatile("s_nop 1\n\tv_mfma_f32_16x16x32_bf16 %0, %1, %2, %0" : "+v"(acc) : "v"(a), "v"(b)); }
DI void mfma32_acc(f32x16& acc, const bf16x8& a, const bf16x8& b) { asm volatile("v_mfma_f32_32x32x16_bf16 %0, %1, %2, %0" : "+v"(acc) : "v"(a), "v"(b)); }
DI void mfma32_ini(f32x16& d, const bf16x8& a, const bf16x8& b, const f32x16& c) { asm volatile("v_mfma_f32_32x32x16_bf16 %0, %1, %2, %3" : "=&v"(d) : "v"(a), "v"(b), "v"(c)); }
DI float v_exp(float x) { float r; asm volatile("v_exp_f32 %0, %1" : "=v"(r) : "v"(x)); return r; }
DI float v_add(float a, float b) { float r; asm volatile("v_add_f32 %0, %1, %2" : "=v"(r) : "v"(a), "v"(b)); return r; }
DI unsigned v_cvt(float lo, float hi) { unsigned r; asm volatile("v_cvt_pk_bf16_f32 %0, %1, %2" : "=v"(r) : "v"(lo), "v"(hi)); return r; }
DI void set_m0(unsigned v) { asm volatile("s_mov_b32 m0, %0" ::"s"(v) : "memory"); }
template <int OFF> DI void dma_nom0(const void* sbase, unsigned voff) { asm volatile("global_load_lds_dwordx4 %0, %1 offset:%2" ::"v"(voff), "s"(sbase), "i"(OFF) : "memory"); }
DI void dma4(unsigned m0v, const void* sbase, unsigned voff) {
    asm volatile("s_mov_b32 m0, %0\n\ts_nop 0\n\tglobal_load_lds_dwordx4 %1, %2\n\tglobal_load_lds_dwordx4 %1, %2 offset:1024\n\tglobal_load_lds_dwordx4 %1, %2 offset:2048\n\tglobal_load_lds_dwordx4 %1, %2 offset:3072"
                 ::"s"(m0v), "v"(voff), "s"(sbase) : "memory");
}

typedef unsigned long long u64;
DI void st_wt(char* p, const u32x4& v) { asm volatile("global_store_dwordx4 %0, %1, off sc1\n\ts_nop 1" ::"v"(p), "v"(v) : "memory"); }
DI void st_wt_o(const char* sbase, unsigned voff, const u32x4& v) { asm volatile("global_store_dwordx4 %0, %1, %2 sc1\n\ts_nop 1" ::"v"(voff), "v"(v), "s"(sbase) : "memory"); }
DI void flag_set(unsigned wid, const char* sbase, unsigned off, u64 tag) {
    const u32x2 v = u32x2{(unsigned)tag, (unsigned)(tag >> 32)};
    u64 ex;
    asm volatile("s_cmp_lg_u32 %1, 0\n\ts_cbranch_scc1 .Lfs%=\n\ts_mov_b64 %0, exec\n\ts_mov_b64 exec, 1\n\t"
                 "global_store_dwordx2 %2, %3, %4 sc1\n\ts_mov_b64 exec, %0\n.Lfs%=:\n\ts_nop 1"
                 : "=&s"(ex) : "s"(wid), "v"(off), "v"(v), "s"(sbase) : "scc", "memory");
}
DI void flag_ld(u32x2& f, const char* sbase, unsigned voff) { asm volatile("global_load_dwordx2 %0, %1, %2 sc1" : "=&v"(f) : "v"(voff), "s"(sbase) : "memory"); }
template <bool ACQ>
DI void flag_wait(unsigned skip, u32x2& f, const char* sbase, unsigned voff, u64 tag) {
    unsigned cnt;
    asm volatile("s_cmp_lg_u32 %2, 0\n\ts_cbranch_scc1 .Lfe%=\n\t"
                 "v_cmp_ne_u64 vcc, %3, %0\n\ts_cbranch_vccz .Lfd%=\n\ts_mov_b32 %1, 0x40000\n"
                 ".Lfl%=:\n\tglobal_load_dwordx2 %0, %4, %5 sc1\n\ts_waitcnt vmcnt(0)\n\tv_cmp_ne_u64 vcc, %3, %0\n\ts_cbranch_vccz .Lfd%=\n\t"
                 "s_sleep 2\n\ts_sub_u32 %1, %1, 1\n\ts_cbranch_scc0 .Lfl%=\n"
                 ".Lfd%=:\n\t.if %6\n\tbuffer_inv sc1\n\t.endif\n"
                 ".Lfe%=:"
                 : "+v"(f), "=&s"(cnt) : "s"(skip), "s"(tag), "v"(voff), "s"(sbase), "i"(ACQ ? 1 : 0) : "vcc", "scc", "memory");
}

constexpr int WQ_PIECE = 1040, W1_PIECE = 1056, W1_OFF = 16 * WQ_PIECE, PSLOT = W1_OFF + 8 * W1_PIECE;
struct PJ { unsigned wqvo, w1vo, xv0, xv1; const char *wqs, *w1s, *xs; };
DI PJ pj_setup(const char* wq, const char* w1, const char* ws, unsigned wid, int head, int f0, bool nft4, int q16_0) {
    const unsigned lane = flane();
    PJ j;
    j.wqvo = ((lane >> 2) & 3) * 8192 + (lane >> 4) * 64 + (lane & 3) * 16;
    j.w1vo = ((lane >> 2) & 7) * 3456 + (nft4 ? (lane >> 5) * 64 : 0) + (lane & 3) * 16;
    j.wqs = uptr(wq + (size_t)wid * 8 * 8192 + head * 256);
    j.w1s = uptr(w1 + (size_t)wid * 8 * 3456 + f0 * 2);
    j.xs = uptr(ws + WS_XF + (size_t)q16_0 * NKS_X * 1024);
    j.xv0 = lane * 16; j.xv1 = lane * 16 + NKS_X * 1024;
    return j;
}
DI void pj_dma(const PJ& j, unsigned lds0, unsigned wid, int c, int slot) {
    dma1<0>(lds0 + slot + (2 * wid) * WQ_PIECE, j.wqs + (size_t)c * 64 * 8192, j.wqvo);
    dma1<0>(lds0 + slot + (2 * wid + 1) * WQ_PIECE, j.wqs + (size_t)c * 64 * 8192 + 4 * 8192, j.wqvo);
    dma1<0>(lds0 + slot + W1_OFF + wid * W1_PIECE, j.w1s + (size_t)c * 64 * 3456, j.w1vo);
}
DI void pj_xload(const PJ& j, u32x4 (&X)[4], int c) {
    const char* p_ = j.xs + c * 2048;
    asm volatile("global_load_dwordx4 %0, %4, %6 sc1\n\tglobal_load_dwordx4 %1, %5, %6 sc1\n\tglobal_load_dwordx4 %2, %4, %6 offset:1024 sc1\n\tglobal_load_dwordx4 %3, %5, %6 offset:1024 sc1"
                 : "=&v"(X[0]), "=&v"(X[1]), "=&v"(X[2]), "=&v"(X[3]) : "v"(j.xv0), "v"(j.xv1), "s"(p_) : "memory");
}

#define PREFETCH 1
template <bool VT, class F>
DI void k1_mma(f32x4 (&acc)[8][4], int half, const bf16x8 (&c)[4], const bf16x8 (&w)[4], F&& filler) {
#pragma unroll
    for (int rt = 0; rt < 4; ++rt)
#pragma unroll
        for (int nt = 0; nt < 4; ++nt) {
            if (half == 0) acc[rt][nt] = VT ? MFMA(c[rt], w[nt], acc[rt][nt]) : MFMA(w[nt], c[rt], acc[rt][nt]);
            else acc[4 + rt][nt] = VT ? MFMA(c[rt], w[nt], acc[4 + rt][nt]) : MFMA(w[nt], c[rt], acc[4 + rt][nt]);
            filler(rt * 4 + nt);
        }
}

template <bool VT>
DI void k1_tile(const char* __restrict__ Abase, const char* __restrict__ Wbase, char* __restrict__ outp, char* lds, const unsigned (&avo)[4], const unsigned (&wvo)[4],
                unsigned wid, int hp, int rb, char* __restrict__ ws, const char* __restrict__ wq, const char* __restrict__ w1, const char* __restrict__ b1, u64 tag,
                int head2, int f0, bool nft4, int q16_0, u32x4 (&xa)[4], u32x4 (&xb)[4], u32x2 (&bb)[4]) {
    char* stamp_base = ws; (void)stamp_base;
    const char* flb = uptr(ws + WS_FL);
    const unsigned wm = wid >> 2, wn = wid & 3;
    const unsigned lds0 = (unsigned)(uintptr_t)lds;
    unsigned aaddr[2], baddr[4];
    {
        const unsigned lane = flane(), c = lane & 15, g = lane >> 4;
        const unsigned x = (c >> 1) & 7;
#pragma unroll
        for (int s = 0; s < 2; ++s) aaddr[s] = (128 * wm + c) * 128 + (((4 * s + g) ^ x) << 4);
        const unsigned q = (lane & 15) >> 2, p = lane & 3, f = q | ((g & 1) << 2);
#pragma unroll
        for (int nt = 0; nt < 4; ++nt) baddr[nt] = 32768 + (8 * g + q) * 512 + (((8 * wn + 2 * nt + (p >> 1)) ^ (f << 1)) << 4) + ((p & 1) << 3);
    }
    f32x4 acc[8][4];
#pragma unroll
    for (int i = 0; i < 8; ++i)
#pragma unroll
        for (int j = 0; j < 4; ++j) acc[i][j] = f32x4{};
    bf16x8 cf[2][4], wf[2][4];
    lds_cptr L = (lds_cptr)lds;
#define K1_RC(buf, stg, s, half) do { _Pragma("unroll") for (int rt = 0; rt < 4; ++rt) cf[buf][rt] = lds_frag(L + (stg) + aaddr[s] + ((half) * 4 + rt) * 2048); } while (0)
#define K1_RW(buf, stg, s) do { _Pragma("unroll") for (int nt = 0; nt < 4; ++nt) wf[buf][nt] = tr_frag(L + (stg) + baddr[nt] + (s) * 16384, L + (stg) + baddr[nt] + (s) * 16384 + 2048); } while (0)
    const char* Aw = Abase + (size_t)wid * 32 * 1024;
    const char* Ww = Wbase + (size_t)wid * 8 * 8192;
    const unsigned m0a = lds0 + wid * 4096, m0w = lds0 + 32768 + wid * 4096;
#define K1_DMA(kt, stg)                                                                   \
    do {                                                                                  \
        const char* a_ = Aw + (kt) * 128; const char* w_ = Ww + (size_t)(kt) * 64 * 8192; \
        dma1<0>(m0a + (stg), a_, avo[0]); dma1<1024>(m0a + (stg), a_, avo[1]); dma1<2048>(m0a + (stg), a_, avo[2]); dma1<3072>(m0a + (stg), a_, avo[3]); \
        dma1<0>(m0w + (stg), w_, wvo[0]); dma1<1024>(m0w + (stg), w_, wvo[1]); dma1<2048>(m0w + (stg), w_, wvo[2]); dma1<3072>(m0w + (stg), w_, wvo[3]); \
    } while (0)
    unsigned pfd = 0;
    u32x2 fx = u32x2{};
#define K1_FXO() (FL_X + (2 * q16_0 + (flane() & 3)) * 8)
    STAMP(1);
    asm volatile("s_waitcnt vmcnt(10)\n\ts_barrier" ::: "memory");
    STAMP(2);
    K1_RW(0, 0, 0); K1_RC(0, 0, 0, 0);
#define K1_RC1(buf, stg, s, half, rt) cf[buf][rt] = lds_frag(L + (stg) + aaddr[s] + ((half) * 4 + (rt)) * 2048)
#define K1_RW1(buf, stg, s, nt) wf[buf][nt] = tr_frag(L + (stg) + baddr[nt] + (s) * 16384, L + (stg) + baddr[nt] + (s) * 16384 + 2048)
#pragma unroll
    for (int kt = 0; kt < 8; ++kt) {
        const unsigned stg = (kt & 1) * 65536, nstg = stg ^ 65536;
        SBAR();
        k1_mma<VT>(acc, 0, cf[0], wf[0], [&](int m) { if (m < 8 && (m & 1) == 0) { K1_RC1(1, stg, 0, 1, m >> 1); SBAR(); } });
        SBAR();
        k1_mma<VT>(acc, 1, cf[1], wf[0], [&](int m) { if (m < 4) { K1_RC1(0, stg, 1, 0, m); SBAR(); } else if (m < 12 && (m & 1) == 0) { K1_RW1(1, stg, 1, (m - 4) >> 1); SBAR(); } });
        SBAR();
        k1_mma<VT>(acc, 0, cf[0], wf[1], [&](int m) { if (m < 8 && (m & 1) == 0) { K1_RC1(1, stg, 1, 1, m >> 1); SBAR(); } });
        SBAR();
        if (kt == 7) asm volatile("s_waitcnt lgkmcnt(0)\n\ts_barrier" ::: "memory");
        else asm volatile("s_waitcnt vmcnt(0) lgkmcnt(0)\n\ts_barrier" : "+v"(fx)::"memory");
        if (kt == 0) flag_set(wid, flb, FL_X + blockIdx.x * 8, tag);
        if (kt == 6) flag_wait<false>(0, fx, flb, K1_FXO(), tag);
        const char* a_ = Aw + (kt + 2) * 128; const char* w_ = Ww + (size_t)(kt + 2) * 64 * 8192;
        k1_mma<VT>(acc, 1, cf[1], wf[1], [&](int m) {
            if (kt + 1 < 8) { if (m < 4) { K1_RW1(0, nstg, 0, m); SBAR(); } else if (m < 8) { K1_RC1(0, nstg, 0, 0, m - 4); SBAR(); } }
            if (kt + 2 < 8) {
                if (m == 1) { dma1<0>(m0a + stg, a_, avo[0]); SBAR(); } if (m == 3) { dma1<1024>(m0a + stg, a_, avo[1]); SBAR(); }
                if (m == 5) { dma1<2048>(m0a + stg, a_, avo[2]); SBAR(); } if (m == 7) { dma1<3072>(m0a + stg, a_, avo[3]); SBAR(); }
                if (m == 9) { dma1<0>(m0w + stg, w_, wvo[0]); SBAR(); } if (m == 11) { dma1<1024>(m0w + stg, w_, wvo[1]); SBAR(); }
                if (m == 13) { dma1<2048>(m0w + stg, w_, wvo[2]); SBAR(); } if (m == 15) { dma1<3072>(m0w + stg, w_, wvo[3]); SBAR(); }
            }
            if (PREFETCH && kt == 5 && m == 15) {
                warm_load(pfd, wq, w1, blockIdx.x * 8 + wid);
                SBAR();
            }
            if (kt == 4 && m == 14) { flag_ld(fx, flb, K1_FXO()); SBAR(); }
            if (kt == 6 && m == 2) {
                const PJ j = pj_setup(wq, w1, ws, wid, head2, f0, nft4, q16_0); pj_dma(j, lds0, wid, 0, 0); pj_dma(j, lds0, wid, 1, PSLOT); SBAR();
            }
            if (kt == 7) {
                if (m == 0) {
                    const char* bp = uptr(b1 + f0 * 2);
                    const unsigned bo = (flane() >> 4) * 8, b2 = bo + (nft4 ? 64 : 0);
                    asm volatile("s_nop 4\n\tglobal_load_dwordx2 %0, %4, %6\n\tglobal_load_dwordx2 %1, %4, %6 offset:32\n\tglobal_load_dwordx2 %2, %5, %6\n\tglobal_load_dwordx2 %3, %5, %6 offset:32"
                                 : "=&v"(bb[0]), "=&v"(bb[1]), "=&v"(bb[2]), "=&v"(bb[3]) : "v"(bo), "v"(b2), "s"(bp) : "memory");
                    SBAR();
                }
                if (m == 4) { const PJ j = pj_setup(wq, w1, ws, wid, head2, f0, nft4, q16_0); pj_xload(j, xa, 0); pj_xload(j, xb, 1); SBAR(); }
            }
        });
        SBAR();
    }
    STAMP(3);
    const int head = 2 * hp + (wn >> 1), d0 = (wn & 1) * 64;
    const int key0 = 256 * rb + 128 * wm;
    const unsigned lane = flane(), c = lane & 15, g = lane >> 4;
    if (VT) {
        const unsigned lo = (g >> 1) * 1024 + (c + 32 * (g & 1)) * 16;
#pragma unroll
        for (int u = 0; u < 4; ++u)
#pragma unroll
            for (int nt = 0; nt < 4; ++nt) {
                const size_t off = ((size_t)(head * 64 + (key0 >> 5) + u) * 4 + (d0 >> 5) + (nt >> 1)) * 2048 + (nt & 1) * 256;
                st_wt_o(uptr(outp + off), lo, pack44(acc[2 * u][nt], acc[2 * u + 1][nt]));
            }
    } else {
        const unsigned lo = (g >> 1) * 1024 + ((c & 7) + 16 * (c >> 3) + 32 * (g & 1)) * 16;
#pragma unroll
        for (int kt = 0; kt < 8; ++kt)
#pragma unroll
            for (int v = 0; v < 2; ++v) {
                const size_t off = ((size_t)(head * 64 + (key0 >> 5) + (kt >> 1)) * 4 + (d0 >> 5) + v) * 2048 + (kt & 1) * 128;
                st_wt_o(uptr(outp + off), lo, pack44(acc[kt][2 * v], acc[kt][2 * v + 1]));
            }
    }
    STAMP(4);
    asm volatile("s_waitcnt vmcnt(16)\n\ts_barrier" : "+v"(pfd), "+v"(xa[0]), "+v"(xa[1]), "+v"(xa[2]), "+v"(xa[3]), "+v"(xb[0]), "+v"(xb[1]), "+v"(xb[2]), "+v"(xb[3]),
                 "+v"(bb[0]), "+v"(bb[1]), "+v"(bb[2]), "+v"(bb[3])::"memory");
    STAMP(5);
#undef K1_RC
#undef K1_RW
#undef K1_DMA
#undef K1_FXO
}

constexpr float QSCALE = 0.08838834764831845f * 1.4426950408889634f;
constexpr float THR = 8.0f;
DI float x4_max(float x) {
    auto s = __builtin_amdgcn_permlane16_swap(__float_as_uint(x), __float_as_uint(x), false, false);
    x = __builtin_fmaxf(__uint_as_float(s[0]), __uint_as_float(s[1]));
    auto t = __builtin_amdgcn_permlane32_swap(__float_as_uint(x), __float_as_uint(x), false, false);
    return __builtin_fmaxf(__uint_as_float(t[0]), __uint_as_float(t[1]));
}
DI float x4_sum(float x) {
    auto s = __builtin_amdgcn_permlane16_swap(__float_as_uint(x), __float_as_uint(x), false, false);
    x = __uint_as_float(s[0]) + __uint_as_float(s[1]);
    auto t = __builtin_amdgcn_permlane32_swap(__float_as_uint(x), __float_as_uint(x), false, false);
    return __uint_as_float(t[0]) + __uint_as_float(t[1]);
}
DI float mish_f(float h) {
    const float e = __builtin_amdgcn_exp2f(__builtin_fminf(h, 30.0f) * 1.4426950408889634f);
    const float n = e * (e + 2.0f);
    return h * n * __builtin_amdgcn_rcpf(n + 2.0f);
}

template <int NFT>
DI void proj_phase(const char* __restrict__ wq, const char* __restrict__ w1, char* __restrict__ ws, char* lds, int head, int f0, int q16_0, bf16x8 (&qf)[2][4],
                   const char* __restrict__ wo, const char* __restrict__ w2, unsigned m0v0, unsigned wid, u32x4 (&xa)[4], u32x4 (&xb)[4], const u32x2 (&bb)[4], u64 tag,
                   unsigned kvflag, int hp2, unsigned m0k0, unsigned m0k1, const char* kptr0, const char* kptr1) {
    const unsigned lane = flane(), g = lane >> 4;
    const unsigned lds0 = (unsigned)(uintptr_t)lds;
    lds_cptr L = (lds_cptr)lds;
    const PJ pj = pj_setup(wq, w1, ws, wid, head, f0, NFT == 4, q16_0);
    const char* flb = uptr(ws + WS_FL);
    u32x2 fkv = u32x2{};
    const unsigned fkvo = FL_KV + (hp2 * 16 + (lane & 15)) * 8;
    const unsigned q = (lane & 15) >> 2, p = lane & 3;
    const unsigned wqb = 2 * g * WQ_PIECE + q * 64 + p * 8;
    const unsigned w1b = W1_OFF + g * W1_PIECE + q * 64 + p * 8;
    f32x4 aq[8][2], ah[NFT][2];
#pragma unroll
    for (int i = 0; i < 8; ++i) { aq[i][0] = f32x4{}; aq[i][1] = f32x4{}; }
#pragma unroll
    for (int i = 0; i < NFT; ++i) { ah[i][0] = f32x4{}; ah[i][1] = f32x4{}; }
    constexpr int NT = 8 + NFT;
    bf16x8 wf[4];
#define PJ_RD(slot, s, n)                                                                                       \
    (((n) < 8) ? tr_frag(L + (slot) + wqb + (8 * (s)) * WQ_PIECE + ((n) >> 1) * 256 + ((n) & 1) * 32, L + (slot) + wqb + (8 * (s) + 1) * WQ_PIECE + ((n) >> 1) * 256 + ((n) & 1) * 32) \
               : tr_frag(L + (slot) + w1b + (4 * (s)) * W1_PIECE + (((n) - 8) >> 1) * 512 + ((n) & 1) * 32, L + (slot) + w1b + (4 * (s)) * W1_PIECE + 256 + (((n) - 8) >> 1) * 512 + ((n) & 1) * 32))
#pragma unroll
    for (int n = 0; n < 3; ++n) wf[n] = PJ_RD(0, 0, n);
#define PJ_CHUNK(c, XC, XN)                                                                                     \
    do {                                                                                                        \
        constexpr int slot_ = ((c) % 3) * PSLOT, nslot_ = (((c) + 1) % 3) * PSLOT, dslot_ = (((c) + 2) % 3) * PSLOT; \
        if ((c) == 4) flag_ld(fkv, flb, fkvo);                                                                  \
          \
        if ((c) == 5) flag_wait<true>(wid, fkv, flb, fkvo, tag);                                                \
        if ((c) >= 1 && (c) + 1 < 10) pj_xload(pj, XN, (c) + 1);                                                \
        if ((c) + 2 < 10) pj_dma(pj, lds0, wid, (c) + 2, dslot_);                                               \
        if ((c) == 9) {       \
            dma4(m0k0, kptr0, lane * 16); dma4(m0k1, kptr1, lane * 16);                                         \
            if (PREFETCH) warm_lds(m0v0, wo, w2, blockIdx.x * 8 + wid);                                         \
        }                                                                                                       \
        SBAR();                                                                                                 \
        _Pragma("unroll") for (int m = 0; m < 2 * NT; ++m) {                                                    \
            const int s = m / NT, n = m % NT;                                                                   \
            if (m == 2 * NT - 3) {       \
                if ((c) == 0) asm volatile("s_waitcnt lgkmcnt(0)\n\ts_barrier" ::: "memory");                   \
                else if ((c) + 2 < 10) asm volatile("s_waitcnt vmcnt(3) lgkmcnt(0)\n\ts_barrier" : "+v"(XN[0]), "+v"(XN[1]), "+v"(XN[2]), "+v"(XN[3]), "+v"(fkv)::"memory"); \
                else if ((c) + 1 < 10) asm volatile("s_waitcnt vmcnt(0) lgkmcnt(0)\n\ts_barrier" : "+v"(XN[0]), "+v"(XN[1]), "+v"(XN[2]), "+v"(XN[3])::"memory"); \
                if ((c) == 1) flag_set(wid, flb, kvflag, tag);        \
            }                                                                                                   \
            const int m3 = m + 3;                                                                               \
            if (m3 < 2 * NT) wf[m3 & 3] = PJ_RD(slot_, m3 / NT, m3 % NT);                                       \
            else if ((c) + 1 < 10) wf[m3 & 3] = PJ_RD(nslot_, 0, m3 - 2 * NT);                                  \
            const bf16x8 x0 = __builtin_bit_cast(bf16x8, XC[2 * s]), x1 = __builtin_bit_cast(bf16x8, XC[2 * s + 1]); \
            if (n < 8) { aq[n & 7][0] = MFMA(wf[m & 3], x0, aq[n & 7][0]); aq[n & 7][1] = MFMA(wf[m & 3], x1, aq[n & 7][1]); } \
            else { ah[(n - 8) % NFT][0] = MFMA(wf[m & 3], x0, ah[(n - 8) % NFT][0]); ah[(n - 8) % NFT][1] = MFMA(wf[m & 3], x1, ah[(n - 8) % NFT][1]); } \
            SBAR();                                                                                             \
        }                                                                                                       \
    } while (0)
    static_assert((2 * NT) % 4 == 0, "fragment window");
    PJ_CHUNK(0, xa, xb); PJ_CHUNK(1, xb, xa); PJ_CHUNK(2, xa, xb); PJ_CHUNK(3, xb, xa); PJ_CHUNK(4, xa, xb);
    PJ_CHUNK(5, xb, xa); PJ_CHUNK(6, xa, xb); PJ_CHUNK(7, xb, xa); PJ_CHUNK(8, xa, xb); PJ_CHUNK(9, xb, xa);
#undef PJ_CHUNK
#undef PJ_RD
    asm volatile("s_waitcnt vmcnt(%0)" ::"i"(PREFETCH ? 1 : 0) : "memory");
#pragma unroll
    for (int a = 0; a < 2; ++a)
#pragma unroll
        for (int v = 0; v < 4; ++v) qf[a][v] = __builtin_bit_cast(bf16x8, pack44s(aq[2 * v][a], aq[2 * v + 1][a], QSCALE));
#pragma unroll
    for (int v = 0; v < NFT / 2; ++v) {
        float bias[8];
#pragma unroll
        for (int t = 0; t < 2; ++t) {
            const u32x2 q2 = bb[2 * v + t];
            bias[4 * t + 0] = bflo(q2[0]); bias[4 * t + 1] = bfhi(q2[0]); bias[4 * t + 2] = bflo(q2[1]); bias[4 * t + 3] = bfhi(q2[1]);
        }
#pragma unroll
        for (int a = 0; a < 2; ++a) {
            f32x4 h0, h1;
#pragma unroll
            for (int i = 0; i < 4; ++i) { h0[i] = mish_f(ah[2 * v][a][i] + bias[i]); h1[i] = mish_f(ah[2 * v + 1][a][i] + bias[4 + i]); }
            *(u32x4*)(ws + WS_CH + ((size_t)(q16_0 + a) * NKS_CH + NKS_C + (f0 >> 5) + v) * 1024 + lane * 16) = pack44(h0, h1);
        }
    }
}

#define NOP_SO(S, o) asm volatile("s_nop 7\n\ts_nop 3" : "+v"(S[0]), "+v"(S[1]), "+v"(o[0]), "+v"(o[1]), "+v"(o[2]), "+v"(o[3]))
#define NOP_A(A) asm volatile("s_nop 7\n\ts_nop 1" : "+v"(A[0][0]), "+v"(A[0][1]), "+v"(A[0][2]), "+v"(A[0][3]), "+v"(A[1][0]), "+v"(A[1][1]), "+v"(A[1][2]), "+v"(A[1][3]), \
    "+v"(A[2][0]), "+v"(A[2][1]), "+v"(A[2][2]), "+v"(A[2][3]), "+v"(A[3][0]), "+v"(A[3][1]), "+v"(A[3][2]), "+v"(A[3][3]), "+v"(A[4][0]), "+v"(A[4][1]), "+v"(A[4][2]), "+v"(A[4][3]))
DI float xh_sum(float v) { auto r = __builtin_amdgcn_permlane32_swap(__float_as_uint(v), __float_as_uint(v), false, false); return __uint_as_float(r[0]) + __uint_as_float(r[1]); }
DI float xh_max(float v) { auto r = __builtin_amdgcn_permlane32_swap(__float_as_uint(v), __float_as_uint(v), false, false); return __builtin_fmaxf(__uint_as_float(r[0]), __uint_as_float(r[1])); }
DI float mul_s(float a, float b) { float r; asm("v_mul_f32 %0, %1, %2" : "=v"(r) : "v"(a), "v"(b)); return r; }
DI float sub_s(float a, float b) { float r; asm("v_sub_f32 %0, %1, %2" : "=v"(r) : "v"(a), "v"(b)); return r; }
DI u32x4 pack_o(const f32x16& a, int y, float sc) {
    u32x4 w; w[0] = cvtpk(mul_s(a[4 * y + 0], sc), mul_s(a[4 * y + 1], sc)); w[1] = cvtpk(mul_s(a[4 * y + 2], sc), mul_s(a[4 * y + 3], sc));
    w[2] = cvtpk(mul_s(a[8 + 4 * y + 0], sc), mul_s(a[8 + 4 * y + 1], sc)); w[3] = cvtpk(mul_s(a[8 + 4 * y + 2], sc), mul_s(a[8 + 4 * y + 3], sc)); return w;
}
#define ABL_NODMA 0
#define ABL_NOEXP 0
#define ABL_NOMFMA 0
#define DMAPOS 2
DI void mask_diag(f32x16& s, int dq) {
    const float ninf = -__builtin_inff();
#pragma unroll
    for (int r0 = 0; r0 < 16; r0 += 4) {
        unsigned long long m0, m1, m2, m3;
        const int kc = 16 * ((r0 >> 2) & 1) + 8 * (r0 >> 3);
        float a = s[r0], b = s[r0 + 1], c = s[r0 + 2], d = s[r0 + 3];
        asm("v_cmp_gt_i32_e64 %4, %9, %8\n\tv_cmp_gt_i32_e64 %5, %10, %8\n\tv_cmp_gt_i32_e64 %6, %11, %8\n\tv_cmp_gt_i32_e64 %7, %12, %8\n\t"
            "v_cndmask_b32_e64 %0, %0, %13, %4\n\tv_cndmask_b32_e64 %1, %1, %13, %5\n\tv_cndmask_b32_e64 %2, %2, %13, %6\n\tv_cndmask_b32_e64 %3, %3, %13, %7"
            : "+v"(a), "+v"(b), "+v"(c), "+v"(d), "=&s"(m0), "=&s"(m1), "=&s"(m2), "=&s"(m3)
            : "v"(dq), "i"(kc), "i"(kc + 1), "i"(kc + 2), "i"(kc + 3), "v"(ninf));
        s[r0] = a; s[r0 + 1] = b; s[r0 + 2] = c; s[r0 + 3] = d;
    }
}
#define FRW 8
#define FRA 6
#define PADN -1
#define MPRIO 2

__global__ __launch_bounds__(512, 2) void k12(const char* __restrict__ x, const char* __restrict__ ckv, const char* __restrict__ wuk, const char* __restrict__ wuv,
                                              const char* __restrict__ wq, const char* __restrict__ w1, const char* __restrict__ b1, char* __restrict__ ws,
                                              const char* __restrict__ wo, const char* __restrict__ w2) {
    extern __shared__ __attribute__((aligned(16))) char lds[];
    const unsigned wid = rfl(threadIdx.x >> 6), grp = wid >> 2, w4 = wid & 3;
    char* stamp_base = ws; (void)stamp_base;
    STAMP(0);
    const int b = blockIdx.x, xcd = b & 7, idx = b >> 3;
    if (threadIdx.x == 0) *(unsigned*)(ws + WS_FL + FL_P + (size_t)b * 128) = 0u;
    const int vt = idx & 1, hp = 2 * xcd + ((idx >> 1) & 1), rb = idx >> 2;
    const int head = 4 * xcd + (idx & 3), pi = idx >> 2;
    const int HB = 15 - pi, LB = pi;
    const int myblk = grp ? LB : HB;
    const int q16_0 = 8 * myblk + 2 * w4;
    const bool nft4 = head < 22;
    const int f0 = nft4 ? 64 * head : 1408 + 32 * (head - 22);
    const char* Abase = ckv + (size_t)rb * 256 * 1024;
    const char* Wbase = (vt ? wuv : wuk) + hp * 512;
    const unsigned lane0 = flane();
#define lane lane0
    const int row = 8 * b + wid;
    const unsigned l40 = lane < 40 ? lane : 39;
    u32x4 x0, x1;
    {
        const char* xr = uptr(x + (size_t)row * 1280);
        const unsigned vo = l40 * 32;
        asm volatile("global_load_dwordx4 %0, %2, %3\n\tglobal_load_dwordx4 %1, %2, %3 offset:16" : "=&v"(x0), "=&v"(x1) : "v"(vo), "s"(xr) : "memory");
    }
    unsigned avo[4], wvo[4];
    {
        const unsigned lds0 = (unsigned)(uintptr_t)lds;
        const char* Aw = Abase + (size_t)wid * 32 * 1024;
        const char* Ww = Wbase + (size_t)wid * 8 * 8192;
        const unsigned m0a = lds0 + wid * 4096, m0w = lds0 + 32768 + wid * 4096;
#pragma unroll
        for (int p = 0; p < 4; ++p) {
            const unsigned rw = 8 * p + (lane >> 3);
            avo[p] = rw * 1024 + (((lane & 7) ^ ((rw >> 1) & 7)) << 4) - 1024 * p;
            const unsigned k = 2 * p + (lane >> 5);
            const unsigned f = (k & 3) | ((wid & 1) << 2);
            wvo[p] = k * 8192 + (((lane & 31) ^ (f << 1)) << 4) - 1024 * p;
        }
#pragma unroll
        for (int kt = 0; kt < 2; ++kt) {
            const char* a_ = Aw + kt * 128; const char* w_ = Ww + (size_t)kt * 64 * 8192; const unsigned stg = kt * 65536;
            dma1<0>(m0a + stg, a_, avo[0]); dma1<1024>(m0a + stg, a_, avo[1]); dma1<2048>(m0a + stg, a_, avo[2]); dma1<3072>(m0a + stg, a_, avo[3]);
            dma1<0>(m0w + stg, w_, wvo[0]); dma1<1024>(m0w + stg, w_, wvo[1]); dma1<2048>(m0w + stg, w_, wvo[2]); dma1<3072>(m0w + stg, w_, wvo[3]);
        }
    }
    u64 tag;
    asm volatile("s_load_dwordx2 %0, %1, 0x0\n\ts_waitcnt lgkmcnt(0)" : "=s"(tag) : "s"(uptr(ws + WS_FL + FL_EPOCH)) : "memory");
    tag += 1;
    {
        asm volatile("s_waitcnt vmcnt(16)" : "+v"(x0), "+v"(x1)::"memory");
        float v[16];
#pragma unroll
        for (int i = 0; i < 4; ++i) { v[2 * i] = bflo(x0[i]); v[2 * i + 1] = bfhi(x0[i]); v[8 + 2 * i] = bflo(x1[i]); v[8 + 2 * i + 1] = bfhi(x1[i]); }
        float s = 0.f;
#pragma unroll
        for (int i = 0; i < 16; ++i) s += v[i];
        if (lane >= 40) s = 0.f;
#pragma unroll
        for (int o = 32; o >= 1; o >>= 1) s += __shfl_xor(s, o);
        const float mean = s * (1.0f / DM);
        float q = 0.f;
#pragma unroll
        for (int i = 0; i < 16; ++i) { v[i] -= mean; q += v[i] * v[i]; }
        if (lane >= 40) q = 0.f;
#pragma unroll
        for (int o = 32; o >= 1; o >>= 1) q += __shfl_xor(q, o);
        const float rstd = 1.0f / sqrtf(q * (1.0f / DM) + 1e-5f);
        u32x4 o0, o1;
#pragma unroll
        for (int i = 0; i < 4; ++i) { o0[i] = cvtpk(v[2 * i] * rstd, v[2 * i + 1] * rstd); o1[i] = cvtpk(v[8 + 2 * i] * rstd, v[8 + 2 * i + 1] * rstd); }
        char* xf = ws + WS_XF + ((size_t)(row >> 4) * NKS_X + (l40 >> 1)) * 1024 + ((row & 15) + 32 * (l40 & 1)) * 16;
        if (lane < 40) { st_wt(xf, o0); st_wt(xf + 256, o1); }
    }
#undef lane
    u32x4 xa[4], xb[4];
    u32x2 bb[4];
    if (vt) k1_tile<true>(Abase, Wbase, ws + WS_VF, lds, avo, wvo, wid, hp, rb, ws, wq, w1, b1, tag, head, f0, nft4, q16_0, xa, xb, bb);
    else    k1_tile<false>(Abase, Wbase, ws + WS_KF, lds, avo, wvo, wid, hp, rb, ws, wq, w1, b1, tag, head, f0, nft4, q16_0, xa, xb, bb);
    STAMP(6);
    const unsigned lds0 = (unsigned)(uintptr_t)lds;
    const unsigned kslot0 = grp ? 114688 : 81920, kslot1 = kslot0 + 16384, kslot2 = grp ? 16384 : 0;
    const unsigned vslot0 = grp ? 65536 : 32768, vslot1 = grp ? 147456 : 49152;
    const int nL = 2 * pi + 2;
    const int nsw = grp ? nL : 1000;
    const size_t jump = (size_t)(17 - nL) * 16384;
    const char* kptr = uptr(ws + WS_KF + (size_t)head * 524288 + w4 * 4096);
    const char* vptr = uptr(ws + WS_VF + (size_t)head * 524288 + w4 * 4096);
    bf16x8 qf[8];
    {
        bf16x8 q16[2][4];
        const unsigned kvflag = FL_KV + (hp * 16 + vt * 8 + rb) * 8;
        if (nft4) proj_phase<4>(wq, w1, ws, lds, head, f0, q16_0, q16, wo, w2, lds0 + vslot0 + w4 * 4096, wid, xa, xb, bb, tag, kvflag, head >> 1, lds0 + kslot0 + w4 * 4096, lds0 + kslot1 + w4 * 4096, kptr, kptr + 16384);
        else      proj_phase<2>(wq, w1, ws, lds, head, f0, q16_0, q16, wo, w2, lds0 + vslot0 + w4 * 4096, wid, xa, xb, bb, tag, kvflag, head >> 1, lds0 + kslot0 + w4 * 4096, lds0 + kslot1 + w4 * 4096, kptr, kptr + 16384);
        kptr += 2 * 16384; if (nsw == 2) kptr += jump;
#pragma unroll
        for (int v = 0; v < 4; ++v) {
            const u32x4 r0 = __builtin_bit_cast(u32x4, q16[0][v]), r1 = __builtin_bit_cast(u32x4, q16[1][v]);
            u32x4 z0, z1;
#pragma unroll
            for (int w = 0; w < 4; ++w) {
                const auto s1 = __builtin_amdgcn_permlane16_swap(r0[w], r1[w], false, false);
                const auto s2 = __builtin_amdgcn_permlane32_swap(s1[0], s1[1], false, false);
                z0[w] = s2[0]; z1[w] = s2[1];
            }
            qf[2 * v] = __builtin_bit_cast(bf16x8, z0); qf[2 * v + 1] = __builtin_bit_cast(bf16x8, z1);
        }
    }
    STAMP(7);
    const unsigned lane = flane();
    if (grp == 0) {
        char* qh = ws + WS_QH + (size_t)b * 32768 + w4 * 8192 + lane * 16;
#pragma unroll
        for (int s = 0; s < 8; ++s) *(bf16x8*)(qh + s * 1024) = qf[s];
    }
    lds_cptr Lb = (lds_cptr)lds + lane * 16;
    const unsigned m0b = lds0 + w4 * 4096;
    const unsigned dvo = lane * 16;
    int q0 = 128 * myblk + 32 * w4;
    int tile = 0;
    asm volatile("s_waitcnt lgkmcnt(0)\n\ts_barrier" ::: "memory");
    dma4(m0b + vslot0, vptr, dvo); vptr += 16384;
    dma4(m0b + kslot2, kptr, dvo); kptr += 16384;
    f32x16 o[4], S[2], negm;
    float mref = 0.f, lsum = 0.f, lfin = 1.f;
    bf16x8 pf[4], fr[FRW];
#pragma unroll
    for (int i = 0; i < 4; ++i) o[i] = f32x16{};
    negm = f32x16{}; S[0] = f32x16{}; S[1] = f32x16{};
    asm volatile("s_nop 1" : "+v"(negm));
    NOP_SO(S, o);
    unsigned ks_cur = kslot0, ks_nxt = kslot1, ks_nn = kslot2;
#define KOFF(i) (((i) & 1) * 8192 + ((i) >> 1) * 1024)
#define VOFF(j) (((j) >> 3) * 8192 + ((j) & 3) * 2048 + (((j) >> 2) & 1) * 1024)
#define EARLY_K(slot) do { _Pragma("unroll") for (int i = 0; i < FRA; ++i) fr[i] = lds_frag(Lb + (slot) + KOFF(i)); } while (0)
    EARLY_K(kslot0);
    if (grp) asm volatile("s_barrier" ::: "memory");
#define PHASE_M(t, QK, PV, VS, DV, DK, WN)      \
    do {                                                                                                                              \
        if (MPRIO == 1) __builtin_amdgcn_s_setprio(1);                                                                                \
        if (MPRIO >= 2) __builtin_amdgcn_s_setprio(0);                                                                                \
        lds_cptr kp_ = Lb + ks_cur; lds_cptr vp_ = Lb + ((VS) ? vslot1 : vslot0);                                                     \
        if (!(QK)) { _Pragma("unroll") for (int i = 16; i < 16 + FRA; ++i) fr[i % FRW] = lds_frag(vp_ + VOFF(i - 16)); }               \
        _Pragma("unroll") for (int i = (QK) ? 0 : 16; i < ((PV) ? 32 : 16); ++i) {                                                    \
            const int n = i + FRA;                                                                                                    \
            if (n < 16) fr[n % FRW] = lds_frag(kp_ + KOFF(n)); else if ((PV) && n < 32) fr[n % FRW] = lds_frag(vp_ + VOFF(n - 16));    \
            SBAR();                                                                                                                   \
            if (ABL_NOMFMA && (i & (ABL_NOMFMA - 1)) != 0) { PINR(fr[i % FRW]); }                                                       \
            else if (i < 2) mfma32_ini(S[i & 1], fr[i % FRW], qf[i >> 1], negm);                                                        \
            else if (i < 16) mfma32_acc(S[i & 1], fr[i % FRW], qf[i >> 1]);                                                             \
            else mfma32_acc(o[i & 3], fr[i % FRW], pf[(i - 16) >> 2]);                                                                  \
            SBAR();                                                                                                                   \
            if (PADN >= 0) {     \
                const bool dm_ = ((DV) && i >= 1 && i <= 7 && (i & 1)) || ((DK) && i >= 9 && i <= 15 && (i & 1));                      \
                if (!dm_) asm volatile("s_nop %0" ::"i"(PADN >= 0 ? PADN : 0)); else if (PADN >= 1) asm volatile("s_nop %0" ::"i"(PADN >= 1 ? PADN - 1 : 0)); \
            }                                                                                                                         \
            if ((QK) && i == 16 && (t) + 1 == nsw) {        \
                const char* qs_ = uptr(ws + WS_QH + (size_t)b * 32768 + w4 * 8192);                                                   \
                asm volatile("s_nop 4\n\tglobal_load_dwordx4 %0, %8, %9\n\tglobal_load_dwordx4 %1, %8, %9 offset:1024\n\tglobal_load_dwordx4 %2, %8, %9 offset:2048\n\tglobal_load_dwordx4 %3, %8, %9 offset:3072" \
                             : "+v"(qf[0]), "+v"(qf[1]), "+v"(qf[2]), "+v"(qf[3]), "+v"(qf[4]), "+v"(qf[5]), "+v"(qf[6]), "+v"(qf[7]) : "v"(dvo), "s"(qs_) : "memory"); \
                asm volatile("global_load_dwordx4 %4, %8, %9\n\tglobal_load_dwordx4 %5, %8, %9 offset:1024\n\tglobal_load_dwordx4 %6, %8, %9 offset:2048\n\tglobal_load_dwordx4 %7, %8, %9 offset:3072" \
                             : "+v"(qf[0]), "+v"(qf[1]), "+v"(qf[2]), "+v"(qf[3]), "+v"(qf[4]), "+v"(qf[5]), "+v"(qf[6]), "+v"(qf[7]) : "v"(dvo), "s"(qs_ + 4096) : "memory"); \
            }                                                                                                                         \
            SBAR();                                                                                                                   \
        }                                                                                                                             \
        if (MPRIO == 1) __builtin_amdgcn_s_setprio(0);                                                                                \
        if (MPRIO >= 2) __builtin_amdgcn_s_setprio(3);                                                                                \
        TICK(tA);                                                                                                                     \
        if ((WN) >= 0) asm volatile("s_waitcnt vmcnt(%0)\n\ts_barrier" ::"i"((WN) >= 0 ? (WN) : 0) : "memory");                       \
        else asm volatile("s_barrier" ::: "memory");                                                                                  \
        TICK(tX);                                                                                                                     \
    } while (0)
#define EXP_PACK()                    \
    do {                                                                                                                              \
        float e_[32], a_[2];                                                                                                          \
        unsigned c_[16];                                                                                                              \
        _Pragma("unroll") for (int k = 0; k < 18; ++k) {                                                                              \
            if (k < 16) { e_[2 * k] = v_exp(S[k >> 3][2 * (k & 7)]); e_[2 * k + 1] = v_exp(S[k >> 3][2 * (k & 7) + 1]); }             \
            if (k == 1) { a_[0] = e_[0]; a_[1] = e_[1]; }                                                                             \
            if (k >= 2 && k < 17) { a_[0] = v_add(a_[0], e_[2 * k - 2]); a_[1] = v_add(a_[1], e_[2 * k - 1]); }                        \
            if (k >= 2) c_[k - 2] = v_cvt(e_[2 * k - 4], e_[2 * k - 3]);                                                              \
        }                                                                                                                             \
        _Pragma("unroll") for (int k = 0; k < 4; ++k) pf[k] = __builtin_bit_cast(bf16x8, u32x4{c_[4 * k], c_[4 * k + 1], c_[4 * k + 2], c_[4 * k + 3]}); \
        ts_ = v_add(a_[0], a_[1]);                                                                                                    \
    } while (0)
#define REBASE(FIRST)                                                          \
    do {                                                                                                                              \
        float rm = S[0][0];                                                                                                           \
        _Pragma("unroll") for (int e = 1; e < 32; ++e) rm = __builtin_fmaxf(rm, S[e >> 4][e & 15]);                                   \
        rm = xh_max(rm);                                                                                                              \
        const float dl = (FIRST) ? (rm == -__builtin_inff() ? 0.f : rm) : __builtin_fmaxf(rm, 0.f);                                   \
        mref += dl;                                                                                                                   \
        if (!(FIRST)) { float f = __builtin_amdgcn_exp2f(-dl); asm volatile("s_nop 1" : "+v"(f)); lsum *= f;               \
            _Pragma("unroll") for (int i = 0; i < 4; ++i) _Pragma("unroll") for (int r = 0; r < 16; ++r) o[i][r] = mul_s(o[i][r], f); } \
        _Pragma("unroll") for (int r = 0; r < 16; ++r) { S[0][r] = sub_s(S[0][r], dl); S[1][r] = sub_s(S[1][r], dl); }                \
        const float nm = -mref;                                                                                                       \
        _Pragma("unroll") for (int r = 0; r < 16; ++r) negm[r] = nm;                                                                  \
    } while (0)
#define PHASE_V(t, WN, LASTV, VS, DK, DV)                                                                                             \
    do {                                                                                                                              \
        const bool swn = ((t) + 1 == nsw), sw0 = ((t) == nsw);                                                                        \
        const bool diag = 64 * tile + 63 > q0 || sw0, first_ = (t) == 0 || sw0;                                                       \
        if (DMAPOS == 2 && !ABL_NODMA) {                                                                                              \
            if (DV) { dma4(m0b + ((VS) ? vslot1 : vslot0), vptr, dvo); vptr += 16384 + ((t) + 2 == nsw ? jump : 0); }      \
            if (DK) { dma4(m0b + ks_cur, kptr, dvo); kptr += 16384 + ((t) + 4 == nsw ? jump : 0); }                       \
        }                                                                                                                             \
        NOP_SO(S, o);                                                                                                                 \
        float ts_;                                                                                                                    \
        if (__builtin_expect(diag || first_, 0)) {                                                                                    \
            if (sw0) {                                                                                \
                float il_ = __builtin_amdgcn_rcpf(xh_sum(lsum));                                                                      \
                asm volatile("s_nop 1" : "+v"(il_));                                                                                  \
                char* cp_ = ws + WS_CH + ((size_t)(8 * LB + 2 * w4 + ((lane >> 4) & 1)) * NKS_CH + 4 * head) * 1024 + ((lane & 15) + 16 * (lane >> 5)) * 16; \
                _Pragma("unroll") for (int dt = 0; dt < 4; ++dt) _Pragma("unroll") for (int y = 0; y < 2; ++y)                        \
                    *(u32x4*)(cp_ + dt * 1024 + y * 512) = pack_o(o[dt], y, il_);                                                     \
                _Pragma("unroll") for (int i = 0; i < 4; ++i) o[i] = f32x16{};                                                        \
                lsum = 0.f; mref = 0.f; q0 = 128 * HB + 32 * w4;                                                                      \
            }                                                                                                                         \
            if (64 * tile + 63 > q0) {         \
                const int dq = (int)(lane & 31) - 4 * (int)(lane >> 5);                                                               \
                _Pragma("unroll") for (int u = 0; u < 2; ++u) {                                                                       \
                    const int kb = 64 * tile + 32 * u;                                                                                \
                    if (kb > q0) { _Pragma("unroll") for (int r = 0; r < 16; ++r) S[u][r] = -__builtin_inff(); }                       \
                    else if (kb == q0) mask_diag(S[u], dq);                                                                           \
                }                                                                                                                     \
            }                                                                                                                         \
            if (first_) REBASE(true);                                                                                                 \
        }                                                                                                                             \
        EXP_PACK();                                                                                                                   \
        if (__builtin_expect(!(ABL_NOMFMA || ABL_NODMA) && __any(ts_ > PLIM), 0)) { REBASE(false); EXP_PACK(); }     \
        lsum += ts_;                                                                                                                  \
        if (!(LASTV)) EARLY_K(ks_nxt);                                                                                                \
        if (__builtin_expect(swn, 0)) {                                                                                               \
            negm = f32x16{};                                                                                                          \
            asm volatile("s_waitcnt vmcnt(%9)" : "+v"(qf[0]), "+v"(qf[1]), "+v"(qf[2]), "+v"(qf[3]), "+v"(qf[4]), "+v"(qf[5]), "+v"(qf[6]), "+v"(qf[7]), "+v"(negm) : "i"(DMAPOS == 2 ? (WN) : 0) : "memory"); \
            tile += 17 - nL;                                                                                                          \
        }                                                                                                                             \
        TICK(tB);                                                                                                                     \
        asm volatile("s_waitcnt vmcnt(%5)\n\ts_barrier" : "+v"(pf[0]), "+v"(pf[1]), "+v"(pf[2]), "+v"(pf[3]), "+v"(negm) : "i"(WN) : "memory"); \
        TICK(tO);                                                                                                                     \
        { const unsigned r_ = ks_cur; ks_cur = ks_nxt; ks_nxt = ks_nn; ks_nn = r_; }                                                  \
        tile += 1;                                                                                                                    \
    } while (0)
    constexpr float PLIM = 256.0f;
    PHASE_M(0, true, false, 1, false, false, -1);
    PHASE_V(0, 8, false, 1, true, true);
    for (int t = 1; t < 13; t += 2) {
        PHASE_M(t, true, true, 0, false, false, -1); PHASE_V(t, 8, false, 0, true, true);
        PHASE_M(t + 1, true, true, 1, false, false, -1); PHASE_V(t + 1, 8, false, 1, true, true);
    }
    PHASE_M(13, true, true, 0, false, false, -1); PHASE_V(13, 8, false, 0, true, true);
    PHASE_M(14, true, true, 1, false, false, -1); PHASE_V(14, 4, false, 1, false, true);
    PHASE_M(15, true, true, 0, false, false, -1); PHASE_V(15, 4, false, 0, false, true);
    PHASE_M(16, true, true, 1, false, false, -1); PHASE_V(16, 0, true, 1, false, false);
    PHASE_M(17, false, true, 0, false, false, -1);
    if (!grp) asm volatile("s_barrier" ::: "memory");
    NOP_SO(S, o);
    STAMP(8);
    {
        const float ls = xh_sum(lsum);
        const float mm = ls > 0.f ? mref : -__builtin_inff();
        lds_ptr X = (lds_ptr)lds + wid * 8192 + lane * 16;
        f32x16 keep[2];
        if (grp) {
#pragma unroll
            for (int i = 0; i < 2; ++i) {
                keep[i] = o[2 + i];
#pragma unroll
                for (int y = 0; y < 4; ++y) *(__attribute__((address_space(3))) f32x4*)(X + (4 * i + y) * 1024) = f32x4{o[i][4 * y], o[i][4 * y + 1], o[i][4 * y + 2], o[i][4 * y + 3]};
            }
        } else {
#pragma unroll
            for (int i = 0; i < 2; ++i) {
                keep[i] = o[i];
#pragma unroll
                for (int y = 0; y < 4; ++y) *(__attribute__((address_space(3))) f32x4*)(X + (4 * i + y) * 1024) = f32x4{o[2 + i][4 * y], o[2 + i][4 * y + 1], o[2 + i][4 * y + 2], o[2 + i][4 * y + 3]};
            }
        }
        lds_ptr Y = (lds_ptr)lds + 65536 + wid * 512 + lane * 8;
        *(__attribute__((address_space(3))) f32x2*)Y = f32x2{mm, ls};
        asm volatile("s_waitcnt lgkmcnt(0)\n\ts_barrier" ::: "memory");
        const unsigned ow = wid ^ 4;
        lds_cptr Xo = (lds_cptr)lds + ow * 8192 + lane * 16;
        const f32x2 oml = *(const __attribute__((address_space(3))) f32x2*)((lds_cptr)lds + 65536 + ow * 512 + lane * 8);
        const float mx = __builtin_fmaxf(mm, oml[0]);
        const float e0 = __builtin_amdgcn_exp2f(mm - mx), e1 = __builtin_amdgcn_exp2f(oml[0] - mx);
        const float inv = __builtin_amdgcn_rcpf(ls * e0 + oml[1] * e1);
        const float fs = e0 * inv, fo = e1 * inv;
        char* cp = ws + WS_CH + ((size_t)(8 * HB + 2 * w4 + ((lane >> 4) & 1)) * NKS_CH + 4 * head + 2 * grp) * 1024 + ((lane & 15) + 16 * (lane >> 5)) * 16;
#pragma unroll
        for (int i = 0; i < 2; ++i) {
            f32x16 m;
#pragma unroll
            for (int y = 0; y < 4; ++y) {
                const f32x4 pv = *(const __attribute__((address_space(3))) f32x4*)(Xo + (4 * i + y) * 1024);
#pragma unroll
                for (int r = 0; r < 4; ++r) m[4 * y + r] = keep[i][4 * y + r] * fs + pv[r] * fo;
            }
#pragma unroll
            for (int y = 0; y < 2; ++y) *(u32x4*)(cp + i * 1024 + y * 512) = pack_o(m, y, 1.f);
        }
    }
    if (b == 0 && wid == 0) {
        const char* flb = uptr(ws + WS_FL);
#pragma unroll
        for (int i = 0; i < 4; ++i) { u32x2 f = u32x2{~(unsigned)tag, 0u}; flag_wait<false>(0, f, flb, FL_X + (64 * i + lane) * 8, tag); }
        flag_set(0, flb, FL_EPOCH, tag);
    }
    STAMP(9);
#undef KOFF
#undef VOFF
#undef EARLY_K
#undef PHASE_M
#undef PHASE_V
#undef EXP_PACK
#undef REBASE
}

typedef __attribute__((address_space(1))) unsigned gu32;
#define K3_NOMFMA 0
#define K3_NODMA 0
#define K3_NORD 0
constexpr int K3_SLOT = 36864;
constexpr int PP_SET = 8 * 5 * 1024;
DI float silu_f(float v) { return v * __builtin_amdgcn_rcpf(1.0f + __builtin_amdgcn_exp2f(-1.4426950408889634f * v)); }
DI f32x4 unpk_lo(const u32x4& w) { return f32x4{bflo(w[0]), bfhi(w[0]), bflo(w[1]), bfhi(w[1])}; }
DI f32x4 unpk_hi(const u32x4& w) { return f32x4{bflo(w[2]), bfhi(w[2]), bflo(w[3]), bfhi(w[3])}; }

template <int KS>
DI void k3_finish(const f32x4 (&res)[10], const f32x4 (&res2)[10], const char* __restrict__ x, char* __restrict__ out, char* __restrict__ ws,
                  int grp64, int qt, int mt) {
    const unsigned tid = threadIdx.x, lane = tid & 63, c = lane & 15, g = lane >> 4;
    const unsigned wid = rfl(tid >> 6), grp = wid >> 2, w4 = wid & 3, qh = w4 >> 1, mh = w4 & 1;
    char* stamp_base = ws; (void)stamp_base;
    const __amdgpu_buffer_rsrc_t rs = __builtin_amdgcn_make_buffer_rsrc(ws + WS_PP + (size_t)grp64 * (5 * PP_SET), (short)0, 5 * PP_SET, 0x00020000);
    const unsigned vo = wid * 5120 + lane * 16;
    const unsigned r = (KS - wid) & 3;
    f32x4 oa[2][2], ob[2][2];
#pragma unroll
    for (int j = 0; j < 5; ++j) {
        if ((j & 3) == r) {
            oa[j >> 2][0] = res[2 * j]; oa[j >> 2][1] = res[2 * j + 1];
            if (KS == 2) { ob[j >> 2][0] = res2[2 * j]; ob[j >> 2][1] = res2[2 * j + 1]; }
        } else {
            __builtin_amdgcn_raw_buffer_store_b128(pack44(res[2 * j], res[2 * j + 1]), rs, vo + j * 1024 + (KS < 2 ? KS : KS + 1) * PP_SET, 0, 16);
            if (KS == 2) __builtin_amdgcn_raw_buffer_store_b128(pack44(res2[2 * j], res2[2 * j + 1]), rs, vo + j * 1024 + 2 * PP_SET, 0, 16);
        }
    }
    const int nown = r == 0 ? 2 : 1;
    u32x2 xv[2][2];
    unsigned oo[2][2];
#pragma unroll
    for (int sl = 0; sl < 2; ++sl)
#pragma unroll
        for (int t = 0; t < 2; ++t) {
            const unsigned i = 2 * (sl ? 4 : r) + t, a = i >= 5 ? 1 : 0, n = i - 5 * a;
            oo[sl][t] = (16 * (8 * qt + 4 * qh + 2 * grp + a) + c) * (DM * 2) + (16 * (10 * mt + 5 * mh + n) + 4 * g) * 2;
            xv[sl][t] = *(const u32x2*)(x + oo[sl][t]);
        }
    STAMP(14);
    asm volatile("s_waitcnt vmcnt(0)\n\ts_barrier" ::: "memory");
    STAMP(15);
    gu32* fl = (gu32*)(ws + WS_FL + FL_P + (size_t)grp64 * 4 * 128);
    if (tid == 0) __hip_atomic_store(fl + KS * 32, 1u, __ATOMIC_RELAXED, __HIP_MEMORY_SCOPE_AGENT);
    for (unsigned spins = 0; spins < (1u << 22); ++spins) {
        unsigned v = 1u;
        if (lane < 4 && lane != KS) v = __hip_atomic_load(fl + lane * 32, __ATOMIC_RELAXED, __HIP_MEMORY_SCOPE_AGENT);
        if (__all(v != 0u)) break;
        __builtin_amdgcn_s_sleep(1);
    }
    asm volatile("" ::: "memory");
    STAMP(16);
    u32x4 p[2][5];
#pragma unroll
    for (int sl = 0; sl < 2; ++sl)
        if (sl < nown) {
            const unsigned j = sl ? 4 : r;
#pragma unroll
            for (int st = 0; st < 5; ++st) {
                const bool own = (st < 3 && st == KS) || (st == 3 && KS == 2) || (st == 4 && KS == 3);
                if (!own) p[sl][st] = __builtin_amdgcn_raw_buffer_load_b128(rs, vo + j * 1024, st * PP_SET, 16);
            }
        }
#pragma unroll
    for (int sl = 0; sl < 2; ++sl)
        if (sl < nown) {
#pragma unroll
            for (int t = 0; t < 2; ++t) {
                f32x4 sv = f32x4{}, bv = f32x4{};
#pragma unroll
                for (int st = 0; st < 5; ++st) {
                    const bool own = (st < 3 && st == KS) || (st == 3 && KS == 2) || (st == 4 && KS == 3);
                    const f32x4 v = own ? ((KS == 2 && st == 2) ? ob[sl][t] : oa[sl][t]) : (t ? unpk_hi(p[sl][st]) : unpk_lo(p[sl][st]));
                    if (st < 3) sv += v; else bv += v;
                }
                const float o0 = bflo(xv[sl][t][0]) + sv[0] * silu_f(bv[0]), o1 = bfhi(xv[sl][t][0]) + sv[1] * silu_f(bv[1]);
                const float o2 = bflo(xv[sl][t][1]) + sv[2] * silu_f(bv[2]), o3 = bfhi(xv[sl][t][1]) + sv[3] * silu_f(bv[3]);
                u32x2 w; w[0] = cvtpk(o0, o1); w[1] = cvtpk(o2, o3);
                *(u32x2*)(out + oo[sl][t]) = w;
            }
        }
}

template <int GRP>
DI void k3_body(const char* __restrict__ x, const char* __restrict__ wo, const char* __restrict__ w2, char* __restrict__ ws, char* __restrict__ out, char* lds) {
    constexpr int P = GRP ? 4 : 5;
    const unsigned tid = threadIdx.x, lane = tid & 63, g = lane >> 4;
    const unsigned wid = rfl(tid >> 6), w4 = wid & 3, qh = w4 >> 1, mh = w4 & 1;
    char* stamp_base = ws; (void)stamp_base;
    const int b = blockIdx.x, xcd = b & 7, idx = b >> 3;
    const int ks = xcd >> 1, qt = 8 * (xcd & 1) + (idx >> 2), mt = idx & 3;
    const int st0 = ks == 0 ? 0 : ks == 1 ? 23 : ks == 2 ? 46 : 68;
    const unsigned lds0 = (unsigned)(uintptr_t)lds;
    lds_cptr L = (lds_cptr)lds;
    const char* aptr = uptr(ws + WS_CH + ((size_t)(8 * qt + wid) * NKS_CH + 2 * st0) * 1024);
    const char* w2b = uptr(w2 + mt * 320);
    const char* wptr = st0 < 64 ? uptr(wo + (size_t)st0 * 64 * 1280 + mt * 320) : w2b + (size_t)(st0 - 64) * 64 * 1280;
    int sdma = st0;
    const unsigned avo = lane * 16;
    unsigned wvo[3];
    const unsigned p0 = GRP ? 12 + 2 * w4 : 3 * w4;
#pragma unroll
    for (int i = 0; i < P - 2; ++i) {
        const unsigned o = (p0 + i) * 1024 + lane * 16, row = o / 320, c16 = (o % 320) >> 4;
        wvo[i] = row * 1280 + ((c16 ^ (((row >> 2) & 1) << 1)) << 4) - 1024 * i;
    }
    const unsigned m0a = lds0 + wid * 2048, m0w = lds0 + 16384 + p0 * 1024;
#define K3_ADV() do { ++sdma; aptr += 2048; wptr = sdma == 64 ? w2b : wptr + 81920; } while (0)
#define K3_DMA(slot) do { set_m0(m0a + (slot)); asm volatile("s_nop 0"); dma_nom0<0>(aptr, avo); dma_nom0<1024>(aptr, avo); \
        set_m0(m0w + (slot)); asm volatile("s_nop 0"); dma_nom0<0>(wptr, wvo[0]); dma_nom0<1024>(wptr, wvo[1]); if (!GRP) dma_nom0<2048>(wptr, wvo[2]); K3_ADV(); } while (0)
    K3_DMA(0); K3_DMA(K3_SLOT); K3_DMA(2 * K3_SLOT); K3_DMA(3 * K3_SLOT);
    const unsigned ard = (8 * qh + GRP) * 1024 + lane * 16;
    const unsigned q = (lane & 15) >> 2, p = lane & 3;
    const unsigned wrow = 16384 + (32 * GRP + 4 * g + q) * 320 + 160 * mh + 8 * p;
    const unsigned sgn = ((g & 1) ? 32u : 0u);
    const unsigned wrdE = (mh & 1) ? wrow - sgn : wrow + sgn;
    const unsigned wrdO = (mh & 1) ? wrow + sgn : wrow - sgn;
    f32x4 acc[5][4], acc2[5][4];
#pragma unroll
    for (int n = 0; n < 5; ++n)
#pragma unroll
        for (int a = 0; a < 4; ++a) { acc[n][a] = f32x4{}; acc2[n][a] = f32x4{}; }
    NOP_A(acc);
    bf16x8 af[2][4], wf[4], w4f[2];
#define K3_RW(slot, n) tr_frag(L + (slot) + (((n) & 1) ? wrdO : wrdE) + 32 * (n), L + (slot) + (((n) & 1) ? wrdO : wrdE) + 32 * (n) + 16 * 320)
#define K3_RA(slot, a) lds_frag(L + (slot) + ard + (a) * 2048)
    asm volatile("s_waitcnt vmcnt(%0)\n\ts_barrier" ::"i"(3 * P) : "memory");
    STAMP(11);
    unsigned cur = 0;
#define K3_ITER(PAR, WN, RD, DM) K3_ITER_(PAR, WN, RD, DM, false)
#define K3_ITER_(PAR, WN, RD, DM, SAFE)                                                                                 \
    do {                                                                                                                \
        asm volatile("s_waitcnt vmcnt(%0) lgkmcnt(0)\n\ts_barrier" ::"i"(WN) : "memory");                               \
        const unsigned nxt_ = cur == 3 * K3_SLOT ? 0u : cur + K3_SLOT;                                                  \
        _Pragma("unroll") for (int n = 0; n < 5; ++n) {                                                                 \
            _Pragma("unroll") for (int a = 0; a < 4; ++a) {                                                             \
                if (SAFE) mfma_acc_s(acc[n][a], n < 4 ? wf[n & 3] : w4f[PAR], af[PAR][a]);                              \
                else if (!K3_NOMFMA) mfma_acc(acc[n][a], n < 4 ? wf[n & 3] : w4f[PAR], af[PAR][a]);                     \
                else { PINR(n < 4 ? wf[n & 3] : w4f[PAR]); PINR(af[PAR][a]); }                                          \
                SBAR();                                                                                                 \
                if (RD && !K3_NORD) {                                                                                               \
                    if (n == 0 && a == 0) w4f[1 - (PAR)] = K3_RW(nxt_, 4);                                              \
                    if (n < 4 && a == 1) af[1 - (PAR)][n & 3] = K3_RA(nxt_, n & 3);                                     \
                    if (n >= 1 && a == 0) wf[(n - 1) & 3] = K3_RW(nxt_, n - 1);                                         \
                }                                                                                                       \
                if (DM && !K3_NODMA) {                                                                                  \
                    if (n == 0 && a == 2) set_m0(m0a + cur);                                                            \
                    if (n == 0 && a == 3) dma_nom0<0>(aptr, avo);                                                       \
                    if (n == 1 && a == 2) dma_nom0<1024>(aptr, avo);                                                    \
                    if (n == 1 && a == 3) set_m0(m0w + cur);                                                            \
                    if (n == 2 && a == 2) dma_nom0<0>(wptr, wvo[0]);                                                    \
                    if (n == 3 && a == 2) dma_nom0<1024>(wptr, wvo[1]);                                                 \
                    if (n == 4 && a == 1 && !GRP) dma_nom0<2048>(wptr, wvo[2]);                                         \
                }                                                                                                       \
                SBAR();                                                                                                 \
            }                                                                                                           \
        }                                                                                                               \
        if (DM) K3_ADV();                                                                                               \
        cur = nxt_;                                                                                                     \
    } while (0)
#define K3_FIRST(PAR) do { _Pragma("unroll") for (int a = 0; a < 4; ++a) af[PAR][a] = K3_RA(0, a);                      \
        _Pragma("unroll") for (int n = 0; n < 4; ++n) wf[n] = K3_RW(0, n);                                              \
        w4f[PAR] = K3_RW(0, 4); } while (0)
    if (ks != 2) { K3_FIRST(1); K3_ITER_(1, 2 * P, true, true, true); }
    else K3_FIRST(0);
    for (int it = 0; it < 9; ++it) { K3_ITER(0, 2 * P, true, true); K3_ITER(1, 2 * P, true, true); }
    if (ks == 2) {
        NOP_A(acc);
#pragma unroll
        for (int n = 0; n < 5; ++n)
#pragma unroll
            for (int a = 0; a < 4; ++a) { acc2[n][a] = acc[n][a]; acc[n][a] = f32x4{}; PIN(acc2[n][a]); }
        NOP_A(acc);
    }
    K3_ITER(0, 2 * P, true, false); K3_ITER(1, P, true, false); K3_ITER(0, 0, true, false); K3_ITER(1, 0, false, false);
    NOP_A(acc);
    STAMP(12);
    f32x4 res[10], res2[10];
    {
        lds_ptr X = (lds_ptr)lds + wid * 10240 + lane * 16;
        lds_cptr Y = (lds_cptr)lds + (wid ^ 4) * 10240 + lane * 16;
#pragma unroll
        for (int a = 0; a < 2; ++a)
#pragma unroll
            for (int n = 0; n < 5; ++n) *(__attribute__((address_space(3))) f32x4*)(X + (a * 5 + n) * 1024) = acc[n][GRP ? a : 2 + a];
        if (ks == 2) {
#pragma unroll
            for (int a = 0; a < 2; ++a)
#pragma unroll
                for (int n = 0; n < 5; ++n) *(__attribute__((address_space(3))) f32x4*)(X + 81920 + (a * 5 + n) * 1024) = acc2[n][GRP ? a : 2 + a];
        }
        asm volatile("s_waitcnt lgkmcnt(0)\n\ts_barrier" ::: "memory");
#pragma unroll
        for (int a = 0; a < 2; ++a)
#pragma unroll
            for (int n = 0; n < 5; ++n) {
                res[a * 5 + n] = acc[n][GRP ? 2 + a : a] + *(const __attribute__((address_space(3))) f32x4*)(Y + (a * 5 + n) * 1024);
                res2[a * 5 + n] = f32x4{};
            }
        if (ks == 2) {
#pragma unroll
            for (int a = 0; a < 2; ++a)
#pragma unroll
                for (int n = 0; n < 5; ++n) res2[a * 5 + n] = acc2[n][GRP ? 2 + a : a] + *(const __attribute__((address_space(3))) f32x4*)(Y + 81920 + (a * 5 + n) * 1024);
        }
    }
    STAMP(17);
    const int grp64 = qt * 4 + mt;
    if (ks == 0) k3_finish<0>(res, res2, x, out, ws, grp64, qt, mt);
    else if (ks == 1) k3_finish<1>(res, res2, x, out, ws, grp64, qt, mt);
    else if (ks == 2) k3_finish<2>(res, res2, x, out, ws, grp64, qt, mt);
    else k3_finish<3>(res, res2, x, out, ws, grp64, qt, mt);
    STAMP(13);
#undef K3_ADV
#undef K3_DMA
#undef K3_RW
#undef K3_RA
#undef K3_ITER
#undef K3_ITER_
#undef K3_FIRST
}

__global__ __launch_bounds__(512, 2) void k3_out(const char* __restrict__ x, const char* __restrict__ wo, const char* __restrict__ w2, char* __restrict__ ws,
                                                 char* __restrict__ out) {
    extern __shared__ __attribute__((aligned(16))) char lds[];
    char* stamp_base = ws; (void)stamp_base;
    const unsigned wid = rfl(threadIdx.x >> 6); (void)wid;
    STAMP(10);
    if (rfl(threadIdx.x >> 8) == 0) k3_body<0>(x, wo, w2, ws, out, lds);
    else k3_body<1>(x, wo, w2, ws, out, lds);
}

extern "C" void kernel_launch(void* const* d_in, const int* in_sizes, int n_in, void* d_out, int out_size, void* d_ws, size_t ws_size,
                              hipStream_t stream) {
    const char* x = (const char*)d_in[0];
    const char* ckv = (const char*)d_in[1];
    const char* wuk = (const char*)d_in[3];
    const char* wuv = (const char*)d_in[4];
    char* ws = (char*)d_ws;
    (void)hipFuncSetAttribute((const void*)k12, hipFuncAttributeMaxDynamicSharedMemorySize, 163840);
    k12<<<256, 512, 163840, stream>>>(x, ckv, wuk, wuv, (const char*)d_in[2], (const char*)d_in[7], (const char*)d_in[8], ws, (const char*)d_in[5], (const char*)d_in[9]);
    (void)hipFuncSetAttribute((const void*)k3_out, hipFuncAttributeMaxDynamicSharedMemorySize, 163840);
    k3_out<<<256, 512, 163840, stream>>>(x, (const char*)d_in[5], (const char*)d_in[9], ws, (char*)d_out);
}
```

```cpp
#include <hip/hip_runtime.h>
#include <hip/hip_bf16.h>
#include <stdint.h>

typedef __bf16 bf16x8 __attribute__((ext_vector_type(8)));
typedef __bf16 bf16x2 __attribute__((ext_vector_type(2)));
typedef short s16x4 __attribute__((ext_vector_type(4)));
typedef short s16x8 __attribute__((ext_vector_type(8)));
typedef short v4i16_t __attribute__((ext_vector_type(4)));
typedef float f32x16 __attribute__((ext_vector_type(16)));
typedef float f32x4 __attribute__((ext_vector_type(4)));
typedef float f32x2 __attribute__((ext_vector_type(2)));
typedef unsigned u32x4 __attribute__((ext_vector_type(4)));
typedef unsigned u32x2 __attribute__((ext_vector_type(2)));
typedef __attribute__((address_space(3))) const char* lds_cptr;
typedef __attribute__((address_space(3))) char* lds_ptr;

#define DI __device__ __forceinline__
#define SBAR() __builtin_amdgcn_sched_barrier(0)
#define MFMA(a, b, c) __builtin_amdgcn_mfma_f32_16x16x32_bf16((a), (b), (c), 0, 0, 0)
#define PIN(x) asm volatile("" : "+v"(x))
#define PINR(x) asm volatile("" ::"v"(x))

constexpr int S_ = 2048, NH = 32, HD = 128, DM = 640, DC = 512, FF = 1728;
constexpr int NKS_X = DM / 32;
constexpr int NKS_C = (NH * HD) / 32;
constexpr int NKS_H = FF / 32;
constexpr int NKS_CH = NKS_C + NKS_H;
constexpr size_t WS_XF = 0;
constexpr size_t WS_KF = WS_XF + (size_t)128 * NKS_X * 1024;
constexpr size_t WS_VF = WS_KF + (size_t)NH * 128 * 4 * 1024;
constexpr size_t WS_CH = WS_VF + (size_t)NH * 64 * 8 * 1024;
constexpr size_t WS_QH = WS_CH + (size_t)128 * NKS_CH * 1024;
constexpr size_t WS_PP = WS_QH + (size_t)256 * 32768;
constexpr size_t PP_BYTES = (size_t)64 * 5 * 8 * 5 * 1024;
constexpr size_t WS_FL = WS_PP + PP_BYTES;
constexpr size_t FL_EPOCH = 0;
constexpr size_t FL_X = 256;
constexpr size_t FL_KV = FL_X + 2048;
constexpr size_t FL_P = 8192;
constexpr size_t WS_END = WS_FL + FL_P + (size_t)256 * 128;

DI unsigned rfl(unsigned x) { return __builtin_amdgcn_readfirstlane(x); }
DI unsigned flane() { unsigned r; asm volatile("v_mbcnt_lo_u32_b32 %0, -1, 0\n\tv_mbcnt_hi_u32_b32 %0, -1, %0" : "=v"(r)); return r; }
#define STAMP(i)
#define TICK(acc)
DI const char* uptr(const char* p) { const unsigned long long v = (unsigned long long)p; const unsigned lo = rfl((unsigned)v), hi = rfl((unsigned)(v >> 32)); return (const char*)(((unsigned long long)hi << 32) | lo); }
DI unsigned cvtpk(float lo, float hi) { f32x2 v = {lo, hi}; bf16x2 b = __builtin_convertvector(v, bf16x2); return __builtin_bit_cast(unsigned, b); }
DI bf16x8 lds_frag(lds_cptr p) { return *(const __attribute__((address_space(3))) bf16x8*)p; }
DI s16x4 lds_tr(lds_cptr p) { return __builtin_bit_cast(s16x4, __builtin_amdgcn_ds_read_tr16_b64_v4i16((__attribute__((address_space(3))) v4i16_t*)p)); }
DI bf16x8 tr_frag(lds_cptr p0, lds_cptr p1) { s16x4 lo = lds_tr(p0), hi = lds_tr(p1); s16x8 v = __builtin_shufflevector(lo, hi, 0, 1, 2, 3, 4, 5, 6, 7); return __builtin_bit_cast(bf16x8, v); }
template <int OFF> DI void dma1(unsigned m0v, const void* sbase, unsigned voff) {
    asm volatile("s_mov_b32 m0, %0\n\ts_nop 0\n\tglobal_load_lds_dwordx4 %1, %2 offset:%3" ::"s"(m0v), "v"(voff), "s"(sbase), "i"(OFF) : "memory");
}
DI u32x4 pack44(const f32x4& a, const f32x4& b) { u32x4 w; w[0] = cvtpk(a[0], a[1]); w[1] = cvtpk(a[2], a[3]); w[2] = cvtpk(b[0], b[1]); w[3] = cvtpk(b[2], b[3]); return w; }
DI u32x4 pack44s(const f32x4& a, const f32x4& b, float sc) { u32x4 w; w[0] = cvtpk(a[0] * sc, a[1] * sc); w[1] = cvtpk(a[2] * sc, a[3] * sc); w[2] = cvtpk(b[0] * sc, b[1] * sc); w[3] = cvtpk(b[2] * sc, b[3] * sc); return w; }
DI u32x4 pack8(const f32x16& a, int b, float sc) {
    u32x4 w; w[0] = cvtpk(a[b + 0] * sc, a[b + 1] * sc); w[1] = cvtpk(a[b + 2] * sc, a[b + 3] * sc);
    w[2] = cvtpk(a[b + 4] * sc, a[b + 5] * sc); w[3] = cvtpk(a[b + 6] * sc, a[b + 7] * sc); return w;
}
DI u32x4 pack8n(const f32x16& a, int b) {
    u32x4 w; w[0] = cvtpk(a[b + 0], a[b + 1]); w[1] = cvtpk(a[b + 2], a[b + 3]); w[2] = cvtpk(a[b + 4], a[b + 5]); w[3] = cvtpk(a[b + 6], a[b + 7]); return w;
}
DI float bf2f(unsigned short u) { return __uint_as_float((unsigned)u << 16); }
DI float bflo(unsigned w) { return __uint_as_float(w << 16); }
DI float bfhi(unsigned w) { return __uint_as_float(w & 0xffff0000u); }
DI float xhalf_sum(float v) { auto r = __builtin_amdgcn_permlane32_swap(__float_as_uint(v), __float_as_uint(v), false, false); return __uint_as_float(r[0]) + __uint_as_float(r[1]); }
DI float xhalf_max(float v) { auto r = __builtin_amdgcn_permlane32_swap(__float_as_uint(v), __float_as_uint(v), false, false); return __builtin_fmaxf(__uint_as_float(r[0]), __uint_as_float(r[1])); }

DI void warm_load(unsigned& d, const char* wa, const char* wb, unsigned id) {
    const unsigned j = id - 1280u;
    const char* p = uptr(id < 1280u ? wa + (size_t)id * 4096 : wb + (size_t)(j < 539u ? j : 539u) * 4096);
    const unsigned voff = flane() * 64;
    asm volatile("s_nop 4\n\tglobal_load_dword %0, %1, %2" : "=&v"(d) : "v"(voff), "s"(p) : "memory");
}
DI void warm_lds(unsigned m0v, const char* wa, const char* wb, unsigned id) {
    const unsigned j = id - 1280u;
    const char* p = uptr(id < 1280u ? wa + (size_t)id * 4096 : wb + (size_t)(j < 539u ? j : 539u) * 4096);
    const unsigned voff = flane() * 64;
    asm volatile("s_mov_b32 m0, %0\n\ts_nop 4\n\tglobal_load_lds_dword %1, %2" ::"s"(m0v), "v"(voff), "s"(p) : "memory");
}

DI void mfma_acc(f32x4& acc, const bf16x8& a, const bf16x8& b) { asm volatile("v_mfma_f32_16x16x32_bf16 %0, %1, %2, %0" : "+v"(acc) : "v"(a), "v"(b)); }
DI void mfma_acc_s(f32x4& acc, const bf16x8& a, const bf16x8& b) { asm volatile("s_nop 1\n\tv_mfma_f32_16x16x32_bf16 %0, %1, %2, %0" : "+v"(acc) : "v"(a), "v"(b)); }
DI void mfma32_acc(f32x16& acc, const bf16x8& a, const bf16x8& b) { asm volatile("v_mfma_f32_32x32x16_bf16 %0, %1, %2, %0" : "+v"(acc) : "v"(a), "v"(b)); }
DI void mfma32_ini(f32x16& d, const bf16x8& a, const bf16x8& b, const f32x16& c) { asm volatile("v_mfma_f32_32x32x16_bf16 %0, %1, %2, %3" : "=&v"(d) : "v"(a), "v"(b), "v"(c)); }
DI float v_exp(float x) { float r; asm volatile("v_exp_f32 %0, %1" : "=v"(r) : "v"(x)); return r; }
DI float v_add(float a, float b) { float r; asm volatile("v_add_f32 %0, %1, %2" : "=v"(r) : "v"(a), "v"(b)); return r; }
DI unsigned v_cvt(float lo, float hi) { unsigned r; asm volatile("v_cvt_pk_bf16_f32 %0, %1, %2" : "=v"(r) : "v"(lo), "v"(hi)); return r; }
DI void set_m0(unsigned v) { asm volatile("s_mov_b32 m0, %0" ::"s"(v) : "memory"); }
template <int OFF> DI void dma_nom0(const void* sbase, unsigned voff) { asm volatile("global_load_lds_dwordx4 %0, %1 offset:%2" ::"v"(voff), "s"(sbase), "i"(OFF) : "memory"); }
DI void dma4(unsigned m0v, const void* sbase, unsigned voff) {
    asm volatile("s_mov_b32 m0, %0\n\ts_nop 0\n\tglobal_load_lds_dwordx4 %1, %2\n\tglobal_load_lds_dwordx4 %1, %2 offset:1024\n\tglobal_load_lds_dwordx4 %1, %2 offset:2048\n\tglobal_load_lds_dwordx4 %1, %2 offset:3072"
                 ::"s"(m0v), "v"(voff), "s"(sbase) : "memory");
}

typedef unsigned long long u64;
DI void st_wt(char* p, const u32x4& v) { asm volatile("global_store_dwordx4 %0, %1, off sc1\n\ts_nop 1" ::"v"(p), "v"(v) : "memory"); }
DI void st_wt_o(const char* sbase, unsigned voff, const u32x4& v) { asm volatile("global_store_dwordx4 %0, %1, %2 sc1\n\ts_nop 1" ::"v"(voff), "v"(v), "s"(sbase) : "memory"); }
DI void flag_set(unsigned wid, const char* sbase, unsigned off, u64 tag) {
    const u32x2 v = u32x2{(unsigned)tag, (unsigned)(tag >> 32)};
    u64 ex;
    asm volatile("s_cmp_lg_u32 %1, 0\n\ts_cbranch_scc1 .Lfs%=\n\ts_mov_b64 %0, exec\n\ts_mov_b64 exec, 1\n\t"
                 "global_store_dwordx2 %2, %3, %4 sc1\n\ts_mov_b64 exec, %0\n.Lfs%=:\n\ts_nop 1"
                 : "=&s"(ex) : "s"(wid), "v"(off), "v"(v), "s"(sbase) : "scc", "memory");
}
DI void flag_ld(u32x2& f, const char* sbase, unsigned voff) { asm volatile("global_load_dwordx2 %0, %1, %2 sc1" : "=&v"(f) : "v"(voff), "s"(sbase) : "memory"); }
template <bool ACQ>
DI void flag_wait(unsigned skip, u32x2& f, const char* sbase, unsigned voff, u64 tag) {
    unsigned cnt;
    asm volatile("s_cmp_lg_u32 %2, 0\n\ts_cbranch_scc1 .Lfe%=\n\t"
                 "v_cmp_ne_u64 vcc, %3, %0\n\ts_cbranch_vccz .Lfd%=\n\ts_mov_b32 %1, 0x40000\n"
                 ".Lfl%=:\n\tglobal_load_dwordx2 %0, %4, %5 sc1\n\ts_waitcnt vmcnt(0)\n\tv_cmp_ne_u64 vcc, %3, %0\n\ts_cbranch_vccz .Lfd%=\n\t"
                 "s_sleep 2\n\ts_sub_u32 %1, %1, 1\n\ts_cbranch_scc0 .Lfl%=\n"
                 ".Lfd%=:\n\t.if %6\n\tbuffer_inv sc1\n\t.endif\n"
                 ".Lfe%=:"
                 : "+v"(f), "=&s"(cnt) : "s"(skip), "s"(tag), "v"(voff), "s"(sbase), "i"(ACQ ? 1 : 0) : "vcc", "scc", "memory");
}

constexpr int WQ_PIECE = 1040, W1_PIECE = 1056, W1_OFF = 16 * WQ_PIECE, PSLOT = W1_OFF + 8 * W1_PIECE;
struct PJ { unsigned wqvo, w1vo, xv0, xv1; const char *wqs, *w1s, *xs; };
DI PJ pj_setup(const char* wq, const char* w1, const char* ws, unsigned wid, int head, int f0, bool nft4, int q16_0) {
    const unsigned lane = flane();
    PJ j;
    j.wqvo = ((lane >> 2) & 3) * 8192 + (lane >> 4) * 64 + (lane & 3) * 16;
    j.w1vo = ((lane >> 2) & 7) * 3456 + (nft4 ? (lane >> 5) * 64 : 0) + (lane & 3) * 16;
    j.wqs = uptr(wq + (size_t)wid * 8 * 8192 + head * 256);
    j.w1s = uptr(w1 + (size_t)wid * 8 * 3456 + f0 * 2);
    j.xs = uptr(ws + WS_XF + (size_t)q16_0 * NKS_X * 1024);
    j.xv0 = lane * 16; j.xv1 = lane * 16 + NKS_X * 1024;
    return j;
}
DI void pj_dma(const PJ& j, unsigned lds0, unsigned wid, int c, int slot) {
    dma1<0>(lds0 + slot + (2 * wid) * WQ_PIECE, j.wqs + (size_t)c * 64 * 8192, j.wqvo);
    dma1<0>(lds0 + slot + (2 * wid + 1) * WQ_PIECE, j.wqs + (size_t)c * 64 * 8192 + 4 * 8192, j.wqvo);
    dma1<0>(lds0 + slot + W1_OFF + wid * W1_PIECE, j.w1s + (size_t)c * 64 * 3456, j.w1vo);
}
DI void pj_xload(const PJ& j, u32x4 (&X)[4], int c) {
    const char* p_ = j.xs + c * 2048;
    asm volatile("global_load_dwordx4 %0, %4, %6 sc1\n\tglobal_load_dwordx4 %1, %5, %6 sc1\n\tglobal_load_dwordx4 %2, %4, %6 offset:1024 sc1\n\tglobal_load_dwordx4 %3, %5, %6 offset:1024 sc1"
                 : "=&v"(X[0]), "=&v"(X[1]), "=&v"(X[2]), "=&v"(X[3]) : "v"(j.xv0), "v"(j.xv1), "s"(p_) : "memory");
}

#define PREFETCH 1
template <bool VT, class F>
DI void k1_mma(f32x4 (&acc)[8][4], int half, const bf16x8 (&c)[4], const bf16x8 (&w)[4], F&& filler) {
#pragma unroll
    for (int rt = 0; rt < 4; ++rt)
#pragma unroll
        for (int nt = 0; nt < 4; ++nt) {
            if (half == 0) acc[rt][nt] = VT ? MFMA(c[rt], w[nt], acc[rt][nt]) : MFMA(w[nt], c[rt], acc[rt][nt]);
            else acc[4 + rt][nt] = VT ? MFMA(c[rt], w[nt], acc[4 + rt][nt]) : MFMA(w[nt], c[rt], acc[4 + rt][nt]);
            filler(rt * 4 + nt);
        }
}

template <bool VT>
DI void k1_tile(const char* __restrict__ Abase, const char* __restrict__ Wbase, char* __restrict__ outp, char* lds, const unsigned (&avo)[4], const unsigned (&wvo)[4],
                unsigned wid, int hp, int rb, char* __restrict__ ws, const char* __restrict__ wq, const char* __restrict__ w1, const char* __restrict__ b1, u64 tag,
                int head2, int f0, bool nft4, int q16_0, u32x4 (&xa)[4], u32x4 (&xb)[4], u32x2 (&bb)[4]) {
    char* stamp_base = ws; (void)stamp_base;
    const char* flb = uptr(ws + WS_FL);
    const unsigned wm = wid >> 2, wn = wid & 3;
    const unsigned lds0 = (unsigned)(uintptr_t)lds;
    unsigned aaddr[2], baddr[4];
    {
        const unsigned lane = flane(), c = lane & 15, g = lane >> 4;
        const unsigned x = (c >> 1) & 7;
#pragma unroll
        for (int s = 0; s < 2; ++s) aaddr[s] = (128 * wm + c) * 128 + (((4 * s + g) ^ x) << 4);
        const unsigned q = (lane & 15) >> 2, p = lane & 3, f = q | ((g & 1) << 2);
#pragma unroll
        for (int nt = 0; nt < 4; ++nt) baddr[nt] = 32768 + (8 * g + q) * 512 + (((8 * wn + 2 * nt + (p >> 1)) ^ (f << 1)) << 4) + ((p & 1) << 3);
    }
    f32x4 acc[8][4];
#pragma unroll
    for (int i = 0; i < 8; ++i)
#pragma unroll
        for (int j = 0; j < 4; ++j) acc[i][j] = f32x4{};
    bf16x8 cf[2][4], wf[2][4];
    lds_cptr L = (lds_cptr)lds;
#define K1_RC(buf, stg, s, half) do { _Pragma("unroll") for (int rt = 0; rt < 4; ++rt) cf[buf][rt] = lds_frag(L + (stg) + aaddr[s] + ((half) * 4 + rt) * 2048); } while (0)
#define K1_RW(buf, stg, s) do { _Pragma("unroll") for (int nt = 0; nt < 4; ++nt) wf[buf][nt] = tr_frag(L + (stg) + baddr[nt] + (s) * 16384, L + (stg) + baddr[nt] + (s) * 16384 + 2048); } while (0)
    const char* Aw = Abase + (size_t)wid * 32 * 1024;
    const char* Ww = Wbase + (size_t)wid * 8 * 8192;
    const unsigned m0a = lds0 + wid * 4096, m0w = lds0 + 32768 + wid * 4096;
#define K1_DMA(kt, stg)                                                                   \
    do {                                                                                  \
        const char* a_ = Aw + (kt) * 128; const char* w_ = Ww + (size_t)(kt) * 64 * 8192; \
        dma1<0>(m0a + (stg), a_, avo[0]); dma1<1024>(m0a + (stg), a_, avo[1]); dma1<2048>(m0a + (stg), a_, avo[2]); dma1<3072>(m0a + (stg), a_, avo[3]); \
        dma1<0>(m0w + (stg), w_, wvo[0]); dma1<1024>(m0w + (stg), w_, wvo[1]); dma1<2048>(m0w + (stg), w_, wvo[2]); dma1<3072>(m0w + (stg), w_, wvo[3]); \
    } while (0)
    unsigned pfd = 0;
    u32x2 fx = u32x2{};
#define K1_FXO() (FL_X + (2 * q16_0 + (flane() & 3)) * 8)
    STAMP(1);
    asm volatile("s_waitcnt vmcnt(10)\n\ts_barrier" ::: "memory");
    STAMP(2);
    K1_RW(0, 0, 0); K1_RC(0, 0, 0, 0);
#define K1_RC1(buf, stg, s, half, rt) cf[buf][rt] = lds_frag(L + (stg) + aaddr[s] + ((half) * 4 + (rt)) * 2048)
#define K1_RW1(buf, stg, s, nt) wf[buf][nt] = tr_frag(L + (stg) + baddr[nt] + (s) * 16384, L + (stg) + baddr[nt] + (s) * 16384 + 2048)
#pragma unroll
    for (int kt = 0; kt < 8; ++kt) {
        const unsigned stg = (kt & 1) * 65536, nstg = stg ^ 65536;
        SBAR();
        k1_mma<VT>(acc, 0, cf[0], wf[0], [&](int m) { if (m < 8 && (m & 1) == 0) { K1_RC1(1, stg, 0, 1, m >> 1); SBAR(); } });
        SBAR();
        k1_mma<VT>(acc, 1, cf[1], wf[0], [&](int m) { if (m < 4) { K1_RC1(0, stg, 1, 0, m); SBAR(); } else if (m < 12 && (m & 1) == 0) { K1_RW1(1, stg, 1, (m - 4) >> 1); SBAR(); } });
        SBAR();
        k1_mma<VT>(acc, 0, cf[0], wf[1], [&](int m) { if (m < 8 && (m & 1) == 0) { K1_RC1(1, stg, 1, 1, m >> 1); SBAR(); } });
        SBAR();
        if (kt == 7) asm volatile("s_waitcnt lgkmcnt(0)\n\ts_barrier" ::: "memory");
        else asm volatile("s_waitcnt vmcnt(0) lgkmcnt(0)\n\ts_barrier" : "+v"(fx)::"memory");
        if (kt == 0) flag_set(wid, flb, FL_X + blockIdx.x * 8, tag);
        if (kt == 6) flag_wait<false>(0, fx, flb, K1_FXO(), tag);
        const char* a_ = Aw + (kt + 2) * 128; const char* w_ = Ww + (size_t)(kt + 2) * 64 * 8192;
        k1_mma<VT>(acc, 1, cf[1], wf[1], [&](int m) {
            if (kt + 1 < 8) { if (m < 4) { K1_RW1(0, nstg, 0, m); SBAR(); } else if (m < 8) { K1_RC1(0, nstg, 0, 0, m - 4); SBAR(); } }
            if (kt + 2 < 8) {
                if (m == 1) { dma1<0>(m0a + stg, a_, avo[0]); SBAR(); } if (m == 3) { dma1<1024>(m0a + stg, a_, avo[1]); SBAR(); }
                if (m == 5) { dma1<2048>(m0a + stg, a_, avo[2]); SBAR(); } if (m == 7) { dma1<3072>(m0a + stg, a_, avo[3]); SBAR(); }
                if (m == 9) { dma1<0>(m0w + stg, w_, wvo[0]); SBAR(); } if (m == 11) { dma1<1024>(m0w + stg, w_, wvo[1]); SBAR(); }
                if (m == 13) { dma1<2048>(m0w + stg, w_, wvo[2]); SBAR(); } if (m == 15) { dma1<3072>(m0w + stg, w_, wvo[3]); SBAR(); }
            }
            if (PREFETCH && kt == 5 && m == 15) {
                warm_load(pfd, wq, w1, blockIdx.x * 8 + wid);
                SBAR();
            }
            if (kt == 4 && m == 14) { flag_ld(fx, flb, K1_FXO()); SBAR(); }
            if (kt == 6 && m == 2) {
                const PJ j = pj_setup(wq, w1, ws, wid, head2, f0, nft4, q16_0); pj_dma(j, lds0, wid, 0, 0); pj_dma(j, lds0, wid, 1, PSLOT); SBAR();
            }
            if (kt == 7) {
                if (m == 0) {
                    const char* bp = uptr(b1 + f0 * 2);
                    const unsigned bo = (flane() >> 4) * 8, b2 = bo + (nft4 ? 64 : 0);
                    asm volatile("s_nop 4\n\tglobal_load_dwordx2 %0, %4, %6\n\tglobal_load_dwordx2 %1, %4, %6 offset:32\n\tglobal_load_dwordx2 %2, %5, %6\n\tglobal_load_dwordx2 %3, %5, %6 offset:32"
                                 : "=&v"(bb[0]), "=&v"(bb[1]), "=&v"(bb[2]), "=&v"(bb[3]) : "v"(bo), "v"(b2), "s"(bp) : "memory");
                    SBAR();
                }
                if (m == 4) { const PJ j = pj_setup(wq, w1, ws, wid, head2, f0, nft4, q16_0); pj_xload(j, xa, 0); pj_xload(j, xb, 1); SBAR(); }
            }
        });
        SBAR();
    }
    STAMP(3);
    const int head = 2 * hp + (wn >> 1), d0 = (wn & 1) * 64;
    const int key0 = 256 * rb + 128 * wm;
    const unsigned lane = flane(), c = lane & 15, g = lane >> 4;
    if (VT) {
        const unsigned lo = (g >> 1) * 1024 + (c + 32 * (g & 1)) * 16;
#pragma unroll
        for (int u = 0; u < 4; ++u)
#pragma unroll
            for (int nt = 0; nt < 4; ++nt) {
                const size_t off = ((size_t)(head * 64 + (key0 >> 5) + u) * 4 + (d0 >> 5) + (nt >> 1)) * 2048 + (nt & 1) * 256;
                st_wt_o(uptr(outp + off), lo, pack44(acc[2 * u][nt], acc[2 * u + 1][nt]));
            }
    } else {
        const unsigned lo = (g >> 1) * 1024 + ((c & 7) + 16 * (c >> 3) + 32 * (g & 1)) * 16;
#pragma unroll
        for (int kt = 0; kt < 8; ++kt)
#pragma unroll
            for (int v = 0; v < 2; ++v) {
                const size_t off = ((size_t)(head * 64 + (key0 >> 5) + (kt >> 1)) * 4 + (d0 >> 5) + v) * 2048 + (kt & 1) * 128;
                st_wt_o(uptr(outp + off), lo, pack44(acc[kt][2 * v], acc[kt][2 * v + 1]));
            }
    }
    STAMP(4);
    asm volatile("s_waitcnt vmcnt(16)\n\ts_barrier" : "+v"(pfd), "+v"(xa[0]), "+v"(xa[1]), "+v"(xa[2]), "+v"(xa[3]), "+v"(xb[0]), "+v"(xb[1]), "+v"(xb[2]), "+v"(xb[3]),
                 "+v"(bb[0]), "+v"(bb[1]), "+v"(bb[2]), "+v"(bb[3])::"memory");
    STAMP(5);
#undef K1_RC
#undef K1_RW
#undef K1_DMA
#undef K1_FXO
}

constexpr float QSCALE = 0.08838834764831845f * 1.4426950408889634f;
constexpr float THR = 8.0f;
DI float x4_max(float x) {
    auto s = __builtin_amdgcn_permlane16_swap(__float_as_uint(x), __float_as_uint(x), false, false);
    x = __builtin_fmaxf(__uint_as_float(s[0]), __uint_as_float(s[1]));
    auto t = __builtin_amdgcn_permlane32_swap(__float_as_uint(x), __float_as_uint(x), false, false);
    return __builtin_fmaxf(__uint_as_float(t[0]), __uint_as_float(t[1]));
}
DI float x4_sum(float x) {
    auto s = __builtin_amdgcn_permlane16_swap(__float_as_uint(x), __float_as_uint(x), false, false);
    x = __uint_as_float(s[0]) + __uint_as_float(s[1]);
    auto t = __builtin_amdgcn_permlane32_swap(__float_as_uint(x), __float_as_uint(x), false, false);
    return __uint_as_float(t[0]) + __uint_as_float(t[1]);
}
DI float mish_f(float h) {
    const float e = __builtin_amdgcn_exp2f(__builtin_fminf(h, 30.0f) * 1.4426950408889634f);
    const float n = e * (e + 2.0f);
    return h * n * __builtin_amdgcn_rcpf(n + 2.0f);
}

template <int NFT>
DI void proj_phase(const char* __restrict__ wq, const char* __restrict__ w1, char* __restrict__ ws, char* lds, int head, int f0, int q16_0, bf16x8 (&qf)[2][4],
                   const char* __restrict__ wo, const char* __restrict__ w2, unsigned m0v0, unsigned wid, u32x4 (&xa)[4], u32x4 (&xb)[4], const u32x2 (&bb)[4], u64 tag,
                   unsigned kvflag, int hp2, unsigned m0k0, unsigned m0k1, const char* kptr0, const char* kptr1) {
    const unsigned lane = flane(), g = lane >> 4;
    const unsigned lds0 = (unsigned)(uintptr_t)lds;
    lds_cptr L = (lds_cptr)lds;
    const PJ pj = pj_setup(wq, w1, ws, wid, head, f0, NFT == 4, q16_0);
    const char* flb = uptr(ws + WS_FL);
    u32x2 fkv = u32x2{};
    const unsigned fkvo = FL_KV + (hp2 * 16 + (lane & 15)) * 8;
    const unsigned q = (lane & 15) >> 2, p = lane & 3;
    const unsigned wqb = 2 * g * WQ_PIECE + q * 64 + p * 8;
    const unsigned w1b = W1_OFF + g * W1_PIECE + q * 64 + p * 8;
    f32x4 aq[8][2], ah[NFT][2];
#pragma unroll
    for (int i = 0; i < 8; ++i) { aq[i][0] = f32x4{}; aq[i][1] = f32x4{}; }
#pragma unroll
    for (int i = 0; i < NFT; ++i) { ah[i][0] = f32x4{}; ah[i][1] = f32x4{}; }
    constexpr int NT = 8 + NFT;
    bf16x8 wf[4];
#define PJ_RD(slot, s, n)                                                                                       \
    (((n) < 8) ? tr_frag(L + (slot) + wqb + (8 * (s)) * WQ_PIECE + ((n) >> 1) * 256 + ((n) & 1) * 32, L + (slot) + wqb + (8 * (s) + 1) * WQ_PIECE + ((n) >> 1) * 256 + ((n) & 1) * 32) \
               : tr_frag(L + (slot) + w1b + (4 * (s)) * W1_PIECE + (((n) - 8) >> 1) * 512 + ((n) & 1) * 32, L + (slot) + w1b + (4 * (s)) * W1_PIECE + 256 + (((n) - 8) >> 1) * 512 + ((n) & 1) * 32))
#pragma unroll
    for (int n = 0; n < 3; ++n) wf[n] = PJ_RD(0, 0, n);
#define PJ_CHUNK(c, XC, XN)                                                                                     \
    do {                                                                                                        \
        constexpr int slot_ = ((c) % 3) * PSLOT, nslot_ = (((c) + 1) % 3) * PSLOT, dslot_ = (((c) + 2) % 3) * PSLOT; \
        if ((c) == 4) flag_ld(fkv, flb, fkvo);                                                                  \
          \
        if ((c) == 5) flag_wait<true>(wid, fkv, flb, fkvo, tag);                                                \
        if ((c) >= 1 && (c) + 1 < 10) pj_xload(pj, XN, (c) + 1);                                                \
        if ((c) + 2 < 10) pj_dma(pj, lds0, wid, (c) + 2, dslot_);                                               \
        if ((c) == 9) {       \
            dma4(m0k0, kptr0, lane * 16); dma4(m0k1, kptr1, lane * 16);                                         \
            if (PREFETCH) warm_lds(m0v0, wo, w2, blockIdx.x * 8 + wid);                                         \
        }                                                                                                       \
        SBAR();                                                                                                 \
        _Pragma("unroll") for (int m = 0; m < 2 * NT; ++m) {                                                    \
            const int s = m / NT, n = m % NT;                                                                   \
            if (m == 2 * NT - 3) {       \
                if ((c) == 0) asm volatile("s_waitcnt lgkmcnt(0)\n\ts_barrier" ::: "memory");                   \
                else if ((c) + 2 < 10) asm volatile("s_waitcnt vmcnt(3) lgkmcnt(0)\n\ts_barrier" : "+v"(XN[0]), "+v"(XN[1]), "+v"(XN[2]), "+v"(XN[3]), "+v"(fkv)::"memory"); \
                else if ((c) + 1 < 10) asm volatile("s_waitcnt vmcnt(0) lgkmcnt(0)\n\ts_barrier" : "+v"(XN[0]), "+v"(XN[1]), "+v"(XN[2]), "+v"(XN[3])::"memory"); \
                if ((c) == 1) flag_set(wid, flb, kvflag, tag);        \
            }                                                                                                   \
            const int m3 = m + 3;                                                                               \
            if (m3 < 2 * NT) wf[m3 & 3] = PJ_RD(slot_, m3 / NT, m3 % NT);                                       \
            else if ((c) + 1 < 10) wf[m3 & 3] = PJ_RD(nslot_, 0, m3 - 2 * NT);                                  \
            const bf16x8 x0 = __builtin_bit_cast(bf16x8, XC[2 * s]), x1 = __builtin_bit_cast(bf16x8, XC[2 * s + 1]); \
            if (n < 8) { aq[n & 7][0] = MFMA(wf[m & 3], x0, aq[n & 7][0]); aq[n & 7][1] = MFMA(wf[m & 3], x1, aq[n & 7][1]); } \
            else { ah[(n - 8) % NFT][0] = MFMA(wf[m & 3], x0, ah[(n - 8) % NFT][0]); ah[(n - 8) % NFT][1] = MFMA(wf[m & 3], x1, ah[(n - 8) % NFT][1]); } \
            SBAR();                                                                                             \
        }                                                                                                       \
    } while (0)
    static_assert((2 * NT) % 4 == 0, "fragment window");
    PJ_CHUNK(0, xa, xb); PJ_CHUNK(1, xb, xa); PJ_CHUNK(2, xa, xb); PJ_CHUNK(3, xb, xa); PJ_CHUNK(4, xa, xb);
    PJ_CHUNK(5, xb, xa); PJ_CHUNK(6, xa, xb); PJ_CHUNK(7, xb, xa); PJ_CHUNK(8, xa, xb); PJ_CHUNK(9, xb, xa);
#undef PJ_CHUNK
#undef PJ_RD
    asm volatile("s_waitcnt vmcnt(%0)" ::"i"(PREFETCH ? 1 : 0) : "memory");
#pragma unroll
    for (int a = 0; a < 2; ++a)
#pragma unroll
        for (int v = 0; v < 4; ++v) qf[a][v] = __builtin_bit_cast(bf16x8, pack44s(aq[2 * v][a], aq[2 * v + 1][a], QSCALE));
#pragma unroll
    for (int v = 0; v < NFT / 2; ++v) {
        float bias[8];
#pragma unroll
        for (int t = 0; t < 2; ++t) {
            const u32x2 q2 = bb[2 * v + t];
            bias[4 * t + 0] = bflo(q2[0]); bias[4 * t + 1] = bfhi(q2[0]); bias[4 * t + 2] = bflo(q2[1]); bias[4 * t + 3] = bfhi(q2[1]);
        }
#pragma unroll
        for (int a = 0; a < 2; ++a) {
            f32x4 h0, h1;
#pragma unroll
            for (int i = 0; i < 4; ++i) { h0[i] = mish_f(ah[2 * v][a][i] + bias[i]); h1[i] = mish_f(ah[2 * v + 1][a][i] + bias[4 + i]); }
            *(u32x4*)(ws + WS_CH + ((size_t)(q16_0 + a) * NKS_CH + NKS_C + (f0 >> 5) + v) * 1024 + lane * 16) = pack44(h0, h1);
        }
    }
}

#define NOP_SO(S, o) asm volatile("s_nop 7\n\ts_nop 3" : "+v"(S[0]), "+v"(S[1]), "+v"(o[0]), "+v"(o[1]), "+v"(o[2]), "+v"(o[3]))
#define NOP_A(A) asm volatile("s_nop 7\n\ts_nop 1" : "+v"(A[0][0]), "+v"(A[0][1]), "+v"(A[0][2]), "+v"(A[0][3]), "+v"(A[1][0]), "+v"(A[1][1]), "+v"(A[1][2]), "+v"(A[1][3]), \
    "+v"(A[2][0]), "+v"(A[2][1]), "+v"(A[2][2]), "+v"(A[2][3]), "+v"(A[3][0]), "+v"(A[3][1]), "+v"(A[3][2]), "+v"(A[3][3]), "+v"(A[4][0]), "+v"(A[4][1]), "+v"(A[4][2]), "+v"(A[4][3]))
DI float xh_sum(float v) { auto r = __builtin_amdgcn_permlane32_swap(__float_as_uint(v), __float_as_uint(v), false, false); return __uint_as_float(r[0]) + __uint_as_float(r[1]); }
DI float xh_max(float v) { auto r = __builtin_amdgcn_permlane32_swap(__float_as_uint(v), __float_as_uint(v), false, false); return __builtin_fmaxf(__uint_as_float(r[0]), __uint_as_float(r[1])); }
DI float mul_s(float a, float b) { float r; asm("v_mul_f32 %0, %1, %2" : "=v"(r) : "v"(a), "v"(b)); return r; }
DI float sub_s(float a, float b) { float r; asm("v_sub_f32 %0, %1, %2" : "=v"(r) : "v"(a), "v"(b)); return r; }
DI u32x4 pack_o(const f32x16& a, int y, float sc) {
    u32x4 w; w[0] = cvtpk(mul_s(a[4 * y + 0], sc), mul_s(a[4 * y + 1], sc)); w[1] = cvtpk(mul_s(a[4 * y + 2], sc), mul_s(a[4 * y + 3], sc));
    w[2] = cvtpk(mul_s(a[8 + 4 * y + 0], sc), mul_s(a[8 + 4 * y + 1], sc)); w[3] = cvtpk(mul_s(a[8 + 4 * y + 2], sc), mul_s(a[8 + 4 * y + 3], sc)); return w;
}
#define ABL_NODMA 0
#define ABL_NOEXP 0
#define ABL_NOMFMA 0
#define DMAPOS 2
DI void mask_diag(f32x16& s, int dq) {
    const float ninf = -__builtin_inff();
#pragma unroll
    for (int r0 = 0; r0 < 16; r0 += 4) {
        unsigned long long m0, m1, m2, m3;
        const int kc = 16 * ((r0 >> 2) & 1) + 8 * (r0 >> 3);
        float a = s[r0], b = s[r0 + 1], c = s[r0 + 2], d = s[r0 + 3];
        asm("v_cmp_gt_i32_e64 %4, %9, %8\n\tv_cmp_gt_i32_e64 %5, %10, %8\n\tv_cmp_gt_i32_e64 %6, %11, %8\n\tv_cmp_gt_i32_e64 %7, %12, %8\n\t"
            "v_cndmask_b32_e64 %0, %0, %13, %4\n\tv_cndmask_b32_e64 %1, %1, %13, %5\n\tv_cndmask_b32_e64 %2, %2, %13, %6\n\tv_cndmask_b32_e64 %3, %3, %13, %7"
            : "+v"(a), "+v"(b), "+v"(c), "+v"(d), "=&s"(m0), "=&s"(m1), "=&s"(m2), "=&s"(m3)
            : "v"(dq), "i"(kc), "i"(kc + 1), "i"(kc + 2), "i"(kc + 3), "v"(ninf));
        s[r0] = a; s[r0 + 1] = b; s[r0 + 2] = c; s[r0 + 3] = d;
    }
}
#define FRW 8
#define FRA 6
#define PADN -1
#define MPRIO 2

__global__ __launch_bounds__(512, 2) void k12(const char* __restrict__ x, const char* __restrict__ ckv, const char* __restrict__ wuk, const char* __restrict__ wuv,
                                              const char* __restrict__ wq, const char* __restrict__ w1, const char* __restrict__ b1, char* __restrict__ ws,
                                              const char* __restrict__ wo, const char* __restrict__ w2, const char* __restrict__ out) {
    extern __shared__ __attribute__((aligned(16))) char lds[];
    const unsigned wid = rfl(threadIdx.x >> 6), grp = wid >> 2, w4 = wid & 3;
    char* stamp_base = ws; (void)stamp_base;
    STAMP(0);
    const int b = blockIdx.x, xcd = b & 7, idx = b >> 3;
    if (threadIdx.x == 0) *(unsigned*)(ws + WS_FL + FL_P + (size_t)b * 128) = 0u;
    const int vt = idx & 1, hp = 2 * xcd + ((idx >> 1) & 1), rb = idx >> 2;
    const int head = 4 * xcd + (idx & 3), pi = idx >> 2;
    const int HB = 15 - pi, LB = pi;
    const int myblk = grp ? LB : HB;
    const int q16_0 = 8 * myblk + 2 * w4;
    const bool nft4 = head < 22;
    const int f0 = nft4 ? 64 * head : 1408 + 32 * (head - 22);
    const char* Abase = ckv + (size_t)rb * 256 * 1024;
    const char* Wbase = (vt ? wuv : wuk) + hp * 512;
    const unsigned lane0 = flane();
#define lane lane0
    const int row = 8 * b + wid;
    const unsigned l40 = lane < 40 ? lane : 39;
    u32x4 x0, x1;
    {
        const char* xr = uptr(x + (size_t)row * 1280);
        const unsigned vo = l40 * 32;
        asm volatile("global_load_dwordx4 %0, %2, %3\n\tglobal_load_dwordx4 %1, %2, %3 offset:16" : "=&v"(x0), "=&v"(x1) : "v"(vo), "s"(xr) : "memory");
    }
    unsigned avo[4], wvo[4];
    {
        const unsigned lds0 = (unsigned)(uintptr_t)lds;
        const char* Aw = Abase + (size_t)wid * 32 * 1024;
        const char* Ww = Wbase + (size_t)wid * 8 * 8192;
        const unsigned m0a = lds0 + wid * 4096, m0w = lds0 + 32768 + wid * 4096;
#pragma unroll
        for (int p = 0; p < 4; ++p) {
            const unsigned rw = 8 * p + (lane >> 3);
            avo[p] = rw * 1024 + (((lane & 7) ^ ((rw >> 1) & 7)) << 4) - 1024 * p;
            const unsigned k = 2 * p + (lane >> 5);
            const unsigned f = (k & 3) | ((wid & 1) << 2);
            wvo[p] = k * 8192 + (((lane & 31) ^ (f << 1)) << 4) - 1024 * p;
        }
#pragma unroll
        for (int kt = 0; kt < 2; ++kt) {
            const char* a_ = Aw + kt * 128; const char* w_ = Ww + (size_t)kt * 64 * 8192; const unsigned stg = kt * 65536;
            dma1<0>(m0a + stg, a_, avo[0]); dma1<1024>(m0a + stg, a_, avo[1]); dma1<2048>(m0a + stg, a_, avo[2]); dma1<3072>(m0a + stg, a_, avo[3]);
            dma1<0>(m0w + stg, w_, wvo[0]); dma1<1024>(m0w + stg, w_, wvo[1]); dma1<2048>(m0w + stg, w_, wvo[2]); dma1<3072>(m0w + stg, w_, wvo[3]);
        }
    }
#define TLBW 0
    if (TLBW && (wid == 6 || (wid == 7 && idx < 2))) {
        const unsigned vo = ((wid == 6 ? 2 * idx : 0) + (lane & 1)) * (2u << 20);
        const char* p_ = uptr(wid == 6 ? (const char*)ws : out);
        asm volatile("s_mov_b32 m0, %0\n\ts_nop 4\n\tglobal_load_lds_dword %1, %2" ::"s"((unsigned)(uintptr_t)lds + 131072 + wid * 256), "v"(vo), "s"(p_) : "memory");
    }
    u64 tag;
    asm volatile("s_load_dwordx2 %0, %1, 0x0\n\ts_waitcnt lgkmcnt(0)" : "=s"(tag) : "s"(uptr(ws + WS_FL + FL_EPOCH)) : "memory");
    tag += 1;
    {
        asm volatile("s_waitcnt vmcnt(16)" : "+v"(x0), "+v"(x1)::"memory");
        float v[16];
#pragma unroll
        for (int i = 0; i < 4; ++i) { v[2 * i] = bflo(x0[i]); v[2 * i + 1] = bfhi(x0[i]); v[8 + 2 * i] = bflo(x1[i]); v[8 + 2 * i + 1] = bfhi(x1[i]); }
        float s = 0.f;
#pragma unroll
        for (int i = 0; i < 16; ++i) s += v[i];
        if (lane >= 40) s = 0.f;
#pragma unroll
        for (int o = 32; o >= 1; o >>= 1) s += __shfl_xor(s, o);
        const float mean = s * (1.0f / DM);
        float q = 0.f;
#pragma unroll
        for (int i = 0; i < 16; ++i) { v[i] -= mean; q += v[i] * v[i]; }
        if (lane >= 40) q = 0.f;
#pragma unroll
        for (int o = 32; o >= 1; o >>= 1) q += __shfl_xor(q, o);
        const float rstd = 1.0f / sqrtf(q * (1.0f / DM) + 1e-5f);
        u32x4 o0, o1;
#pragma unroll
        for (int i = 0; i < 4; ++i) { o0[i] = cvtpk(v[2 * i] * rstd, v[2 * i + 1] * rstd); o1[i] = cvtpk(v[8 + 2 * i] * rstd, v[8 + 2 * i + 1] * rstd); }
        char* xf = ws + WS_XF + ((size_t)(row >> 4) * NKS_X + (l40 >> 1)) * 1024 + ((row & 15) + 32 * (l40 & 1)) * 16;
        if (lane < 40) { st_wt(xf, o0); st_wt(xf + 256, o1); }
    }
#undef lane
    u32x4 xa[4], xb[4];
    u32x2 bb[4];
    if (vt) k1_tile<true>(Abase, Wbase, ws + WS_VF, lds, avo, wvo, wid, hp, rb, ws, wq, w1, b1, tag, head, f0, nft4, q16_0, xa, xb, bb);
    else    k1_tile<false>(Abase, Wbase, ws + WS_KF, lds, avo, wvo, wid, hp, rb, ws, wq, w1, b1, tag, head, f0, nft4, q16_0, xa, xb, bb);
    STAMP(6);
    const unsigned lds0 = (unsigned)(uintptr_t)lds;
    const unsigned kslot0 = grp ? 114688 : 81920, kslot1 = kslot0 + 16384, kslot2 = grp ? 16384 : 0;
    const unsigned vslot0 = grp ? 65536 : 32768, vslot1 = grp ? 147456 : 49152;
    const int nL = 2 * pi + 2;
    const int nsw = grp ? nL : 1000;
    const size_t jump = (size_t)(17 - nL) * 16384;
    const char* kptr = uptr(ws + WS_KF + (size_t)head * 524288 + w4 * 4096);
    const char* vptr = uptr(ws + WS_VF + (size_t)head * 524288 + w4 * 4096);
    bf16x8 qf[8];
    {
        bf16x8 q16[2][4];
        const unsigned kvflag = FL_KV + (hp * 16 + vt * 8 + rb) * 8;
        if (nft4) proj_phase<4>(wq, w1, ws, lds, head, f0, q16_0, q16, wo, w2, lds0 + vslot0 + w4 * 4096, wid, xa, xb, bb, tag, kvflag, head >> 1, lds0 + kslot0 + w4 * 4096, lds0 + kslot1 + w4 * 4096, kptr, kptr + 16384);
        else      proj_phase<2>(wq, w1, ws, lds, head, f0, q16_0, q16, wo, w2, lds0 + vslot0 + w4 * 4096, wid, xa, xb, bb, tag, kvflag, head >> 1, lds0 + kslot0 + w4 * 4096, lds0 + kslot1 + w4 * 4096, kptr, kptr + 16384);
        kptr += 2 * 16384; if (nsw == 2) kptr += jump;
#pragma unroll
        for (int v = 0; v < 4; ++v) {
            const u32x4 r0 = __builtin_bit_cast(u32x4, q16[0][v]), r1 = __builtin_bit_cast(u32x4, q16[1][v]);
            u32x4 z0, z1;
#pragma unroll
            for (int w = 0; w < 4; ++w) {
                const auto s1 = __builtin_amdgcn_permlane16_swap(r0[w], r1[w], false, false);
                const auto s2 = __builtin_amdgcn_permlane32_swap(s1[0], s1[1], false, false);
                z0[w] = s2[0]; z1[w] = s2[1];
            }
            qf[2 * v] = __builtin_bit_cast(bf16x8, z0); qf[2 * v + 1] = __builtin_bit_cast(bf16x8, z1);
        }
    }
    STAMP(7);
    const unsigned lane = flane();
    if (grp == 0) {
        char* qh = ws + WS_QH + (size_t)b * 32768 + w4 * 8192 + lane * 16;
#pragma unroll
        for (int s = 0; s < 8; ++s) *(bf16x8*)(qh + s * 1024) = qf[s];
    }
    lds_cptr Lb = (lds_cptr)lds + lane * 16;
    const unsigned m0b = lds0 + w4 * 4096;
    const unsigned dvo = lane * 16;
    int q0 = 128 * myblk + 32 * w4;
    int tile = 0;
    asm volatile("s_waitcnt lgkmcnt(0)\n\ts_barrier" ::: "memory");
    dma4(m0b + vslot0, vptr, dvo); vptr += 16384;
    dma4(m0b + kslot2, kptr, dvo); kptr += 16384;
    f32x16 o[4], S[2], negm;
    float mref = 0.f, lsum = 0.f, lfin = 1.f;
    bf16x8 pf[4], fr[FRW];
#pragma unroll
    for (int i = 0; i < 4; ++i) o[i] = f32x16{};
    negm = f32x16{}; S[0] = f32x16{}; S[1] = f32x16{};
    asm volatile("s_nop 1" : "+v"(negm));
    NOP_SO(S, o);
    unsigned ks_cur = kslot0, ks_nxt = kslot1, ks_nn = kslot2;
#define KOFF(i) (((i) & 1) * 8192 + ((i) >> 1) * 1024)
#define VOFF(j) (((j) >> 3) * 8192 + ((j) & 3) * 2048 + (((j) >> 2) & 1) * 1024)
#define EARLY_K(slot) do { _Pragma("unroll") for (int i = 0; i < FRA; ++i) fr[i] = lds_frag(Lb + (slot) + KOFF(i)); } while (0)
    EARLY_K(kslot0);
    if (grp) asm volatile("s_barrier" ::: "memory");
#define PHASE_M(t, QK, PV, VS, DV, DK, WN)      \
    do {                                                                                                                              \
        if (MPRIO == 1) __builtin_amdgcn_s_setprio(1);                                                                                \
        if (MPRIO >= 2) __builtin_amdgcn_s_setprio(0);                                                                                \
        lds_cptr kp_ = Lb + ks_cur; lds_cptr vp_ = Lb + ((VS) ? vslot1 : vslot0);                                                     \
        if (!(QK)) { _Pragma("unroll") for (int i = 16; i < 16 + FRA; ++i) fr[i % FRW] = lds_frag(vp_ + VOFF(i - 16)); }               \
        _Pragma("unroll") for (int i = (QK) ? 0 : 16; i < ((PV) ? 32 : 16); ++i) {                                                    \
            const int n = i + FRA;                                                                                                    \
            if (n < 16) fr[n % FRW] = lds_frag(kp_ + KOFF(n)); else if ((PV) && n < 32) fr[n % FRW] = lds_frag(vp_ + VOFF(n - 16));    \
            SBAR();                                                                                                                   \
            if (ABL_NOMFMA && (i & (ABL_NOMFMA - 1)) != 0) { PINR(fr[i % FRW]); }                                                       \
            else if (i < 2) mfma32_ini(S[i & 1], fr[i % FRW], qf[i >> 1], negm);                                                        \
            else if (i < 16) mfma32_acc(S[i & 1], fr[i % FRW], qf[i >> 1]);                                                             \
            else mfma32_acc(o[i & 3], fr[i % FRW], pf[(i - 16) >> 2]);                                                                  \
            SBAR();                                                                                                                   \
            if (PADN >= 0) {     \
                const bool dm_ = ((DV) && i >= 1 && i <= 7 && (i & 1)) || ((DK) && i >= 9 && i <= 15 && (i & 1));                      \
                if (!dm_) asm volatile("s_nop %0" ::"i"(PADN >= 0 ? PADN : 0)); else if (PADN >= 1) asm volatile("s_nop %0" ::"i"(PADN >= 1 ? PADN - 1 : 0)); \
            }                                                                                                                         \
            if ((QK) && i == 16 && (t) + 1 == nsw) {        \
                const char* qs_ = uptr(ws + WS_QH + (size_t)b * 32768 + w4 * 8192);                                                   \
                asm volatile("s_nop 4\n\tglobal_load_dwordx4 %0, %8, %9\n\tglobal_load_dwordx4 %1, %8, %9 offset:1024\n\tglobal_load_dwordx4 %2, %8, %9 offset:2048\n\tglobal_load_dwordx4 %3, %8, %9 offset:3072" \
                             : "+v"(qf[0]), "+v"(qf[1]), "+v"(qf[2]), "+v"(qf[3]), "+v"(qf[4]), "+v"(qf[5]), "+v"(qf[6]), "+v"(qf[7]) : "v"(dvo), "s"(qs_) : "memory"); \
                asm volatile("global_load_dwordx4 %4, %8, %9\n\tglobal_load_dwordx4 %5, %8, %9 offset:1024\n\tglobal_load_dwordx4 %6, %8, %9 offset:2048\n\tglobal_load_dwordx4 %7, %8, %9 offset:3072" \
                             : "+v"(qf[0]), "+v"(qf[1]), "+v"(qf[2]), "+v"(qf[3]), "+v"(qf[4]), "+v"(qf[5]), "+v"(qf[6]), "+v"(qf[7]) : "v"(dvo), "s"(qs_ + 4096) : "memory"); \
            }                                                                                                                         \
            SBAR();                                                                                                                   \
        }                                                                                                                             \
        if (MPRIO == 1) __builtin_amdgcn_s_setprio(0);                                                                                \
        if (MPRIO >= 2) __builtin_amdgcn_s_setprio(3);                                                                                \
        TICK(tA);                                                                                                                     \
        if ((WN) >= 0) asm volatile("s_waitcnt vmcnt(%0)\n\ts_barrier" ::"i"((WN) >= 0 ? (WN) : 0) : "memory");                       \
        else asm volatile("s_barrier" ::: "memory");                                                                                  \
        TICK(tX);                                                                                                                     \
    } while (0)
#define EXP_PACK()                    \
    do {                                                                                                                              \
        float e_[32], a_[2];                                                                                                          \
        unsigned c_[16];                                                                                                              \
        _Pragma("unroll") for (int k = 0; k < 18; ++k) {                                                                              \
            if (k < 16) { e_[2 * k] = v_exp(S[k >> 3][2 * (k & 7)]); e_[2 * k + 1] = v_exp(S[k >> 3][2 * (k & 7) + 1]); }             \
            if (k == 1) { a_[0] = e_[0]; a_[1] = e_[1]; }                                                                             \
            if (k >= 2 && k < 17) { a_[0] = v_add(a_[0], e_[2 * k - 2]); a_[1] = v_add(a_[1], e_[2 * k - 1]); }                        \
            if (k >= 2) c_[k - 2] = v_cvt(e_[2 * k - 4], e_[2 * k - 3]);                                                              \
        }                                                                                                                             \
        _Pragma("unroll") for (int k = 0; k < 4; ++k) pf[k] = __builtin_bit_cast(bf16x8, u32x4{c_[4 * k], c_[4 * k + 1], c_[4 * k + 2], c_[4 * k + 3]}); \
        ts_ = v_add(a_[0], a_[1]);                                                                                                    \
    } while (0)
#define REBASE(FIRST)                                                          \
    do {                                                                                                                              \
        float rm = S[0][0];                                                                                                           \
        _Pragma("unroll") for (int e = 1; e < 32; ++e) rm = __builtin_fmaxf(rm, S[e >> 4][e & 15]);                                   \
        rm = xh_max(rm);                                                                                                              \
        const float dl = (FIRST) ? (rm == -__builtin_inff() ? 0.f : rm) : __builtin_fmaxf(rm, 0.f);                                   \
        mref += dl;                                                                                                                   \
        if (!(FIRST)) { float f = __builtin_amdgcn_exp2f(-dl); asm volatile("s_nop 1" : "+v"(f)); lsum *= f;               \
            _Pragma("unroll") for (int i = 0; i < 4; ++i) _Pragma("unroll") for (int r = 0; r < 16; ++r) o[i][r] = mul_s(o[i][r], f); } \
        _Pragma("unroll") for (int r = 0; r < 16; ++r) { S[0][r] = sub_s(S[0][r], dl); S[1][r] = sub_s(S[1][r], dl); }                \
        const float nm = -mref;                                                                                                       \
        _Pragma("unroll") for (int r = 0; r < 16; ++r) negm[r] = nm;                                                                  \
    } while (0)
#define PHASE_V(t, WN, LASTV, VS, DK, DV)                                                                                             \
    do {                                                                                                                              \
        const bool swn = ((t) + 1 == nsw), sw0 = ((t) == nsw);                                                                        \
        const bool diag = 64 * tile + 63 > q0 || sw0, first_ = (t) == 0 || sw0;                                                       \
        if (DMAPOS == 2 && !ABL_NODMA) {                                                                                              \
            if (DV) { dma4(m0b + ((VS) ? vslot1 : vslot0), vptr, dvo); vptr += 16384 + ((t) + 2 == nsw ? jump : 0); }      \
            if (DK) { dma4(m0b + ks_cur, kptr, dvo); kptr += 16384 + ((t) + 4 == nsw ? jump : 0); }                       \
        }                                                                                                                             \
        NOP_SO(S, o);                                                                                                                 \
        float ts_;                                                                                                                    \
        if (__builtin_expect(diag || first_, 0)) {                                                                                    \
            if (sw0) {                                                                                \
                float il_ = __builtin_amdgcn_rcpf(xh_sum(lsum));                                                                      \
                asm volatile("s_nop 1" : "+v"(il_));                                                                                  \
                char* cp_ = ws + WS_CH + ((size_t)(8 * LB + 2 * w4 + ((lane >> 4) & 1)) * NKS_CH + 4 * head) * 1024 + ((lane & 15) + 16 * (lane >> 5)) * 16; \
                _Pragma("unroll") for (int dt = 0; dt < 4; ++dt) _Pragma("unroll") for (int y = 0; y < 2; ++y)                        \
                    *(u32x4*)(cp_ + dt * 1024 + y * 512) = pack_o(o[dt], y, il_);                                                     \
                _Pragma("unroll") for (int i = 0; i < 4; ++i) o[i] = f32x16{};                                                        \
                lsum = 0.f; mref = 0.f; q0 = 128 * HB + 32 * w4;                                                                      \
            }                                                                                                                         \
            if (64 * tile + 63 > q0) {         \
                const int dq = (int)(lane & 31) - 4 * (int)(lane >> 5);                                                               \
                _Pragma("unroll") for (int u = 0; u < 2; ++u) {                                                                       \
                    const int kb = 64 * tile + 32 * u;                                                                                \
                    if (kb > q0) { _Pragma("unroll") for (int r = 0; r < 16; ++r) S[u][r] = -__builtin_inff(); }                       \
                    else if (kb == q0) mask_diag(S[u], dq);                                                                           \
                }                                                                                                                     \
            }                                                                                                                         \
            if (first_) REBASE(true);                                                                                                 \
        }                                                                                                                             \
        EXP_PACK();                                                                                                                   \
        if (__builtin_expect(!(ABL_NOMFMA || ABL_NODMA) && __any(ts_ > PLIM), 0)) { REBASE(false); EXP_PACK(); }     \
        lsum += ts_;                                                                                                                  \
        if (!(LASTV)) EARLY_K(ks_nxt);                                                                                                \
        if (__builtin_expect(swn, 0)) {                                                                                               \
            negm = f32x16{};                                                                                                          \
            asm volatile("s_waitcnt vmcnt(%9)" : "+v"(qf[0]), "+v"(qf[1]), "+v"(qf[2]), "+v"(qf[3]), "+v"(qf[4]), "+v"(qf[5]), "+v"(qf[6]), "+v"(qf[7]), "+v"(negm) : "i"(DMAPOS == 2 ? (WN) : 0) : "memory"); \
            tile += 17 - nL;                                                                                                          \
        }                                                                                                                             \
        TICK(tB);                                                                                                                     \
        asm volatile("s_waitcnt vmcnt(%5)\n\ts_barrier" : "+v"(pf[0]), "+v"(pf[1]), "+v"(pf[2]), "+v"(pf[3]), "+v"(negm) : "i"(WN) : "memory"); \
        TICK(tO);                                                                                                                     \
        { const unsigned r_ = ks_cur; ks_cur = ks_nxt; ks_nxt = ks_nn; ks_nn = r_; }                                                  \
        tile += 1;                                                                                                                    \
    } while (0)
    constexpr float PLIM = 256.0f;
    PHASE_M(0, true, false, 1, false, false, -1);
    PHASE_V(0, 8, false, 1, true, true);
    for (int t = 1; t < 13; t += 2) {
        PHASE_M(t, true, true, 0, false, false, -1); PHASE_V(t, 8, false, 0, true, true);
        PHASE_M(t + 1, true, true, 1, false, false, -1); PHASE_V(t + 1, 8, false, 1, true, true);
    }
    PHASE_M(13, true, true, 0, false, false, -1); PHASE_V(13, 8, false, 0, true, true);
    PHASE_M(14, true, true, 1, false, false, -1); PHASE_V(14, 4, false, 1, false, true);
    PHASE_M(15, true, true, 0, false, false, -1); PHASE_V(15, 4, false, 0, false, true);
    PHASE_M(16, true, true, 1, false, false, -1); PHASE_V(16, 0, true, 1, false, false);
    PHASE_M(17, false, true, 0, false, false, -1);
    if (!grp) asm volatile("s_barrier" ::: "memory");
    NOP_SO(S, o);
    STAMP(8);
    {
        const float ls = xh_sum(lsum);
        const float mm = ls > 0.f ? mref : -__builtin_inff();
        lds_ptr X = (lds_ptr)lds + wid * 8192 + lane * 16;
        f32x16 keep[2];
        if (grp) {
#pragma unroll
            for (int i = 0; i < 2; ++i) {
                keep[i] = o[2 + i];
#pragma unroll
                for (int y = 0; y < 4; ++y) *(__attribute__((address_space(3))) f32x4*)(X + (4 * i + y) * 1024) = f32x4{o[i][4 * y], o[i][4 * y + 1], o[i][4 * y + 2], o[i][4 * y + 3]};
            }
        } else {
#pragma unroll
            for (int i = 0; i < 2; ++i) {
                keep[i] = o[i];
#pragma unroll
                for (int y = 0; y < 4; ++y) *(__attribute__((address_space(3))) f32x4*)(X + (4 * i + y) * 1024) = f32x4{o[2 + i][4 * y], o[2 + i][4 * y + 1], o[2 + i][4 * y + 2], o[2 + i][4 * y + 3]};
            }
        }
        lds_ptr Y = (lds_ptr)lds + 65536 + wid * 512 + lane * 8;
        *(__attribute__((address_space(3))) f32x2*)Y = f32x2{mm, ls};
        asm volatile("s_waitcnt lgkmcnt(0)\n\ts_barrier" ::: "memory");
        const unsigned ow = wid ^ 4;
        lds_cptr Xo = (lds_cptr)lds + ow * 8192 + lane * 16;
        const f32x2 oml = *(const __attribute__((address_space(3))) f32x2*)((lds_cptr)lds + 65536 + ow * 512 + lane * 8);
        const float mx = __builtin_fmaxf(mm, oml[0]);
        const float e0 = __builtin_amdgcn_exp2f(mm - mx), e1 = __builtin_amdgcn_exp2f(oml[0] - mx);
        const float inv = __builtin_amdgcn_rcpf(ls * e0 + oml[1] * e1);
        const float fs = e0 * inv, fo = e1 * inv;
        char* cp = ws + WS_CH + ((size_t)(8 * HB + 2 * w4 + ((lane >> 4) & 1)) * NKS_CH + 4 * head + 2 * grp) * 1024 + ((lane & 15) + 16 * (lane >> 5)) * 16;
#pragma unroll
        for (int i = 0; i < 2; ++i) {
            f32x16 m;
#pragma unroll
            for (int y = 0; y < 4; ++y) {
                const f32x4 pv = *(const __attribute__((address_space(3))) f32x4*)(Xo + (4 * i + y) * 1024);
#pragma unroll
                for (int r = 0; r < 4; ++r) m[4 * y + r] = keep[i][4 * y + r] * fs + pv[r] * fo;
            }
#pragma unroll
            for (int y = 0; y < 2; ++y) *(u32x4*)(cp + i * 1024 + y * 512) = pack_o(m, y, 1.f);
        }
    }
    if (b == 0 && wid == 0) {
        const char* flb = uptr(ws + WS_FL);
#pragma unroll
        for (int i = 0; i < 4; ++i) { u32x2 f = u32x2{~(unsigned)tag, 0u}; flag_wait<false>(0, f, flb, FL_X + (64 * i + lane) * 8, tag); }
        flag_set(0, flb, FL_EPOCH, tag);
    }
    STAMP(9);
#undef KOFF
#undef VOFF
#undef EARLY_K
#undef PHASE_M
#undef PHASE_V
#undef EXP_PACK
#undef REBASE
}

typedef __attribute__((address_space(1))) unsigned gu32;
#define K3_NOMFMA 0
#define K3_NODMA 0
#define K3_NORD 0
constexpr int K3_SLOT = 36864;
constexpr int PP_SET = 8 * 5 * 1024;
DI float silu_f(float v) { return v * __builtin_amdgcn_rcpf(1.0f + __builtin_amdgcn_exp2f(-1.4426950408889634f * v)); }
DI f32x4 unpk_lo(const u32x4& w) { return f32x4{bflo(w[0]), bfhi(w[0]), bflo(w[1]), bfhi(w[1])}; }
DI f32x4 unpk_hi(const u32x4& w) { return f32x4{bflo(w[2]), bfhi(w[2]), bflo(w[3]), bfhi(w[3])}; }

template <int KS>
DI void k3_finish(const f32x4 (&res)[10], const f32x4 (&res2)[10], const char* __restrict__ x, char* __restrict__ out, char* __restrict__ ws,
                  int grp64, int qt, int mt) {
    const unsigned tid = threadIdx.x, lane = tid & 63, c = lane & 15, g = lane >> 4;
    const unsigned wid = rfl(tid >> 6), grp = wid >> 2, w4 = wid & 3, qh = w4 >> 1, mh = w4 & 1;
    char* stamp_base = ws; (void)stamp_base;
    const __amdgpu_buffer_rsrc_t rs = __builtin_amdgcn_make_buffer_rsrc(ws + WS_PP + (size_t)grp64 * (5 * PP_SET), (short)0, 5 * PP_SET, 0x00020000);
    const unsigned vo = wid * 5120 + lane * 16;
    const unsigned r = (KS - wid) & 3;
    f32x4 oa[2][2], ob[2][2];
#pragma unroll
    for (int j = 0; j < 5; ++j) {
        if ((j & 3) == r) {
            oa[j >> 2][0] = res[2 * j]; oa[j >> 2][1] = res[2 * j + 1];
            if (KS == 2) { ob[j >> 2][0] = res2[2 * j]; ob[j >> 2][1] = res2[2 * j + 1]; }
        } else {
            __builtin_amdgcn_raw_buffer_store_b128(pack44(res[2 * j], res[2 * j + 1]), rs, vo + j * 1024 + (KS < 2 ? KS : KS + 1) * PP_SET, 0, 16);
            if (KS == 2) __builtin_amdgcn_raw_buffer_store_b128(pack44(res2[2 * j], res2[2 * j + 1]), rs, vo + j * 1024 + 2 * PP_SET, 0, 16);
        }
    }
    const int nown = r == 0 ? 2 : 1;
    u32x2 xv[2][2];
    unsigned oo[2][2];
#pragma unroll
    for (int sl = 0; sl < 2; ++sl)
#pragma unroll
        for (int t = 0; t < 2; ++t) {
            const unsigned i = 2 * (sl ? 4 : r) + t, a = i >= 5 ? 1 : 0, n = i - 5 * a;
            oo[sl][t] = (16 * (8 * qt + 4 * qh + 2 * grp + a) + c) * (DM * 2) + (16 * (10 * mt + 5 * mh + n) + 4 * g) * 2;
            xv[sl][t] = *(const u32x2*)(x + oo[sl][t]);
        }
    STAMP(14);
    asm volatile("s_waitcnt vmcnt(0)\n\ts_barrier" ::: "memory");
    STAMP(15);
    gu32* fl = (gu32*)(ws + WS_FL + FL_P + (size_t)grp64 * 4 * 128);
    if (tid == 0) __hip_atomic_store(fl + KS * 32, 1u, __ATOMIC_RELAXED, __HIP_MEMORY_SCOPE_AGENT);
    for (unsigned spins = 0; spins < (1u << 22); ++spins) {
        unsigned v = 1u;
        if (lane < 4 && lane != KS) v = __hip_atomic_load(fl + lane * 32, __ATOMIC_RELAXED, __HIP_MEMORY_SCOPE_AGENT);
        if (__all(v != 0u)) break;
        __builtin_amdgcn_s_sleep(1);
    }
    asm volatile("" ::: "memory");
    STAMP(16);
    u32x4 p[2][5];
#pragma unroll
    for (int sl = 0; sl < 2; ++sl)
        if (sl < nown) {
            const unsigned j = sl ? 4 : r;
#pragma unroll
            for (int st = 0; st < 5; ++st) {
                const bool own = (st < 3 && st == KS) || (st == 3 && KS == 2) || (st == 4 && KS == 3);
                if (!own) p[sl][st] = __builtin_amdgcn_raw_buffer_load_b128(rs, vo + j * 1024, st * PP_SET, 16);
            }
        }
#pragma unroll
    for (int sl = 0; sl < 2; ++sl)
        if (sl < nown) {
#pragma unroll
            for (int t = 0; t < 2; ++t) {
                f32x4 sv = f32x4{}, bv = f32x4{};
#pragma unroll
                for (int st = 0; st < 5; ++st) {
                    const bool own = (st < 3 && st == KS) || (st == 3 && KS == 2) || (st == 4 && KS == 3);
                    const f32x4 v = own ? ((KS == 2 && st == 2) ? ob[sl][t] : oa[sl][t]) : (t ? unpk_hi(p[sl][st]) : unpk_lo(p[sl][st]));
                    if (st < 3) sv += v; else bv += v;
                }
                const float o0 = bflo(xv[sl][t][0]) + sv[0] * silu_f(bv[0]), o1 = bfhi(xv[sl][t][0]) + sv[1] * silu_f(bv[1]);
                const float o2 = bflo(xv[sl][t][1]) + sv[2] * silu_f(bv[2]), o3 = bfhi(xv[sl][t][1]) + sv[3] * silu_f(bv[3]);
                u32x2 w; w[0] = cvtpk(o0, o1); w[1] = cvtpk(o2, o3);
                *(u32x2*)(out + oo[sl][t]) = w;
            }
        }
}

template <int GRP>
DI void k3_body(const char* __restrict__ x, const char* __restrict__ wo, const char* __restrict__ w2, char* __restrict__ ws, char* __restrict__ out, char* lds) {
    constexpr int P = GRP ? 4 : 5;
    const unsigned tid = threadIdx.x, lane = tid & 63, g = lane >> 4;
    const unsigned wid = rfl(tid >> 6), w4 = wid & 3, qh = w4 >> 1, mh = w4 & 1;
    char* stamp_base = ws; (void)stamp_base;
    const int b = blockIdx.x, xcd = b & 7, idx = b >> 3;
    const int ks = xcd >> 1, qt = 8 * (xcd & 1) + (idx >> 2), mt = idx & 3;
    const int st0 = ks == 0 ? 0 : ks == 1 ? 23 : ks == 2 ? 46 : 68;
    const unsigned lds0 = (unsigned)(uintptr_t)lds;
    lds_cptr L = (lds_cptr)lds;
    const char* aptr = uptr(ws + WS_CH + ((size_t)(8 * qt + wid) * NKS_CH + 2 * st0) * 1024);
    const char* w2b = uptr(w2 + mt * 320);
    const char* wptr = st0 < 64 ? uptr(wo + (size_t)st0 * 64 * 1280 + mt * 320) : w2b + (size_t)(st0 - 64) * 64 * 1280;
    int sdma = st0;
    const unsigned avo = lane * 16;
    unsigned wvo[3];
    const unsigned p0 = GRP ? 12 + 2 * w4 : 3 * w4;
#pragma unroll
    for (int i = 0; i < P - 2; ++i) {
        const unsigned o = (p0 + i) * 1024 + lane * 16, row = o / 320, c16 = (o % 320) >> 4;
        wvo[i] = row * 1280 + ((c16 ^ (((row >> 2) & 1) << 1)) << 4) - 1024 * i;
    }
    const unsigned m0a = lds0 + wid * 2048, m0w = lds0 + 16384 + p0 * 1024;
#define K3_ADV() do { ++sdma; aptr += 2048; wptr = sdma == 64 ? w2b : wptr + 81920; } while (0)
#define K3_DMA(slot) do { set_m0(m0a + (slot)); asm volatile("s_nop 0"); dma_nom0<0>(aptr, avo); dma_nom0<1024>(aptr, avo); \
        set_m0(m0w + (slot)); asm volatile("s_nop 0"); dma_nom0<0>(wptr, wvo[0]); dma_nom0<1024>(wptr, wvo[1]); if (!GRP) dma_nom0<2048>(wptr, wvo[2]); K3_ADV(); } while (0)
    K3_DMA(0); K3_DMA(K3_SLOT); K3_DMA(2 * K3_SLOT); K3_DMA(3 * K3_SLOT);
    const unsigned ard = (8 * qh + GRP) * 1024 + lane * 16;
    const unsigned q = (lane & 15) >> 2, p = lane & 3;
    const unsigned wrow = 16384 + (32 * GRP + 4 * g + q) * 320 + 160 * mh + 8 * p;
    const unsigned sgn = ((g & 1) ? 32u : 0u);
    const unsigned wrdE = (mh & 1) ? wrow - sgn : wrow + sgn;
    const unsigned wrdO = (mh & 1) ? wrow + sgn : wrow - sgn;
    f32x4 acc[5][4], acc2[5][4];
#pragma unroll
    for (int n = 0; n < 5; ++n)
#pragma unroll
        for (int a = 0; a < 4; ++a) { acc[n][a] = f32x4{}; acc2[n][a] = f32x4{}; }
    NOP_A(acc);
    bf16x8 af[2][4], wf[4], w4f[2];
#define K3_RW(slot, n) tr_frag(L + (slot) + (((n) & 1) ? wrdO : wrdE) + 32 * (n), L + (slot) + (((n) & 1) ? wrdO : wrdE) + 32 * (n) + 16 * 320)
#define K3_RA(slot, a) lds_frag(L + (slot) + ard + (a) * 2048)
    asm volatile("s_waitcnt vmcnt(%0)\n\ts_barrier" ::"i"(3 * P) : "memory");
    STAMP(11);
    unsigned cur = 0;
#define K3_ITER(PAR, WN, RD, DM) K3_ITER_(PAR, WN, RD, DM, false)
#define K3_ITER_(PAR, WN, RD, DM, SAFE)                                                                                 \
    do {                                                                                                                \
        asm volatile("s_waitcnt vmcnt(%0) lgkmcnt(0)\n\ts_barrier" ::"i"(WN) : "memory");                               \
        const unsigned nxt_ = cur == 3 * K3_SLOT ? 0u : cur + K3_SLOT;                                                  \
        _Pragma("unroll") for (int n = 0; n < 5; ++n) {                                                                 \
            _Pragma("unroll") for (int a = 0; a < 4; ++a) {                                                             \
                if (SAFE) mfma_acc_s(acc[n][a], n < 4 ? wf[n & 3] : w4f[PAR], af[PAR][a]);                              \
                else if (!K3_NOMFMA) mfma_acc(acc[n][a], n < 4 ? wf[n & 3] : w4f[PAR], af[PAR][a]);                     \
                else { PINR(n < 4 ? wf[n & 3] : w4f[PAR]); PINR(af[PAR][a]); }                                          \
                SBAR();                                                                                                 \
                if (RD && !K3_NORD) {                                                                                               \
                    if (n == 0 && a == 0) w4f[1 - (PAR)] = K3_RW(nxt_, 4);                                              \
                    if (n < 4 && a == 1) af[1 - (PAR)][n & 3] = K3_RA(nxt_, n & 3);                                     \
                    if (n >= 1 && a == 0) wf[(n - 1) & 3] = K3_RW(nxt_, n - 1);                                         \
                }                                                                                                       \
                if (DM && !K3_NODMA) {                                                                                  \
                    if (n == 0 && a == 2) set_m0(m0a + cur);                                                            \
                    if (n == 0 && a == 3) dma_nom0<0>(aptr, avo);                                                       \
                    if (n == 1 && a == 2) dma_nom0<1024>(aptr, avo);                                                    \
                    if (n == 1 && a == 3) set_m0(m0w + cur);                                                            \
                    if (n == 2 && a == 2) dma_nom0<0>(wptr, wvo[0]);                                                    \
                    if (n == 3 && a == 2) dma_nom0<1024>(wptr, wvo[1]);                                                 \
                    if (n == 4 && a == 1 && !GRP) dma_nom0<2048>(wptr, wvo[2]);                                         \
                }                                                                                                       \
                SBAR();                                                                                                 \
            }                                                                                                           \
        }                                                                                                               \
        if (DM) K3_ADV();                                                                                               \
        cur = nxt_;                                                                                                     \
    } while (0)
#define K3_FIRST(PAR) do { _Pragma("unroll") for (int a = 0; a < 4; ++a) af[PAR][a] = K3_RA(0, a);                      \
        _Pragma("unroll") for (int n = 0; n < 4; ++n) wf[n] = K3_RW(0, n);                                              \
        w4f[PAR] = K3_RW(0, 4); } while (0)
    if (ks != 2) { K3_FIRST(1); K3_ITER_(1, 2 * P, true, true, true); }
    else K3_FIRST(0);
    for (int it = 0; it < 9; ++it) { K3_ITER(0, 2 * P, true, true); K3_ITER(1, 2 * P, true, true); }
    if (ks == 2) {
        NOP_A(acc);
#pragma unroll
        for (int n = 0; n < 5; ++n)
#pragma unroll
            for (int a = 0; a < 4; ++a) { acc2[n][a] = acc[n][a]; acc[n][a] = f32x4{}; PIN(acc2[n][a]); }
        NOP_A(acc);
    }
    K3_ITER(0, 2 * P, true, false); K3_ITER(1, P, true, false); K3_ITER(0, 0, true, false); K3_ITER(1, 0, false, false);
    NOP_A(acc);
    STAMP(12);
    f32x4 res[10], res2[10];
    {
        lds_ptr X = (lds_ptr)lds + wid * 10240 + lane * 16;
        lds_cptr Y = (lds_cptr)lds + (wid ^ 4) * 10240 + lane * 16;
#pragma unroll
        for (int a = 0; a < 2; ++a)
#pragma unroll
            for (int n = 0; n < 5; ++n) *(__attribute__((address_space(3))) f32x4*)(X + (a * 5 + n) * 1024) = acc[n][GRP ? a : 2 + a];
        if (ks == 2) {
#pragma unroll
            for (int a = 0; a < 2; ++a)
#pragma unroll
                for (int n = 0; n < 5; ++n) *(__attribute__((address_space(3))) f32x4*)(X + 81920 + (a * 5 + n) * 1024) = acc2[n][GRP ? a : 2 + a];
        }
        asm volatile("s_waitcnt lgkmcnt(0)\n\ts_barrier" ::: "memory");
#pragma unroll
        for (int a = 0; a < 2; ++a)
#pragma unroll
            for (int n = 0; n < 5; ++n) {
                res[a * 5 + n] = acc[n][GRP ? 2 + a : a] + *(const __attribute__((address_space(3))) f32x4*)(Y + (a * 5 + n) * 1024);
                res2[a * 5 + n] = f32x4{};
            }
        if (ks == 2) {
#pragma unroll
            for (int a = 0; a < 2; ++a)
#pragma unroll
                for (int n = 0; n < 5; ++n) res2[a * 5 + n] = acc2[n][GRP ? 2 + a : a] + *(const __attribute__((address_space(3))) f32x4*)(Y + 81920 + (a * 5 + n) * 1024);
        }
    }
    STAMP(17);
    const int grp64 = qt * 4 + mt;
    if (ks == 0) k3_finish<0>(res, res2, x, out, ws, grp64, qt, mt);
    else if (ks == 1) k3_finish<1>(res, res2, x, out, ws, grp64, qt, mt);
    else if (ks == 2) k3_finish<2>(res, res2, x, out, ws, grp64, qt, mt);
    else k3_finish<3>(res, res2, x, out, ws, grp64, qt, mt);
    STAMP(13);
#undef K3_ADV
#undef K3_DMA
#undef K3_RW
#undef K3_RA
#undef K3_ITER
#undef K3_ITER_
#undef K3_FIRST
}

__global__ __launch_bounds__(512, 2) void k3_out(const char* __restrict__ x, const char* __restrict__ wo, const char* __restrict__ w2, char* __restrict__ ws,
                                                 char* __restrict__ out) {
    extern __shared__ __attribute__((aligned(16))) char lds[];
    char* stamp_base = ws; (void)stamp_base;
    const unsigned wid = rfl(threadIdx.x >> 6); (void)wid;
    STAMP(10);
    if (rfl(threadIdx.x >> 8) == 0) k3_body<0>(x, wo, w2, ws, out, lds);
    else k3_body<1>(x, wo, w2, ws, out, lds);
}

extern "C" void kernel_launch(void* const* d_in, const int* in_sizes, int n_in, void* d_out, int out_size, void* d_ws, size_t ws_size,
                              hipStream_t stream) {
    const char* x = (const char*)d_in[0];
    const char* ckv = (const char*)d_in[1];
    const char* wuk = (const char*)d_in[3];
    const char* wuv = (const char*)d_in[4];
    char* ws = (char*)d_ws;
    (void)hipFuncSetAttribute((const void*)k12, hipFuncAttributeMaxDynamicSharedMemorySize, 163840);
    k12<<<256, 512, 163840, stream>>>(x, ckv, wuk, wuv, (const char*)d_in[2], (const char*)d_in[7], (const char*)d_in[8], ws, (const char*)d_in[5], (const char*)d_in[9], (const char*)d_out);
    (void)hipFuncSetAttribute((const void*)k3_out, hipFuncAttributeMaxDynamicSharedMemorySize, 163840);
    k3_out<<<256, 512, 163840, stream>>>(x, (const char*)d_in[5], (const char*)d_in[9], ws, (char*)d_out);
}
```

```cpp
#include <hip/hip_runtime.h>
#include <hip/hip_bf16.h>
#include <stdint.h>

typedef __bf16 bf16x8 __attribute__((ext_vector_type(8)));
typedef __bf16 bf16x2 __attribute__((ext_vector_type(2)));
typedef short s16x4 __attribute__((ext_vector_type(4)));
typedef short s16x8 __attribute__((ext_vector_type(8)));
typedef short v4i16_t __attribute__((ext_vector_type(4)));
typedef float f32x16 __attribute__((ext_vector_type(16)));
typedef float f32x4 __attribute__((ext_vector_type(4)));
typedef float f32x2 __attribute__((ext_vector_type(2)));
typedef unsigned u32x4 __attribute__((ext_vector_type(4)));
typedef unsigned u32x2 __attribute__((ext_vector_type(2)));
typedef __attribute__((address_space(3))) const char* lds_cptr;
typedef __attribute__((address_space(3))) char* lds_ptr;

#define DI __device__ __forceinline__
#define SBAR() __builtin_amdgcn_sched_barrier(0)
#define MFMA(a, b, c) __builtin_amdgcn_mfma_f32_16x16x32_bf16((a), (b), (c), 0, 0, 0)
#define PIN(x) asm volatile("" : "+v"(x))
#define PINR(x) asm volatile("" ::"v"(x))

constexpr int S_ = 2048, NH = 32, HD = 128, DM = 640, DC = 512, FF = 1728;
constexpr int NKS_X = DM / 32;
constexpr int NKS_C = (NH * HD) / 32;
constexpr int NKS_H = FF / 32;
constexpr int NKS_CH = NKS_C + NKS_H;
constexpr size_t WS_XF = 0;
constexpr size_t WS_KF = WS_XF + (size_t)128 * NKS_X * 1024;
constexpr size_t WS_VF = WS_KF + (size_t)NH * 128 * 4 * 1024;
constexpr size_t WS_CH = WS_VF + (size_t)NH * 64 * 8 * 1024;
constexpr size_t WS_QH = WS_CH + (size_t)128 * NKS_CH * 1024;
constexpr size_t WS_PP = WS_QH + (size_t)256 * 32768;
constexpr size_t PP_BYTES = (size_t)64 * 8 * 8 * 5 * 1024;
constexpr size_t WS_FL = WS_PP + PP_BYTES;
constexpr size_t FL_EPOCH = 0;
constexpr size_t FL_X = 256;
constexpr size_t FL_KV = FL_X + 2048;
constexpr size_t FL_H = FL_KV + 2048;
constexpr size_t FL_C = FL_H + 4096;
constexpr size_t FL_S = 16384;
constexpr size_t WS_END = WS_FL + FL_S + (size_t)256 * 128;

DI unsigned rfl(unsigned x) { return __builtin_amdgcn_readfirstlane(x); }
DI unsigned flane() { unsigned r; asm volatile("v_mbcnt_lo_u32_b32 %0, -1, 0\n\tv_mbcnt_hi_u32_b32 %0, -1, %0" : "=v"(r)); return r; }
#define STAMP(i)
#define TICK(acc)
DI const char* uptr(const char* p) { const unsigned long long v = (unsigned long long)p; const unsigned lo = rfl((unsigned)v), hi = rfl((unsigned)(v >> 32)); return (const char*)(((unsigned long long)hi << 32) | lo); }
DI unsigned cvtpk(float lo, float hi) { f32x2 v = {lo, hi}; bf16x2 b = __builtin_convertvector(v, bf16x2); return __builtin_bit_cast(unsigned, b); }
DI bf16x8 lds_frag(lds_cptr p) { return *(const __attribute__((address_space(3))) bf16x8*)p; }
DI s16x4 lds_tr(lds_cptr p) { return __builtin_bit_cast(s16x4, __builtin_amdgcn_ds_read_tr16_b64_v4i16((__attribute__((address_space(3))) v4i16_t*)p)); }
DI bf16x8 tr_frag(lds_cptr p0, lds_cptr p1) { s16x4 lo = lds_tr(p0), hi = lds_tr(p1); s16x8 v = __builtin_shufflevector(lo, hi, 0, 1, 2, 3, 4, 5, 6, 7); return __builtin_bit_cast(bf16x8, v); }
template <int OFF> DI void dma1(unsigned m0v, const void* sbase, unsigned voff) {
    asm volatile("s_mov_b32 m0, %0\n\ts_nop 0\n\tglobal_load_lds_dwordx4 %1, %2 offset:%3" ::"s"(m0v), "v"(voff), "s"(sbase), "i"(OFF) : "memory");
}
DI u32x4 pack44(const f32x4& a, const f32x4& b) { u32x4 w; w[0] = cvtpk(a[0], a[1]); w[1] = cvtpk(a[2], a[3]); w[2] = cvtpk(b[0], b[1]); w[3] = cvtpk(b[2], b[3]); return w; }
DI u32x4 pack44s(const f32x4& a, const f32x4& b, float sc) { u32x4 w; w[0] = cvtpk(a[0] * sc, a[1] * sc); w[1] = cvtpk(a[2] * sc, a[3] * sc); w[2] = cvtpk(b[0] * sc, b[1] * sc); w[3] = cvtpk(b[2] * sc, b[3] * sc); return w; }
DI u32x4 pack8(const f32x16& a, int b, float sc) {
    u32x4 w; w[0] = cvtpk(a[b + 0] * sc, a[b + 1] * sc); w[1] = cvtpk(a[b + 2] * sc, a[b + 3] * sc);
    w[2] = cvtpk(a[b + 4] * sc, a[b + 5] * sc); w[3] = cvtpk(a[b + 6] * sc, a[b + 7] * sc); return w;
}
DI u32x4 pack8n(const f32x16& a, int b) {
    u32x4 w; w[0] = cvtpk(a[b + 0], a[b + 1]); w[1] = cvtpk(a[b + 2], a[b + 3]); w[2] = cvtpk(a[b + 4], a[b + 5]); w[3] = cvtpk(a[b + 6], a[b + 7]); return w;
}
DI float bf2f(unsigned short u) { return __uint_as_float((unsigned)u << 16); }
DI float bflo(unsigned w) { return __uint_as_float(w << 16); }
DI float bfhi(unsigned w) { return __uint_as_float(w & 0xffff0000u); }
DI float xhalf_sum(float v) { auto r = __builtin_amdgcn_permlane32_swap(__float_as_uint(v), __float_as_uint(v), false, false); return __uint_as_float(r[0]) + __uint_as_float(r[1]); }
DI float xhalf_max(float v) { auto r = __builtin_amdgcn_permlane32_swap(__float_as_uint(v), __float_as_uint(v), false, false); return __builtin_fmaxf(__uint_as_float(r[0]), __uint_as_float(r[1])); }

DI void warm_load(unsigned& d, const char* wa, const char* wb, unsigned id) {
    const unsigned j = id - 1280u;
    const char* p = uptr(id < 1280u ? wa + (size_t)id * 4096 : wb + (size_t)(j < 539u ? j : 539u) * 4096);
    const unsigned voff = flane() * 64;
    asm volatile("s_nop 4\n\tglobal_load_dword %0, %1, %2" : "=&v"(d) : "v"(voff), "s"(p) : "memory");
}
DI void warm_lds(unsigned m0v, const char* wa, const char* wb, unsigned id) {
    const unsigned j = id - 1280u;
    const char* p = uptr(id < 1280u ? wa + (size_t)id * 4096 : wb + (size_t)(j < 539u ? j : 539u) * 4096);
    const unsigned voff = flane() * 64;
    asm volatile("s_mov_b32 m0, %0\n\ts_nop 4\n\tglobal_load_lds_dword %1, %2" ::"s"(m0v), "v"(voff), "s"(p) : "memory");
}

DI void mfma_acc(f32x4& acc, const bf16x8& a, const bf16x8& b) { asm volatile("v_mfma_f32_16x16x32_bf16 %0, %1, %2, %0" : "+v"(acc) : "v"(a), "v"(b)); }
DI void mfma_acc_s(f32x4& acc, const bf16x8& a, const bf16x8& b) { asm volatile("s_nop 1\n\tv_mfma_f32_16x16x32_bf16 %0, %1, %2, %0" : "+v"(acc) : "v"(a), "v"(b)); }
DI void mfma32_acc(f32x16& acc, const bf16x8& a, const bf16x8& b) { asm volatile("v_mfma_f32_32x32x16_bf16 %0, %1, %2, %0" : "+v"(acc) : "v"(a), "v"(b)); }
DI void mfma32_ini(f32x16& d, const bf16x8& a, const bf16x8& b, const f32x16& c) { asm volatile("v_mfma_f32_32x32x16_bf16 %0, %1, %2, %3" : "=&v"(d) : "v"(a), "v"(b), "v"(c)); }
DI float v_exp(float x) { float r; asm volatile("v_exp_f32 %0, %1" : "=v"(r) : "v"(x)); return r; }
DI float v_add(float a, float b) { float r; asm volatile("v_add_f32 %0, %1, %2" : "=v"(r) : "v"(a), "v"(b)); return r; }
DI unsigned v_cvt(float lo, float hi) { unsigned r; asm volatile("v_cvt_pk_bf16_f32 %0, %1, %2" : "=v"(r) : "v"(lo), "v"(hi)); return r; }
DI void set_m0(unsigned v) { asm volatile("s_mov_b32 m0, %0" ::"s"(v) : "memory"); }
template <int OFF> DI void dma_nom0(const void* sbase, unsigned voff) { asm volatile("global_load_lds_dwordx4 %0, %1 offset:%2" ::"v"(voff), "s"(sbase), "i"(OFF) : "memory"); }
DI void dma4(unsigned m0v, const void* sbase, unsigned voff) {
    asm volatile("s_mov_b32 m0, %0\n\ts_nop 0\n\tglobal_load_lds_dwordx4 %1, %2\n\tglobal_load_lds_dwordx4 %1, %2 offset:1024\n\tglobal_load_lds_dwordx4 %1, %2 offset:2048\n\tglobal_load_lds_dwordx4 %1, %2 offset:3072"
                 ::"s"(m0v), "v"(voff), "s"(sbase) : "memory");
}

typedef unsigned long long u64;
DI void st_wt(char* p, const u32x4& v) { asm volatile("global_store_dwordx4 %0, %1, off sc1\n\ts_nop 1" ::"v"(p), "v"(v) : "memory"); }
DI void st_wt_o(const char* sbase, unsigned voff, const u32x4& v) { asm volatile("global_store_dwordx4 %0, %1, %2 sc1\n\ts_nop 1" ::"v"(voff), "v"(v), "s"(sbase) : "memory"); }
template <int OFF> DI void st_wt_i(const char* sbase, unsigned voff, const u32x4& v) {
    asm volatile("global_store_dwordx4 %0, %1, %2 offset:%3 sc1\n\ts_nop 1" ::"v"(voff), "v"(v), "s"(sbase), "i"(OFF) : "memory");
}
DI void ld_wt(u32x4& d, const char* sbase, unsigned voff) { asm volatile("global_load_dwordx4 %0, %1, %2 sc1" : "=&v"(d) : "v"(voff), "s"(sbase) : "memory"); }
DI void flag_set(unsigned wid, const char* sbase, unsigned off, u64 tag) {
    const u32x2 v = u32x2{(unsigned)tag, (unsigned)(tag >> 32)};
    u64 ex;
    asm volatile("s_cmp_lg_u32 %1, 0\n\ts_cbranch_scc1 .Lfs%=\n\ts_mov_b64 %0, exec\n\ts_mov_b64 exec, 1\n\t"
                 "global_store_dwordx2 %2, %3, %4 sc1\n\ts_mov_b64 exec, %0\n.Lfs%=:\n\ts_nop 1"
                 : "=&s"(ex) : "s"(wid), "v"(off), "v"(v), "s"(sbase) : "scc", "memory");
}
DI void flag_ld(u32x2& f, const char* sbase, unsigned voff) { asm volatile("global_load_dwordx2 %0, %1, %2 sc1" : "=&v"(f) : "v"(voff), "s"(sbase) : "memory"); }
template <bool ACQ>
DI void flag_wait(unsigned skip, u32x2& f, const char* sbase, unsigned voff, u64 tag) {
    unsigned cnt;
    asm volatile("s_cmp_lg_u32 %2, 0\n\ts_cbranch_scc1 .Lfe%=\n\t"
                 "v_cmp_ne_u64 vcc, %3, %0\n\ts_cbranch_vccz .Lfd%=\n\ts_mov_b32 %1, 0x40000\n"
                 ".Lfl%=:\n\tglobal_load_dwordx2 %0, %4, %5 sc1\n\ts_waitcnt vmcnt(0)\n\tv_cmp_ne_u64 vcc, %3, %0\n\ts_cbranch_vccz .Lfd%=\n\t"
                 "s_sleep 2\n\ts_sub_u32 %1, %1, 1\n\ts_cbranch_scc0 .Lfl%=\n"
                 ".Lfd%=:\n\t.if %6\n\tbuffer_inv sc1\n\t.endif\n"
                 ".Lfe%=:"
                 : "+v"(f), "=&s"(cnt) : "s"(skip), "s"(tag), "v"(voff), "s"(sbase), "i"(ACQ ? 1 : 0) : "vcc", "scc", "memory");
}

constexpr int WQ_PIECE = 1040, W1_PIECE = 1056, W1_OFF = 16 * WQ_PIECE, PSLOT = W1_OFF + 8 * W1_PIECE;
struct PJ { unsigned wqvo, w1vo, xv0, xv1; const char *wqs, *w1s, *xs; };
DI PJ pj_setup(const char* wq, const char* w1, const char* ws, unsigned wid, int head, int f0, bool nft4, int q16_0) {
    const unsigned lane = flane();
    PJ j;
    j.wqvo = ((lane >> 2) & 3) * 8192 + (lane >> 4) * 64 + (lane & 3) * 16;
    j.w1vo = ((lane >> 2) & 7) * 3456 + (nft4 ? (lane >> 5) * 64 : 0) + (lane & 3) * 16;
    j.wqs = uptr(wq + (size_t)wid * 8 * 8192 + head * 256);
    j.w1s = uptr(w1 + (size_t)wid * 8 * 3456 + f0 * 2);
    j.xs = uptr(ws + WS_XF + (size_t)q16_0 * NKS_X * 1024);
    j.xv0 = lane * 16; j.xv1 = lane * 16 + NKS_X * 1024;
    return j;
}
DI void pj_dma(const PJ& j, unsigned lds0, unsigned wid, int c, int slot) {
    dma1<0>(lds0 + slot + (2 * wid) * WQ_PIECE, j.wqs + (size_t)c * 64 * 8192, j.wqvo);
    dma1<0>(lds0 + slot + (2 * wid + 1) * WQ_PIECE, j.wqs + (size_t)c * 64 * 8192 + 4 * 8192, j.wqvo);
    dma1<0>(lds0 + slot + W1_OFF + wid * W1_PIECE, j.w1s + (size_t)c * 64 * 3456, j.w1vo);
}
DI void pj_xload(const PJ& j, u32x4 (&X)[4], int c) {
    const char* p_ = j.xs + c * 2048;
    asm volatile("global_load_dwordx4 %0, %4, %6 sc1\n\tglobal_load_dwordx4 %1, %5, %6 sc1\n\tglobal_load_dwordx4 %2, %4, %6 offset:1024 sc1\n\tglobal_load_dwordx4 %3, %5, %6 offset:1024 sc1"
                 : "=&v"(X[0]), "=&v"(X[1]), "=&v"(X[2]), "=&v"(X[3]) : "v"(j.xv0), "v"(j.xv1), "s"(p_) : "memory");
}

template <bool VT, class F>
DI void k1_mma(f32x4 (&acc)[8][4], int half, const bf16x8 (&c)[4], const bf16x8 (&w)[4], F&& filler) {
#pragma unroll
    for (int rt = 0; rt < 4; ++rt)
#pragma unroll
        for (int nt = 0; nt < 4; ++nt) {
            if (half == 0) acc[rt][nt] = VT ? MFMA(c[rt], w[nt], acc[rt][nt]) : MFMA(w[nt], c[rt], acc[rt][nt]);
            else acc[4 + rt][nt] = VT ? MFMA(c[rt], w[nt], acc[4 + rt][nt]) : MFMA(w[nt], c[rt], acc[4 + rt][nt]);
            filler(rt * 4 + nt);
        }
}

template <bool VT>
DI void k1_tile(const char* __restrict__ Abase, const char* __restrict__ Wbase, char* __restrict__ outp, char* lds, const unsigned (&avo)[4], const unsigned (&wvo)[4],
                unsigned wid, int hp, int rb, char* __restrict__ ws, const char* __restrict__ wq, const char* __restrict__ w1, const char* __restrict__ b1, u64 tag,
                int head2, int f0, bool nft4, int q16_0, u32x4 (&xa)[4], u32x4 (&xb)[4], u32x2 (&bb)[4]) {
    char* stamp_base = ws; (void)stamp_base;
    const char* flb = uptr(ws + WS_FL);
    const unsigned wm = wid >> 2, wn = wid & 3;
    const unsigned lds0 = (unsigned)(uintptr_t)lds;
    unsigned aaddr[2], baddr[4];
    {
        const unsigned lane = flane(), c = lane & 15, g = lane >> 4;
        const unsigned x = (c >> 1) & 7;
#pragma unroll
        for (int s = 0; s < 2; ++s) aaddr[s] = (128 * wm + c) * 128 + (((4 * s + g) ^ x) << 4);
        const unsigned q = (lane & 15) >> 2, p = lane & 3, f = q | ((g & 1) << 2);
#pragma unroll
        for (int nt = 0; nt < 4; ++nt) baddr[nt] = 32768 + (8 * g + q) * 512 + (((8 * wn + 2 * nt + (p >> 1)) ^ (f << 1)) << 4) + ((p & 1) << 3);
    }
    f32x4 acc[8][4];
#pragma unroll
    for (int i = 0; i < 8; ++i)
#pragma unroll
        for (int j = 0; j < 4; ++j) acc[i][j] = f32x4{};
    bf16x8 cf[2][4], wf[2][4];
    lds_cptr L = (lds_cptr)lds;
#define K1_RC(buf, stg, s, half) do { _Pragma("unroll") for (int rt = 0; rt < 4; ++rt) cf[buf][rt] = lds_frag(L + (stg) + aaddr[s] + ((half) * 4 + rt) * 2048); } while (0)
#define K1_RW(buf, stg, s) do { _Pragma("unroll") for (int nt = 0; nt < 4; ++nt) wf[buf][nt] = tr_frag(L + (stg) + baddr[nt] + (s) * 16384, L + (stg) + baddr[nt] + (s) * 16384 + 2048); } while (0)
    const char* Aw = Abase + (size_t)wid * 32 * 1024;
    const char* Ww = Wbase + (size_t)wid * 8 * 8192;
    const unsigned m0a = lds0 + wid * 4096, m0w = lds0 + 32768 + wid * 4096;
#define K1_DMA(kt, stg)                                                                   \
    do {                                                                                  \
        const char* a_ = Aw + (kt) * 128; const char* w_ = Ww + (size_t)(kt) * 64 * 8192; \
        dma1<0>(m0a + (stg), a_, avo[0]); dma1<1024>(m0a + (stg), a_, avo[1]); dma1<2048>(m0a + (stg), a_, avo[2]); dma1<3072>(m0a + (stg), a_, avo[3]); \
        dma1<0>(m0w + (stg), w_, wvo[0]); dma1<1024>(m0w + (stg), w_, wvo[1]); dma1<2048>(m0w + (stg), w_, wvo[2]); dma1<3072>(m0w + (stg), w_, wvo[3]); \
    } while (0)
    unsigned pfd = 0;
    u32x2 fx = u32x2{};
#define K1_FXO() (FL_X + (2 * q16_0 + (flane() & 3)) * 8)
    STAMP(1);
    asm volatile("s_waitcnt vmcnt(10)\n\ts_barrier" ::: "memory");
    STAMP(2);
    K1_RW(0, 0, 0); K1_RC(0, 0, 0, 0);
#define K1_RC1(buf, stg, s, half, rt) cf[buf][rt] = lds_frag(L + (stg) + aaddr[s] + ((half) * 4 + (rt)) * 2048)
#define K1_RW1(buf, stg, s, nt) wf[buf][nt] = tr_frag(L + (stg) + baddr[nt] + (s) * 16384, L + (stg) + baddr[nt] + (s) * 16384 + 2048)
#pragma unroll
    for (int kt = 0; kt < 8; ++kt) {
        const unsigned stg = (kt & 1) * 65536, nstg = stg ^ 65536;
        SBAR();
        k1_mma<VT>(acc, 0, cf[0], wf[0], [&](int m) { if (m < 8 && (m & 1) == 0) { K1_RC1(1, stg, 0, 1, m >> 1); SBAR(); } });
        SBAR();
        k1_mma<VT>(acc, 1, cf[1], wf[0], [&](int m) { if (m < 4) { K1_RC1(0, stg, 1, 0, m); SBAR(); } else if (m < 12 && (m & 1) == 0) { K1_RW1(1, stg, 1, (m - 4) >> 1); SBAR(); } });
        SBAR();
        k1_mma<VT>(acc, 0, cf[0], wf[1], [&](int m) { if (m < 8 && (m & 1) == 0) { K1_RC1(1, stg, 1, 1, m >> 1); SBAR(); } });
        SBAR();
        if (kt == 7) asm volatile("s_waitcnt lgkmcnt(0)\n\ts_barrier" ::: "memory");
        else asm volatile("s_waitcnt vmcnt(0) lgkmcnt(0)\n\ts_barrier" : "+v"(fx)::"memory");
        if (kt == 0) flag_set(wid, flb, FL_X + blockIdx.x * 8, tag);
        if (kt == 6) flag_wait<false>(0, fx, flb, K1_FXO(), tag);
        const char* a_ = Aw + (kt + 2) * 128; const char* w_ = Ww + (size_t)(kt + 2) * 64 * 8192;
        k1_mma<VT>(acc, 1, cf[1], wf[1], [&](int m) {
            if (kt + 1 < 8) { if (m < 4) { K1_RW1(0, nstg, 0, m); SBAR(); } else if (m < 8) { K1_RC1(0, nstg, 0, 0, m - 4); SBAR(); } }
            if (kt + 2 < 8) {
                if (m == 1) { dma1<0>(m0a + stg, a_, avo[0]); SBAR(); } if (m == 3) { dma1<1024>(m0a + stg, a_, avo[1]); SBAR(); }
                if (m == 5) { dma1<2048>(m0a + stg, a_, avo[2]); SBAR(); } if (m == 7) { dma1<3072>(m0a + stg, a_, avo[3]); SBAR(); }
                if (m == 9) { dma1<0>(m0w + stg, w_, wvo[0]); SBAR(); } if (m == 11) { dma1<1024>(m0w + stg, w_, wvo[1]); SBAR(); }
                if (m == 13) { dma1<2048>(m0w + stg, w_, wvo[2]); SBAR(); } if (m == 15) { dma1<3072>(m0w + stg, w_, wvo[3]); SBAR(); }
            }
            if (kt == 5 && m == 15) {
                warm_load(pfd, wq, w1, blockIdx.x * 8 + wid);
                SBAR();
            }
            if (kt == 4 && m == 14) { flag_ld(fx, flb, K1_FXO()); SBAR(); }
            if (kt == 6 && m == 2) {
                const PJ j = pj_setup(wq, w1, ws, wid, head2, f0, nft4, q16_0); pj_dma(j, lds0, wid, 0, 0); pj_dma(j, lds0, wid, 1, PSLOT); SBAR();
            }
            if (kt == 7) {
                if (m == 0) {
                    const char* bp = uptr(b1 + f0 * 2);
                    const unsigned bo = (flane() >> 4) * 8, b2 = bo + (nft4 ? 64 : 0);
                    asm volatile("s_nop 4\n\tglobal_load_dwordx2 %0, %4, %6\n\tglobal_load_dwordx2 %1, %4, %6 offset:32\n\tglobal_load_dwordx2 %2, %5, %6\n\tglobal_load_dwordx2 %3, %5, %6 offset:32"
                                 : "=&v"(bb[0]), "=&v"(bb[1]), "=&v"(bb[2]), "=&v"(bb[3]) : "v"(bo), "v"(b2), "s"(bp) : "memory");
                    SBAR();
                }
                if (m == 4) { const PJ j = pj_setup(wq, w1, ws, wid, head2, f0, nft4, q16_0); pj_xload(j, xa, 0); pj_xload(j, xb, 1); SBAR(); }
            }
        });
        SBAR();
    }
    STAMP(3);
    const int head = 2 * hp + (wn >> 1), d0 = (wn & 1) * 64;
    const int key0 = 256 * rb + 128 * wm;
    const unsigned lane = flane(), c = lane & 15, g = lane >> 4;
    if (VT) {
        const unsigned lo = (g >> 1) * 1024 + (c + 32 * (g & 1)) * 16;
#pragma unroll
        for (int u = 0; u < 4; ++u)
#pragma unroll
            for (int nt = 0; nt < 4; ++nt) {
                const size_t off = ((size_t)(head * 64 + (key0 >> 5) + u) * 4 + (d0 >> 5) + (nt >> 1)) * 2048 + (nt & 1) * 256;
                st_wt_o(uptr(outp + off), lo, pack44(acc[2 * u][nt], acc[2 * u + 1][nt]));
            }
    } else {
        const unsigned lo = (g >> 1) * 1024 + ((c & 7) + 16 * (c >> 3) + 32 * (g & 1)) * 16;
#pragma unroll
        for (int kt = 0; kt < 8; ++kt)
#pragma unroll
            for (int v = 0; v < 2; ++v) {
                const size_t off = ((size_t)(head * 64 + (key0 >> 5) + (kt >> 1)) * 4 + (d0 >> 5) + v) * 2048 + (kt & 1) * 128;
                st_wt_o(uptr(outp + off), lo, pack44(acc[kt][2 * v], acc[kt][2 * v + 1]));
            }
    }
    STAMP(4);
    asm volatile("s_waitcnt vmcnt(16)\n\ts_barrier" : "+v"(pfd), "+v"(xa[0]), "+v"(xa[1]), "+v"(xa[2]), "+v"(xa[3]), "+v"(xb[0]), "+v"(xb[1]), "+v"(xb[2]), "+v"(xb[3]),
                 "+v"(bb[0]), "+v"(bb[1]), "+v"(bb[2]), "+v"(bb[3])::"memory");
    STAMP(5);
#undef K1_RC
#undef K1_RW
#undef K1_DMA
#undef K1_FXO
}

constexpr float QSCALE = 0.08838834764831845f * 1.4426950408889634f;
constexpr float THR = 8.0f;
DI float x4_max(float x) {
    auto s = __builtin_amdgcn_permlane16_swap(__float_as_uint(x), __float_as_uint(x), false, false);
    x = __builtin_fmaxf(__uint_as_float(s[0]), __uint_as_float(s[1]));
    auto t = __builtin_amdgcn_permlane32_swap(__float_as_uint(x), __float_as_uint(x), false, false);
    return __builtin_fmaxf(__uint_as_float(t[0]), __uint_as_float(t[1]));
}
DI float x4_sum(float x) {
    auto s = __builtin_amdgcn_permlane16_swap(__float_as_uint(x), __float_as_uint(x), false, false);
    x = __uint_as_float(s[0]) + __uint_as_float(s[1]);
    auto t = __builtin_amdgcn_permlane32_swap(__float_as_uint(x), __float_as_uint(x), false, false);
    return __uint_as_float(t[0]) + __uint_as_float(t[1]);
}
DI float mish_f(float h) {
    const float e = __builtin_amdgcn_exp2f(__builtin_fminf(h, 30.0f) * 1.4426950408889634f);
    const float n = e * (e + 2.0f);
    return h * n * __builtin_amdgcn_rcpf(n + 2.0f);
}

template <int NFT>
DI void proj_phase(const char* __restrict__ wq, const char* __restrict__ w1, char* __restrict__ ws, char* lds, int head, int f0, int q16_0, bf16x8 (&qf)[2][4],
                   const char* __restrict__ wo, const char* __restrict__ w2, unsigned m0v0, unsigned wid, u32x4 (&xa)[4], u32x4 (&xb)[4], const u32x2 (&bb)[4], u64 tag,
                   unsigned kvflag, int hp2, unsigned m0k0, unsigned m0k1, const char* kptr0, const char* kptr1) {
    const unsigned lane = flane(), g = lane >> 4;
    const unsigned lds0 = (unsigned)(uintptr_t)lds;
    lds_cptr L = (lds_cptr)lds;
    const PJ pj = pj_setup(wq, w1, ws, wid, head, f0, NFT == 4, q16_0);
    const char* flb = uptr(ws + WS_FL);
    u32x2 fkv = u32x2{};
    const unsigned fkvo = FL_KV + (hp2 * 16 + (lane & 15)) * 8;
    const unsigned q = (lane & 15) >> 2, p = lane & 3;
    const unsigned wqb = 2 * g * WQ_PIECE + q * 64 + p * 8;
    const unsigned w1b = W1_OFF + g * W1_PIECE + q * 64 + p * 8;
    f32x4 aq[8][2], ah[NFT][2];
#pragma unroll
    for (int i = 0; i < 8; ++i) { aq[i][0] = f32x4{}; aq[i][1] = f32x4{}; }
#pragma unroll
    for (int i = 0; i < NFT; ++i) { ah[i][0] = f32x4{}; ah[i][1] = f32x4{}; }
    constexpr int NT = 8 + NFT;
    bf16x8 wf[4];
#define PJ_RD(slot, s, n)                                                                                       \
    (((n) < 8) ? tr_frag(L + (slot) + wqb + (8 * (s)) * WQ_PIECE + ((n) >> 1) * 256 + ((n) & 1) * 32, L + (slot) + wqb + (8 * (s) + 1) * WQ_PIECE + ((n) >> 1) * 256 + ((n) & 1) * 32) \
               : tr_frag(L + (slot) + w1b + (4 * (s)) * W1_PIECE + (((n) - 8) >> 1) * 512 + ((n) & 1) * 32, L + (slot) + w1b + (4 * (s)) * W1_PIECE + 256 + (((n) - 8) >> 1) * 512 + ((n) & 1) * 32))
#pragma unroll
    for (int n = 0; n < 3; ++n) wf[n] = PJ_RD(0, 0, n);
#define PJ_CHUNK(c, XC, XN)                                                                                     \
    do {                                                                                                        \
        constexpr int slot_ = ((c) % 3) * PSLOT, nslot_ = (((c) + 1) % 3) * PSLOT, dslot_ = (((c) + 2) % 3) * PSLOT; \
        if ((c) == 4) flag_ld(fkv, flb, fkvo);                                                                  \
          \
        if ((c) == 5) flag_wait<true>(wid, fkv, flb, fkvo, tag);                                                \
        if ((c) >= 1 && (c) + 1 < 10) pj_xload(pj, XN, (c) + 1);                                                \
        if ((c) + 2 < 10) pj_dma(pj, lds0, wid, (c) + 2, dslot_);                                               \
        if ((c) == 9) {       \
            dma4(m0k0, kptr0, lane * 16); dma4(m0k1, kptr1, lane * 16);                                         \
            warm_lds(m0v0, wo, w2, blockIdx.x * 8 + wid);                                                       \
        }                                                                                                       \
        SBAR();                                                                                                 \
        _Pragma("unroll") for (int m = 0; m < 2 * NT; ++m) {                                                    \
            const int s = m / NT, n = m % NT;                                                                   \
            if (m == 2 * NT - 3) {       \
                if ((c) == 0) asm volatile("s_waitcnt lgkmcnt(0)\n\ts_barrier" ::: "memory");                   \
                else if ((c) + 2 < 10) asm volatile("s_waitcnt vmcnt(3) lgkmcnt(0)\n\ts_barrier" : "+v"(XN[0]), "+v"(XN[1]), "+v"(XN[2]), "+v"(XN[3]), "+v"(fkv)::"memory"); \
                else if ((c) + 1 < 10) asm volatile("s_waitcnt vmcnt(0) lgkmcnt(0)\n\ts_barrier" : "+v"(XN[0]), "+v"(XN[1]), "+v"(XN[2]), "+v"(XN[3])::"memory"); \
                if ((c) == 1) flag_set(wid, flb, kvflag, tag);        \
            }                                                                                                   \
            const int m3 = m + 3;                                                                               \
            if (m3 < 2 * NT) wf[m3 & 3] = PJ_RD(slot_, m3 / NT, m3 % NT);                                       \
            else if ((c) + 1 < 10) wf[m3 & 3] = PJ_RD(nslot_, 0, m3 - 2 * NT);                                  \
            const bf16x8 x0 = __builtin_bit_cast(bf16x8, XC[2 * s]), x1 = __builtin_bit_cast(bf16x8, XC[2 * s + 1]); \
            if (n < 8) { aq[n & 7][0] = MFMA(wf[m & 3], x0, aq[n & 7][0]); aq[n & 7][1] = MFMA(wf[m & 3], x1, aq[n & 7][1]); } \
            else { ah[(n - 8) % NFT][0] = MFMA(wf[m & 3], x0, ah[(n - 8) % NFT][0]); ah[(n - 8) % NFT][1] = MFMA(wf[m & 3], x1, ah[(n - 8) % NFT][1]); } \
            SBAR();                                                                                             \
        }                                                                                                       \
    } while (0)
    static_assert((2 * NT) % 4 == 0, "fragment window");
    PJ_CHUNK(0, xa, xb); PJ_CHUNK(1, xb, xa); PJ_CHUNK(2, xa, xb); PJ_CHUNK(3, xb, xa); PJ_CHUNK(4, xa, xb);
    PJ_CHUNK(5, xb, xa); PJ_CHUNK(6, xa, xb); PJ_CHUNK(7, xb, xa); PJ_CHUNK(8, xa, xb); PJ_CHUNK(9, xb, xa);
#undef PJ_CHUNK
#undef PJ_RD
    asm volatile("s_waitcnt vmcnt(1)" ::: "memory");
#pragma unroll
    for (int a = 0; a < 2; ++a)
#pragma unroll
        for (int v = 0; v < 4; ++v) qf[a][v] = __builtin_bit_cast(bf16x8, pack44s(aq[2 * v][a], aq[2 * v + 1][a], QSCALE));
#pragma unroll
    for (int v = 0; v < NFT / 2; ++v) {
        float bias[8];
#pragma unroll
        for (int t = 0; t < 2; ++t) {
            const u32x2 q2 = bb[2 * v + t];
            bias[4 * t + 0] = bflo(q2[0]); bias[4 * t + 1] = bfhi(q2[0]); bias[4 * t + 2] = bflo(q2[1]); bias[4 * t + 3] = bfhi(q2[1]);
        }
#pragma unroll
        for (int a = 0; a < 2; ++a) {
            f32x4 h0, h1;
#pragma unroll
            for (int i = 0; i < 4; ++i) { h0[i] = mish_f(ah[2 * v][a][i] + bias[i]); h1[i] = mish_f(ah[2 * v + 1][a][i] + bias[4 + i]); }
            st_wt_o(uptr(ws + WS_CH + ((size_t)(q16_0 + a) * NKS_CH + NKS_C + (f0 >> 5) + v) * 1024), lane * 16, pack44(h0, h1));
        }
    }
}

#define NOP_SO(S, o) asm volatile("s_nop 7\n\ts_nop 3" : "+v"(S[0]), "+v"(S[1]), "+v"(o[0]), "+v"(o[1]), "+v"(o[2]), "+v"(o[3]))
#define NOP_A(A) asm volatile("s_nop 7\n\ts_nop 1" : "+v"(A[0][0]), "+v"(A[0][1]), "+v"(A[0][2]), "+v"(A[0][3]), "+v"(A[1][0]), "+v"(A[1][1]), "+v"(A[1][2]), "+v"(A[1][3]), \
    "+v"(A[2][0]), "+v"(A[2][1]), "+v"(A[2][2]), "+v"(A[2][3]), "+v"(A[3][0]), "+v"(A[3][1]), "+v"(A[3][2]), "+v"(A[3][3]), "+v"(A[4][0]), "+v"(A[4][1]), "+v"(A[4][2]), "+v"(A[4][3]))
DI float xh_sum(float v) { auto r = __builtin_amdgcn_permlane32_swap(__float_as_uint(v), __float_as_uint(v), false, false); return __uint_as_float(r[0]) + __uint_as_float(r[1]); }
DI float xh_max(float v) { auto r = __builtin_amdgcn_permlane32_swap(__float_as_uint(v), __float_as_uint(v), false, false); return __builtin_fmaxf(__uint_as_float(r[0]), __uint_as_float(r[1])); }
DI float mul_s(float a, float b) { float r; asm("v_mul_f32 %0, %1, %2" : "=v"(r) : "v"(a), "v"(b)); return r; }
DI float sub_s(float a, float b) { float r; asm("v_sub_f32 %0, %1, %2" : "=v"(r) : "v"(a), "v"(b)); return r; }
DI u32x4 pack_o(const f32x16& a, int y, float sc) {
    u32x4 w; w[0] = cvtpk(mul_s(a[4 * y + 0], sc), mul_s(a[4 * y + 1], sc)); w[1] = cvtpk(mul_s(a[4 * y + 2], sc), mul_s(a[4 * y + 3], sc));
    w[2] = cvtpk(mul_s(a[8 + 4 * y + 0], sc), mul_s(a[8 + 4 * y + 1], sc)); w[3] = cvtpk(mul_s(a[8 + 4 * y + 2], sc), mul_s(a[8 + 4 * y + 3], sc)); return w;
}
DI void mask_diag(f32x16& s, int dq) {
    const float ninf = -__builtin_inff();
#pragma unroll
    for (int r0 = 0; r0 < 16; r0 += 4) {
        unsigned long long m0, m1, m2, m3;
        const int kc = 16 * ((r0 >> 2) & 1) + 8 * (r0 >> 3);
        float a = s[r0], b = s[r0 + 1], c = s[r0 + 2], d = s[r0 + 3];
        asm("v_cmp_gt_i32_e64 %4, %9, %8\n\tv_cmp_gt_i32_e64 %5, %10, %8\n\tv_cmp_gt_i32_e64 %6, %11, %8\n\tv_cmp_gt_i32_e64 %7, %12, %8\n\t"
            "v_cndmask_b32_e64 %0, %0, %13, %4\n\tv_cndmask_b32_e64 %1, %1, %13, %5\n\tv_cndmask_b32_e64 %2, %2, %13, %6\n\tv_cndmask_b32_e64 %3, %3, %13, %7"
            : "+v"(a), "+v"(b), "+v"(c), "+v"(d), "=&s"(m0), "=&s"(m1), "=&s"(m2), "=&s"(m3)
            : "v"(dq), "i"(kc), "i"(kc + 1), "i"(kc + 2), "i"(kc + 3), "v"(ninf));
        s[r0] = a; s[r0 + 1] = b; s[r0 + 2] = c; s[r0 + 3] = d;
    }
}
#define FRW 8
#define FRA 6

constexpr int K3_SLOT = 36864;
constexpr int K3_RED = 2 * K3_SLOT;
constexpr int PP_SET = 8 * 5 * 1024;
DI float silu_f(float v) { return v * __builtin_amdgcn_rcpf(1.0f + __builtin_amdgcn_exp2f(-1.4426950408889634f * v)); }
DI f32x4 unpk_lo(const u32x4& w) { return f32x4{bflo(w[0]), bfhi(w[0]), bflo(w[1]), bfhi(w[1])}; }
DI f32x4 unpk_hi(const u32x4& w) { return f32x4{bflo(w[2]), bfhi(w[2]), bflo(w[3]), bfhi(w[3])}; }
template <int OFF> DI void dma_sc1(const void* sbase, unsigned voff) { asm volatile("global_load_lds_dwordx4 %0, %1 offset:%2 sc1" ::"v"(voff), "s"(sbase), "i"(OFF) : "memory"); }

struct K3S { const char *aptr, *wptr; unsigned avo, wvo[3], m0a, m0w; };
DI K3S k3_setup(unsigned lds0, unsigned wid, unsigned lane) {
    K3S k;
    k.aptr = nullptr; k.wptr = nullptr;
    k.avo = lane * 16;
    const unsigned p0 = wid >= 4 ? 12 + 2 * (wid & 3) : 3 * wid;
#pragma unroll
    for (int i = 0; i < 3; ++i) {
        const unsigned o = (p0 + i) * 1024 + lane * 16, row = o / 320, c16 = (o % 320) >> 4;
        k.wvo[i] = row * 1280 + ((c16 ^ (((row >> 2) & 1) << 1)) << 4) - 1024 * i;
    }
    k.m0a = lds0 + wid * 2048; k.m0w = lds0 + 16384 + p0 * 1024;
    return k;
}
DI void k3_point(K3S& k, const char* ws, const char* w, unsigned wid, int qt, int mt, int kstep, int stage) {
    k.aptr = uptr(ws + WS_CH + ((size_t)(8 * qt + wid) * NKS_CH + kstep) * 1024);
    k.wptr = uptr(w + (size_t)stage * 64 * 1280 + mt * 320);
    asm volatile("s_nop 4" : "+s"(k.aptr), "+s"(k.wptr));
}
DI void k3_stage(K3S& k, unsigned slot, bool three) {
    set_m0(k.m0a + slot); asm volatile("s_nop 0"); dma_sc1<0>(k.aptr, k.avo); dma_sc1<1024>(k.aptr, k.avo);
    set_m0(k.m0w + slot); asm volatile("s_nop 0"); dma_nom0<0>(k.wptr, k.wvo[0]); dma_nom0<1024>(k.wptr, k.wvo[1]);
    if (three) dma_nom0<2048>(k.wptr, k.wvo[2]);
    k.aptr += 2048; k.wptr += 81920;
}
DI void k3_publish(const f32x4 (&res)[10], f32x4 (&own)[2][2], const char* pset, unsigned vo, unsigned r) {
    own[1][0] = f32x4{}; own[1][1] = f32x4{};
#pragma unroll
    for (int j = 0; j < 5; ++j) {
        if ((j & 3) == r) { own[j >> 2][0] = res[2 * j]; own[j >> 2][1] = res[2 * j + 1]; }
        else st_wt_o(pset + j * 1024, vo, pack44(res[2 * j], res[2 * j + 1]));
    }
}

template <int GRP>
DI void k3_body(K3S& k, const char* __restrict__ x, const char* __restrict__ wo, char* __restrict__ ws, char* __restrict__ out, char* lds, unsigned wid, u64 tag,
                int ks, int qt, int mt, unsigned cflag) {
    constexpr int P = GRP ? 4 : 5;
    const unsigned lane = flane(), c = lane & 15, g = lane >> 4;
    const unsigned w4 = wid & 3, qh = w4 >> 1, mh = w4 & 1;
    char* stamp_base = ws; (void)stamp_base;
    const char* flb = uptr(ws + WS_FL);
    lds_cptr L = (lds_cptr)lds;
    const unsigned ard = (8 * qh + GRP) * 1024 + lane * 16;
    const unsigned q = (lane & 15) >> 2, p = lane & 3;
    const unsigned wrow = 16384 + (32 * GRP + 4 * g + q) * 320 + 160 * mh + 8 * p;
    const unsigned sgn = ((g & 1) ? 32u : 0u);
    const unsigned wrdE = (mh & 1) ? wrow - sgn : wrow + sgn;
    const unsigned wrdO = (mh & 1) ? wrow + sgn : wrow - sgn;
    f32x4 acc[5][4];
    bf16x8 af[2][4], wf[4], w4f[2];
    unsigned cur;
#define K3_RW(slot, n) tr_frag(L + (slot) + (((n) & 1) ? wrdO : wrdE) + 32 * (n), L + (slot) + (((n) & 1) ? wrdO : wrdE) + 32 * (n) + 16 * 320)
#define K3_RA(slot, a) lds_frag(L + (slot) + ard + (a) * 2048)
#define K3_ZERO() do { _Pragma("unroll") for (int n = 0; n < 5; ++n) _Pragma("unroll") for (int a = 0; a < 4; ++a) acc[n][a] = f32x4{}; NOP_A(acc); cur = 0; } while (0)
#define K3_ITER(PAR, WN, RD, DM, SAFE, HOOK)                                                                            \
    do {                                                                                                                \
        asm volatile("s_waitcnt vmcnt(%0) lgkmcnt(0)\n\ts_barrier" ::"i"(WN) : "memory");                               \
        HOOK;                                                                                                           \
        const unsigned nxt_ = cur == 3 * K3_SLOT ? 0u : cur + K3_SLOT;                                                  \
        _Pragma("unroll") for (int n = 0; n < 5; ++n) {                                                                 \
            _Pragma("unroll") for (int a = 0; a < 4; ++a) {                                                             \
                if (SAFE) mfma_acc_s(acc[n][a], n < 4 ? wf[n & 3] : w4f[PAR], af[PAR][a]);                              \
                else mfma_acc(acc[n][a], n < 4 ? wf[n & 3] : w4f[PAR], af[PAR][a]);                                     \
                SBAR();                                                                                                 \
                if (RD) {                                                                                               \
                    if (n == 0 && a == 0) w4f[1 - (PAR)] = K3_RW(nxt_, 4);                                              \
                    if (n < 4 && a == 1) af[1 - (PAR)][n & 3] = K3_RA(nxt_, n & 3);                                     \
                    if (n >= 1 && a == 0) wf[(n - 1) & 3] = K3_RW(nxt_, n - 1);                                         \
                }                                                                                                       \
                if (DM) {                                                                                               \
                    if (n == 0 && a == 2) set_m0(k.m0a + cur);                                                          \
                    if (n == 0 && a == 3) dma_sc1<0>(k.aptr, k.avo);                                                    \
                    if (n == 1 && a == 2) dma_sc1<1024>(k.aptr, k.avo);                                                 \
                    if (n == 1 && a == 3) set_m0(k.m0w + cur);                                                          \
                    if (n == 2 && a == 2) dma_nom0<0>(k.wptr, k.wvo[0]);                                                \
                    if (n == 3 && a == 2) dma_nom0<1024>(k.wptr, k.wvo[1]);                                             \
                    if (n == 4 && a == 1 && !GRP) dma_nom0<2048>(k.wptr, k.wvo[2]);                                     \
                }                                                                                                       \
                SBAR();                                                                                                 \
            }                                                                                                           \
        }                                                                                                               \
        if (DM) { k.aptr += 2048; k.wptr += 81920; }                                                                    \
        cur = nxt_;                                                                                                     \
    } while (0)
#define K3_FIRST(PAR) do { _Pragma("unroll") for (int a = 0; a < 4; ++a) af[PAR][a] = K3_RA(0, a);                      \
        _Pragma("unroll") for (int n = 0; n < 4; ++n) wf[n] = K3_RW(0, n);                                              \
        w4f[PAR] = K3_RW(0, 4); } while (0)
#define K3_REDUCE(res)                                                                                                  \
    do {                                                                                                                \
        NOP_A(acc);                                                                                                     \
        lds_ptr X_ = (lds_ptr)lds + K3_RED + wid * 10240 + lane * 16;                                                   \
        lds_cptr Y_ = (lds_cptr)lds + K3_RED + (wid ^ 4) * 10240 + lane * 16;                                           \
        _Pragma("unroll") for (int a = 0; a < 2; ++a) _Pragma("unroll") for (int n = 0; n < 5; ++n)                     \
            *(__attribute__((address_space(3))) f32x4*)(X_ + (a * 5 + n) * 1024) = acc[n][GRP ? a : 2 + a];             \
        asm volatile("s_waitcnt lgkmcnt(0)\n\ts_barrier" ::: "memory");                                                 \
        _Pragma("unroll") for (int a = 0; a < 2; ++a) _Pragma("unroll") for (int n = 0; n < 5; ++n)                     \
            res[a * 5 + n] = acc[n][GRP ? 2 + a : a] + *(const __attribute__((address_space(3))) f32x4*)(Y_ + (a * 5 + n) * 1024); \
    } while (0)
#define NOHOOK (void)0
    const int grp64 = qt * 4 + mt;
    const char* pbase = uptr(ws + WS_PP + (size_t)grp64 * (8 * PP_SET) + wid * 5120);
    const unsigned vo = lane * 16;
    const unsigned r = (ks - wid) & 3;
    f32x4 res[10], ownB[2][2], ownS[2][2];
    asm volatile("s_waitcnt vmcnt(%0) lgkmcnt(0)\n\ts_barrier" ::"i"(P + 4) : "memory");
    STAMP(10);
    k3_stage(k, 2 * K3_SLOT, !GRP); k3_stage(k, 3 * K3_SLOT, !GRP);
    K3_ZERO();
    if (ks != 3) { K3_FIRST(1); K3_ITER(1, 2 * P + 4, true, true, true, NOHOOK); }
    else K3_FIRST(0);
    K3_ITER(0, 2 * P, true, true, true, flag_set(wid, flb, cflag, tag));
    K3_ITER(1, 2 * P, true, true, false, NOHOOK);
    K3_ITER(0, 2 * P, true, false, false, NOHOOK); K3_ITER(1, P, true, false, false, NOHOOK); K3_ITER(0, 0, true, false, false, NOHOOK);
    STAMP(11);
    {
        u32x2 f = u32x2{~(unsigned)tag, 0u};
        flag_wait<false>(wid, f, flb, FL_C + ((qt < 8 ? qt : 15 - qt) * 32 + 8 * ks + (lane & 7)) * 8, tag);
    }
    STAMP(18);
    k3_point(k, ws, wo, wid, qt, mt, 32 * ks, 16 * ks);
    K3_ITER(1, 0, false, false, false, (k3_stage(k, 0, !GRP), k3_stage(k, K3_SLOT, !GRP)));
    K3_REDUCE(res);
    asm volatile("s_waitcnt vmcnt(%0) lgkmcnt(0)\n\ts_barrier" ::"i"(P) : "memory");
    STAMP(19);
    k3_stage(k, 2 * K3_SLOT, !GRP); k3_stage(k, 3 * K3_SLOT, !GRP);
    k3_publish(res, ownB, pbase + (4 + ks) * PP_SET, vo, r);
    K3_ZERO();
    K3_FIRST(0);
    K3_ITER(0, 2 * P + 3, true, true, true, NOHOOK); K3_ITER(1, 2 * P + 3, true, true, false, NOHOOK);
    for (int it = 0; it < 5; ++it) { K3_ITER(0, 2 * P, true, true, false, NOHOOK); K3_ITER(1, 2 * P, true, true, false, NOHOOK); }
    u32x2 xv[2][2];
    unsigned oo[2][2];
#pragma unroll
    for (int sl = 0; sl < 2; ++sl)
#pragma unroll
        for (int t = 0; t < 2; ++t) {
            const unsigned i = 2 * (sl ? 4 : r) + t, a = i >= 5 ? 1 : 0, n = i - 5 * a;
            oo[sl][t] = (16 * (8 * qt + 4 * qh + 2 * GRP + a) + c) * (DM * 2) + (16 * (10 * mt + 5 * mh + n) + 4 * g) * 2;
        }
#define K3_XLD() asm volatile("global_load_dwordx2 %0, %4, %8\n\tglobal_load_dwordx2 %1, %5, %8\n\tglobal_load_dwordx2 %2, %6, %8\n\tglobal_load_dwordx2 %3, %7, %8" \
        : "=&v"(xv[0][0]), "=&v"(xv[0][1]), "=&v"(xv[1][0]), "=&v"(xv[1][1]) : "v"(oo[0][0]), "v"(oo[0][1]), "v"(oo[1][0]), "v"(oo[1][1]), "s"(x) : "memory")
    K3_ITER(0, 2 * P, true, false, false, NOHOOK); K3_ITER(1, P, true, false, false, NOHOOK); K3_ITER(0, 0, true, false, false, K3_XLD()); K3_ITER(1, 0, false, false, false, NOHOOK);
    STAMP(12);
    K3_REDUCE(res);
    STAMP(17);
    k3_publish(res, ownS, pbase + ks * PP_SET, vo, r);
    u32x2 fe[4];
    const bool bump = blockIdx.x == 0 && wid == 0;
    if (bump) {
#pragma unroll
        for (int i = 0; i < 4; ++i) flag_ld(fe[i], flb, FL_X + (64 * i + lane) * 8);
    }
    STAMP(14);
    asm volatile("s_waitcnt vmcnt(0)\n\ts_barrier" : "+v"(xv[0][0]), "+v"(xv[0][1]), "+v"(xv[1][0]), "+v"(xv[1][1]), "+v"(fe[0]), "+v"(fe[1]), "+v"(fe[2]), "+v"(fe[3])::"memory");
    STAMP(15);
    flag_set(wid, flb, FL_S + (grp64 * 4 + ks) * 128, tag);
    {
        u32x2 f = u32x2{~(unsigned)tag, 0u};
        const unsigned o3 = (lane & 3) == (unsigned)ks ? (ks + 1) & 3 : lane & 3;
        flag_wait<false>(0, f, flb, FL_S + (grp64 * 4 + o3) * 128, tag);
    }
    STAMP(16);
    const int nown = r == 0 ? 2 : 1;
    u32x4 pp[2][6];
#pragma unroll
    for (int sl = 0; sl < 2; ++sl)
        if (sl < nown) {
            const char* pj = pbase + (sl ? 4 : r) * 1024;
#pragma unroll
            for (int i = 0; i < 3; ++i) { const unsigned st = (ks + 1 + i) & 3; ld_wt(pp[sl][i], pj + st * PP_SET, vo); ld_wt(pp[sl][3 + i], pj + (4 + st) * PP_SET, vo); }
        }
    asm volatile("s_waitcnt vmcnt(0)" : "+v"(pp[0][0]), "+v"(pp[0][1]), "+v"(pp[0][2]), "+v"(pp[0][3]), "+v"(pp[0][4]), "+v"(pp[0][5]),
                 "+v"(pp[1][0]), "+v"(pp[1][1]), "+v"(pp[1][2]), "+v"(pp[1][3]), "+v"(pp[1][4]), "+v"(pp[1][5])::"memory");
#pragma unroll
    for (int sl = 0; sl < 2; ++sl)
        if (sl < nown) {
#pragma unroll
            for (int t = 0; t < 2; ++t) {
                f32x4 sv = ownS[sl][t], bv = ownB[sl][t];
#pragma unroll
                for (int i = 0; i < 3; ++i) { sv += t ? unpk_hi(pp[sl][i]) : unpk_lo(pp[sl][i]); bv += t ? unpk_hi(pp[sl][3 + i]) : unpk_lo(pp[sl][3 + i]); }
                const float o0 = bflo(xv[sl][t][0]) + sv[0] * silu_f(bv[0]), o1 = bfhi(xv[sl][t][0]) + sv[1] * silu_f(bv[1]);
                const float o2 = bflo(xv[sl][t][1]) + sv[2] * silu_f(bv[2]), o3 = bfhi(xv[sl][t][1]) + sv[3] * silu_f(bv[3]);
                u32x2 w; w[0] = cvtpk(o0, o1); w[1] = cvtpk(o2, o3);
                *(u32x2*)(out + oo[sl][t]) = w;
            }
        }
    if (bump) {
#pragma unroll
        for (int i = 0; i < 4; ++i) flag_wait<false>(0, fe[i], flb, FL_X + (64 * i + lane) * 8, tag);
        flag_set(0, flb, FL_EPOCH, tag);
    }
    STAMP(13);
#undef K3_RW
#undef K3_RA
#undef K3_ZERO
#undef K3_ITER
#undef K3_FIRST
#undef K3_REDUCE
#undef K3_XLD
#undef NOHOOK
}

__global__ __launch_bounds__(512, 2) void fused(const char* __restrict__ x, const char* __restrict__ ckv, const char* __restrict__ wuk, const char* __restrict__ wuv,
                                              const char* __restrict__ wq, const char* __restrict__ w1, const char* __restrict__ b1, char* __restrict__ ws,
                                              const char* __restrict__ wo, const char* __restrict__ w2, char* __restrict__ out) {
    extern __shared__ __attribute__((aligned(16))) char lds[];
    const unsigned wid = rfl(threadIdx.x >> 6), grp = wid >> 2, w4 = wid & 3;
    char* stamp_base = ws; (void)stamp_base;
    STAMP(0);
    const int b = blockIdx.x, xcd = b & 7, idx = b >> 3;
    const int vt = idx & 1, hp = 2 * xcd + ((idx >> 1) & 1), rb = idx >> 2;
    const int head = 4 * xcd + (idx & 3), pi = idx >> 2;
    const int HB = 15 - pi, LB = pi;
    const int myblk = grp ? LB : HB;
    const int q16_0 = 8 * myblk + 2 * w4;
    const bool nft4 = head < 22;
    const int f0 = nft4 ? 64 * head : 1408 + 32 * (head - 22);
    const char* Abase = ckv + (size_t)rb * 256 * 1024;
    const char* Wbase = (vt ? wuv : wuk) + hp * 512;
    const unsigned lane0 = flane();
#define lane lane0
    const int row = 8 * b + wid;
    const unsigned l40 = lane < 40 ? lane : 39;
    u32x4 x0, x1;
    {
        const char* xr = uptr(x + (size_t)row * 1280);
        const unsigned vo = l40 * 32;
        asm volatile("global_load_dwordx4 %0, %2, %3\n\tglobal_load_dwordx4 %1, %2, %3 offset:16" : "=&v"(x0), "=&v"(x1) : "v"(vo), "s"(xr) : "memory");
    }
    unsigned avo[4], wvo[4];
    {
        const unsigned lds0 = (unsigned)(uintptr_t)lds;
        const char* Aw = Abase + (size_t)wid * 32 * 1024;
        const char* Ww = Wbase + (size_t)wid * 8 * 8192;
        const unsigned m0a = lds0 + wid * 4096, m0w = lds0 + 32768 + wid * 4096;
#pragma unroll
        for (int p = 0; p < 4; ++p) {
            const unsigned rw = 8 * p + (lane >> 3);
            avo[p] = rw * 1024 + (((lane & 7) ^ ((rw >> 1) & 7)) << 4) - 1024 * p;
            const unsigned k = 2 * p + (lane >> 5);
            const unsigned f = (k & 3) | ((wid & 1) << 2);
            wvo[p] = k * 8192 + (((lane & 31) ^ (f << 1)) << 4) - 1024 * p;
        }
#pragma unroll
        for (int kt = 0; kt < 2; ++kt) {
            const char* a_ = Aw + kt * 128; const char* w_ = Ww + (size_t)kt * 64 * 8192; const unsigned stg = kt * 65536;
            dma1<0>(m0a + stg, a_, avo[0]); dma1<1024>(m0a + stg, a_, avo[1]); dma1<2048>(m0a + stg, a_, avo[2]); dma1<3072>(m0a + stg, a_, avo[3]);
            dma1<0>(m0w + stg, w_, wvo[0]); dma1<1024>(m0w + stg, w_, wvo[1]); dma1<2048>(m0w + stg, w_, wvo[2]); dma1<3072>(m0w + stg, w_, wvo[3]);
        }
    }
    u64 tag;
    asm volatile("s_load_dwordx2 %0, %1, 0x0\n\ts_waitcnt lgkmcnt(0)" : "=s"(tag) : "s"(uptr(ws + WS_FL + FL_EPOCH)) : "memory");
    tag += 1;
    {
        asm volatile("s_waitcnt vmcnt(16)" : "+v"(x0), "+v"(x1)::"memory");
        float v[16];
#pragma unroll
        for (int i = 0; i < 4; ++i) { v[2 * i] = bflo(x0[i]); v[2 * i + 1] = bfhi(x0[i]); v[8 + 2 * i] = bflo(x1[i]); v[8 + 2 * i + 1] = bfhi(x1[i]); }
        float s = 0.f;
#pragma unroll
        for (int i = 0; i < 16; ++i) s += v[i];
        if (lane >= 40) s = 0.f;
#pragma unroll
        for (int o = 32; o >= 1; o >>= 1) s += __shfl_xor(s, o);
        const float mean = s * (1.0f / DM);
        float q = 0.f;
#pragma unroll
        for (int i = 0; i < 16; ++i) { v[i] -= mean; q += v[i] * v[i]; }
        if (lane >= 40) q = 0.f;
#pragma unroll
        for (int o = 32; o >= 1; o >>= 1) q += __shfl_xor(q, o);
        const float rstd = 1.0f / sqrtf(q * (1.0f / DM) + 1e-5f);
        u32x4 o0, o1;
#pragma unroll
        for (int i = 0; i < 4; ++i) { o0[i] = cvtpk(v[2 * i] * rstd, v[2 * i + 1] * rstd); o1[i] = cvtpk(v[8 + 2 * i] * rstd, v[8 + 2 * i + 1] * rstd); }
        char* xf = ws + WS_XF + ((size_t)(row >> 4) * NKS_X + (l40 >> 1)) * 1024 + ((row & 15) + 32 * (l40 & 1)) * 16;
        if (lane < 40) { st_wt(xf, o0); st_wt(xf + 256, o1); }
    }
#undef lane
    u32x4 xa[4], xb[4];
    u32x2 bb[4];
    if (vt) k1_tile<true>(Abase, Wbase, ws + WS_VF, lds, avo, wvo, wid, hp, rb, ws, wq, w1, b1, tag, head, f0, nft4, q16_0, xa, xb, bb);
    else    k1_tile<false>(Abase, Wbase, ws + WS_KF, lds, avo, wvo, wid, hp, rb, ws, wq, w1, b1, tag, head, f0, nft4, q16_0, xa, xb, bb);
    STAMP(6);
    const unsigned lds0 = (unsigned)(uintptr_t)lds;
    const unsigned kslot0 = grp ? 114688 : 81920, kslot1 = kslot0 + 16384, kslot2 = grp ? 16384 : 0;
    const unsigned vslot0 = grp ? 147456 : 32768, vslot1 = grp ? 65536 : 49152;
    const int nL = 2 * pi + 2;
    const int nsw = grp ? nL : 1000;
    const size_t jump = (size_t)(17 - nL) * 16384;
    const char* kptr = uptr(ws + WS_KF + (size_t)head * 524288 + w4 * 4096);
    const char* vptr = uptr(ws + WS_VF + (size_t)head * 524288 + w4 * 4096);
    bf16x8 qf[8];
    {
        bf16x8 q16[2][4];
        const unsigned kvflag = FL_KV + (hp * 16 + vt * 8 + rb) * 8;
        if (nft4) proj_phase<4>(wq, w1, ws, lds, head, f0, q16_0, q16, wo, w2, lds0 + vslot0 + w4 * 4096, wid, xa, xb, bb, tag, kvflag, head >> 1, lds0 + kslot0 + w4 * 4096, lds0 + kslot1 + w4 * 4096, kptr, kptr + 16384);
        else      proj_phase<2>(wq, w1, ws, lds, head, f0, q16_0, q16, wo, w2, lds0 + vslot0 + w4 * 4096, wid, xa, xb, bb, tag, kvflag, head >> 1, lds0 + kslot0 + w4 * 4096, lds0 + kslot1 + w4 * 4096, kptr, kptr + 16384);
        kptr += 2 * 16384; if (nsw == 2) kptr += jump;
#pragma unroll
        for (int v = 0; v < 4; ++v) {
            const u32x4 r0 = __builtin_bit_cast(u32x4, q16[0][v]), r1 = __builtin_bit_cast(u32x4, q16[1][v]);
            u32x4 z0, z1;
#pragma unroll
            for (int w = 0; w < 4; ++w) {
                const auto s1 = __builtin_amdgcn_permlane16_swap(r0[w], r1[w], false, false);
                const auto s2 = __builtin_amdgcn_permlane32_swap(s1[0], s1[1], false, false);
                z0[w] = s2[0]; z1[w] = s2[1];
            }
            qf[2 * v] = __builtin_bit_cast(bf16x8, z0); qf[2 * v + 1] = __builtin_bit_cast(bf16x8, z1);
        }
    }
    STAMP(7);
    const unsigned lane = flane();
    if (grp == 0) {
        char* qh = ws + WS_QH + (size_t)b * 32768 + w4 * 8192 + lane * 16;
#pragma unroll
        for (int s = 0; s < 8; ++s) *(bf16x8*)(qh + s * 1024) = qf[s];
    }
    const char* flb = uptr(ws + WS_FL);
    lds_cptr Lb = (lds_cptr)lds + lane * 16;
    const unsigned m0b = lds0 + w4 * 4096;
    const unsigned dvo = lane * 16;
    int q0 = 128 * myblk + 32 * w4;
    int tile = 0;
    asm volatile("s_waitcnt lgkmcnt(0)\n\ts_barrier" ::: "memory");
    dma4(m0b + vslot0, vptr, dvo); vptr += 16384;
    dma4(m0b + kslot2, kptr, dvo); kptr += 16384;
    f32x16 o[4], S[2], negm;
    float mref = 0.f, lsum = 0.f, lfin = 1.f;
    bf16x8 pf[4], fr[FRW];
#pragma unroll
    for (int i = 0; i < 4; ++i) o[i] = f32x16{};
    negm = f32x16{}; S[0] = f32x16{}; S[1] = f32x16{};
    asm volatile("s_nop 1" : "+v"(negm));
    NOP_SO(S, o);
    unsigned ks_cur = kslot0, ks_nxt = kslot1, ks_nn = kslot2;
#define CTX_VO(l) ((((l) >> 4) & 1) * (NKS_CH * 1024) + (((l) & 15) + 16 * ((l) >> 5)) * 16)
#define KOFF(i) (((i) & 1) * 8192 + ((i) >> 1) * 1024)
#define VOFF(j) (((j) >> 3) * 8192 + ((j) & 3) * 2048 + (((j) >> 2) & 1) * 1024)
#define EARLY_K(slot) do { _Pragma("unroll") for (int i = 0; i < FRA; ++i) fr[i] = lds_frag(Lb + (slot) + KOFF(i)); } while (0)
    EARLY_K(kslot0);
    if (grp) asm volatile("s_barrier" ::: "memory");
#define PHASE_M(t, QK, PV, VS)                                                                              \
    do {                                                                                                                              \
        __builtin_amdgcn_s_setprio(0);                                                                                                \
        lds_cptr kp_ = Lb + ks_cur; lds_cptr vp_ = Lb + ((VS) ? vslot1 : vslot0);                                                     \
        if (!(QK)) { _Pragma("unroll") for (int i = 16; i < 16 + FRA; ++i) fr[i % FRW] = lds_frag(vp_ + VOFF(i - 16)); }               \
        _Pragma("unroll") for (int i = (QK) ? 0 : 16; i < ((PV) ? 32 : 16); ++i) {                                                    \
            const int n = i + FRA;                                                                                                    \
            if (n < 16) fr[n % FRW] = lds_frag(kp_ + KOFF(n)); else if ((PV) && n < 32) fr[n % FRW] = lds_frag(vp_ + VOFF(n - 16));    \
            SBAR();                                                                                                                   \
            if (i < 2) mfma32_ini(S[i & 1], fr[i % FRW], qf[i >> 1], negm);                                                        \
            else if (i < 16) mfma32_acc(S[i & 1], fr[i % FRW], qf[i >> 1]);                                                             \
            else mfma32_acc(o[i & 3], fr[i % FRW], pf[(i - 16) >> 2]);                                                                  \
            SBAR();                                                                                                                   \
            if ((QK) && i == 16 && (t) + 1 == nsw) {        \
                const char* qs_ = uptr(ws + WS_QH + (size_t)b * 32768 + w4 * 8192);                                                   \
                asm volatile("s_nop 4\n\tglobal_load_dwordx4 %0, %8, %9\n\tglobal_load_dwordx4 %1, %8, %9 offset:1024\n\tglobal_load_dwordx4 %2, %8, %9 offset:2048\n\tglobal_load_dwordx4 %3, %8, %9 offset:3072" \
                             : "+v"(qf[0]), "+v"(qf[1]), "+v"(qf[2]), "+v"(qf[3]), "+v"(qf[4]), "+v"(qf[5]), "+v"(qf[6]), "+v"(qf[7]) : "v"(dvo), "s"(qs_) : "memory"); \
                asm volatile("global_load_dwordx4 %4, %8, %9\n\tglobal_load_dwordx4 %5, %8, %9 offset:1024\n\tglobal_load_dwordx4 %6, %8, %9 offset:2048\n\tglobal_load_dwordx4 %7, %8, %9 offset:3072" \
                             : "+v"(qf[0]), "+v"(qf[1]), "+v"(qf[2]), "+v"(qf[3]), "+v"(qf[4]), "+v"(qf[5]), "+v"(qf[6]), "+v"(qf[7]) : "v"(dvo), "s"(qs_ + 4096) : "memory"); \
            }                                                                                                                         \
            SBAR();                                                                                                                   \
        }                                                                                                                             \
        __builtin_amdgcn_s_setprio(3);                                                                                                \
        TICK(tA);                                                                                                                     \
        asm volatile("s_barrier" ::: "memory");                                                                                       \
        TICK(tX);                                                                                                                     \
    } while (0)
#define EXP_PACK()                    \
    do {                                                                                                                              \
        float e_[32], a_[2];                                                                                                          \
        unsigned c_[16];                                                                                                              \
        _Pragma("unroll") for (int k = 0; k < 18; ++k) {                                                                              \
            if (k < 16) { e_[2 * k] = v_exp(S[k >> 3][2 * (k & 7)]); e_[2 * k + 1] = v_exp(S[k >> 3][2 * (k & 7) + 1]); }             \
            if (k == 1) { a_[0] = e_[0]; a_[1] = e_[1]; }                                                                             \
            if (k >= 2 && k < 17) { a_[0] = v_add(a_[0], e_[2 * k - 2]); a_[1] = v_add(a_[1], e_[2 * k - 1]); }                        \
            if (k >= 2) c_[k - 2] = v_cvt(e_[2 * k - 4], e_[2 * k - 3]);                                                              \
        }                                                                                                                             \
        _Pragma("unroll") for (int k = 0; k < 4; ++k) pf[k] = __builtin_bit_cast(bf16x8, u32x4{c_[4 * k], c_[4 * k + 1], c_[4 * k + 2], c_[4 * k + 3]}); \
        ts_ = v_add(a_[0], a_[1]);                                                                                                    \
    } while (0)
#define REBASE(FIRST)                                                          \
    do {                                                                                                                              \
        float rm = S[0][0];                                                                                                           \
        _Pragma("unroll") for (int e = 1; e < 32; ++e) rm = __builtin_fmaxf(rm, S[e >> 4][e & 15]);                                   \
        rm = xh_max(rm);                                                                                                              \
        const float dl = (FIRST) ? (rm == -__builtin_inff() ? 0.f : rm) : __builtin_fmaxf(rm, 0.f);                                   \
        mref += dl;                                                                                                                   \
        if (!(FIRST)) { float f = __builtin_amdgcn_exp2f(-dl); asm volatile("s_nop 1" : "+v"(f)); lsum *= f;               \
            _Pragma("unroll") for (int i = 0; i < 4; ++i) _Pragma("unroll") for (int r = 0; r < 16; ++r) o[i][r] = mul_s(o[i][r], f); } \
        _Pragma("unroll") for (int r = 0; r < 16; ++r) { S[0][r] = sub_s(S[0][r], dl); S[1][r] = sub_s(S[1][r], dl); }                \
        const float nm = -mref;                                                                                                       \
        _Pragma("unroll") for (int r = 0; r < 16; ++r) negm[r] = nm;                                                                  \
    } while (0)
#define PHASE_V(t, WN, LASTV, VS, DK, DV)                                                                                             \
    do {                                                                                                                              \
        const bool swn = ((t) + 1 == nsw), sw0 = ((t) == nsw);                                                                        \
        const bool diag = 64 * tile + 63 > q0 || sw0, first_ = (t) == 0 || sw0;                                                       \
        if (DV) { dma4(m0b + ((VS) ? vslot1 : vslot0), vptr, dvo); vptr += 16384 + ((t) + 2 == nsw ? jump : 0); }          \
        if (DK) { dma4(m0b + ks_cur, kptr, dvo); kptr += 16384 + ((t) + 4 == nsw ? jump : 0); }                           \
        NOP_SO(S, o);                                                                                                                 \
        float ts_;                                                                                                                    \
        if (__builtin_expect(diag || first_, 0)) {                                                                                    \
            if (sw0) {                                                                                \
                float il_ = __builtin_amdgcn_rcpf(xh_sum(lsum));                                                                      \
                asm volatile("s_nop 1" : "+v"(il_));                                                                                  \
                const char* cp_ = uptr(ws + WS_CH + ((size_t)(8 * LB + 2 * w4) * NKS_CH + 4 * head) * 1024);                          \
                const unsigned co_ = CTX_VO(flane());                                                                                 \
                asm volatile("s_nop 4");                                                                                              \
                st_wt_i<0>(cp_, co_, pack_o(o[0], 0, il_)); st_wt_i<512>(cp_, co_, pack_o(o[0], 1, il_));                             \
                st_wt_i<1024>(cp_, co_, pack_o(o[1], 0, il_)); st_wt_i<1536>(cp_, co_, pack_o(o[1], 1, il_));                         \
                st_wt_i<2048>(cp_, co_, pack_o(o[2], 0, il_)); st_wt_i<2560>(cp_, co_, pack_o(o[2], 1, il_));                         \
                st_wt_i<3072>(cp_, co_, pack_o(o[3], 0, il_)); st_wt_i<3584>(cp_, co_, pack_o(o[3], 1, il_));                         \
                _Pragma("unroll") for (int i = 0; i < 4; ++i) o[i] = f32x16{};                                                        \
                lsum = 0.f; mref = 0.f; q0 = 128 * HB + 32 * w4;                                                                      \
            }                                                                                                                         \
            if (64 * tile + 63 > q0) {         \
                const int dq = (int)(lane & 31) - 4 * (int)(lane >> 5);                                                               \
                _Pragma("unroll") for (int u = 0; u < 2; ++u) {                                                                       \
                    const int kb = 64 * tile + 32 * u;                                                                                \
                    if (kb > q0) { _Pragma("unroll") for (int r = 0; r < 16; ++r) S[u][r] = -__builtin_inff(); }                       \
                    else if (kb == q0) mask_diag(S[u], dq);                                                                           \
                }                                                                                                                     \
            }                                                                                                                         \
            if (first_) REBASE(true);                                                                                                 \
        }                                                                                                                             \
        EXP_PACK();                                                                                                                   \
        if (__builtin_expect(__any(ts_ > PLIM), 0)) { REBASE(false); EXP_PACK(); }     \
        lsum += ts_;                                                                                                                  \
        if (!(LASTV)) EARLY_K(ks_nxt);                                                                                                \
        if (__builtin_expect(swn, 0)) {                                                                                               \
            negm = f32x16{};                                                                                                          \
            asm volatile("s_waitcnt vmcnt(%9)" : "+v"(qf[0]), "+v"(qf[1]), "+v"(qf[2]), "+v"(qf[3]), "+v"(qf[4]), "+v"(qf[5]), "+v"(qf[6]), "+v"(qf[7]), "+v"(negm) : "i"(WN) : "memory"); \
            tile += 17 - nL;                                                                                                          \
        }                                                                                                                             \
        TICK(tB);                                                                                                                     \
        asm volatile("s_cmp_eq_u32 %7, 0\n\ts_cbranch_scc1 .Lv1%=\n\ts_waitcnt vmcnt(%5)\n\ts_branch .Lv2%=\n.Lv1%=:\n\ts_waitcnt vmcnt(%6)\n.Lv2%=:\n\ts_barrier"                \
                     : "+v"(pf[0]), "+v"(pf[1]), "+v"(pf[2]), "+v"(pf[3]), "+v"(negm) : "i"(WN), "i"((WN) + 8), "s"(nsw - (t)) : "scc", "memory");   \
        TICK(tO);                                                                                                                     \
        { const unsigned r_ = ks_cur; ks_cur = ks_nxt; ks_nxt = ks_nn; ks_nn = r_; }                                                  \
        tile += 1;                                                                                                                    \
    } while (0)
    constexpr float PLIM = 256.0f;
    PHASE_M(0, true, false, 1);
    PHASE_V(0, 8, false, 1, true, true);
    flag_set(w4, flb, FL_H + (myblk * 32 + head) * 8, tag);
    for (int t = 1; t < 13; t += 2) {
        PHASE_M(t, true, true, 0); PHASE_V(t, 8, false, 0, true, true);
        PHASE_M(t + 1, true, true, 1); PHASE_V(t + 1, 8, false, 1, true, true);
    }
    PHASE_M(13, true, true, 0); PHASE_V(13, 8, false, 0, true, true);
    PHASE_M(14, true, true, 1); PHASE_V(14, 4, false, 1, false, true);
    PHASE_M(15, true, true, 0);
    const int ks = xcd >> 1, qt = 8 * (xcd & 1) + (idx >> 2), mt = idx & 3;
    u32x2 fh;
    const unsigned fho = FL_H + (qt * 32 + (lane & 31)) * 8;
    flag_ld(fh, flb, fho);
    PHASE_V(15, 4, false, 0, false, true);
    PIN(fh);
    PHASE_M(16, true, true, 1); PHASE_V(16, 0, true, 1, false, false);
    PHASE_M(17, false, true, 0);
    flag_wait<false>(0, fh, flb, fho, tag);
    if (!grp) asm volatile("s_barrier" ::: "memory");
    NOP_SO(S, o);
    K3S k3 = k3_setup(lds0, wid, lane);
    k3_point(k3, ws, w2, wid, qt, mt, NKS_C + 14 * ks, 7 * ks);
    k3_stage(k3, 0, grp == 0); k3_stage(k3, K3_SLOT, grp == 0);
    STAMP(8);
    {
        const float ls = xh_sum(lsum);
        const float mm = ls > 0.f ? mref : -__builtin_inff();
        lds_ptr X = (lds_ptr)lds + 81920 + wid * 8192 + lane * 16;
        f32x16 keep[2];
        if (grp) {
#pragma unroll
            for (int i = 0; i < 2; ++i) {
                keep[i] = o[2 + i];
#pragma unroll
                for (int y = 0; y < 4; ++y) *(__attribute__((address_space(3))) f32x4*)(X + (4 * i + y) * 1024) = f32x4{o[i][4 * y], o[i][4 * y + 1], o[i][4 * y + 2], o[i][4 * y + 3]};
            }
        } else {
#pragma unroll
            for (int i = 0; i < 2; ++i) {
                keep[i] = o[i];
#pragma unroll
                for (int y = 0; y < 4; ++y) *(__attribute__((address_space(3))) f32x4*)(X + (4 * i + y) * 1024) = f32x4{o[2 + i][4 * y], o[2 + i][4 * y + 1], o[2 + i][4 * y + 2], o[2 + i][4 * y + 3]};
            }
        }
        lds_ptr Y = (lds_ptr)lds + 73728 + wid * 512 + lane * 8;
        *(__attribute__((address_space(3))) f32x2*)Y = f32x2{mm, ls};
        asm volatile("s_waitcnt lgkmcnt(0)\n\ts_barrier" ::: "memory");
        const unsigned ow = wid ^ 4;
        lds_cptr Xo = (lds_cptr)lds + 81920 + ow * 8192 + lane * 16;
        const f32x2 oml = *(const __attribute__((address_space(3))) f32x2*)((lds_cptr)lds + 73728 + ow * 512 + lane * 8);
        const float mx = __builtin_fmaxf(mm, oml[0]);
        const float e0 = __builtin_amdgcn_exp2f(mm - mx), e1 = __builtin_amdgcn_exp2f(oml[0] - mx);
        const float inv = __builtin_amdgcn_rcpf(ls * e0 + oml[1] * e1);
        const float fs = e0 * inv, fo = e1 * inv;
        const char* cp = uptr(ws + WS_CH + ((size_t)(8 * HB + 2 * w4) * NKS_CH + 4 * head + 2 * grp) * 1024);
        const unsigned co = CTX_VO(lane);
#pragma unroll
        for (int i = 0; i < 2; ++i) {
            f32x16 m;
#pragma unroll
            for (int y = 0; y < 4; ++y) {
                const f32x4 pv = *(const __attribute__((address_space(3))) f32x4*)(Xo + (4 * i + y) * 1024);
#pragma unroll
                for (int r = 0; r < 4; ++r) m[4 * y + r] = keep[i][4 * y + r] * fs + pv[r] * fo;
            }
            if (i == 0) { st_wt_i<0>(cp, co, pack_o(m, 0, 1.f)); st_wt_i<512>(cp, co, pack_o(m, 1, 1.f)); }
            else { st_wt_i<1024>(cp, co, pack_o(m, 0, 1.f)); st_wt_i<1536>(cp, co, pack_o(m, 1, 1.f)); }
        }
    }
    STAMP(9);
#undef KOFF
#undef CTX_VO
#undef VOFF
#undef EARLY_K
#undef PHASE_M
#undef PHASE_V
#undef EXP_PACK
#undef REBASE
    if (grp == 0) k3_body<0>(k3, x, wo, ws, out, lds, wid, tag, ks, qt, mt, FL_C + (pi * 32 + head) * 8);
    else k3_body<1>(k3, x, wo, ws, out, lds, wid, tag, ks, qt, mt, FL_C + (pi * 32 + head) * 8);
}

extern "C" void kernel_launch(void* const* d_in, const int* in_sizes, int n_in, void* d_out, int out_size, void* d_ws, size_t ws_size,
                              hipStream_t stream) {
    const char* x = (const char*)d_in[0];
    const char* ckv = (const char*)d_in[1];
    const char* wuk = (const char*)d_in[3];
    const char* wuv = (const char*)d_in[4];
    char* ws = (char*)d_ws;
    (void)hipFuncSetAttribute((const void*)fused, hipFuncAttributeMaxDynamicSharedMemorySize, 163840);
    fused<<<256, 512, 163840, stream>>>(x, ckv, wuk, wuv, (const char*)d_in[2], (const char*)d_in[7], (const char*)d_in[8], ws, (const char*)d_in[5], (const char*)d_in[9], (char*)d_out);
}
```

```cpp
#include <hip/hip_runtime.h>
#include <hip/hip_bf16.h>
#include <stdint.h>

typedef __bf16 bf16x8 __attribute__((ext_vector_type(8)));
typedef __bf16 bf16x2 __attribute__((ext_vector_type(2)));
typedef short s16x4 __attribute__((ext_vector_type(4)));
typedef short s16x8 __attribute__((ext_vector_type(8)));
typedef short v4i16_t __attribute__((ext_vector_type(4)));
typedef float f32x16 __attribute__((ext_vector_type(16)));
typedef float f32x4 __attribute__((ext_vector_type(4)));
typedef float f32x2 __attribute__((ext_vector_type(2)));
typedef unsigned u32x4 __attribute__((ext_vector_type(4)));
typedef unsigned u32x2 __attribute__((ext_vector_type(2)));
typedef __attribute__((address_space(3))) const char* lds_cptr;
typedef __attribute__((address_space(3))) char* lds_ptr;

#define DI __device__ __forceinline__
#define SBAR() __builtin_amdgcn_sched_barrier(0)
#define MFMA(a, b, c) __builtin_amdgcn_mfma_f32_16x16x32_bf16((a), (b), (c), 0, 0, 0)
#define PIN(x) asm volatile("" : "+v"(x))
#define PINR(x) asm volatile("" ::"v"(x))

constexpr int S_ = 2048, NH = 32, HD = 128, DM = 640, DC = 512, FF = 1728;
constexpr int NKS_X = DM / 32;
constexpr int NKS_C = (NH * HD) / 32;
constexpr int NKS_H = FF / 32;
constexpr int NKS_CH = NKS_C + NKS_H;
constexpr size_t WS_XF = 0;
constexpr size_t WS_KF = WS_XF + (size_t)128 * NKS_X * 1024;
constexpr size_t WS_VF = WS_KF + (size_t)NH * 128 * 4 * 1024;
constexpr size_t WS_CH = WS_VF + (size_t)NH * 64 * 8 * 1024;
constexpr size_t WS_QH = WS_CH + (size_t)128 * NKS_CH * 1024;
constexpr size_t WS_PP = WS_QH + (size_t)256 * 32768;
constexpr size_t PP_BYTES = (size_t)64 * 8 * 8 * 5 * 1024;
constexpr size_t WS_FL = WS_PP + PP_BYTES;
constexpr size_t FL_EPOCH = 0;
constexpr size_t FL_X = 256;
constexpr size_t FL_KV = FL_X + 2048;
constexpr size_t FL_H = FL_KV + 2048;
constexpr size_t FL_C = FL_H + 4096;
constexpr size_t FL_S = 16384;
constexpr size_t WS_END = WS_FL + FL_S + (size_t)256 * 128;

DI unsigned rfl(unsigned x) { return __builtin_amdgcn_readfirstlane(x); }
DI unsigned flane() { unsigned r; asm volatile("v_mbcnt_lo_u32_b32 %0, -1, 0\n\tv_mbcnt_hi_u32_b32 %0, -1, %0" : "=v"(r)); return r; }
#define STAMP(i)
#define TICK(acc)
DI const char* uptr(const char* p) { const unsigned long long v = (unsigned long long)p; const unsigned lo = rfl((unsigned)v), hi = rfl((unsigned)(v >> 32)); return (const char*)(((unsigned long long)hi << 32) | lo); }
DI unsigned cvtpk(float lo, float hi) { f32x2 v = {lo, hi}; bf16x2 b = __builtin_convertvector(v, bf16x2); return __builtin_bit_cast(unsigned, b); }
DI bf16x8 lds_frag(lds_cptr p) { return *(const __attribute__((address_space(3))) bf16x8*)p; }
DI s16x4 lds_tr(lds_cptr p) { return __builtin_bit_cast(s16x4, __builtin_amdgcn_ds_read_tr16_b64_v4i16((__attribute__((address_space(3))) v4i16_t*)p)); }
DI bf16x8 tr_frag(lds_cptr p0, lds_cptr p1) { s16x4 lo = lds_tr(p0), hi = lds_tr(p1); s16x8 v = __builtin_shufflevector(lo, hi, 0, 1, 2, 3, 4, 5, 6, 7); return __builtin_bit_cast(bf16x8, v); }
template <int OFF> DI void dma1(unsigned m0v, const void* sbase, unsigned voff) {
    asm volatile("s_mov_b32 m0, %0\n\ts_nop 0\n\tglobal_load_lds_dwordx4 %1, %2 offset:%3" ::"s"(m0v), "v"(voff), "s"(sbase), "i"(OFF) : "memory");
}
DI u32x4 pack44(const f32x4& a, const f32x4& b) { u32x4 w; w[0] = cvtpk(a[0], a[1]); w[1] = cvtpk(a[2], a[3]); w[2] = cvtpk(b[0], b[1]); w[3] = cvtpk(b[2], b[3]); return w; }
DI u32x4 pack44s(const f32x4& a, const f32x4& b, float sc) { u32x4 w; w[0] = cvtpk(a[0] * sc, a[1] * sc); w[1] = cvtpk(a[2] * sc, a[3] * sc); w[2] = cvtpk(b[0] * sc, b[1] * sc); w[3] = cvtpk(b[2] * sc, b[3] * sc); return w; }
DI u32x4 pack8(const f32x16& a, int b, float sc) {
    u32x4 w; w[0] = cvtpk(a[b + 0] * sc, a[b + 1] * sc); w[1] = cvtpk(a[b + 2] * sc, a[b + 3] * sc);
    w[2] = cvtpk(a[b + 4] * sc, a[b + 5] * sc); w[3] = cvtpk(a[b + 6] * sc, a[b + 7] * sc); return w;
}
DI u32x4 pack8n(const f32x16& a, int b) {
    u32x4 w; w[0] = cvtpk(a[b + 0], a[b + 1]); w[1] = cvtpk(a[b + 2], a[b + 3]); w[2] = cvtpk(a[b + 4], a[b + 5]); w[3] = cvtpk(a[b + 6], a[b + 7]); return w;
}
DI float bf2f(unsigned short u) { return __uint_as_float((unsigned)u << 16); }
DI float bflo(unsigned w) { return __uint_as_float(w << 16); }
DI float bfhi(unsigned w) { return __uint_as_float(w & 0xffff0000u); }
DI float xhalf_sum(float v) { auto r = __builtin_amdgcn_permlane32_swap(__float_as_uint(v), __float_as_uint(v), false, false); return __uint_as_float(r[0]) + __uint_as_float(r[1]); }
DI float xhalf_max(float v) { auto r = __builtin_amdgcn_permlane32_swap(__float_as_uint(v), __float_as_uint(v), false, false); return __builtin_fmaxf(__uint_as_float(r[0]), __uint_as_float(r[1])); }

DI void warm_load(unsigned& d, const char* wa, const char* wb, unsigned id) {
    const unsigned j = id - 1280u;
    const char* p = uptr(id < 1280u ? wa + (size_t)id * 4096 : wb + (size_t)(j < 539u ? j : 539u) * 4096);
    const unsigned voff = flane() * 64;
    asm volatile("s_nop 4\n\tglobal_load_dword %0, %1, %2" : "=&v"(d) : "v"(voff), "s"(p) : "memory");
}
DI void warm_lds(unsigned m0v, const char* wa, const char* wb, unsigned id) {
    const unsigned j = id - 1280u;
    const char* p = uptr(id < 1280u ? wa + (size_t)id * 4096 : wb + (size_t)(j < 539u ? j : 539u) * 4096);
    const unsigned voff = flane() * 64;
    asm volatile("s_mov_b32 m0, %0\n\ts_nop 4\n\tglobal_load_lds_dword %1, %2" ::"s"(m0v), "v"(voff), "s"(p) : "memory");
}

DI void mfma_acc(f32x4& acc, const bf16x8& a, const bf16x8& b) { asm volatile("v_mfma_f32_16x16x32_bf16 %0, %1, %2, %0" : "+v"(acc) : "v"(a), "v"(b)); }
DI void mfma_acc_s(f32x4& acc, const bf16x8& a, const bf16x8& b) { asm volatile("s_nop 1\n\tv_mfma_f32_16x16x32_bf16 %0, %1, %2, %0" : "+v"(acc) : "v"(a), "v"(b)); }
DI void mfma32_acc(f32x16& acc, const bf16x8& a, const bf16x8& b) { asm volatile("v_mfma_f32_32x32x16_bf16 %0, %1, %2, %0" : "+v"(acc) : "v"(a), "v"(b)); }
DI void mfma32_ini(f32x16& d, const bf16x8& a, const bf16x8& b, const f32x16& c) { asm volatile("v_mfma_f32_32x32x16_bf16 %0, %1, %2, %3" : "=&v"(d) : "v"(a), "v"(b), "v"(c)); }
DI float v_exp(float x) { float r; asm volatile("v_exp_f32 %0, %1" : "=v"(r) : "v"(x)); return r; }
DI float v_add(float a, float b) { float r; asm volatile("v_add_f32 %0, %1, %2" : "=v"(r) : "v"(a), "v"(b)); return r; }
DI unsigned v_cvt(float lo, float hi) { unsigned r; asm volatile("v_cvt_pk_bf16_f32 %0, %1, %2" : "=v"(r) : "v"(lo), "v"(hi)); return r; }
DI void set_m0(unsigned v) { asm volatile("s_mov_b32 m0, %0" ::"s"(v) : "memory"); }
template <int OFF> DI void dma_nom0(const void* sbase, unsigned voff) { asm volatile("global_load_lds_dwordx4 %0, %1 offset:%2" ::"v"(voff), "s"(sbase), "i"(OFF) : "memory"); }
DI void dma4(unsigned m0v, const void* sbase, unsigned voff) {
    asm volatile("s_mov_b32 m0, %0\n\ts_nop 0\n\tglobal_load_lds_dwordx4 %1, %2\n\tglobal_load_lds_dwordx4 %1, %2 offset:1024\n\tglobal_load_lds_dwordx4 %1, %2 offset:2048\n\tglobal_load_lds_dwordx4 %1, %2 offset:3072"
                 ::"s"(m0v), "v"(voff), "s"(sbase) : "memory");
}

typedef unsigned long long u64;
DI void st_wt(char* p, const u32x4& v) { asm volatile("global_store_dwordx4 %0, %1, off sc1\n\ts_nop 1" ::"v"(p), "v"(v) : "memory"); }
DI void st_wt_o(const char* sbase, unsigned voff, const u32x4& v) { asm volatile("global_store_dwordx4 %0, %1, %2 sc1\n\ts_nop 1" ::"v"(voff), "v"(v), "s"(sbase) : "memory"); }
template <int OFF> DI void st_wt_i(const char* sbase, unsigned voff, const u32x4& v) {
    asm volatile("global_store_dwordx4 %0, %1, %2 offset:%3 sc1\n\ts_nop 1" ::"v"(voff), "v"(v), "s"(sbase), "i"(OFF) : "memory");
}
DI void ld_wt(u32x4& d, const char* sbase, unsigned voff) { asm volatile("global_load_dwordx4 %0, %1, %2 sc1" : "=&v"(d) : "v"(voff), "s"(sbase) : "memory"); }
DI void flag_set(unsigned wid, const char* sbase, unsigned off, u64 tag) {
    const u32x2 v = u32x2{(unsigned)tag, (unsigned)(tag >> 32)};
    u64 ex;
    asm volatile("s_cmp_lg_u32 %1, 0\n\ts_cbranch_scc1 .Lfs%=\n\ts_mov_b64 %0, exec\n\ts_mov_b64 exec, 1\n\t"
                 "global_store_dwordx2 %2, %3, %4 sc1\n\ts_mov_b64 exec, %0\n.Lfs%=:\n\ts_nop 1"
                 : "=&s"(ex) : "s"(wid), "v"(off), "v"(v), "s"(sbase) : "scc", "memory");
}
DI void flag_ld(u32x2& f, const char* sbase, unsigned voff) { asm volatile("global_load_dwordx2 %0, %1, %2 sc1" : "=&v"(f) : "v"(voff), "s"(sbase) : "memory"); }
template <bool ACQ>
DI void flag_wait(unsigned skip, u32x2& f, const char* sbase, unsigned voff, u64 tag) {
    unsigned cnt;
    asm volatile("s_cmp_lg_u32 %2, 0\n\ts_cbranch_scc1 .Lfe%=\n\t"
                 "v_cmp_ne_u64 vcc, %3, %0\n\ts_cbranch_vccz .Lfd%=\n\ts_mov_b32 %1, 0x40000\n"
                 ".Lfl%=:\n\tglobal_load_dwordx2 %0, %4, %5 sc1\n\ts_waitcnt vmcnt(0)\n\tv_cmp_ne_u64 vcc, %3, %0\n\ts_cbranch_vccz .Lfd%=\n\t"
                 "s_sleep 2\n\ts_sub_u32 %1, %1, 1\n\ts_cbranch_scc0 .Lfl%=\n"
                 ".Lfd%=:\n\t.if %6\n\tbuffer_inv sc1\n\t.endif\n"
                 ".Lfe%=:"
                 : "+v"(f), "=&s"(cnt) : "s"(skip), "s"(tag), "v"(voff), "s"(sbase), "i"(ACQ ? 1 : 0) : "vcc", "scc", "memory");
}

constexpr int WQ_PIECE = 1040, W1_PIECE = 1056, W1_OFF = 16 * WQ_PIECE, PSLOT = W1_OFF + 8 * W1_PIECE;
struct PJ { unsigned wqvo, w1vo, xv0, xv1; const char *wqs, *w1s, *xs; };
DI PJ pj_setup(const char* wq, const char* w1, const char* ws, unsigned wid, int head, int f0, bool nft4, int q16_0) {
    const unsigned lane = flane();
    PJ j;
    j.wqvo = ((lane >> 2) & 3) * 8192 + (lane >> 4) * 64 + (lane & 3) * 16;
    j.w1vo = ((lane >> 2) & 7) * 3456 + (nft4 ? (lane >> 5) * 64 : 0) + (lane & 3) * 16;
    j.wqs = uptr(wq + (size_t)wid * 8 * 8192 + head * 256);
    j.w1s = uptr(w1 + (size_t)wid * 8 * 3456 + f0 * 2);
    j.xs = uptr(ws + WS_XF + (size_t)q16_0 * NKS_X * 1024);
    j.xv0 = lane * 16; j.xv1 = lane * 16 + NKS_X * 1024;
    return j;
}
DI void pj_dma(const PJ& j, unsigned lds0, unsigned wid, int c, int slot) {
    dma1<0>(lds0 + slot + (2 * wid) * WQ_PIECE, j.wqs + (size_t)c * 64 * 8192, j.wqvo);
    dma1<0>(lds0 + slot + (2 * wid + 1) * WQ_PIECE, j.wqs + (size_t)c * 64 * 8192 + 4 * 8192, j.wqvo);
    dma1<0>(lds0 + slot + W1_OFF + wid * W1_PIECE, j.w1s + (size_t)c * 64 * 3456, j.w1vo);
}
DI void pj_xload(const PJ& j, u32x4 (&X)[4], int c) {
    const char* p_ = j.xs + c * 2048;
    asm volatile("global_load_dwordx4 %0, %4, %6 sc1\n\tglobal_load_dwordx4 %1, %5, %6 sc1\n\tglobal_load_dwordx4 %2, %4, %6 offset:1024 sc1\n\tglobal_load_dwordx4 %3, %5, %6 offset:1024 sc1"
                 : "=&v"(X[0]), "=&v"(X[1]), "=&v"(X[2]), "=&v"(X[3]) : "v"(j.xv0), "v"(j.xv1), "s"(p_) : "memory");
}

#define K1PF 0
template <bool VT, class F>
DI void k1_mma(f32x4 (&acc)[8][4], int half, const bf16x8 (&c)[4], const bf16x8 (&w)[4], F&& filler) {
#pragma unroll
    for (int rt = 0; rt < 4; ++rt)
#pragma unroll
        for (int nt = 0; nt < 4; ++nt) {
            if (half == 0) acc[rt][nt] = VT ? MFMA(c[rt], w[nt], acc[rt][nt]) : MFMA(w[nt], c[rt], acc[rt][nt]);
            else acc[4 + rt][nt] = VT ? MFMA(c[rt], w[nt], acc[4 + rt][nt]) : MFMA(w[nt], c[rt], acc[4 + rt][nt]);
            filler(rt * 4 + nt);
        }
}

template <bool VT>
DI void k1_tile(const char* __restrict__ Abase, const char* __restrict__ Wbase, char* __restrict__ outp, char* lds, const unsigned (&avo)[4], const unsigned (&wvo)[4],
                unsigned wid, int hp, int rb, char* __restrict__ ws, const char* __restrict__ wq, const char* __restrict__ w1, const char* __restrict__ b1, u64 tag,
                int head2, int f0, bool nft4, int q16_0, u32x4 (&xa)[4], u32x4 (&xb)[4], u32x2 (&bb)[4], const char* const (&pfb)[3], const unsigned (&pfo)[3]) {
    char* stamp_base = ws; (void)stamp_base;
    const char* flb = uptr(ws + WS_FL);
    const unsigned wm = wid >> 2, wn = wid & 3;
    const unsigned lds0 = (unsigned)(uintptr_t)lds;
    unsigned aaddr[2], baddr[4];
    {
        const unsigned lane = flane(), c = lane & 15, g = lane >> 4;
        const unsigned x = (c >> 1) & 7;
#pragma unroll
        for (int s = 0; s < 2; ++s) aaddr[s] = (128 * wm + c) * 128 + (((4 * s + g) ^ x) << 4);
        const unsigned q = (lane & 15) >> 2, p = lane & 3, f = q | ((g & 1) << 2);
#pragma unroll
        for (int nt = 0; nt < 4; ++nt) baddr[nt] = 32768 + (8 * g + q) * 512 + (((8 * wn + 2 * nt + (p >> 1)) ^ (f << 1)) << 4) + ((p & 1) << 3);
    }
    f32x4 acc[8][4];
#pragma unroll
    for (int i = 0; i < 8; ++i)
#pragma unroll
        for (int j = 0; j < 4; ++j) acc[i][j] = f32x4{};
    bf16x8 cf[2][4], wf[2][4];
    lds_cptr L = (lds_cptr)lds;
#define K1_RC(buf, stg, s, half) do { _Pragma("unroll") for (int rt = 0; rt < 4; ++rt) cf[buf][rt] = lds_frag(L + (stg) + aaddr[s] + ((half) * 4 + rt) * 2048); } while (0)
#define K1_RW(buf, stg, s) do { _Pragma("unroll") for (int nt = 0; nt < 4; ++nt) wf[buf][nt] = tr_frag(L + (stg) + baddr[nt] + (s) * 16384, L + (stg) + baddr[nt] + (s) * 16384 + 2048); } while (0)
    const char* Aw = Abase + (size_t)wid * 32 * 1024;
    const char* Ww = Wbase + (size_t)wid * 8 * 8192;
    const unsigned m0a = lds0 + wid * 4096, m0w = lds0 + 32768 + wid * 4096;
#define K1_DMA(kt, stg)                                                                   \
    do {                                                                                  \
        const char* a_ = Aw + (kt) * 128; const char* w_ = Ww + (size_t)(kt) * 64 * 8192; \
        dma1<0>(m0a + (stg), a_, avo[0]); dma1<1024>(m0a + (stg), a_, avo[1]); dma1<2048>(m0a + (stg), a_, avo[2]); dma1<3072>(m0a + (stg), a_, avo[3]); \
        dma1<0>(m0w + (stg), w_, wvo[0]); dma1<1024>(m0w + (stg), w_, wvo[1]); dma1<2048>(m0w + (stg), w_, wvo[2]); dma1<3072>(m0w + (stg), w_, wvo[3]); \
    } while (0)
    unsigned pfd = 0;
    u32x2 fx = u32x2{};
#define K1_FXO() (FL_X + (2 * q16_0 + (flane() & 3)) * 8)
    STAMP(1);
    asm volatile("s_waitcnt vmcnt(10)\n\ts_barrier" ::: "memory");
    STAMP(2);
    if (K1PF) {
#pragma unroll
        for (int i = 0; i < 3; ++i)
            asm volatile("s_mov_b32 m0, %0\n\ts_nop 0\n\tglobal_load_lds_dword %1, %2" ::"s"(lds0 + 131072 + wid * 256), "v"(pfo[i]), "s"(pfb[i]) : "memory");
    }
    K1_RW(0, 0, 0); K1_RC(0, 0, 0, 0);
#define K1_RC1(buf, stg, s, half, rt) cf[buf][rt] = lds_frag(L + (stg) + aaddr[s] + ((half) * 4 + (rt)) * 2048)
#define K1_RW1(buf, stg, s, nt) wf[buf][nt] = tr_frag(L + (stg) + baddr[nt] + (s) * 16384, L + (stg) + baddr[nt] + (s) * 16384 + 2048)
#pragma unroll
    for (int kt = 0; kt < 8; ++kt) {
        const unsigned stg = (kt & 1) * 65536, nstg = stg ^ 65536;
        SBAR();
        k1_mma<VT>(acc, 0, cf[0], wf[0], [&](int m) { if (m < 8 && (m & 1) == 0) { K1_RC1(1, stg, 0, 1, m >> 1); SBAR(); } });
        SBAR();
        k1_mma<VT>(acc, 1, cf[1], wf[0], [&](int m) { if (m < 4) { K1_RC1(0, stg, 1, 0, m); SBAR(); } else if (m < 12 && (m & 1) == 0) { K1_RW1(1, stg, 1, (m - 4) >> 1); SBAR(); } });
        SBAR();
        k1_mma<VT>(acc, 0, cf[0], wf[1], [&](int m) { if (m < 8 && (m & 1) == 0) { K1_RC1(1, stg, 1, 1, m >> 1); SBAR(); } });
        SBAR();
        if (kt == 7) asm volatile("s_waitcnt lgkmcnt(0)\n\ts_barrier" ::: "memory");
        else if (kt == 0) asm volatile("s_waitcnt vmcnt(%0) lgkmcnt(0)\n\ts_barrier" ::"i"(K1PF) : "memory");
        else asm volatile("s_waitcnt vmcnt(0) lgkmcnt(0)\n\ts_barrier" : "+v"(fx)::"memory");
        if (kt == 0) flag_set(wid, flb, FL_X + blockIdx.x * 8, tag);
        if (kt == 6) flag_wait<false>(0, fx, flb, K1_FXO(), tag);
        const char* a_ = Aw + (kt + 2) * 128; const char* w_ = Ww + (size_t)(kt + 2) * 64 * 8192;
        k1_mma<VT>(acc, 1, cf[1], wf[1], [&](int m) {
            if (kt + 1 < 8) { if (m < 4) { K1_RW1(0, nstg, 0, m); SBAR(); } else if (m < 8) { K1_RC1(0, nstg, 0, 0, m - 4); SBAR(); } }
            if (kt + 2 < 8) {
                if (m == 1) { dma1<0>(m0a + stg, a_, avo[0]); SBAR(); } if (m == 3) { dma1<1024>(m0a + stg, a_, avo[1]); SBAR(); }
                if (m == 5) { dma1<2048>(m0a + stg, a_, avo[2]); SBAR(); } if (m == 7) { dma1<3072>(m0a + stg, a_, avo[3]); SBAR(); }
                if (m == 9) { dma1<0>(m0w + stg, w_, wvo[0]); SBAR(); } if (m == 11) { dma1<1024>(m0w + stg, w_, wvo[1]); SBAR(); }
                if (m == 13) { dma1<2048>(m0w + stg, w_, wvo[2]); SBAR(); } if (m == 15) { dma1<3072>(m0w + stg, w_, wvo[3]); SBAR(); }
            }
            if (kt == 5 && m == 15) {
                warm_load(pfd, wq, w1, blockIdx.x * 8 + wid);
                SBAR();
            }
            if (kt == 4 && m == 14) { flag_ld(fx, flb, K1_FXO()); SBAR(); }
            if (kt == 6 && m == 2) {
                const PJ j = pj_setup(wq, w1, ws, wid, head2, f0, nft4, q16_0); pj_dma(j, lds0, wid, 0, 0); pj_dma(j, lds0, wid, 1, PSLOT); SBAR();
            }
            if (kt == 7) {
                if (m == 0) {
                    const char* bp = uptr(b1 + f0 * 2);
                    const unsigned bo = (flane() >> 4) * 8, b2 = bo + (nft4 ? 64 : 0);
                    asm volatile("s_nop 4\n\tglobal_load_dwordx2 %0, %4, %6\n\tglobal_load_dwordx2 %1, %4, %6 offset:32\n\tglobal_load_dwordx2 %2, %5, %6\n\tglobal_load_dwordx2 %3, %5, %6 offset:32"
                                 : "=&v"(bb[0]), "=&v"(bb[1]), "=&v"(bb[2]), "=&v"(bb[3]) : "v"(bo), "v"(b2), "s"(bp) : "memory");
                    SBAR();
                }
                if (m == 4) { const PJ j = pj_setup(wq, w1, ws, wid, head2, f0, nft4, q16_0); pj_xload(j, xa, 0); pj_xload(j, xb, 1); SBAR(); }
            }
        });
        SBAR();
    }
    STAMP(3);
    const int head = 2 * hp + (wn >> 1), d0 = (wn & 1) * 64;
    const int key0 = 256 * rb + 128 * wm;
    const unsigned lane = flane(), c = lane & 15, g = lane >> 4;
    if (VT) {
        const unsigned lo = (g >> 1) * 1024 + (c + 32 * (g & 1)) * 16;
#pragma unroll
        for (int u = 0; u < 4; ++u)
#pragma unroll
            for (int nt = 0; nt < 4; ++nt) {
                const size_t off = ((size_t)(head * 64 + (key0 >> 5) + u) * 4 + (d0 >> 5) + (nt >> 1)) * 2048 + (nt & 1) * 256;
                st_wt_o(uptr(outp + off), lo, pack44(acc[2 * u][nt], acc[2 * u + 1][nt]));
            }
    } else {
        const unsigned lo = (g >> 1) * 1024 + ((c & 7) + 16 * (c >> 3) + 32 * (g & 1)) * 16;
#pragma unroll
        for (int kt = 0; kt < 8; ++kt)
#pragma unroll
            for (int v = 0; v < 2; ++v) {
                const size_t off = ((size_t)(head * 64 + (key0 >> 5) + (kt >> 1)) * 4 + (d0 >> 5) + v) * 2048 + (kt & 1) * 128;
                st_wt_o(uptr(outp + off), lo, pack44(acc[kt][2 * v], acc[kt][2 * v + 1]));
            }
    }
    STAMP(4);
    asm volatile("s_waitcnt vmcnt(16)\n\ts_barrier" : "+v"(pfd), "+v"(xa[0]), "+v"(xa[1]), "+v"(xa[2]), "+v"(xa[3]), "+v"(xb[0]), "+v"(xb[1]), "+v"(xb[2]), "+v"(xb[3]),
                 "+v"(bb[0]), "+v"(bb[1]), "+v"(bb[2]), "+v"(bb[3])::"memory");
    STAMP(5);
#undef K1_RC
#undef K1_RW
#undef K1_DMA
#undef K1_FXO
}

constexpr float QSCALE = 0.08838834764831845f * 1.4426950408889634f;
constexpr float THR = 8.0f;
DI float x4_max(float x) {
    auto s = __builtin_amdgcn_permlane16_swap(__float_as_uint(x), __float_as_uint(x), false, false);
    x = __builtin_fmaxf(__uint_as_float(s[0]), __uint_as_float(s[1]));
    auto t = __builtin_amdgcn_permlane32_swap(__float_as_uint(x), __float_as_uint(x), false, false);
    return __builtin_fmaxf(__uint_as_float(t[0]), __uint_as_float(t[1]));
}
DI float x4_sum(float x) {
    auto s = __builtin_amdgcn_permlane16_swap(__float_as_uint(x), __float_as_uint(x), false, false);
    x = __uint_as_float(s[0]) + __uint_as_float(s[1]);
    auto t = __builtin_amdgcn_permlane32_swap(__float_as_uint(x), __float_as_uint(x), false, false);
    return __uint_as_float(t[0]) + __uint_as_float(t[1]);
}
DI float mish_f(float h) {
    const float e = __builtin_amdgcn_exp2f(__builtin_fminf(h, 30.0f) * 1.4426950408889634f);
    const float n = e * (e + 2.0f);
    return h * n * __builtin_amdgcn_rcpf(n + 2.0f);
}

#define ABL_PJ 0
template <int NFT>
DI void proj_phase(const char* __restrict__ wq, const char* __restrict__ w1, char* __restrict__ ws, char* lds, int head, int f0, int q16_0, bf16x8 (&qf)[2][4],
                   const char* __restrict__ wo, const char* __restrict__ w2, unsigned m0v0, unsigned wid, u32x4 (&xa)[4], u32x4 (&xb)[4], const u32x2 (&bb)[4], u64 tag,
                   unsigned kvflag, int hp2, unsigned m0k0, unsigned m0k1, const char* kptr0, const char* kptr1) {
    const unsigned lane = flane(), g = lane >> 4;
    const unsigned lds0 = (unsigned)(uintptr_t)lds;
    lds_cptr L = (lds_cptr)lds;
    const PJ pj = pj_setup(wq, w1, ws, wid, head, f0, NFT == 4, q16_0);
    const char* flb = uptr(ws + WS_FL);
    u32x2 fkv = u32x2{};
    const unsigned fkvo = FL_KV + (hp2 * 16 + (lane & 15)) * 8;
    const unsigned q = (lane & 15) >> 2, p = lane & 3;
    const unsigned wqb = 2 * g * WQ_PIECE + q * 64 + p * 8;
    const unsigned w1b = W1_OFF + g * W1_PIECE + q * 64 + p * 8;
    f32x4 aq[8][2], ah[NFT][2];
#pragma unroll
    for (int i = 0; i < 8; ++i) { aq[i][0] = f32x4{}; aq[i][1] = f32x4{}; }
#pragma unroll
    for (int i = 0; i < NFT; ++i) { ah[i][0] = f32x4{}; ah[i][1] = f32x4{}; }
    constexpr int NT = 8 + NFT;
    bf16x8 wf[4];
#define PJ_RD(slot, s, n)                                                                                       \
    (((n) < 8) ? tr_frag(L + (slot) + wqb + (8 * (s)) * WQ_PIECE + ((n) >> 1) * 256 + ((n) & 1) * 32, L + (slot) + wqb + (8 * (s) + 1) * WQ_PIECE + ((n) >> 1) * 256 + ((n) & 1) * 32) \
               : tr_frag(L + (slot) + w1b + (4 * (s)) * W1_PIECE + (((n) - 8) >> 1) * 512 + ((n) & 1) * 32, L + (slot) + w1b + (4 * (s)) * W1_PIECE + 256 + (((n) - 8) >> 1) * 512 + ((n) & 1) * 32))
#pragma unroll
    for (int n = 0; n < 3; ++n) wf[n] = PJ_RD(0, 0, n);
#define PJ_CHUNK(c, XC, XN)                                                                                     \
    do {                                                                                                        \
        constexpr int slot_ = ((c) % 3) * PSLOT, nslot_ = (((c) + 1) % 3) * PSLOT, dslot_ = (((c) + 2) % 3) * PSLOT; \
        if ((c) == 4) flag_ld(fkv, flb, fkvo);                                                                  \
          \
        if ((c) == 5) flag_wait<true>(wid, fkv, flb, fkvo, tag);                                                \
        if (!(ABL_PJ & 2) && (c) >= 1 && (c) + 1 < 10) pj_xload(pj, XN, (c) + 1);                               \
        if (!(ABL_PJ & 4) && (c) + 2 < 10) pj_dma(pj, lds0, wid, (c) + 2, dslot_);                              \
        if ((c) == 9) {       \
            dma4(m0k0, kptr0, lane * 16); dma4(m0k1, kptr1, lane * 16);                                         \
            warm_lds(m0v0, wo, w2, blockIdx.x * 8 + wid);                                                       \
        }                                                                                                       \
        SBAR();                                                                                                 \
        _Pragma("unroll") for (int m = 0; m < 2 * NT; ++m) {                                                    \
            const int s = m / NT, n = m % NT;                                                                   \
            if (m == 2 * NT - 3 && (!(ABL_PJ & 8) || (c) == 1 || (c) >= 8)) {       \
                if ((c) == 0) asm volatile("s_waitcnt lgkmcnt(0)\n\ts_barrier" ::: "memory");                   \
                else if ((c) + 2 < 10) asm volatile("s_waitcnt vmcnt(3) lgkmcnt(0)\n\ts_barrier" : "+v"(XN[0]), "+v"(XN[1]), "+v"(XN[2]), "+v"(XN[3]), "+v"(fkv)::"memory"); \
                else if ((c) + 1 < 10) asm volatile("s_waitcnt vmcnt(0) lgkmcnt(0)\n\ts_barrier" : "+v"(XN[0]), "+v"(XN[1]), "+v"(XN[2]), "+v"(XN[3])::"memory"); \
                if ((c) == 1) flag_set(wid, flb, kvflag, tag);        \
            }                                                                                                   \
            const int m3 = m + 3;                                                                               \
            if (ABL_PJ & 1) { PIN(wf[m3 & 3]); }                                                                \
            else if (m3 < 2 * NT) wf[m3 & 3] = PJ_RD(slot_, m3 / NT, m3 % NT);                                  \
            else if ((c) + 1 < 10) wf[m3 & 3] = PJ_RD(nslot_, 0, m3 - 2 * NT);                                  \
            const bf16x8 x0 = __builtin_bit_cast(bf16x8, XC[2 * s]), x1 = __builtin_bit_cast(bf16x8, XC[2 * s + 1]); \
            if (n < 8) { aq[n & 7][0] = MFMA(wf[m & 3], x0, aq[n & 7][0]); aq[n & 7][1] = MFMA(wf[m & 3], x1, aq[n & 7][1]); } \
            else { ah[(n - 8) % NFT][0] = MFMA(wf[m & 3], x0, ah[(n - 8) % NFT][0]); ah[(n - 8) % NFT][1] = MFMA(wf[m & 3], x1, ah[(n - 8) % NFT][1]); } \
            SBAR();                                                                                             \
        }                                                                                                       \
    } while (0)
    static_assert((2 * NT) % 4 == 0, "fragment window");
    PJ_CHUNK(0, xa, xb); PJ_CHUNK(1, xb, xa); PJ_CHUNK(2, xa, xb); PJ_CHUNK(3, xb, xa); PJ_CHUNK(4, xa, xb);
    PJ_CHUNK(5, xb, xa); PJ_CHUNK(6, xa, xb); PJ_CHUNK(7, xb, xa); PJ_CHUNK(8, xa, xb); PJ_CHUNK(9, xb, xa);
#undef PJ_CHUNK
#undef PJ_RD
    asm volatile("s_waitcnt vmcnt(1)" ::: "memory");
#pragma unroll
    for (int a = 0; a < 2; ++a)
#pragma unroll
        for (int v = 0; v < 4; ++v) qf[a][v] = __builtin_bit_cast(bf16x8, pack44s(aq[2 * v][a], aq[2 * v + 1][a], QSCALE));
#pragma unroll
    for (int v = 0; v < NFT / 2; ++v) {
        float bias[8];
#pragma unroll
        for (int t = 0; t < 2; ++t) {
            const u32x2 q2 = bb[2 * v + t];
            bias[4 * t + 0] = bflo(q2[0]); bias[4 * t + 1] = bfhi(q2[0]); bias[4 * t + 2] = bflo(q2[1]); bias[4 * t + 3] = bfhi(q2[1]);
        }
#pragma unroll
        for (int a = 0; a < 2; ++a) {
            f32x4 h0, h1;
#pragma unroll
            for (int i = 0; i < 4; ++i) { h0[i] = mish_f(ah[2 * v][a][i] + bias[i]); h1[i] = mish_f(ah[2 * v + 1][a][i] + bias[4 + i]); }
            st_wt_o(uptr(ws + WS_CH + ((size_t)(q16_0 + a) * NKS_CH + NKS_C + (f0 >> 5) + v) * 1024), lane * 16, pack44(h0, h1));
        }
    }
}

#define NOP_SO(S, o) asm volatile("s_nop 7\n\ts_nop 3" : "+v"(S[0]), "+v"(S[1]), "+v"(o[0]), "+v"(o[1]), "+v"(o[2]), "+v"(o[3]))
#define NOP_A(A) asm volatile("s_nop 7\n\ts_nop 1" : "+v"(A[0][0]), "+v"(A[0][1]), "+v"(A[0][2]), "+v"(A[0][3]), "+v"(A[1][0]), "+v"(A[1][1]), "+v"(A[1][2]), "+v"(A[1][3]), \
    "+v"(A[2][0]), "+v"(A[2][1]), "+v"(A[2][2]), "+v"(A[2][3]), "+v"(A[3][0]), "+v"(A[3][1]), "+v"(A[3][2]), "+v"(A[3][3]), "+v"(A[4][0]), "+v"(A[4][1]), "+v"(A[4][2]), "+v"(A[4][3]))
DI float xh_sum(float v) { auto r = __builtin_amdgcn_permlane32_swap(__float_as_uint(v), __float_as_uint(v), false, false); return __uint_as_float(r[0]) + __uint_as_float(r[1]); }
DI float xh_max(float v) { auto r = __builtin_amdgcn_permlane32_swap(__float_as_uint(v), __float_as_uint(v), false, false); return __builtin_fmaxf(__uint_as_float(r[0]), __uint_as_float(r[1])); }
DI float mul_s(float a, float b) { float r; asm("v_mul_f32 %0, %1, %2" : "=v"(r) : "v"(a), "v"(b)); return r; }
DI float sub_s(float a, float b) { float r; asm("v_sub_f32 %0, %1, %2" : "=v"(r) : "v"(a), "v"(b)); return r; }
DI u32x4 pack_o(const f32x16& a, int y, float sc) {
    u32x4 w; w[0] = cvtpk(mul_s(a[4 * y + 0], sc), mul_s(a[4 * y + 1], sc)); w[1] = cvtpk(mul_s(a[4 * y + 2], sc), mul_s(a[4 * y + 3], sc));
    w[2] = cvtpk(mul_s(a[8 + 4 * y + 0], sc), mul_s(a[8 + 4 * y + 1], sc)); w[3] = cvtpk(mul_s(a[8 + 4 * y + 2], sc), mul_s(a[8 + 4 * y + 3], sc)); return w;
}
DI void mask_diag(f32x16& s, int dq) {
    const float ninf = -__builtin_inff();
#pragma unroll
    for (int r0 = 0; r0 < 16; r0 += 4) {
        unsigned long long m0, m1, m2, m3;
        const int kc = 16 * ((r0 >> 2) & 1) + 8 * (r0 >> 3);
        float a = s[r0], b = s[r0 + 1], c = s[r0 + 2], d = s[r0 + 3];
        asm("v_cmp_gt_i32_e64 %4, %9, %8\n\tv_cmp_gt_i32_e64 %5, %10, %8\n\tv_cmp_gt_i32_e64 %6, %11, %8\n\tv_cmp_gt_i32_e64 %7, %12, %8\n\t"
            "v_cndmask_b32_e64 %0, %0, %13, %4\n\tv_cndmask_b32_e64 %1, %1, %13, %5\n\tv_cndmask_b32_e64 %2, %2, %13, %6\n\tv_cndmask_b32_e64 %3, %3, %13, %7"
            : "+v"(a), "+v"(b), "+v"(c), "+v"(d), "=&s"(m0), "=&s"(m1), "=&s"(m2), "=&s"(m3)
            : "v"(dq), "i"(kc), "i"(kc + 1), "i"(kc + 2), "i"(kc + 3), "v"(ninf));
        s[r0] = a; s[r0 + 1] = b; s[r0 + 2] = c; s[r0 + 3] = d;
    }
}
#define FRW 8
#define FRA 6

#define ABL_K3 0
constexpr int K3_SLOT = 36864;
constexpr int K3_RED = 2 * K3_SLOT;
constexpr int PP_SET = 8 * 5 * 1024;
DI float silu_f(float v) { return v * __builtin_amdgcn_rcpf(1.0f + __builtin_amdgcn_exp2f(-1.4426950408889634f * v)); }
DI f32x4 unpk_lo(const u32x4& w) { return f32x4{bflo(w[0]), bfhi(w[0]), bflo(w[1]), bfhi(w[1])}; }
DI f32x4 unpk_hi(const u32x4& w) { return f32x4{bflo(w[2]), bfhi(w[2]), bflo(w[3]), bfhi(w[3])}; }
template <int OFF> DI void dma_sc1(const void* sbase, unsigned voff) { asm volatile("global_load_lds_dwordx4 %0, %1 offset:%2 sc1" ::"v"(voff), "s"(sbase), "i"(OFF) : "memory"); }

struct K3S { const char *aptr, *wptr; unsigned avo, wvo[3], m0a, m0w; };
DI K3S k3_setup(unsigned lds0, unsigned wid, unsigned lane) {
    K3S k;
    k.aptr = nullptr; k.wptr = nullptr;
    k.avo = lane * 16;
    const unsigned p0 = wid >= 4 ? 12 + 2 * (wid & 3) : 3 * wid;
#pragma unroll
    for (int i = 0; i < 3; ++i) {
        const unsigned o = (p0 + i) * 1024 + lane * 16, row = o / 320, c16 = (o % 320) >> 4;
        k.wvo[i] = row * 1280 + ((c16 ^ (((row >> 2) & 1) << 1)) << 4) - 1024 * i;
    }
    k.m0a = lds0 + wid * 2048; k.m0w = lds0 + 16384 + p0 * 1024;
    return k;
}
DI void k3_point(K3S& k, const char* ws, const char* w, unsigned wid, int qt, int mt, int kstep, int stage) {
    k.aptr = uptr(ws + WS_CH + ((size_t)(8 * qt + wid) * NKS_CH + kstep) * 1024);
    k.wptr = uptr(w + (size_t)stage * 64 * 1280 + mt * 320);
    asm volatile("s_nop 4" : "+s"(k.aptr), "+s"(k.wptr));
}
DI void k3_stage(K3S& k, unsigned slot, bool three) {
    set_m0(k.m0a + slot); asm volatile("s_nop 0"); dma_sc1<0>(k.aptr, k.avo); dma_sc1<1024>(k.aptr, k.avo);
    set_m0(k.m0w + slot); asm volatile("s_nop 0"); dma_nom0<0>(k.wptr, k.wvo[0]); dma_nom0<1024>(k.wptr, k.wvo[1]);
    if (three) dma_nom0<2048>(k.wptr, k.wvo[2]);
    k.aptr += 2048; k.wptr += 81920;
}
DI void k3_publish(const f32x4 (&res)[10], f32x4 (&own)[2][2], const char* pset, unsigned vo, unsigned r) {
    own[1][0] = f32x4{}; own[1][1] = f32x4{};
#pragma unroll
    for (int j = 0; j < 5; ++j) {
        if ((j & 3) == r) { own[j >> 2][0] = res[2 * j]; own[j >> 2][1] = res[2 * j + 1]; }
        else st_wt_o(pset + j * 1024, vo, pack44(res[2 * j], res[2 * j + 1]));
    }
}

template <int GRP>
DI void k3_body(K3S& k, const char* __restrict__ x, const char* __restrict__ wo, char* __restrict__ ws, char* __restrict__ out, char* lds, unsigned wid, u64 tag,
                int ks, int qt, int mt, unsigned cflag) {
    constexpr int P = GRP ? 4 : 5;
    const unsigned lane = flane(), c = lane & 15, g = lane >> 4;
    const unsigned w4 = wid & 3, qh = w4 >> 1, mh = w4 & 1;
    char* stamp_base = ws; (void)stamp_base;
    const char* flb = uptr(ws + WS_FL);
    lds_cptr L = (lds_cptr)lds;
    const unsigned ard = (8 * qh + GRP) * 1024 + lane * 16;
    const unsigned q = (lane & 15) >> 2, p = lane & 3;
    const unsigned wrow = 16384 + (32 * GRP + 4 * g + q) * 320 + 160 * mh + 8 * p;
    const unsigned sgn = ((g & 1) ? 32u : 0u);
    const unsigned wrdE = (mh & 1) ? wrow - sgn : wrow + sgn;
    const unsigned wrdO = (mh & 1) ? wrow + sgn : wrow - sgn;
    f32x4 acc[5][4];
    bf16x8 af[2][4], wf[4], w4f[2];
    unsigned cur;
#define K3_RW(slot, n) tr_frag(L + (slot) + (((n) & 1) ? wrdO : wrdE) + 32 * (n), L + (slot) + (((n) & 1) ? wrdO : wrdE) + 32 * (n) + 16 * 320)
#define K3_RA(slot, a) lds_frag(L + (slot) + ard + (a) * 2048)
#define K3_ZERO() do { _Pragma("unroll") for (int n = 0; n < 5; ++n) _Pragma("unroll") for (int a = 0; a < 4; ++a) acc[n][a] = f32x4{}; NOP_A(acc); cur = 0; } while (0)
#define K3_ITER(PAR, WN, RD, DM, SAFE, HOOK)                                                                            \
    do {                                                                                                                \
        if (ABL_K3 & 1) asm volatile("s_waitcnt vmcnt(%0) lgkmcnt(0)" ::"i"(WN) : "memory");                            \
        else if (ABL_K3 & 8) asm volatile("s_waitcnt lgkmcnt(0)\n\ts_barrier" ::: "memory");                            \
        else asm volatile("s_waitcnt vmcnt(%0) lgkmcnt(0)\n\ts_barrier" ::"i"(WN) : "memory");                          \
        HOOK;                                                                                                           \
        const unsigned nxt_ = cur == 3 * K3_SLOT ? 0u : cur + K3_SLOT;                                                  \
        _Pragma("unroll") for (int n = 0; n < 5; ++n) {                                                                 \
            _Pragma("unroll") for (int a = 0; a < 4; ++a) {                                                             \
                if (SAFE) mfma_acc_s(acc[n][a], n < 4 ? wf[n & 3] : w4f[PAR], af[PAR][a]);                              \
                else mfma_acc(acc[n][a], n < 4 ? wf[n & 3] : w4f[PAR], af[PAR][a]);                                     \
                SBAR();                                                                                                 \
                if (RD && !(ABL_K3 & 2)) {                                                                              \
                    if (n == 0 && a == 0) w4f[1 - (PAR)] = K3_RW(nxt_, 4);                                              \
                    if (n < 4 && a == 1) af[1 - (PAR)][n & 3] = K3_RA(nxt_, n & 3);                                     \
                    if (n >= 1 && a == 0) wf[(n - 1) & 3] = K3_RW(nxt_, n - 1);                                         \
                }                                                                                                       \
                if (DM && !(ABL_K3 & 4)) {                                                                              \
                    if (n == 0 && a == 2) set_m0(k.m0a + cur);                                                          \
                    if (n == 0 && a == 3) dma_sc1<0>(k.aptr, k.avo);                                                    \
                    if (n == 1 && a == 2) dma_sc1<1024>(k.aptr, k.avo);                                                 \
                    if (n == 1 && a == 3) set_m0(k.m0w + cur);                                                          \
                    if (n == 2 && a == 2) dma_nom0<0>(k.wptr, k.wvo[0]);                                                \
                    if (n == 3 && a == 2) dma_nom0<1024>(k.wptr, k.wvo[1]);                                             \
                    if (n == 4 && a == 1 && !GRP) dma_nom0<2048>(k.wptr, k.wvo[2]);                                     \
                }                                                                                                       \
                SBAR();                                                                                                 \
            }                                                                                                           \
        }                                                                                                               \
        if (DM) { k.aptr += 2048; k.wptr += 81920; }                                                                    \
        cur = nxt_;                                                                                                     \
    } while (0)
#define K3_FIRST(PAR) do { _Pragma("unroll") for (int a = 0; a < 4; ++a) af[PAR][a] = K3_RA(0, a);                      \
        _Pragma("unroll") for (int n = 0; n < 4; ++n) wf[n] = K3_RW(0, n);                                              \
        w4f[PAR] = K3_RW(0, 4); } while (0)
#define K3_REDUCE(res)                                                                                                  \
    do {                                                                                                                \
        NOP_A(acc);                                                                                                     \
        lds_ptr X_ = (lds_ptr)lds + K3_RED + wid * 10240 + lane * 16;                                                   \
        lds_cptr Y_ = (lds_cptr)lds + K3_RED + (wid ^ 4) * 10240 + lane * 16;                                           \
        _Pragma("unroll") for (int a = 0; a < 2; ++a) _Pragma("unroll") for (int n = 0; n < 5; ++n)                     \
            *(__attribute__((address_space(3))) f32x4*)(X_ + (a * 5 + n) * 1024) = acc[n][GRP ? a : 2 + a];             \
        asm volatile("s_waitcnt lgkmcnt(0)\n\ts_barrier" ::: "memory");                                                 \
        _Pragma("unroll") for (int a = 0; a < 2; ++a) _Pragma("unroll") for (int n = 0; n < 5; ++n)                     \
            res[a * 5 + n] = acc[n][GRP ? 2 + a : a] + *(const __attribute__((address_space(3))) f32x4*)(Y_ + (a * 5 + n) * 1024); \
    } while (0)
#define NOHOOK (void)0
    const int grp64 = qt * 4 + mt;
    const char* pbase = uptr(ws + WS_PP + (size_t)grp64 * (8 * PP_SET) + wid * 5120);
    const unsigned vo = lane * 16;
    const unsigned r = (ks - wid) & 3;
    f32x4 res[10], ownB[2][2], ownS[2][2];
    asm volatile("s_waitcnt vmcnt(%0) lgkmcnt(0)\n\ts_barrier" ::"i"(P + 4) : "memory");
    STAMP(10);
    k3_stage(k, 2 * K3_SLOT, !GRP); k3_stage(k, 3 * K3_SLOT, !GRP);
    K3_ZERO();
    if (ks != 3) { K3_FIRST(1); K3_ITER(1, 2 * P + 4, true, true, true, NOHOOK); }
    else K3_FIRST(0);
    K3_ITER(0, 2 * P, true, true, true, flag_set(wid, flb, cflag, tag));
    K3_ITER(1, 2 * P, true, true, false, NOHOOK);
    K3_ITER(0, 2 * P, true, false, false, NOHOOK); K3_ITER(1, P, true, false, false, NOHOOK); K3_ITER(0, 0, true, false, false, NOHOOK);
    STAMP(11);
    {
        u32x2 f = u32x2{~(unsigned)tag, 0u};
        flag_wait<false>(wid, f, flb, FL_C + ((qt < 8 ? qt : 15 - qt) * 32 + 8 * ks + (lane & 7)) * 8, tag);
    }
    STAMP(18);
    k3_point(k, ws, wo, wid, qt, mt, 32 * ks, 16 * ks);
    K3_ITER(1, 0, false, false, false, (k3_stage(k, 0, !GRP), k3_stage(k, K3_SLOT, !GRP)));
    K3_REDUCE(res);
    asm volatile("s_waitcnt vmcnt(%0) lgkmcnt(0)\n\ts_barrier" ::"i"(P) : "memory");
    STAMP(19);
    k3_stage(k, 2 * K3_SLOT, !GRP); k3_stage(k, 3 * K3_SLOT, !GRP);
    k3_publish(res, ownB, pbase + (4 + ks) * PP_SET, vo, r);
    K3_ZERO();
    K3_FIRST(0);
    K3_ITER(0, 2 * P + 3, true, true, true, NOHOOK); K3_ITER(1, 2 * P + 3, true, true, false, NOHOOK);
    for (int it = 0; it < 5; ++it) { K3_ITER(0, 2 * P, true, true, false, NOHOOK); K3_ITER(1, 2 * P, true, true, false, NOHOOK); }
    u32x2 xv[2][2];
    unsigned oo[2][2];
#pragma unroll
    for (int sl = 0; sl < 2; ++sl)
#pragma unroll
        for (int t = 0; t < 2; ++t) {
            const unsigned i = 2 * (sl ? 4 : r) + t, a = i >= 5 ? 1 : 0, n = i - 5 * a;
            oo[sl][t] = (16 * (8 * qt + 4 * qh + 2 * GRP + a) + c) * (DM * 2) + (16 * (10 * mt + 5 * mh + n) + 4 * g) * 2;
        }
#define K3_XLD() asm volatile("global_load_dwordx2 %0, %4, %8\n\tglobal_load_dwordx2 %1, %5, %8\n\tglobal_load_dwordx2 %2, %6, %8\n\tglobal_load_dwordx2 %3, %7, %8" \
        : "=&v"(xv[0][0]), "=&v"(xv[0][1]), "=&v"(xv[1][0]), "=&v"(xv[1][1]) : "v"(oo[0][0]), "v"(oo[0][1]), "v"(oo[1][0]), "v"(oo[1][1]), "s"(x) : "memory")
    K3_ITER(0, 2 * P, true, false, false, NOHOOK); K3_ITER(1, P, true, false, false, NOHOOK); K3_ITER(0, 0, true, false, false, K3_XLD()); K3_ITER(1, 0, false, false, false, NOHOOK);
    STAMP(12);
    K3_REDUCE(res);
    STAMP(17);
    k3_publish(res, ownS, pbase + ks * PP_SET, vo, r);
    u32x2 fe[4];
    const bool bump = blockIdx.x == 0 && wid == 0;
    if (bump) {
#pragma unroll
        for (int i = 0; i < 4; ++i) flag_ld(fe[i], flb, FL_X + (64 * i + lane) * 8);
    }
    STAMP(14);
    asm volatile("s_waitcnt vmcnt(0)\n\ts_barrier" : "+v"(xv[0][0]), "+v"(xv[0][1]), "+v"(xv[1][0]), "+v"(xv[1][1]), "+v"(fe[0]), "+v"(fe[1]), "+v"(fe[2]), "+v"(fe[3])::"memory");
    STAMP(15);
    flag_set(wid, flb, FL_S + (grp64 * 4 + ks) * 128, tag);
    {
        u32x2 f = u32x2{~(unsigned)tag, 0u};
        const unsigned o3 = (lane & 3) == (unsigned)ks ? (ks + 1) & 3 : lane & 3;
        flag_wait<false>(0, f, flb, FL_S + (grp64 * 4 + o3) * 128, tag);
    }
    STAMP(16);
    const int nown = r == 0 ? 2 : 1;
    u32x4 pp[2][6];
#pragma unroll
    for (int sl = 0; sl < 2; ++sl)
        if (sl < nown) {
            const char* pj = pbase + (sl ? 4 : r) * 1024;
#pragma unroll
            for (int i = 0; i < 3; ++i) { const unsigned st = (ks + 1 + i) & 3; ld_wt(pp[sl][i], pj + st * PP_SET, vo); ld_wt(pp[sl][3 + i], pj + (4 + st) * PP_SET, vo); }
        }
    asm volatile("s_waitcnt vmcnt(0)" : "+v"(pp[0][0]), "+v"(pp[0][1]), "+v"(pp[0][2]), "+v"(pp[0][3]), "+v"(pp[0][4]), "+v"(pp[0][5]),
                 "+v"(pp[1][0]), "+v"(pp[1][1]), "+v"(pp[1][2]), "+v"(pp[1][3]), "+v"(pp[1][4]), "+v"(pp[1][5])::"memory");
#pragma unroll
    for (int sl = 0; sl < 2; ++sl)
        if (sl < nown) {
#pragma unroll
            for (int t = 0; t < 2; ++t) {
                f32x4 sv = ownS[sl][t], bv = ownB[sl][t];
#pragma unroll
                for (int i = 0; i < 3; ++i) { sv += t ? unpk_hi(pp[sl][i]) : unpk_lo(pp[sl][i]); bv += t ? unpk_hi(pp[sl][3 + i]) : unpk_lo(pp[sl][3 + i]); }
                const float o0 = bflo(xv[sl][t][0]) + sv[0] * silu_f(bv[0]), o1 = bfhi(xv[sl][t][0]) + sv[1] * silu_f(bv[1]);
                const float o2 = bflo(xv[sl][t][1]) + sv[2] * silu_f(bv[2]), o3 = bfhi(xv[sl][t][1]) + sv[3] * silu_f(bv[3]);
                u32x2 w; w[0] = cvtpk(o0, o1); w[1] = cvtpk(o2, o3);
                *(u32x2*)(out + oo[sl][t]) = w;
            }
        }
    if (bump) {
#pragma unroll
        for (int i = 0; i < 4; ++i) flag_wait<false>(0, fe[i], flb, FL_X + (64 * i + lane) * 8, tag);
        flag_set(0, flb, FL_EPOCH, tag);
    }
    STAMP(13);
#undef K3_RW
#undef K3_RA
#undef K3_ZERO
#undef K3_ITER
#undef K3_FIRST
#undef K3_REDUCE
#undef K3_XLD
#undef NOHOOK
}

__global__ __launch_bounds__(512, 2) void fused(const char* __restrict__ x, const char* __restrict__ ckv, const char* __restrict__ wuk, const char* __restrict__ wuv,
                                              const char* __restrict__ wq, const char* __restrict__ w1, const char* __restrict__ b1, char* __restrict__ ws,
                                              const char* __restrict__ wo, const char* __restrict__ w2, char* __restrict__ out) {
    extern __shared__ __attribute__((aligned(16))) char lds[];
    const unsigned wid = rfl(threadIdx.x >> 6), grp = wid >> 2, w4 = wid & 3;
    char* stamp_base = ws; (void)stamp_base;
    STAMP(0);
    const int b = blockIdx.x, xcd = b & 7, idx = b >> 3;
    const int vt = idx & 1, hp = 2 * xcd + ((idx >> 1) & 1), rb = idx >> 2;
    const int head = 4 * xcd + (idx & 3), pi = idx >> 2;
    const int HB = 15 - pi, LB = pi;
    const int myblk = grp ? LB : HB;
    const int q16_0 = 8 * myblk + 2 * w4;
    const bool nft4 = head < 22;
    const int f0 = nft4 ? 64 * head : 1408 + 32 * (head - 22);
    const char* Abase = ckv + (size_t)rb * 256 * 1024;
    const char* Wbase = (vt ? wuv : wuk) + hp * 512;
    const unsigned lane0 = flane();
#define lane lane0
    const int row = 8 * b + wid;
    const unsigned l40 = lane < 40 ? lane : 39;
    u32x4 x0, x1;
    {
        const char* xr = uptr(x + (size_t)row * 1280);
        const unsigned vo = l40 * 32;
        asm volatile("global_load_dwordx4 %0, %2, %3\n\tglobal_load_dwordx4 %1, %2, %3 offset:16" : "=&v"(x0), "=&v"(x1) : "v"(vo), "s"(xr) : "memory");
    }
    unsigned avo[4], wvo[4];
    {
        const unsigned lds0 = (unsigned)(uintptr_t)lds;
        const char* Aw = Abase + (size_t)wid * 32 * 1024;
        const char* Ww = Wbase + (size_t)wid * 8 * 8192;
        const unsigned m0a = lds0 + wid * 4096, m0w = lds0 + 32768 + wid * 4096;
#pragma unroll
        for (int p = 0; p < 4; ++p) {
            const unsigned rw = 8 * p + (lane >> 3);
            avo[p] = rw * 1024 + (((lane & 7) ^ ((rw >> 1) & 7)) << 4) - 1024 * p;
            const unsigned k = 2 * p + (lane >> 5);
            const unsigned f = (k & 3) | ((wid & 1) << 2);
            wvo[p] = k * 8192 + (((lane & 31) ^ (f << 1)) << 4) - 1024 * p;
        }
#pragma unroll
        for (int kt = 0; kt < 2; ++kt) {
            const char* a_ = Aw + kt * 128; const char* w_ = Ww + (size_t)kt * 64 * 8192; const unsigned stg = kt * 65536;
            dma1<0>(m0a + stg, a_, avo[0]); dma1<1024>(m0a + stg, a_, avo[1]); dma1<2048>(m0a + stg, a_, avo[2]); dma1<3072>(m0a + stg, a_, avo[3]);
            dma1<0>(m0w + stg, w_, wvo[0]); dma1<1024>(m0w + stg, w_, wvo[1]); dma1<2048>(m0w + stg, w_, wvo[2]); dma1<3072>(m0w + stg, w_, wvo[3]);
        }
    }
    u64 tag;
    asm volatile("s_load_dwordx2 %0, %1, 0x0\n\ts_waitcnt lgkmcnt(0)" : "=s"(tag) : "s"(uptr(ws + WS_FL + FL_EPOCH)) : "memory");
    tag += 1;
    {
        asm volatile("s_waitcnt vmcnt(16)" : "+v"(x0), "+v"(x1)::"memory");
        float v[16];
#pragma unroll
        for (int i = 0; i < 4; ++i) { v[2 * i] = bflo(x0[i]); v[2 * i + 1] = bfhi(x0[i]); v[8 + 2 * i] = bflo(x1[i]); v[8 + 2 * i + 1] = bfhi(x1[i]); }
        float s = 0.f;
#pragma unroll
        for (int i = 0; i < 16; ++i) s += v[i];
        if (lane >= 40) s = 0.f;
#pragma unroll
        for (int o = 32; o >= 1; o >>= 1) s += __shfl_xor(s, o);
        const float mean = s * (1.0f / DM);
        float q = 0.f;
#pragma unroll
        for (int i = 0; i < 16; ++i) { v[i] -= mean; q += v[i] * v[i]; }
        if (lane >= 40) q = 0.f;
#pragma unroll
        for (int o = 32; o >= 1; o >>= 1) q += __shfl_xor(q, o);
        const float rstd = 1.0f / sqrtf(q * (1.0f / DM) + 1e-5f);
        u32x4 o0, o1;
#pragma unroll
        for (int i = 0; i < 4; ++i) { o0[i] = cvtpk(v[2 * i] * rstd, v[2 * i + 1] * rstd); o1[i] = cvtpk(v[8 + 2 * i] * rstd, v[8 + 2 * i + 1] * rstd); }
        char* xf = ws + WS_XF + ((size_t)(row >> 4) * NKS_X + (l40 >> 1)) * 1024 + ((row & 15) + 32 * (l40 & 1)) * 16;
        if (lane < 40) { st_wt(xf, o0); st_wt(xf + 256, o1); }
    }
    const char* pfb[3];
    unsigned pfo[3];
#pragma unroll
    for (int i = 0; i < 3; ++i) {
        unsigned p = (idx * 8 + wid) * 3 + i;
        if (p >= 576) p -= 192;
        if (p < 192) {
            const unsigned combo = p / 48, r8 = p % 48;
            pfb[i] = uptr(((combo & 1) ? wuv : wuk) + (size_t)(128 + 8 * r8) * 8192 + (2 * xcd + (combo >> 1)) * 512);
            pfo[i] = (lane >> 3) * 8192 + (lane & 7) * 64;
        } else {
            const unsigned l = (p - 192) * 64 + lane, rw = l / 12;
            pfb[i] = uptr(ckv + 256);
            pfo[i] = rw * 1024 + (l - rw * 12) * 64;
        }
    }
#undef lane
    u32x4 xa[4], xb[4];
    u32x2 bb[4];
    if (vt) k1_tile<true>(Abase, Wbase, ws + WS_VF, lds, avo, wvo, wid, hp, rb, ws, wq, w1, b1, tag, head, f0, nft4, q16_0, xa, xb, bb, pfb, pfo);
    else    k1_tile<false>(Abase, Wbase, ws + WS_KF, lds, avo, wvo, wid, hp, rb, ws, wq, w1, b1, tag, head, f0, nft4, q16_0, xa, xb, bb, pfb, pfo);
    STAMP(6);
    const unsigned lds0 = (unsigned)(uintptr_t)lds;
    const unsigned kslot0 = grp ? 114688 : 81920, kslot1 = kslot0 + 16384, kslot2 = grp ? 16384 : 0;
    const unsigned vslot0 = grp ? 147456 : 32768, vslot1 = grp ? 65536 : 49152;
    const int nL = 2 * pi + 2;
    const int nsw = grp ? nL : 1000;
    const size_t jump = (size_t)(17 - nL) * 16384;
    const char* kptr = uptr(ws + WS_KF + (size_t)head * 524288 + w4 * 4096);
    const char* vptr = uptr(ws + WS_VF + (size_t)head * 524288 + w4 * 4096);
    bf16x8 qf[8];
    {
        bf16x8 q16[2][4];
        const unsigned kvflag = FL_KV + (hp * 16 + vt * 8 + rb) * 8;
        if (nft4) proj_phase<4>(wq, w1, ws, lds, head, f0, q16_0, q16, wo, w2, lds0 + vslot0 + w4 * 4096, wid, xa, xb, bb, tag, kvflag, head >> 1, lds0 + kslot0 + w4 * 4096, lds0 + kslot1 + w4 * 4096, kptr, kptr + 16384);
        else      proj_phase<2>(wq, w1, ws, lds, head, f0, q16_0, q16, wo, w2, lds0 + vslot0 + w4 * 4096, wid, xa, xb, bb, tag, kvflag, head >> 1, lds0 + kslot0 + w4 * 4096, lds0 + kslot1 + w4 * 4096, kptr, kptr + 16384);
        kptr += 2 * 16384; if (nsw == 2) kptr += jump;
#pragma unroll
        for (int v = 0; v < 4; ++v) {
            const u32x4 r0 = __builtin_bit_cast(u32x4, q16[0][v]), r1 = __builtin_bit_cast(u32x4, q16[1][v]);
            u32x4 z0, z1;
#pragma unroll
            for (int w = 0; w < 4; ++w) {
                const auto s1 = __builtin_amdgcn_permlane16_swap(r0[w], r1[w], false, false);
                const auto s2 = __builtin_amdgcn_permlane32_swap(s1[0], s1[1], false, false);
                z0[w] = s2[0]; z1[w] = s2[1];
            }
            qf[2 * v] = __builtin_bit_cast(bf16x8, z0); qf[2 * v + 1] = __builtin_bit_cast(bf16x8, z1);
        }
    }
    STAMP(7);
    const unsigned lane = flane();
    if (grp == 0) {
        char* qh = ws + WS_QH + (size_t)b * 32768 + w4 * 8192 + lane * 16;
#pragma unroll
        for (int s = 0; s < 8; ++s) *(bf16x8*)(qh + s * 1024) = qf[s];
    }
    const char* flb = uptr(ws + WS_FL);
    lds_cptr Lb = (lds_cptr)lds + lane * 16;
    const unsigned m0b = lds0 + w4 * 4096;
    const unsigned dvo = lane * 16;
    int q0 = 128 * myblk + 32 * w4;
    int tile = 0;
    asm volatile("s_waitcnt lgkmcnt(0)\n\ts_barrier" ::: "memory");
    dma4(m0b + vslot0, vptr, dvo); vptr += 16384;
    dma4(m0b + kslot2, kptr, dvo); kptr += 16384;
    f32x16 o[4], S[2], negm;
    float mref = 0.f, lsum = 0.f, lfin = 1.f;
    bf16x8 pf[4], fr[FRW];
#pragma unroll
    for (int i = 0; i < 4; ++i) o[i] = f32x16{};
    negm = f32x16{}; S[0] = f32x16{}; S[1] = f32x16{};
    asm volatile("s_nop 1" : "+v"(negm));
    NOP_SO(S, o);
    unsigned ks_cur = kslot0, ks_nxt = kslot1, ks_nn = kslot2;
#define CTX_VO(l) ((((l) >> 4) & 1) * (NKS_CH * 1024) + (((l) & 15) + 16 * ((l) >> 5)) * 16)
#define KOFF(i) (((i) & 1) * 8192 + ((i) >> 1) * 1024)
#define VOFF(j) (((j) >> 3) * 8192 + ((j) & 3) * 2048 + (((j) >> 2) & 1) * 1024)
#define EARLY_K(slot) do { _Pragma("unroll") for (int i = 0; i < FRA; ++i) fr[i] = lds_frag(Lb + (slot) + KOFF(i)); } while (0)
    EARLY_K(kslot0);
    if (grp) asm volatile("s_barrier" ::: "memory");
#define PHASE_M(t, QK, PV, VS)                                                                              \
    do {                                                                                                                              \
        __builtin_amdgcn_s_setprio(0);                                                                                                \
        lds_cptr kp_ = Lb + ks_cur; lds_cptr vp_ = Lb + ((VS) ? vslot1 : vslot0);                                                     \
        if (!(QK)) { _Pragma("unroll") for (int i = 16; i < 16 + FRA; ++i) fr[i % FRW] = lds_frag(vp_ + VOFF(i - 16)); }               \
        _Pragma("unroll") for (int i = (QK) ? 0 : 16; i < ((PV) ? 32 : 16); ++i) {                                                    \
            const int n = i + FRA;                                                                                                    \
            if (n < 16) fr[n % FRW] = lds_frag(kp_ + KOFF(n)); else if ((PV) && n < 32) fr[n % FRW] = lds_frag(vp_ + VOFF(n - 16));    \
            SBAR();                                                                                                                   \
            if (i < 2) mfma32_ini(S[i & 1], fr[i % FRW], qf[i >> 1], negm);                                                        \
            else if (i < 16) mfma32_acc(S[i & 1], fr[i % FRW], qf[i >> 1]);                                                             \
            else mfma32_acc(o[i & 3], fr[i % FRW], pf[(i - 16) >> 2]);                                                                  \
            SBAR();                                                                                                                   \
            if ((QK) && i == 16 && (t) + 1 == nsw) {        \
                const char* qs_ = uptr(ws + WS_QH + (size_t)b * 32768 + w4 * 8192);                                                   \
                asm volatile("s_nop 4\n\tglobal_load_dwordx4 %0, %8, %9\n\tglobal_load_dwordx4 %1, %8, %9 offset:1024\n\tglobal_load_dwordx4 %2, %8, %9 offset:2048\n\tglobal_load_dwordx4 %3, %8, %9 offset:3072" \
                             : "+v"(qf[0]), "+v"(qf[1]), "+v"(qf[2]), "+v"(qf[3]), "+v"(qf[4]), "+v"(qf[5]), "+v"(qf[6]), "+v"(qf[7]) : "v"(dvo), "s"(qs_) : "memory"); \
                asm volatile("global_load_dwordx4 %4, %8, %9\n\tglobal_load_dwordx4 %5, %8, %9 offset:1024\n\tglobal_load_dwordx4 %6, %8, %9 offset:2048\n\tglobal_load_dwordx4 %7, %8, %9 offset:3072" \
                             : "+v"(qf[0]), "+v"(qf[1]), "+v"(qf[2]), "+v"(qf[3]), "+v"(qf[4]), "+v"(qf[5]), "+v"(qf[6]), "+v"(qf[7]) : "v"(dvo), "s"(qs_ + 4096) : "memory"); \
            }                                                                                                                         \
            SBAR();                                                                                                                   \
        }                                                                                                                             \
        __builtin_amdgcn_s_setprio(3);                                                                                                \
        TICK(tA);                                                                                                                     \
        asm volatile("s_barrier" ::: "memory");                                                                                       \
        TICK(tX);                                                                                                                     \
    } while (0)
#define EXP_PACK()                    \
    do {                                                                                                                              \
        float e_[32], a_[2];                                                                                                          \
        unsigned c_[16];                                                                                                              \
        _Pragma("unroll") for (int k = 0; k < 18; ++k) {                                                                              \
            if (k < 16) { e_[2 * k] = v_exp(S[k >> 3][2 * (k & 7)]); e_[2 * k + 1] = v_exp(S[k >> 3][2 * (k & 7) + 1]); }             \
            if (k == 1) { a_[0] = e_[0]; a_[1] = e_[1]; }                                                                             \
            if (k >= 2 && k < 17) { a_[0] = v_add(a_[0], e_[2 * k - 2]); a_[1] = v_add(a_[1], e_[2 * k - 1]); }                        \
            if (k >= 2) c_[k - 2] = v_cvt(e_[2 * k - 4], e_[2 * k - 3]);                                                              \
        }                                                                                                                             \
        _Pragma("unroll") for (int k = 0; k < 4; ++k) pf[k] = __builtin_bit_cast(bf16x8, u32x4{c_[4 * k], c_[4 * k + 1], c_[4 * k + 2], c_[4 * k + 3]}); \
        ts_ = v_add(a_[0], a_[1]);                                                                                                    \
    } while (0)
#define REBASE(FIRST)                                                          \
    do {                                                                                                                              \
        float rm = S[0][0];                                                                                                           \
        _Pragma("unroll") for (int e = 1; e < 32; ++e) rm = __builtin_fmaxf(rm, S[e >> 4][e & 15]);                                   \
        rm = xh_max(rm);                                                                                                              \
        const float dl = (FIRST) ? (rm == -__builtin_inff() ? 0.f : rm) : __builtin_fmaxf(rm, 0.f);                                   \
        mref += dl;                                                                                                                   \
        if (!(FIRST)) { float f = __builtin_amdgcn_exp2f(-dl); asm volatile("s_nop 1" : "+v"(f)); lsum *= f;               \
            _Pragma("unroll") for (int i = 0; i < 4; ++i) _Pragma("unroll") for (int r = 0; r < 16; ++r) o[i][r] = mul_s(o[i][r], f); } \
        _Pragma("unroll") for (int r = 0; r < 16; ++r) { S[0][r] = sub_s(S[0][r], dl); S[1][r] = sub_s(S[1][r], dl); }                \
        const float nm = -mref;                                                                                                       \
        _Pragma("unroll") for (int r = 0; r < 16; ++r) negm[r] = nm;                                                                  \
    } while (0)
#define PHASE_V(t, WN, LASTV, VS, DK, DV)                                                                                             \
    do {                                                                                                                              \
        const bool swn = ((t) + 1 == nsw), sw0 = ((t) == nsw);                                                                        \
        const bool diag = 64 * tile + 63 > q0 || sw0, first_ = (t) == 0 || sw0;                                                       \
        if (DV) { dma4(m0b + ((VS) ? vslot1 : vslot0), vptr, dvo); vptr += 16384 + ((t) + 2 == nsw ? jump : 0); }          \
        if (DK) { dma4(m0b + ks_cur, kptr, dvo); kptr += 16384 + ((t) + 4 == nsw ? jump : 0); }                           \
        NOP_SO(S, o);                                                                                                                 \
        float ts_;                                                                                                                    \
        if (__builtin_expect(diag || first_, 0)) {                                                                                    \
            if (sw0) {                                                                                \
                float il_ = __builtin_amdgcn_rcpf(xh_sum(lsum));                                                                      \
                asm volatile("s_nop 1" : "+v"(il_));                                                                                  \
                const char* cp_ = uptr(ws + WS_CH + ((size_t)(8 * LB + 2 * w4) * NKS_CH + 4 * head) * 1024);                          \
                const unsigned co_ = CTX_VO(flane());                                                                                 \
                asm volatile("s_nop 4");                                                                                              \
                st_wt_i<0>(cp_, co_, pack_o(o[0], 0, il_)); st_wt_i<512>(cp_, co_, pack_o(o[0], 1, il_));                             \
                st_wt_i<1024>(cp_, co_, pack_o(o[1], 0, il_)); st_wt_i<1536>(cp_, co_, pack_o(o[1], 1, il_));                         \
                st_wt_i<2048>(cp_, co_, pack_o(o[2], 0, il_)); st_wt_i<2560>(cp_, co_, pack_o(o[2], 1, il_));                         \
                st_wt_i<3072>(cp_, co_, pack_o(o[3], 0, il_)); st_wt_i<3584>(cp_, co_, pack_o(o[3], 1, il_));                         \
                _Pragma("unroll") for (int i = 0; i < 4; ++i) o[i] = f32x16{};                                                        \
                lsum = 0.f; mref = 0.f; q0 = 128 * HB + 32 * w4;                                                                      \
            }                                                                                                                         \
            if (64 * tile + 63 > q0) {         \
                const int dq = (int)(lane & 31) - 4 * (int)(lane >> 5);                                                               \
                _Pragma("unroll") for (int u = 0; u < 2; ++u) {                                                                       \
                    const int kb = 64 * tile + 32 * u;                                                                                \
                    if (kb > q0) { _Pragma("unroll") for (int r = 0; r < 16; ++r) S[u][r] = -__builtin_inff(); }                       \
                    else if (kb == q0) mask_diag(S[u], dq);                                                                           \
                }                                                                                                                     \
            }                                                                                                                         \
            if (first_) REBASE(true);                                                                                                 \
        }                                                                                                                             \
        EXP_PACK();                                                                                                                   \
        if (__builtin_expect(__any(ts_ > PLIM), 0)) { REBASE(false); EXP_PACK(); }     \
        lsum += ts_;                                                                                                                  \
        if (!(LASTV)) EARLY_K(ks_nxt);                                                                                                \
        if (__builtin_expect(swn, 0)) {                                                                                               \
            negm = f32x16{};                                                                                                          \
            asm volatile("s_waitcnt vmcnt(%9)" : "+v"(qf[0]), "+v"(qf[1]), "+v"(qf[2]), "+v"(qf[3]), "+v"(qf[4]), "+v"(qf[5]), "+v"(qf[6]), "+v"(qf[7]), "+v"(negm) : "i"(WN) : "memory"); \
            tile += 17 - nL;                                                                                                          \
        }                                                                                                                             \
        TICK(tB);                                                                                                                     \
        asm volatile("s_cmp_eq_u32 %7, 0\n\ts_cbranch_scc1 .Lv1%=\n\ts_waitcnt vmcnt(%5)\n\ts_branch .Lv2%=\n.Lv1%=:\n\ts_waitcnt vmcnt(%6)\n.Lv2%=:\n\ts_barrier"                \
                     : "+v"(pf[0]), "+v"(pf[1]), "+v"(pf[2]), "+v"(pf[3]), "+v"(negm) : "i"(WN), "i"((WN) + 8), "s"(nsw - (t)) : "scc", "memory");   \
        TICK(tO);                                                                                                                     \
        { const unsigned r_ = ks_cur; ks_cur = ks_nxt; ks_nxt = ks_nn; ks_nn = r_; }                                                  \
        tile += 1;                                                                                                                    \
    } while (0)
    constexpr float PLIM = 256.0f;
    PHASE_M(0, true, false, 1);
    PHASE_V(0, 8, false, 1, true, true);
    flag_set(w4, flb, FL_H + (myblk * 32 + head) * 8, tag);
    for (int t = 1; t < 13; t += 2) {
        PHASE_M(t, true, true, 0); PHASE_V(t, 8, false, 0, true, true);
        PHASE_M(t + 1, true, true, 1); PHASE_V(t + 1, 8, false, 1, true, true);
    }
    PHASE_M(13, true, true, 0); PHASE_V(13, 8, false, 0, true, true);
    PHASE_M(14, true, true, 1); PHASE_V(14, 4, false, 1, false, true);
    PHASE_M(15, true, true, 0);
    const int ks = xcd >> 1, qt = 8 * (xcd & 1) + (idx >> 2), mt = idx & 3;
    u32x2 fh;
    const unsigned fho = FL_H + (qt * 32 + (lane & 31)) * 8;
    flag_ld(fh, flb, fho);
    PHASE_V(15, 4, false, 0, false, true);
    PIN(fh);
    PHASE_M(16, true, true, 1); PHASE_V(16, 0, true, 1, false, false);
    PHASE_M(17, false, true, 0);
    flag_wait<false>(0, fh, flb, fho, tag);
    if (!grp) asm volatile("s_barrier" ::: "memory");
    NOP_SO(S, o);
    K3S k3 = k3_setup(lds0, wid, lane);
    k3_point(k3, ws, w2, wid, qt, mt, NKS_C + 14 * ks, 7 * ks);
    k3_stage(k3, 0, grp == 0); k3_stage(k3, K3_SLOT, grp == 0);
    STAMP(8);
    {
        const float ls = xh_sum(lsum);
        const float mm = ls > 0.f ? mref : -__builtin_inff();
        lds_ptr X = (lds_ptr)lds + 81920 + wid * 8192 + lane * 16;
        f32x16 keep[2];
        if (grp) {
#pragma unroll
            for (int i = 0; i < 2; ++i) {
                keep[i] = o[2 + i];
#pragma unroll
                for (int y = 0; y < 4; ++y) *(__attribute__((address_space(3))) f32x4*)(X + (4 * i + y) * 1024) = f32x4{o[i][4 * y], o[i][4 * y + 1], o[i][4 * y + 2], o[i][4 * y + 3]};
            }
        } else {
#pragma unroll
            for (int i = 0; i < 2; ++i) {
                keep[i] = o[i];
#pragma unroll
                for (int y = 0; y < 4; ++y) *(__attribute__((address_space(3))) f32x4*)(X + (4 * i + y) * 1024) = f32x4{o[2 + i][4 * y], o[2 + i][4 * y + 1], o[2 + i][4 * y + 2], o[2 + i][4 * y + 3]};
            }
        }
        lds_ptr Y = (lds_ptr)lds + 73728 + wid * 512 + lane * 8;
        *(__attribute__((address_space(3))) f32x2*)Y = f32x2{mm, ls};
        asm volatile("s_waitcnt lgkmcnt(0)\n\ts_barrier" ::: "memory");
        const unsigned ow = wid ^ 4;
        lds_cptr Xo = (lds_cptr)lds + 81920 + ow * 8192 + lane * 16;
        const f32x2 oml = *(const __attribute__((address_space(3))) f32x2*)((lds_cptr)lds + 73728 + ow * 512 + lane * 8);
        const float mx = __builtin_fmaxf(mm, oml[0]);
        const float e0 = __builtin_amdgcn_exp2f(mm - mx), e1 = __builtin_amdgcn_exp2f(oml[0] - mx);
        const float inv = __builtin_amdgcn_rcpf(ls * e0 + oml[1] * e1);
        const float fs = e0 * inv, fo = e1 * inv;
        const char* cp = uptr(ws + WS_CH + ((size_t)(8 * HB + 2 * w4) * NKS_CH + 4 * head + 2 * grp) * 1024);
        const unsigned co = CTX_VO(lane);
#pragma unroll
        for (int i = 0; i < 2; ++i) {
            f32x16 m;
#pragma unroll
            for (int y = 0; y < 4; ++y) {
                const f32x4 pv = *(const __attribute__((address_space(3))) f32x4*)(Xo + (4 * i + y) * 1024);
#pragma unroll
                for (int r = 0; r < 4; ++r) m[4 * y + r] = keep[i][4 * y + r] * fs + pv[r] * fo;
            }
            if (i == 0) { st_wt_i<0>(cp, co, pack_o(m, 0, 1.f)); st_wt_i<512>(cp, co, pack_o(m, 1, 1.f)); }
            else { st_wt_i<1024>(cp, co, pack_o(m, 0, 1.f)); st_wt_i<1536>(cp, co, pack_o(m, 1, 1.f)); }
        }
    }
    STAMP(9);
#undef KOFF
#undef CTX_VO
#undef VOFF
#undef EARLY_K
#undef PHASE_M
#undef PHASE_V
#undef EXP_PACK
#undef REBASE
    if (grp == 0) k3_body<0>(k3, x, wo, ws, out, lds, wid, tag, ks, qt, mt, FL_C + (pi * 32 + head) * 8);
    else k3_body<1>(k3, x, wo, ws, out, lds, wid, tag, ks, qt, mt, FL_C + (pi * 32 + head) * 8);
}

extern "C" void kernel_launch(void* const* d_in, const int* in_sizes, int n_in, void* d_out, int out_size, void* d_ws, size_t ws_size,
                              hipStream_t stream) {
    const char* x = (const char*)d_in[0];
    const char* ckv = (const char*)d_in[1];
    const char* wuk = (const char*)d_in[3];
    const char* wuv = (const char*)d_in[4];
    char* ws = (char*)d_ws;
    (void)hipFuncSetAttribute((const void*)fused, hipFuncAttributeMaxDynamicSharedMemorySize, 163840);
    fused<<<256, 512, 163840, stream>>>(x, ckv, wuk, wuv, (const char*)d_in[2], (const char*)d_in[7], (const char*)d_in[8], ws, (const char*)d_in[5], (const char*)d_in[9], (char*)d_out);
}
```

```cpp
#include <hip/hip_runtime.h>
#include <hip/hip_bf16.h>
#include <stdint.h>

typedef __bf16 bf16x8 __attribute__((ext_vector_type(8)));
typedef __bf16 bf16x2 __attribute__((ext_vector_type(2)));
typedef short s16x4 __attribute__((ext_vector_type(4)));
typedef short s16x8 __attribute__((ext_vector_type(8)));
typedef short v4i16_t __attribute__((ext_vector_type(4)));
typedef float f32x16 __attribute__((ext_vector_type(16)));
typedef float f32x4 __attribute__((ext_vector_type(4)));
typedef float f32x2 __attribute__((ext_vector_type(2)));
typedef unsigned u32x4 __attribute__((ext_vector_type(4)));
typedef unsigned u32x2 __attribute__((ext_vector_type(2)));
typedef __attribute__((address_space(3))) const char* lds_cptr;
typedef __attribute__((address_space(3))) char* lds_ptr;

#define DI __device__ __forceinline__
#define SBAR() __builtin_amdgcn_sched_barrier(0)
#define MFMA(a, b, c) __builtin_amdgcn_mfma_f32_16x16x32_bf16((a), (b), (c), 0, 0, 0)
#define PIN(x) asm volatile("" : "+v"(x))
#define PINR(x) asm volatile("" ::"v"(x))

constexpr int S_ = 2048, NH = 32, HD = 128, DM = 640, DC = 512, FF = 1728;
constexpr int NKS_X = DM / 32;
constexpr int NKS_C = (NH * HD) / 32;
constexpr int NKS_H = FF / 32;
constexpr int NKS_CH = NKS_C + NKS_H;
constexpr size_t WS_XF = 0;
constexpr size_t WS_KF = WS_XF + (size_t)128 * NKS_X * 1024;
constexpr size_t WS_VF = WS_KF + (size_t)NH * 128 * 4 * 1024;
constexpr size_t WS_CH = WS_VF + (size_t)NH * 64 * 8 * 1024;
constexpr size_t WS_QH = WS_CH + (size_t)128 * NKS_CH * 1024;
constexpr size_t WS_PP = WS_QH + (size_t)256 * 32768;
constexpr size_t PP_BYTES = (size_t)64 * 8 * 8 * 5 * 1024;
constexpr size_t WS_FL = WS_PP + PP_BYTES;
constexpr size_t FL_EPOCH = 0;
constexpr size_t FL_X = 256;
constexpr size_t FL_KV = FL_X + 2048;
constexpr size_t FL_H = FL_KV + 2048;
constexpr size_t FL_C = FL_H + 4096;
constexpr size_t FL_S = 16384;
constexpr size_t WS_END = WS_FL + FL_S + (size_t)256 * 128;

DI unsigned rfl(unsigned x) { return __builtin_amdgcn_readfirstlane(x); }
DI unsigned flane() { unsigned r; asm volatile("v_mbcnt_lo_u32_b32 %0, -1, 0\n\tv_mbcnt_hi_u32_b32 %0, -1, %0" : "=v"(r)); return r; }
#define STAMP(i)
#define TICK(acc)
DI const char* uptr(const char* p) { const unsigned long long v = (unsigned long long)p; const unsigned lo = rfl((unsigned)v), hi = rfl((unsigned)(v >> 32)); return (const char*)(((unsigned long long)hi << 32) | lo); }
DI unsigned cvtpk(float lo, float hi) { f32x2 v = {lo, hi}; bf16x2 b = __builtin_convertvector(v, bf16x2); return __builtin_bit_cast(unsigned, b); }
DI bf16x8 lds_frag(lds_cptr p) { return *(const __attribute__((address_space(3))) bf16x8*)p; }
DI s16x4 lds_tr(lds_cptr p) { return __builtin_bit_cast(s16x4, __builtin_amdgcn_ds_read_tr16_b64_v4i16((__attribute__((address_space(3))) v4i16_t*)p)); }
DI bf16x8 tr_frag(lds_cptr p0, lds_cptr p1) { s16x4 lo = lds_tr(p0), hi = lds_tr(p1); s16x8 v = __builtin_shufflevector(lo, hi, 0, 1, 2, 3, 4, 5, 6, 7); return __builtin_bit_cast(bf16x8, v); }
template <int OFF> DI void dma1(unsigned m0v, const void* sbase, unsigned voff) {
    asm volatile("s_mov_b32 m0, %0\n\ts_nop 0\n\tglobal_load_lds_dwordx4 %1, %2 offset:%3" ::"s"(m0v), "v"(voff), "s"(sbase), "i"(OFF) : "memory");
}
DI u32x4 pack44(const f32x4& a, const f32x4& b) { u32x4 w; w[0] = cvtpk(a[0], a[1]); w[1] = cvtpk(a[2], a[3]); w[2] = cvtpk(b[0], b[1]); w[3] = cvtpk(b[2], b[3]); return w; }
DI u32x4 pack44s(const f32x4& a, const f32x4& b, float sc) { u32x4 w; w[0] = cvtpk(a[0] * sc, a[1] * sc); w[1] = cvtpk(a[2] * sc, a[3] * sc); w[2] = cvtpk(b[0] * sc, b[1] * sc); w[3] = cvtpk(b[2] * sc, b[3] * sc); return w; }
DI u32x4 pack8(const f32x16& a, int b, float sc) {
    u32x4 w; w[0] = cvtpk(a[b + 0] * sc, a[b + 1] * sc); w[1] = cvtpk(a[b + 2] * sc, a[b + 3] * sc);
    w[2] = cvtpk(a[b + 4] * sc, a[b + 5] * sc); w[3] = cvtpk(a[b + 6] * sc, a[b + 7] * sc); return w;
}
DI u32x4 pack8n(const f32x16& a, int b) {
    u32x4 w; w[0] = cvtpk(a[b + 0], a[b + 1]); w[1] = cvtpk(a[b + 2], a[b + 3]); w[2] = cvtpk(a[b + 4], a[b + 5]); w[3] = cvtpk(a[b + 6], a[b + 7]); return w;
}
DI float bf2f(unsigned short u) { return __uint_as_float((unsigned)u << 16); }
DI float bflo(unsigned w) { return __uint_as_float(w << 16); }
DI float bfhi(unsigned w) { return __uint_as_float(w & 0xffff0000u); }
DI float xhalf_sum(float v) { auto r = __builtin_amdgcn_permlane32_swap(__float_as_uint(v), __float_as_uint(v), false, false); return __uint_as_float(r[0]) + __uint_as_float(r[1]); }
DI float xhalf_max(float v) { auto r = __builtin_amdgcn_permlane32_swap(__float_as_uint(v), __float_as_uint(v), false, false); return __builtin_fmaxf(__uint_as_float(r[0]), __uint_as_float(r[1])); }

DI void warm_load(unsigned& d, const char* wa, const char* wb, unsigned id) {
    const unsigned j = id - 1280u;
    const char* p = uptr(id < 1280u ? wa + (size_t)id * 4096 : wb + (size_t)(j < 539u ? j : 539u) * 4096);
    const unsigned voff = flane() * 64;
    asm volatile("s_nop 4\n\tglobal_load_dword %0, %1, %2" : "=&v"(d) : "v"(voff), "s"(p) : "memory");
}
DI void warm_lds(unsigned m0v, const char* wa, const char* wb, unsigned id) {
    const unsigned j = id - 1280u;
    const char* p = uptr(id < 1280u ? wa + (size_t)id * 4096 : wb + (size_t)(j < 539u ? j : 539u) * 4096);
    const unsigned voff = flane() * 64;
    asm volatile("s_mov_b32 m0, %0\n\ts_nop 4\n\tglobal_load_lds_dword %1, %2" ::"s"(m0v), "v"(voff), "s"(p) : "memory");
}

DI void mfma_acc(f32x4& acc, const bf16x8& a, const bf16x8& b) { asm volatile("v_mfma_f32_16x16x32_bf16 %0, %1, %2, %0" : "+v"(acc) : "v"(a), "v"(b)); }
DI void mfma_acc_s(f32x4& acc, const bf16x8& a, const bf16x8& b) { asm volatile("s_nop 1\n\tv_mfma_f32_16x16x32_bf16 %0, %1, %2, %0" : "+v"(acc) : "v"(a), "v"(b)); }
DI void mfma32_acc(f32x16& acc, const bf16x8& a, const bf16x8& b) { asm volatile("v_mfma_f32_32x32x16_bf16 %0, %1, %2, %0" : "+v"(acc) : "v"(a), "v"(b)); }
DI void mfma32_ini(f32x16& d, const bf16x8& a, const bf16x8& b, const f32x16& c) { asm volatile("v_mfma_f32_32x32x16_bf16 %0, %1, %2, %3" : "=&v"(d) : "v"(a), "v"(b), "v"(c)); }
DI float v_exp(float x) { float r; asm volatile("v_exp_f32 %0, %1" : "=v"(r) : "v"(x)); return r; }
DI float v_add(float a, float b) { float r; asm volatile("v_add_f32 %0, %1, %2" : "=v"(r) : "v"(a), "v"(b)); return r; }
DI unsigned v_cvt(float lo, float hi) { unsigned r; asm volatile("v_cvt_pk_bf16_f32 %0, %1, %2" : "=v"(r) : "v"(lo), "v"(hi)); return r; }
DI void set_m0(unsigned v) { asm volatile("s_mov_b32 m0, %0" ::"s"(v) : "memory"); }
template <int OFF> DI void dma_nom0(const void* sbase, unsigned voff) { asm volatile("global_load_lds_dwordx4 %0, %1 offset:%2" ::"v"(voff), "s"(sbase), "i"(OFF) : "memory"); }
DI void dma4(unsigned m0v, const void* sbase, unsigned voff) {
    asm volatile("s_mov_b32 m0, %0\n\ts_nop 0\n\tglobal_load_lds_dwordx4 %1, %2\n\tglobal_load_lds_dwordx4 %1, %2 offset:1024\n\tglobal_load_lds_dwordx4 %1, %2 offset:2048\n\tglobal_load_lds_dwordx4 %1, %2 offset:3072"
                 ::"s"(m0v), "v"(voff), "s"(sbase) : "memory");
}

typedef unsigned long long u64;
DI void st_wt(char* p, const u32x4& v) { asm volatile("global_store_dwordx4 %0, %1, off sc1\n\ts_nop 1" ::"v"(p), "v"(v) : "memory"); }
DI void st_wt_o(const char* sbase, unsigned voff, const u32x4& v) { asm volatile("global_store_dwordx4 %0, %1, %2 sc1\n\ts_nop 1" ::"v"(voff), "v"(v), "s"(sbase) : "memory"); }
template <int OFF> DI void st_wt_i(const char* sbase, unsigned voff, const u32x4& v) {
    asm volatile("global_store_dwordx4 %0, %1, %2 offset:%3 sc1\n\ts_nop 1" ::"v"(voff), "v"(v), "s"(sbase), "i"(OFF) : "memory");
}
DI void ld_wt(u32x4& d, const char* sbase, unsigned voff) { asm volatile("global_load_dwordx4 %0, %1, %2 sc1" : "=&v"(d) : "v"(voff), "s"(sbase) : "memory"); }
DI void flag_set(unsigned wid, const char* sbase, unsigned off, u64 tag) {
    const u32x2 v = u32x2{(unsigned)tag, (unsigned)(tag >> 32)};
    u64 ex;
    asm volatile("s_cmp_lg_u32 %1, 0\n\ts_cbranch_scc1 .Lfs%=\n\ts_mov_b64 %0, exec\n\ts_mov_b64 exec, 1\n\t"
                 "global_store_dwordx2 %2, %3, %4 sc1\n\ts_mov_b64 exec, %0\n.Lfs%=:\n\ts_nop 1"
                 : "=&s"(ex) : "s"(wid), "v"(off), "v"(v), "s"(sbase) : "scc", "memory");
}
DI void flag_ld(u32x2& f, const char* sbase, unsigned voff) { asm volatile("global_load_dwordx2 %0, %1, %2 sc1" : "=&v"(f) : "v"(voff), "s"(sbase) : "memory"); }
template <bool ACQ>
DI void flag_wait(unsigned skip, u32x2& f, const char* sbase, unsigned voff, u64 tag) {
    unsigned cnt;
    asm volatile("s_cmp_lg_u32 %2, 0\n\ts_cbranch_scc1 .Lfe%=\n\t"
                 "v_cmp_ne_u64 vcc, %3, %0\n\ts_cbranch_vccz .Lfd%=\n\ts_mov_b32 %1, 0x40000\n"
                 ".Lfl%=:\n\tglobal_load_dwordx2 %0, %4, %5 sc1\n\ts_waitcnt vmcnt(0)\n\tv_cmp_ne_u64 vcc, %3, %0\n\ts_cbranch_vccz .Lfd%=\n\t"
                 "s_sleep 2\n\ts_sub_u32 %1, %1, 1\n\ts_cbranch_scc0 .Lfl%=\n"
                 ".Lfd%=:\n\t.if %6\n\tbuffer_inv sc1\n\t.endif\n"
                 ".Lfe%=:"
                 : "+v"(f), "=&s"(cnt) : "s"(skip), "s"(tag), "v"(voff), "s"(sbase), "i"(ACQ ? 1 : 0) : "vcc", "scc", "memory");
}

constexpr int WQ_PIECE = 1040, W1_PIECE = 1056, W1_OFF = 16 * WQ_PIECE, PSLOT = W1_OFF + 8 * W1_PIECE;
struct PJ { unsigned wqvo, w1vo, xv0, xv1; const char *wqs, *w1s, *xs; };
DI PJ pj_setup(const char* wq, const char* w1, const char* ws, unsigned wid, int head, int f0, bool nft4, int q16_0) {
    const unsigned lane = flane();
    PJ j;
    j.wqvo = ((lane >> 2) & 3) * 8192 + (lane >> 4) * 64 + (lane & 3) * 16;
    j.w1vo = ((lane >> 2) & 7) * 3456 + (nft4 ? (lane >> 5) * 64 : 0) + (lane & 3) * 16;
    j.wqs = uptr(wq + (size_t)wid * 8 * 8192 + head * 256);
    j.w1s = uptr(w1 + (size_t)wid * 8 * 3456 + f0 * 2);
    j.xs = uptr(ws + WS_XF + (size_t)q16_0 * NKS_X * 1024);
    j.xv0 = lane * 16; j.xv1 = lane * 16 + NKS_X * 1024;
    return j;
}
DI void pj_dma(const PJ& j, unsigned lds0, unsigned wid, int c, int slot) {
    dma1<0>(lds0 + slot + (2 * wid) * WQ_PIECE, j.wqs + (size_t)c * 64 * 8192, j.wqvo);
    dma1<0>(lds0 + slot + (2 * wid + 1) * WQ_PIECE, j.wqs + (size_t)c * 64 * 8192 + 4 * 8192, j.wqvo);
    dma1<0>(lds0 + slot + W1_OFF + wid * W1_PIECE, j.w1s + (size_t)c * 64 * 3456, j.w1vo);
}
DI void pj_dma1(const PJ& j, unsigned lds0, unsigned wid, int c, int slot, int i) {
    if (i < 2) dma1<0>(lds0 + slot + (2 * wid + i) * WQ_PIECE, j.wqs + (size_t)c * 64 * 8192 + i * 4 * 8192, j.wqvo);
    else dma1<0>(lds0 + slot + W1_OFF + wid * W1_PIECE, j.w1s + (size_t)c * 64 * 3456, j.w1vo);
}
DI void pj_xload1(const PJ& j, u32x4& X, int c, int i) {
    const char* p_ = j.xs + c * 2048 + (i >> 1) * 1024;
    asm volatile("global_load_dwordx4 %0, %1, %2 sc1" : "=&v"(X) : "v"((i & 1) ? j.xv1 : j.xv0), "s"(p_) : "memory");
}
struct KVO { const char* base; unsigned lo[2]; };
DI void kv_store(const KVO& o, const u32x4& v, int i) {
    if (i & 2) st_wt_i<2048>(o.base + (i >> 2) * 8192, o.lo[i & 1], v); else st_wt_i<0>(o.base + (i >> 2) * 8192, o.lo[i & 1], v);
}
DI void pj_xload(const PJ& j, u32x4 (&X)[4], int c) {
    const char* p_ = j.xs + c * 2048;
    asm volatile("global_load_dwordx4 %0, %4, %6 sc1\n\tglobal_load_dwordx4 %1, %5, %6 sc1\n\tglobal_load_dwordx4 %2, %4, %6 offset:1024 sc1\n\tglobal_load_dwordx4 %3, %5, %6 offset:1024 sc1"
                 : "=&v"(X[0]), "=&v"(X[1]), "=&v"(X[2]), "=&v"(X[3]) : "v"(j.xv0), "v"(j.xv1), "s"(p_) : "memory");
}

#define K1PF 0
template <bool VT, class F>
DI void k1_mma(f32x4 (&acc)[8][4], int half, const bf16x8 (&c)[4], const bf16x8 (&w)[4], F&& filler) {
#pragma unroll
    for (int rt = 0; rt < 4; ++rt)
#pragma unroll
        for (int nt = 0; nt < 4; ++nt) {
            if (half == 0) acc[rt][nt] = VT ? MFMA(c[rt], w[nt], acc[rt][nt]) : MFMA(w[nt], c[rt], acc[rt][nt]);
            else acc[4 + rt][nt] = VT ? MFMA(c[rt], w[nt], acc[4 + rt][nt]) : MFMA(w[nt], c[rt], acc[4 + rt][nt]);
            filler(rt * 4 + nt);
        }
}

template <bool VT>
DI void k1_tile(const char* __restrict__ Abase, const char* __restrict__ Wbase, char* __restrict__ outp, char* lds, const unsigned (&avo)[4], const unsigned (&wvo)[4],
                const char* __restrict__ ckv, const char* __restrict__ wuk, const char* __restrict__ wuv, unsigned wid, int hp, int rb, char* __restrict__ ws, const char* __restrict__ wq, const char* __restrict__ w1, const char* __restrict__ b1, u64 tag,
                int head2, int f0, bool nft4, int q16_0, u32x4 (&xa)[4], u32x2 (&bb)[4], u32x4 (&kv)[16], KVO& kvo) {
    char* stamp_base = ws; (void)stamp_base;
    const char* flb = uptr(ws + WS_FL);
    const unsigned wm = wid >> 2, wn = wid & 3;
    const unsigned lds0 = (unsigned)(uintptr_t)lds;
    unsigned aaddr[2], baddr[4];
    {
        const unsigned lane = flane(), c = lane & 15, g = lane >> 4;
        const unsigned x = (c >> 1) & 7;
#pragma unroll
        for (int s = 0; s < 2; ++s) aaddr[s] = (128 * wm + c) * 128 + (((4 * s + g) ^ x) << 4);
        const unsigned q = (lane & 15) >> 2, p = lane & 3, f = q | ((g & 1) << 2);
#pragma unroll
        for (int nt = 0; nt < 4; ++nt) baddr[nt] = 32768 + (8 * g + q) * 512 + (((8 * wn + 2 * nt + (p >> 1)) ^ (f << 1)) << 4) + ((p & 1) << 3);
    }
    f32x4 acc[8][4];
#pragma unroll
    for (int i = 0; i < 8; ++i)
#pragma unroll
        for (int j = 0; j < 4; ++j) acc[i][j] = f32x4{};
    bf16x8 cf[2][4], wf[2][4];
    lds_cptr L = (lds_cptr)lds;
#define K1_RC(buf, stg, s, half) do { _Pragma("unroll") for (int rt = 0; rt < 4; ++rt) cf[buf][rt] = lds_frag(L + (stg) + aaddr[s] + ((half) * 4 + rt) * 2048); } while (0)
#define K1_RW(buf, stg, s) do { _Pragma("unroll") for (int nt = 0; nt < 4; ++nt) wf[buf][nt] = tr_frag(L + (stg) + baddr[nt] + (s) * 16384, L + (stg) + baddr[nt] + (s) * 16384 + 2048); } while (0)
    const char* Aw = Abase + (size_t)wid * 32 * 1024;
    const char* Ww = Wbase + (size_t)wid * 8 * 8192;
    const unsigned m0a = lds0 + wid * 4096, m0w = lds0 + 32768 + wid * 4096;
#define K1_DMA(kt, stg)                                                                   \
    do {                                                                                  \
        const char* a_ = Aw + (kt) * 128; const char* w_ = Ww + (size_t)(kt) * 64 * 8192; \
        dma1<0>(m0a + (stg), a_, avo[0]); dma1<1024>(m0a + (stg), a_, avo[1]); dma1<2048>(m0a + (stg), a_, avo[2]); dma1<3072>(m0a + (stg), a_, avo[3]); \
        dma1<0>(m0w + (stg), w_, wvo[0]); dma1<1024>(m0w + (stg), w_, wvo[1]); dma1<2048>(m0w + (stg), w_, wvo[2]); dma1<3072>(m0w + (stg), w_, wvo[3]); \
    } while (0)
    unsigned pfd = 0;
    u32x2 fx = u32x2{};
#define K1_FXO() (FL_X + (2 * q16_0 + (flane() & 3)) * 8)
    STAMP(1);
    asm volatile("s_waitcnt vmcnt(10)\n\ts_barrier" ::: "memory");
    STAMP(2);
    K1_RW(0, 0, 0); K1_RC(0, 0, 0, 0);
#define K1_RC1(buf, stg, s, half, rt) cf[buf][rt] = lds_frag(L + (stg) + aaddr[s] + ((half) * 4 + (rt)) * 2048)
#define K1_RW1(buf, stg, s, nt) wf[buf][nt] = tr_frag(L + (stg) + baddr[nt] + (s) * 16384, L + (stg) + baddr[nt] + (s) * 16384 + 2048)
#pragma unroll
    for (int kt = 0; kt < 8; ++kt) {
        const unsigned stg = (kt & 1) * 65536, nstg = stg ^ 65536;
        SBAR();
        k1_mma<VT>(acc, 0, cf[0], wf[0], [&](int m) { if (m < 8 && (m & 1) == 0) { K1_RC1(1, stg, 0, 1, m >> 1); SBAR(); } });
        SBAR();
        k1_mma<VT>(acc, 1, cf[1], wf[0], [&](int m) { if (m < 4) { K1_RC1(0, stg, 1, 0, m); SBAR(); } else if (m < 12 && (m & 1) == 0) { K1_RW1(1, stg, 1, (m - 4) >> 1); SBAR(); } });
        SBAR();
        k1_mma<VT>(acc, 0, cf[0], wf[1], [&](int m) { if (m < 8 && (m & 1) == 0) { K1_RC1(1, stg, 1, 1, m >> 1); SBAR(); } });
        SBAR();
        if (kt == 7) asm volatile("s_waitcnt vmcnt(0) lgkmcnt(0)\n\ts_barrier" : "+v"(pfd)::"memory");
        else if (kt == 1) asm volatile("s_waitcnt vmcnt(%0) lgkmcnt(0)\n\ts_barrier" ::"i"(K1PF) : "memory");
        else asm volatile("s_waitcnt vmcnt(0) lgkmcnt(0)\n\ts_barrier" : "+v"(fx)::"memory");
        if (kt == 0) flag_set(wid, flb, FL_X + blockIdx.x * 8, tag);
        if (kt == 6) flag_wait<false>(0, fx, flb, K1_FXO(), tag);
        const char* a_ = Aw + (kt + 2) * 128; const char* w_ = Ww + (size_t)(kt + 2) * 64 * 8192;
        k1_mma<VT>(acc, 1, cf[1], wf[1], [&](int m) {
            if (kt + 1 < 8) { if (m < 4) { K1_RW1(0, nstg, 0, m); SBAR(); } else if (m < 8) { K1_RC1(0, nstg, 0, 0, m - 4); SBAR(); } }
            if (kt + 2 < 8) {
                if (m == 1) { dma1<0>(m0a + stg, a_, avo[0]); SBAR(); } if (m == 3) { dma1<1024>(m0a + stg, a_, avo[1]); SBAR(); }
                if (m == 5) { dma1<2048>(m0a + stg, a_, avo[2]); SBAR(); } if (m == 7) { dma1<3072>(m0a + stg, a_, avo[3]); SBAR(); }
                if (m == 9) { dma1<0>(m0w + stg, w_, wvo[0]); SBAR(); } if (m == 11) { dma1<1024>(m0w + stg, w_, wvo[1]); SBAR(); }
                if (m == 13) { dma1<2048>(m0w + stg, w_, wvo[2]); SBAR(); } if (m == 15) { dma1<3072>(m0w + stg, w_, wvo[3]); SBAR(); }
            }
            if (K1PF && kt == 0 && m == 15) {
                const unsigned l_ = flane(), id_ = (blockIdx.x >> 3) * 8 + wid, gid_ = (blockIdx.x * 8 + wid) & 511;
                const unsigned p_ = id_ < 160 ? id_ : id_ - 160, combo_ = p_ / 40, r8_ = p_ % 40;
                const char* wp_ = uptr(((combo_ & 1) ? wuv : wuk) + (size_t)(192 + 8 * r8_) * 8192 + (2 * (blockIdx.x & 7) + (combo_ >> 1)) * 512);
                const unsigned wo_ = (l_ >> 3) * 8192 + (l_ & 7) * 64;
                asm volatile("s_mov_b32 m0, %0\n\ts_nop 4\n\tglobal_load_lds_dword %1, %2" ::"s"(lds0 + 131072 + wid * 256), "v"(wo_), "s"(wp_) : "memory");
                if (K1PF == 2) {
                    const unsigned li_ = gid_ * 40 + (l_ < 40 ? l_ : 39), rw_ = li_ / 10;
                    const unsigned ao_ = rw_ * 1024 + 384 + (li_ - rw_ * 10) * 64;
                    asm volatile("global_load_lds_dword %0, %1" ::"v"(ao_), "s"(uptr(ckv)) : "memory");
                }
                SBAR();
            }
            if (kt == 5 && m == 15) {
                warm_load(pfd, wq, w1, blockIdx.x * 8 + wid);
                SBAR();
            }
            if (kt == 4 && m == 14) { flag_ld(fx, flb, K1_FXO()); SBAR(); }
            if (kt == 6 && m == 2) {
                const PJ j = pj_setup(wq, w1, ws, wid, head2, f0, nft4, q16_0); pj_dma(j, lds0, wid, 0, 0); pj_dma(j, lds0, wid, 1, PSLOT); SBAR();
            }
            if (kt == 7 && m == 0) { const PJ j = pj_setup(wq, w1, ws, wid, head2, f0, nft4, q16_0); asm volatile("s_nop 4"); pj_xload(j, xa, 0); SBAR(); }
            if (kt == 7 && m == 2) {
                const char* bp = uptr(b1 + f0 * 2);
                const unsigned bo = (flane() >> 4) * 8, b2 = bo + (nft4 ? 64 : 0);
                asm volatile("s_nop 4\n\tglobal_load_dwordx2 %0, %4, %6\n\tglobal_load_dwordx2 %1, %4, %6 offset:32\n\tglobal_load_dwordx2 %2, %5, %6\n\tglobal_load_dwordx2 %3, %5, %6 offset:32"
                             : "=&v"(bb[0]), "=&v"(bb[1]), "=&v"(bb[2]), "=&v"(bb[3]) : "v"(bo), "v"(b2), "s"(bp) : "memory");
                SBAR();
            }
        });
        SBAR();
    }
    STAMP(3);
    const int head = 2 * hp + (wn >> 1), d0 = (wn & 1) * 64;
    const int key0 = 256 * rb + 128 * wm;
    const unsigned lane = flane(), c = lane & 15, g = lane >> 4;
    kvo.base = uptr(outp + ((size_t)(head * 64 + (key0 >> 5)) * 4 + (d0 >> 5)) * 2048);
    kvo.lo[0] = (g >> 1) * 1024 + ((VT ? c : (c & 7) + 16 * (c >> 3)) + 32 * (g & 1)) * 16;
    kvo.lo[1] = kvo.lo[0] + (VT ? 256 : 128);
#pragma unroll
    for (int i = 0; i < 16; ++i) {
        const int u = i >> 2, a = (i >> 1) & 1, b2 = i & 1;
        kv[i] = VT ? pack44(acc[2 * u][2 * a + b2], acc[2 * u + 1][2 * a + b2]) : pack44(acc[2 * u + b2][2 * a], acc[2 * u + b2][2 * a + 1]);
    }
    asm volatile("s_waitcnt vmcnt(0)" : "+v"(xa[0]), "+v"(xa[1]), "+v"(xa[2]), "+v"(xa[3]), "+v"(bb[0]), "+v"(bb[1]), "+v"(bb[2]), "+v"(bb[3])::"memory");
    STAMP(5);
#undef K1_RC
#undef K1_RW
#undef K1_DMA
#undef K1_FXO
}

constexpr float QSCALE = 0.08838834764831845f * 1.4426950408889634f;
constexpr float THR = 8.0f;
DI float x4_max(float x) {
    auto s = __builtin_amdgcn_permlane16_swap(__float_as_uint(x), __float_as_uint(x), false, false);
    x = __builtin_fmaxf(__uint_as_float(s[0]), __uint_as_float(s[1]));
    auto t = __builtin_amdgcn_permlane32_swap(__float_as_uint(x), __float_as_uint(x), false, false);
    return __builtin_fmaxf(__uint_as_float(t[0]), __uint_as_float(t[1]));
}
DI float x4_sum(float x) {
    auto s = __builtin_amdgcn_permlane16_swap(__float_as_uint(x), __float_as_uint(x), false, false);
    x = __uint_as_float(s[0]) + __uint_as_float(s[1]);
    auto t = __builtin_amdgcn_permlane32_swap(__float_as_uint(x), __float_as_uint(x), false, false);
    return __uint_as_float(t[0]) + __uint_as_float(t[1]);
}
DI float mish_f(float h) {
    const float e = __builtin_amdgcn_exp2f(__builtin_fminf(h, 30.0f) * 1.4426950408889634f);
    const float n = e * (e + 2.0f);
    return h * n * __builtin_amdgcn_rcpf(n + 2.0f);
}

#define SPC 4
#define KVP0 3
#define KVPS 4
#define NOKV 0
template <int NFT>
DI void proj_phase(const char* __restrict__ wq, const char* __restrict__ w1, char* __restrict__ ws, char* lds, int head, int f0, int q16_0, bf16x8 (&qf)[2][4],
                   const char* __restrict__ wo, const char* __restrict__ w2, unsigned m0v0, unsigned wid, u32x4 (&xa)[4], u32x4 (&xb)[4], const u32x2 (&bb)[4], u64 tag,
                   unsigned kvflag, int hp2, unsigned m0k0, unsigned m0k1, const char* kptr0, const char* kptr1, const u32x4 (&kv)[16], const KVO& kvo) {
    const unsigned lane = flane(), g = lane >> 4;
    const unsigned lds0 = (unsigned)(uintptr_t)lds;
    lds_cptr L = (lds_cptr)lds;
    const PJ pj = pj_setup(wq, w1, ws, wid, head, f0, NFT == 4, q16_0);
    const char* flb = uptr(ws + WS_FL);
    u32x2 fkv = u32x2{};
    const unsigned fkvo = FL_KV + (hp2 * 16 + (lane & 15)) * 8;
    const unsigned q = (lane & 15) >> 2, p = lane & 3;
    const unsigned wqb = 2 * g * WQ_PIECE + q * 64 + p * 8;
    const unsigned w1b = W1_OFF + g * W1_PIECE + q * 64 + p * 8;
    f32x4 aq[8][2], ah[NFT][2];
#pragma unroll
    for (int i = 0; i < 8; ++i) { aq[i][0] = f32x4{}; aq[i][1] = f32x4{}; }
#pragma unroll
    for (int i = 0; i < NFT; ++i) { ah[i][0] = f32x4{}; ah[i][1] = f32x4{}; }
    constexpr int NT = 8 + NFT;
    bf16x8 wf[4];
#define PJ_RD(slot, s, n)                                                                                       \
    (((n) < 8) ? tr_frag(L + (slot) + wqb + (8 * (s)) * WQ_PIECE + ((n) >> 1) * 256 + ((n) & 1) * 32, L + (slot) + wqb + (8 * (s) + 1) * WQ_PIECE + ((n) >> 1) * 256 + ((n) & 1) * 32) \
               : tr_frag(L + (slot) + w1b + (4 * (s)) * W1_PIECE + (((n) - 8) >> 1) * 512 + ((n) & 1) * 32, L + (slot) + w1b + (4 * (s)) * W1_PIECE + 256 + (((n) - 8) >> 1) * 512 + ((n) & 1) * 32))
#pragma unroll
    for (int n = 0; n < 3; ++n) wf[n] = PJ_RD(0, 0, n);
#define PJ_CHUNK(c, XC, XN)                                                                                     \
    do {                                                                                                        \
        constexpr int slot_ = ((c) % 3) * PSLOT, nslot_ = (((c) + 1) % 3) * PSLOT, dslot_ = (((c) + 2) % 3) * PSLOT; \
        if ((c) == 7) flag_ld(fkv, flb, fkvo);                                                                  \
          \
        if ((c) == 8) flag_wait<true>(wid, fkv, flb, fkvo, tag);                                                \
        SBAR();                                                                                                 \
        _Pragma("unroll") for (int m = 0; m < 2 * NT; ++m) {                                                    \
            const int s = m / NT, n = m % NT;                                                                   \
            if (m == 2 * NT - 3) {       \
                if ((c) < 16 / SPC) asm volatile("s_waitcnt vmcnt(%4) lgkmcnt(0)\n\ts_barrier" : "+v"(XN[0]), "+v"(XN[1]), "+v"(XN[2]), "+v"(XN[3]) : "i"(3 + SPC) : "memory"); \
                else if ((c) + 2 < 10) asm volatile("s_waitcnt vmcnt(3) lgkmcnt(0)\n\ts_barrier" : "+v"(XN[0]), "+v"(XN[1]), "+v"(XN[2]), "+v"(XN[3]), "+v"(fkv)::"memory"); \
                else if ((c) + 1 < 10) asm volatile("s_waitcnt vmcnt(0) lgkmcnt(0)\n\ts_barrier" : "+v"(XN[0]), "+v"(XN[1]), "+v"(XN[2]), "+v"(XN[3])::"memory"); \
                if ((c) == 16 / SPC) flag_set(wid, flb, kvflag, tag);        \
            }                                                                                                   \
            const int m3 = m + 3;                                                                               \
            if (m3 < 2 * NT) wf[m3 & 3] = PJ_RD(slot_, m3 / NT, m3 % NT);                                       \
            else if ((c) + 1 < 10) wf[m3 & 3] = PJ_RD(nslot_, 0, m3 - 2 * NT);                                  \
            const bf16x8 x0 = __builtin_bit_cast(bf16x8, XC[2 * s]), x1 = __builtin_bit_cast(bf16x8, XC[2 * s + 1]); \
            if (n < 8) { aq[n & 7][0] = MFMA(wf[m & 3], x0, aq[n & 7][0]); aq[n & 7][1] = MFMA(wf[m & 3], x1, aq[n & 7][1]); } \
            else { ah[(n - 8) % NFT][0] = MFMA(wf[m & 3], x0, ah[(n - 8) % NFT][0]); ah[(n - 8) % NFT][1] = MFMA(wf[m & 3], x1, ah[(n - 8) % NFT][1]); } \
            SBAR();                                                                                             \
              \
            if ((c) + 1 < 10 && m < 4) pj_xload1(pj, XN[m & 3], (c) + 1, m);                        \
            if ((c) + 2 < 10 && (m == 4 || m == 6 || m == 8)) pj_dma1(pj, lds0, wid, (c) + 2, dslot_, (m - 4) >> 1); \
            if (!NOKV && (c) < 16 / SPC && m >= KVP0 && (m - KVP0) % KVPS == 0 && (m - KVP0) / KVPS < SPC) {     \
                const int i_ = (SPC * (c) + (m - KVP0) / KVPS) & 15; kv_store(kvo, kv[i_], i_); }                \
            if ((c) == 9 && m < 16 && !(m & 1)) {                           \
                const int i_ = m >> 1;                                                                          \
                if ((i_ & 3) == 0) dma1<0>(i_ < 4 ? m0k0 : m0k1, i_ < 4 ? kptr0 : kptr1, lane * 16);            \
                if ((i_ & 3) == 1) dma1<1024>(i_ < 4 ? m0k0 : m0k1, i_ < 4 ? kptr0 : kptr1, lane * 16);         \
                if ((i_ & 3) == 2) dma1<2048>(i_ < 4 ? m0k0 : m0k1, i_ < 4 ? kptr0 : kptr1, lane * 16);         \
                if ((i_ & 3) == 3) dma1<3072>(i_ < 4 ? m0k0 : m0k1, i_ < 4 ? kptr0 : kptr1, lane * 16);         \
            }                                                                                                   \
            if ((c) == 9 && m == 16) warm_lds(m0v0, wo, w2, blockIdx.x * 8 + wid);        \
            SBAR();                                                                                             \
        }                                                                                                       \
    } while (0)
    static_assert((2 * NT) % 4 == 0, "fragment window");
    PJ_CHUNK(0, xa, xb); PJ_CHUNK(1, xb, xa); PJ_CHUNK(2, xa, xb); PJ_CHUNK(3, xb, xa); PJ_CHUNK(4, xa, xb);
    PJ_CHUNK(5, xb, xa); PJ_CHUNK(6, xa, xb); PJ_CHUNK(7, xb, xa); PJ_CHUNK(8, xa, xb); PJ_CHUNK(9, xb, xa);
#undef PJ_CHUNK
#undef PJ_RD
    asm volatile("s_waitcnt vmcnt(1)" ::: "memory");
#pragma unroll
    for (int a = 0; a < 2; ++a)
#pragma unroll
        for (int v = 0; v < 4; ++v) qf[a][v] = __builtin_bit_cast(bf16x8, pack44s(aq[2 * v][a], aq[2 * v + 1][a], QSCALE));
#pragma unroll
    for (int v = 0; v < NFT / 2; ++v) {
        float bias[8];
#pragma unroll
        for (int t = 0; t < 2; ++t) {
            const u32x2 q2 = bb[2 * v + t];
            bias[4 * t + 0] = bflo(q2[0]); bias[4 * t + 1] = bfhi(q2[0]); bias[4 * t + 2] = bflo(q2[1]); bias[4 * t + 3] = bfhi(q2[1]);
        }
#pragma unroll
        for (int a = 0; a < 2; ++a) {
            f32x4 h0, h1;
#pragma unroll
            for (int i = 0; i < 4; ++i) { h0[i] = mish_f(ah[2 * v][a][i] + bias[i]); h1[i] = mish_f(ah[2 * v + 1][a][i] + bias[4 + i]); }
            st_wt_o(uptr(ws + WS_CH + ((size_t)(q16_0 + a) * NKS_CH + NKS_C + (f0 >> 5) + v) * 1024), lane * 16, pack44(h0, h1));
        }
    }
}

#define NOP_SO(S, o) asm volatile("s_nop 7\n\ts_nop 3" : "+v"(S[0]), "+v"(S[1]), "+v"(o[0]), "+v"(o[1]), "+v"(o[2]), "+v"(o[3]))
#define NOP_A(A) asm volatile("s_nop 7\n\ts_nop 1" : "+v"(A[0][0]), "+v"(A[0][1]), "+v"(A[0][2]), "+v"(A[0][3]), "+v"(A[1][0]), "+v"(A[1][1]), "+v"(A[1][2]), "+v"(A[1][3]), \
    "+v"(A[2][0]), "+v"(A[2][1]), "+v"(A[2][2]), "+v"(A[2][3]), "+v"(A[3][0]), "+v"(A[3][1]), "+v"(A[3][2]), "+v"(A[3][3]), "+v"(A[4][0]), "+v"(A[4][1]), "+v"(A[4][2]), "+v"(A[4][3]))
DI float xh_sum(float v) { auto r = __builtin_amdgcn_permlane32_swap(__float_as_uint(v), __float_as_uint(v), false, false); return __uint_as_float(r[0]) + __uint_as_float(r[1]); }
DI float xh_max(float v) { auto r = __builtin_amdgcn_permlane32_swap(__float_as_uint(v), __float_as_uint(v), false, false); return __builtin_fmaxf(__uint_as_float(r[0]), __uint_as_float(r[1])); }
DI float mul_s(float a, float b) { float r; asm("v_mul_f32 %0, %1, %2" : "=v"(r) : "v"(a), "v"(b)); return r; }
DI float sub_s(float a, float b) { float r; asm("v_sub_f32 %0, %1, %2" : "=v"(r) : "v"(a), "v"(b)); return r; }
DI u32x4 pack_o(const f32x16& a, int y, float sc) {
    u32x4 w; w[0] = cvtpk(mul_s(a[4 * y + 0], sc), mul_s(a[4 * y + 1], sc)); w[1] = cvtpk(mul_s(a[4 * y + 2], sc), mul_s(a[4 * y + 3], sc));
    w[2] = cvtpk(mul_s(a[8 + 4 * y + 0], sc), mul_s(a[8 + 4 * y + 1], sc)); w[3] = cvtpk(mul_s(a[8 + 4 * y + 2], sc), mul_s(a[8 + 4 * y + 3], sc)); return w;
}
DI void mask_diag(f32x16& s, int dq) {
    const float ninf = -__builtin_inff();
#pragma unroll
    for (int r0 = 0; r0 < 16; r0 += 4) {
        unsigned long long m0, m1, m2, m3;
        const int kc = 16 * ((r0 >> 2) & 1) + 8 * (r0 >> 3);
        float a = s[r0], b = s[r0 + 1], c = s[r0 + 2], d = s[r0 + 3];
        asm("v_cmp_gt_i32_e64 %4, %9, %8\n\tv_cmp_gt_i32_e64 %5, %10, %8\n\tv_cmp_gt_i32_e64 %6, %11, %8\n\tv_cmp_gt_i32_e64 %7, %12, %8\n\t"
            "v_cndmask_b32_e64 %0, %0, %13, %4\n\tv_cndmask_b32_e64 %1, %1, %13, %5\n\tv_cndmask_b32_e64 %2, %2, %13, %6\n\tv_cndmask_b32_e64 %3, %3, %13, %7"
            : "+v"(a), "+v"(b), "+v"(c), "+v"(d), "=&s"(m0), "=&s"(m1), "=&s"(m2), "=&s"(m3)
            : "v"(dq), "i"(kc), "i"(kc + 1), "i"(kc + 2), "i"(kc + 3), "v"(ninf));
        s[r0] = a; s[r0 + 1] = b; s[r0 + 2] = c; s[r0 + 3] = d;
    }
}
#define VIL 1
#define VIL0 0
#define FRW 8
#define FRA 6

#define ABL_K3 0
constexpr int K3_SLOT = 36864;
constexpr int K3_RED = 2 * K3_SLOT;
constexpr int PP_SET = 8 * 5 * 1024;
DI float silu_f(float v) { return v * __builtin_amdgcn_rcpf(1.0f + __builtin_amdgcn_exp2f(-1.4426950408889634f * v)); }
DI f32x4 unpk_lo(const u32x4& w) { return f32x4{bflo(w[0]), bfhi(w[0]), bflo(w[1]), bfhi(w[1])}; }
DI f32x4 unpk_hi(const u32x4& w) { return f32x4{bflo(w[2]), bfhi(w[2]), bflo(w[3]), bfhi(w[3])}; }
template <int OFF> DI void dma_sc1(const void* sbase, unsigned voff) { asm volatile("global_load_lds_dwordx4 %0, %1 offset:%2 sc1" ::"v"(voff), "s"(sbase), "i"(OFF) : "memory"); }

struct K3S { const char *aptr, *wptr; unsigned avo, wvo[3], m0a, m0w; };
DI K3S k3_setup(unsigned lds0, unsigned wid, unsigned lane) {
    K3S k;
    k.aptr = nullptr; k.wptr = nullptr;
    k.avo = lane * 16;
    const unsigned p0 = wid >= 4 ? 12 + 2 * (wid & 3) : 3 * wid;
#pragma unroll
    for (int i = 0; i < 3; ++i) {
        const unsigned o = (p0 + i) * 1024 + lane * 16, row = o / 320, c16 = (o % 320) >> 4;
        k.wvo[i] = row * 1280 + ((c16 ^ (((row >> 2) & 1) << 1)) << 4) - 1024 * i;
    }
    k.m0a = lds0 + wid * 2048; k.m0w = lds0 + 16384 + p0 * 1024;
    return k;
}
DI void k3_point(K3S& k, const char* ws, const char* w, unsigned wid, int qt, int mt, int kstep, int stage) {
    k.aptr = uptr(ws + WS_CH + ((size_t)(8 * qt + wid) * NKS_CH + kstep) * 1024);
    k.wptr = uptr(w + (size_t)stage * 64 * 1280 + mt * 320);
    asm volatile("s_nop 4" : "+s"(k.aptr), "+s"(k.wptr));
}
DI void k3_stage(K3S& k, unsigned slot, bool three) {
    set_m0(k.m0a + slot); asm volatile("s_nop 0"); dma_sc1<0>(k.aptr, k.avo); dma_sc1<1024>(k.aptr, k.avo);
    set_m0(k.m0w + slot); asm volatile("s_nop 0"); dma_nom0<0>(k.wptr, k.wvo[0]); dma_nom0<1024>(k.wptr, k.wvo[1]);
    if (three) dma_nom0<2048>(k.wptr, k.wvo[2]);
    k.aptr += 2048; k.wptr += 81920;
}
DI void k3_publish(const f32x4 (&res)[10], f32x4 (&own)[2][2], const char* pset, unsigned vo, unsigned r) {
    own[1][0] = f32x4{}; own[1][1] = f32x4{};
#pragma unroll
    for (int j = 0; j < 5; ++j) {
        if ((j & 3) == r) { own[j >> 2][0] = res[2 * j]; own[j >> 2][1] = res[2 * j + 1]; }
        else st_wt_o(pset + j * 1024, vo, pack44(res[2 * j], res[2 * j + 1]));
    }
}

template <int GRP>
DI void k3_body(K3S& k, const char* __restrict__ x, const char* __restrict__ wo, char* __restrict__ ws, char* __restrict__ out, char* lds, unsigned wid, u64 tag,
                int ks, int qt, int mt, unsigned cflag) {
    constexpr int P = GRP ? 4 : 5;
    const unsigned lane = flane(), c = lane & 15, g = lane >> 4;
    const unsigned w4 = wid & 3, qh = w4 >> 1, mh = w4 & 1;
    char* stamp_base = ws; (void)stamp_base;
    const char* flb = uptr(ws + WS_FL);
    lds_cptr L = (lds_cptr)lds;
    const unsigned ard = (8 * qh + GRP) * 1024 + lane * 16;
    const unsigned q = (lane & 15) >> 2, p = lane & 3;
    const unsigned wrow = 16384 + (32 * GRP + 4 * g + q) * 320 + 160 * mh + 8 * p;
    const unsigned sgn = ((g & 1) ? 32u : 0u);
    const unsigned wrdE = (mh & 1) ? wrow - sgn : wrow + sgn;
    const unsigned wrdO = (mh & 1) ? wrow + sgn : wrow - sgn;
    f32x4 acc[5][4];
    bf16x8 af[2][4], wf[4], w4f[2];
    unsigned cur;
#define K3_RW(slot, n) tr_frag(L + (slot) + (((n) & 1) ? wrdO : wrdE) + 32 * (n), L + (slot) + (((n) & 1) ? wrdO : wrdE) + 32 * (n) + 16 * 320)
#define K3_RA(slot, a) lds_frag(L + (slot) + ard + (a) * 2048)
#define K3_ZERO() do { _Pragma("unroll") for (int n = 0; n < 5; ++n) _Pragma("unroll") for (int a = 0; a < 4; ++a) acc[n][a] = f32x4{}; NOP_A(acc); cur = 0; } while (0)
#define K3_ITER(PAR, WN, RD, DM, SAFE, HOOK)                                                                            \
    do {                                                                                                                \
        if (ABL_K3 & 1) asm volatile("s_waitcnt vmcnt(%0) lgkmcnt(0)" ::"i"(WN) : "memory");                            \
        else if (ABL_K3 & 8) asm volatile("s_waitcnt lgkmcnt(0)\n\ts_barrier" ::: "memory");                            \
        else asm volatile("s_waitcnt vmcnt(%0) lgkmcnt(0)\n\ts_barrier" ::"i"(WN) : "memory");                          \
        HOOK;                                                                                                           \
        const unsigned nxt_ = cur == 3 * K3_SLOT ? 0u : cur + K3_SLOT;                                                  \
        _Pragma("unroll") for (int n = 0; n < 5; ++n) {                                                                 \
            _Pragma("unroll") for (int a = 0; a < 4; ++a) {                                                             \
                if (SAFE) mfma_acc_s(acc[n][a], n < 4 ? wf[n & 3] : w4f[PAR], af[PAR][a]);                              \
                else mfma_acc(acc[n][a], n < 4 ? wf[n & 3] : w4f[PAR], af[PAR][a]);                                     \
                SBAR();                                                                                                 \
                if (RD && !(ABL_K3 & 2)) {                                                                              \
                    if (n == 0 && a == 0) w4f[1 - (PAR)] = K3_RW(nxt_, 4);                                              \
                    if (n < 4 && a == 1) af[1 - (PAR)][n & 3] = K3_RA(nxt_, n & 3);                                     \
                    if (n >= 1 && a == 0) wf[(n - 1) & 3] = K3_RW(nxt_, n - 1);                                         \
                }                                                                                                       \
                if (DM && !(ABL_K3 & 4)) {                                                                              \
                    if (n == 0 && a == 2) set_m0(k.m0a + cur);                                                          \
                    if (n == 0 && a == 3) dma_sc1<0>(k.aptr, k.avo);                                                    \
                    if (n == 1 && a == 2) dma_sc1<1024>(k.aptr, k.avo);                                                 \
                    if (n == 1 && a == 3) set_m0(k.m0w + cur);                                                          \
                    if (n == 2 && a == 2) dma_nom0<0>(k.wptr, k.wvo[0]);                                                \
                    if (n == 3 && a == 2) dma_nom0<1024>(k.wptr, k.wvo[1]);                                             \
                    if (n == 4 && a == 1 && !GRP) dma_nom0<2048>(k.wptr, k.wvo[2]);                                     \
                }                                                                                                       \
                SBAR();                                                                                                 \
            }                                                                                                           \
        }                                                                                                               \
        if (DM) { k.aptr += 2048; k.wptr += 81920; }                                                                    \
        cur = nxt_;                                                                                                     \
    } while (0)
#define K3_FIRST(PAR) do { _Pragma("unroll") for (int a = 0; a < 4; ++a) af[PAR][a] = K3_RA(0, a);                      \
        _Pragma("unroll") for (int n = 0; n < 4; ++n) wf[n] = K3_RW(0, n);                                              \
        w4f[PAR] = K3_RW(0, 4); } while (0)
#define K3_REDUCE(res)                                                                                                  \
    do {                                                                                                                \
        NOP_A(acc);                                                                                                     \
        lds_ptr X_ = (lds_ptr)lds + K3_RED + wid * 10240 + lane * 16;                                                   \
        lds_cptr Y_ = (lds_cptr)lds + K3_RED + (wid ^ 4) * 10240 + lane * 16;                                           \
        _Pragma("unroll") for (int a = 0; a < 2; ++a) _Pragma("unroll") for (int n = 0; n < 5; ++n)                     \
            *(__attribute__((address_space(3))) f32x4*)(X_ + (a * 5 + n) * 1024) = acc[n][GRP ? a : 2 + a];             \
        asm volatile("s_waitcnt lgkmcnt(0)\n\ts_barrier" ::: "memory");                                                 \
        _Pragma("unroll") for (int a = 0; a < 2; ++a) _Pragma("unroll") for (int n = 0; n < 5; ++n)                     \
            res[a * 5 + n] = acc[n][GRP ? 2 + a : a] + *(const __attribute__((address_space(3))) f32x4*)(Y_ + (a * 5 + n) * 1024); \
    } while (0)
#define NOHOOK (void)0
    const int grp64 = qt * 4 + mt;
    const char* pbase = uptr(ws + WS_PP + (size_t)grp64 * (8 * PP_SET) + wid * 5120);
    const unsigned vo = lane * 16;
    const unsigned r = (ks - wid) & 3;
    f32x4 res[10], ownB[2][2], ownS[2][2];
    asm volatile("s_waitcnt vmcnt(%0) lgkmcnt(0)\n\ts_barrier" ::"i"(P + 4) : "memory");
    STAMP(10);
    k3_stage(k, 2 * K3_SLOT, !GRP); k3_stage(k, 3 * K3_SLOT, !GRP);
    K3_ZERO();
    if (ks != 3) { K3_FIRST(1); K3_ITER(1, 2 * P + 4, true, true, true, NOHOOK); }
    else K3_FIRST(0);
    K3_ITER(0, 2 * P, true, true, true, flag_set(wid, flb, cflag, tag));
    K3_ITER(1, 2 * P, true, true, false, NOHOOK);
    K3_ITER(0, 2 * P, true, false, false, NOHOOK); K3_ITER(1, P, true, false, false, NOHOOK); K3_ITER(0, 0, true, false, false, NOHOOK);
    STAMP(11);
    {
        u32x2 f = u32x2{~(unsigned)tag, 0u};
        flag_wait<false>(wid, f, flb, FL_C + ((qt < 8 ? qt : 15 - qt) * 32 + 8 * ks + (lane & 7)) * 8, tag);
    }
    STAMP(18);
    k3_point(k, ws, wo, wid, qt, mt, 32 * ks, 16 * ks);
    K3_ITER(1, 0, false, false, false, (k3_stage(k, 0, !GRP), k3_stage(k, K3_SLOT, !GRP)));
    K3_REDUCE(res);
    asm volatile("s_waitcnt vmcnt(%0) lgkmcnt(0)\n\ts_barrier" ::"i"(P) : "memory");
    STAMP(19);
    k3_stage(k, 2 * K3_SLOT, !GRP); k3_stage(k, 3 * K3_SLOT, !GRP);
    k3_publish(res, ownB, pbase + (4 + ks) * PP_SET, vo, r);
    K3_ZERO();
    K3_FIRST(0);
    K3_ITER(0, 2 * P + 3, true, true, true, NOHOOK); K3_ITER(1, 2 * P + 3, true, true, false, NOHOOK);
    for (int it = 0; it < 5; ++it) { K3_ITER(0, 2 * P, true, true, false, NOHOOK); K3_ITER(1, 2 * P, true, true, false, NOHOOK); }
    u32x2 xv[2][2];
    unsigned oo[2][2];
#pragma unroll
    for (int sl = 0; sl < 2; ++sl)
#pragma unroll
        for (int t = 0; t < 2; ++t) {
            const unsigned i = 2 * (sl ? 4 : r) + t, a = i >= 5 ? 1 : 0, n = i - 5 * a;
            oo[sl][t] = (16 * (8 * qt + 4 * qh + 2 * GRP + a) + c) * (DM * 2) + (16 * (10 * mt + 5 * mh + n) + 4 * g) * 2;
        }
#define K3_XLD() asm volatile("global_load_dwordx2 %0, %4, %8\n\tglobal_load_dwordx2 %1, %5, %8\n\tglobal_load_dwordx2 %2, %6, %8\n\tglobal_load_dwordx2 %3, %7, %8" \
        : "=&v"(xv[0][0]), "=&v"(xv[0][1]), "=&v"(xv[1][0]), "=&v"(xv[1][1]) : "v"(oo[0][0]), "v"(oo[0][1]), "v"(oo[1][0]), "v"(oo[1][1]), "s"(x) : "memory")
    K3_ITER(0, 2 * P, true, false, false, NOHOOK); K3_ITER(1, P, true, false, false, NOHOOK); K3_ITER(0, 0, true, false, false, K3_XLD()); K3_ITER(1, 0, false, false, false, NOHOOK);
    STAMP(12);
    K3_REDUCE(res);
    STAMP(17);
    k3_publish(res, ownS, pbase + ks * PP_SET, vo, r);
    u32x2 fe[4];
    const bool bump = blockIdx.x == 0 && wid == 0;
    if (bump) {
#pragma unroll
        for (int i = 0; i < 4; ++i) flag_ld(fe[i], flb, FL_X + (64 * i + lane) * 8);
    }
    STAMP(14);
    asm volatile("s_waitcnt vmcnt(0)\n\ts_barrier" : "+v"(xv[0][0]), "+v"(xv[0][1]), "+v"(xv[1][0]), "+v"(xv[1][1]), "+v"(fe[0]), "+v"(fe[1]), "+v"(fe[2]), "+v"(fe[3])::"memory");
    STAMP(15);
    flag_set(wid, flb, FL_S + (grp64 * 4 + ks) * 128, tag);
    {
        u32x2 f = u32x2{~(unsigned)tag, 0u};
        const unsigned o3 = (lane & 3) == (unsigned)ks ? (ks + 1) & 3 : lane & 3;
        flag_wait<false>(0, f, flb, FL_S + (grp64 * 4 + o3) * 128, tag);
    }
    STAMP(16);
    const int nown = r == 0 ? 2 : 1;
    u32x4 pp[2][6];
#pragma unroll
    for (int sl = 0; sl < 2; ++sl)
        if (sl < nown) {
            const char* pj = pbase + (sl ? 4 : r) * 1024;
#pragma unroll
            for (int i = 0; i < 3; ++i) { const unsigned st = (ks + 1 + i) & 3; ld_wt(pp[sl][i], pj + st * PP_SET, vo); ld_wt(pp[sl][3 + i], pj + (4 + st) * PP_SET, vo); }
        }
    asm volatile("s_waitcnt vmcnt(0)" : "+v"(pp[0][0]), "+v"(pp[0][1]), "+v"(pp[0][2]), "+v"(pp[0][3]), "+v"(pp[0][4]), "+v"(pp[0][5]),
                 "+v"(pp[1][0]), "+v"(pp[1][1]), "+v"(pp[1][2]), "+v"(pp[1][3]), "+v"(pp[1][4]), "+v"(pp[1][5])::"memory");
#pragma unroll
    for (int sl = 0; sl < 2; ++sl)
        if (sl < nown) {
#pragma unroll
            for (int t = 0; t < 2; ++t) {
                f32x4 sv = ownS[sl][t], bv = ownB[sl][t];
#pragma unroll
                for (int i = 0; i < 3; ++i) { sv += t ? unpk_hi(pp[sl][i]) : unpk_lo(pp[sl][i]); bv += t ? unpk_hi(pp[sl][3 + i]) : unpk_lo(pp[sl][3 + i]); }
                const float o0 = bflo(xv[sl][t][0]) + sv[0] * silu_f(bv[0]), o1 = bfhi(xv[sl][t][0]) + sv[1] * silu_f(bv[1]);
                const float o2 = bflo(xv[sl][t][1]) + sv[2] * silu_f(bv[2]), o3 = bfhi(xv[sl][t][1]) + sv[3] * silu_f(bv[3]);
                u32x2 w; w[0] = cvtpk(o0, o1); w[1] = cvtpk(o2, o3);
                *(u32x2*)(out + oo[sl][t]) = w;
            }
        }
    if (bump) {
#pragma unroll
        for (int i = 0; i < 4; ++i) flag_wait<false>(0, fe[i], flb, FL_X + (64 * i + lane) * 8, tag);
        flag_set(0, flb, FL_EPOCH, tag);
    }
    STAMP(13);
#undef K3_RW
#undef K3_RA
#undef K3_ZERO
#undef K3_ITER
#undef K3_FIRST
#undef K3_REDUCE
#undef K3_XLD
#undef NOHOOK
}

__global__ __launch_bounds__(512, 2) void fused(const char* __restrict__ x, const char* __restrict__ ckv, const char* __restrict__ wuk, const char* __restrict__ wuv,
                                              const char* __restrict__ wq, const char* __restrict__ w1, const char* __restrict__ b1, char* __restrict__ ws,
                                              const char* __restrict__ wo, const char* __restrict__ w2, char* __restrict__ out) {
    extern __shared__ __attribute__((aligned(16))) char lds[];
    const unsigned wid = rfl(threadIdx.x >> 6), grp = wid >> 2, w4 = wid & 3;
    char* stamp_base = ws; (void)stamp_base;
    STAMP(0);
    const int b = blockIdx.x, xcd = b & 7, idx = b >> 3;
    const int vt = idx & 1, hp = 2 * xcd + ((idx >> 1) & 1), rb = idx >> 2;
    const int head = 4 * xcd + (idx & 3), pi = idx >> 2;
    const int HB = 15 - pi, LB = pi;
    const int myblk = grp ? LB : HB;
    const int q16_0 = 8 * myblk + 2 * w4;
    const bool nft4 = head < 22;
    const int f0 = nft4 ? 64 * head : 1408 + 32 * (head - 22);
    const char* Abase = ckv + (size_t)rb * 256 * 1024;
    const char* Wbase = (vt ? wuv : wuk) + hp * 512;
    const unsigned lane0 = flane();
#define lane lane0
    const int row = 8 * b + wid;
    const unsigned l40 = lane < 40 ? lane : 39;
    u32x4 x0, x1;
    {
        const char* xr = uptr(x + (size_t)row * 1280);
        const unsigned vo = l40 * 32;
        asm volatile("global_load_dwordx4 %0, %2, %3\n\tglobal_load_dwordx4 %1, %2, %3 offset:16" : "=&v"(x0), "=&v"(x1) : "v"(vo), "s"(xr) : "memory");
    }
    unsigned avo[4], wvo[4];
    {
        const unsigned lds0 = (unsigned)(uintptr_t)lds;
        const char* Aw = Abase + (size_t)wid * 32 * 1024;
        const char* Ww = Wbase + (size_t)wid * 8 * 8192;
        const unsigned m0a = lds0 + wid * 4096, m0w = lds0 + 32768 + wid * 4096;
#pragma unroll
        for (int p = 0; p < 4; ++p) {
            const unsigned rw = 8 * p + (lane >> 3);
            avo[p] = rw * 1024 + (((lane & 7) ^ ((rw >> 1) & 7)) << 4) - 1024 * p;
            const unsigned k = 2 * p + (lane >> 5);
            const unsigned f = (k & 3) | ((wid & 1) << 2);
            wvo[p] = k * 8192 + (((lane & 31) ^ (f << 1)) << 4) - 1024 * p;
        }
#pragma unroll
        for (int kt = 0; kt < 2; ++kt) {
            const char* a_ = Aw + kt * 128; const char* w_ = Ww + (size_t)kt * 64 * 8192; const unsigned stg = kt * 65536;
            dma1<0>(m0a + stg, a_, avo[0]); dma1<1024>(m0a + stg, a_, avo[1]); dma1<2048>(m0a + stg, a_, avo[2]); dma1<3072>(m0a + stg, a_, avo[3]);
            dma1<0>(m0w + stg, w_, wvo[0]); dma1<1024>(m0w + stg, w_, wvo[1]); dma1<2048>(m0w + stg, w_, wvo[2]); dma1<3072>(m0w + stg, w_, wvo[3]);
        }
    }
    u64 tag;
    asm volatile("s_load_dwordx2 %0, %1, 0x0\n\ts_waitcnt lgkmcnt(0)" : "=s"(tag) : "s"(uptr(ws + WS_FL + FL_EPOCH)) : "memory");
    tag += 1;
    {
        asm volatile("s_waitcnt vmcnt(16)" : "+v"(x0), "+v"(x1)::"memory");
        float v[16];
#pragma unroll
        for (int i = 0; i < 4; ++i) { v[2 * i] = bflo(x0[i]); v[2 * i + 1] = bfhi(x0[i]); v[8 + 2 * i] = bflo(x1[i]); v[8 + 2 * i + 1] = bfhi(x1[i]); }
        float s = 0.f;
#pragma unroll
        for (int i = 0; i < 16; ++i) s += v[i];
        if (lane >= 40) s = 0.f;
#pragma unroll
        for (int o = 32; o >= 1; o >>= 1) s += __shfl_xor(s, o);
        const float mean = s * (1.0f / DM);
        float q = 0.f;
#pragma unroll
        for (int i = 0; i < 16; ++i) { v[i] -= mean; q += v[i] * v[i]; }
        if (lane >= 40) q = 0.f;
#pragma unroll
        for (int o = 32; o >= 1; o >>= 1) q += __shfl_xor(q, o);
        const float rstd = 1.0f / sqrtf(q * (1.0f / DM) + 1e-5f);
        u32x4 o0, o1;
#pragma unroll
        for (int i = 0; i < 4; ++i) { o0[i] = cvtpk(v[2 * i] * rstd, v[2 * i + 1] * rstd); o1[i] = cvtpk(v[8 + 2 * i] * rstd, v[8 + 2 * i + 1] * rstd); }
        char* xf = ws + WS_XF + ((size_t)(row >> 4) * NKS_X + (l40 >> 1)) * 1024 + ((row & 15) + 32 * (l40 & 1)) * 16;
        if (lane < 40) { st_wt(xf, o0); st_wt(xf + 256, o1); }
    }
#undef lane
    u32x4 xa[4], xb[4], kv[16];
    u32x2 bb[4];
    KVO kvo;
    if (vt) k1_tile<true>(Abase, Wbase, ws + WS_VF, lds, avo, wvo, ckv, wuk, wuv, wid, hp, rb, ws, wq, w1, b1, tag, head, f0, nft4, q16_0, xa, bb, kv, kvo);
    else    k1_tile<false>(Abase, Wbase, ws + WS_KF, lds, avo, wvo, ckv, wuk, wuv, wid, hp, rb, ws, wq, w1, b1, tag, head, f0, nft4, q16_0, xa, bb, kv, kvo);
    STAMP(6);
    const unsigned lds0 = (unsigned)(uintptr_t)lds;
    const unsigned kslot0 = grp ? 114688 : 81920, kslot1 = kslot0 + 16384, kslot2 = grp ? 16384 : 0;
    const unsigned vslot0 = grp ? 147456 : 32768, vslot1 = grp ? 65536 : 49152;
    const int nL = 2 * pi + 2;
    const int nsw = grp ? nL : 1000;
    const size_t jump = (size_t)(17 - nL) * 16384;
    const char* kptr = uptr(ws + WS_KF + (size_t)head * 524288 + w4 * 4096);
    const char* vptr = uptr(ws + WS_VF + (size_t)head * 524288 + w4 * 4096);
    bf16x8 qf[8];
    {
        bf16x8 q16[2][4];
        const unsigned kvflag = FL_KV + (hp * 16 + vt * 8 + rb) * 8;
        if (nft4) proj_phase<4>(wq, w1, ws, lds, head, f0, q16_0, q16, wo, w2, lds0 + vslot0 + w4 * 4096, wid, xa, xb, bb, tag, kvflag, head >> 1, lds0 + kslot0 + w4 * 4096, lds0 + kslot1 + w4 * 4096, kptr, kptr + 16384, kv, kvo);
        else      proj_phase<2>(wq, w1, ws, lds, head, f0, q16_0, q16, wo, w2, lds0 + vslot0 + w4 * 4096, wid, xa, xb, bb, tag, kvflag, head >> 1, lds0 + kslot0 + w4 * 4096, lds0 + kslot1 + w4 * 4096, kptr, kptr + 16384, kv, kvo);
        kptr += 2 * 16384; if (nsw == 2) kptr += jump;
#pragma unroll
        for (int v = 0; v < 4; ++v) {
            const u32x4 r0 = __builtin_bit_cast(u32x4, q16[0][v]), r1 = __builtin_bit_cast(u32x4, q16[1][v]);
            u32x4 z0, z1;
#pragma unroll
            for (int w = 0; w < 4; ++w) {
                const auto s1 = __builtin_amdgcn_permlane16_swap(r0[w], r1[w], false, false);
                const auto s2 = __builtin_amdgcn_permlane32_swap(s1[0], s1[1], false, false);
                z0[w] = s2[0]; z1[w] = s2[1];
            }
            qf[2 * v] = __builtin_bit_cast(bf16x8, z0); qf[2 * v + 1] = __builtin_bit_cast(bf16x8, z1);
        }
    }
    STAMP(7);
    const unsigned lane = flane();
    if (grp == 0) {
        char* qh = ws + WS_QH + (size_t)b * 32768 + w4 * 8192 + lane * 16;
#pragma unroll
        for (int s = 0; s < 8; ++s) *(bf16x8*)(qh + s * 1024) = qf[s];
    }
    const char* flb = uptr(ws + WS_FL);
    lds_cptr Lb = (lds_cptr)lds + lane * 16;
    const unsigned m0b = lds0 + w4 * 4096;
    const unsigned dvo = lane * 16;
    int q0 = 128 * myblk + 32 * w4;
    int tile = 0;
    asm volatile("s_waitcnt lgkmcnt(0)\n\ts_barrier" ::: "memory");
    dma4(m0b + vslot0, vptr, dvo); vptr += 16384;
    dma4(m0b + kslot2, kptr, dvo); kptr += 16384;
    f32x16 o[4], S[2], negm;
    float mref = 0.f, lsum = 0.f, lfin = 1.f;
    bf16x8 pf[4], fr[FRW];
#pragma unroll
    for (int i = 0; i < 4; ++i) o[i] = f32x16{};
    negm = f32x16{}; S[0] = f32x16{}; S[1] = f32x16{};
    asm volatile("s_nop 1" : "+v"(negm));
    NOP_SO(S, o);
    unsigned ks_cur = kslot0, ks_nxt = kslot1, ks_nn = kslot2;
#define CTX_VO(l) ((((l) >> 4) & 1) * (NKS_CH * 1024) + (((l) & 15) + 16 * ((l) >> 5)) * 16)
#define KOFF(i) (((i) & 1) * 8192 + ((i) >> 1) * 1024)
#define VOFF(j) (((j) >> 3) * 8192 + ((j) & 3) * 2048 + (((j) >> 2) & 1) * 1024)
#define EARLY_K(slot) do { _Pragma("unroll") for (int i = 0; i < FRA; ++i) fr[i] = lds_frag(Lb + (slot) + KOFF(i)); } while (0)
    EARLY_K(kslot0);
    if (grp) asm volatile("s_barrier" ::: "memory");
#define PHASE_M(t, QK, PV, VS)                                                                              \
    do {                                                                                                                              \
        __builtin_amdgcn_s_setprio(0);                                                                                                \
        lds_cptr kp_ = Lb + ks_cur; lds_cptr vp_ = Lb + ((VS) ? vslot1 : vslot0);                                                     \
        if (!(QK)) { _Pragma("unroll") for (int i = 16; i < 16 + FRA; ++i) fr[i % FRW] = lds_frag(vp_ + VOFF(i - 16)); }               \
        _Pragma("unroll") for (int i = (QK) ? 0 : 16; i < ((PV) ? 32 : 16); ++i) {                                                    \
            const int n = i + FRA;                                                                                                    \
            if (n < 16) fr[n % FRW] = lds_frag(kp_ + KOFF(n)); else if ((PV) && n < 32) fr[n % FRW] = lds_frag(vp_ + VOFF(n - 16));    \
            SBAR();                                                                                                                   \
            if (i < 2) mfma32_ini(S[i & 1], fr[i % FRW], qf[i >> 1], negm);                                                        \
            else if (i < 16) mfma32_acc(S[i & 1], fr[i % FRW], qf[i >> 1]);                                                             \
            else mfma32_acc(o[i & 3], fr[i % FRW], pf[(i - 16) >> 2]);                                                                  \
            SBAR();                                                                                                                   \
            if ((QK) && i == 16 && (t) + 1 == nsw) {        \
                const char* qs_ = uptr(ws + WS_QH + (size_t)b * 32768 + w4 * 8192);                                                   \
                asm volatile("s_nop 4\n\tglobal_load_dwordx4 %0, %8, %9\n\tglobal_load_dwordx4 %1, %8, %9 offset:1024\n\tglobal_load_dwordx4 %2, %8, %9 offset:2048\n\tglobal_load_dwordx4 %3, %8, %9 offset:3072" \
                             : "+v"(qf[0]), "+v"(qf[1]), "+v"(qf[2]), "+v"(qf[3]), "+v"(qf[4]), "+v"(qf[5]), "+v"(qf[6]), "+v"(qf[7]) : "v"(dvo), "s"(qs_) : "memory"); \
                asm volatile("global_load_dwordx4 %4, %8, %9\n\tglobal_load_dwordx4 %5, %8, %9 offset:1024\n\tglobal_load_dwordx4 %6, %8, %9 offset:2048\n\tglobal_load_dwordx4 %7, %8, %9 offset:3072" \
                             : "+v"(qf[0]), "+v"(qf[1]), "+v"(qf[2]), "+v"(qf[3]), "+v"(qf[4]), "+v"(qf[5]), "+v"(qf[6]), "+v"(qf[7]) : "v"(dvo), "s"(qs_ + 4096) : "memory"); \
            }                                                                                                                         \
            SBAR();                                                                                                                   \
        }                                                                                                                             \
        __builtin_amdgcn_s_setprio(3);                                                                                                \
        TICK(tA);                                                                                                                     \
        asm volatile("s_barrier" ::: "memory");                                                                                       \
        TICK(tX);                                                                                                                     \
    } while (0)
#define EXP_PACK(DV, DK, VS)         \
    do {                                                                                                                              \
        float e_[32], a_[2];                                                                                                          \
        unsigned c_[16];                                                                                                              \
        _Pragma("unroll") for (int k = 0; k < 18; ++k) {                                                                              \
            if (k < 16) { e_[2 * k] = v_exp(S[k >> 3][2 * (k & 7)]); e_[2 * k + 1] = v_exp(S[k >> 3][2 * (k & 7) + 1]); }             \
            if (k == 1) { a_[0] = e_[0]; a_[1] = e_[1]; }                                                                             \
            if (k >= 2 && k < 17) { a_[0] = v_add(a_[0], e_[2 * k - 2]); a_[1] = v_add(a_[1], e_[2 * k - 1]); }                        \
            if (k >= 2) c_[k - 2] = v_cvt(e_[2 * k - 4], e_[2 * k - 3]);                                                              \
            if (VIL) {                                                   \
                if ((DV) && k == VIL0) set_m0(m0b + ((VS) ? vslot1 : vslot0));                                                        \
                if ((DV) && k == VIL0 + 1) dma_nom0<0>(vptr, dvo);                                                                    \
                if ((DV) && k == VIL0 + 3) dma_nom0<1024>(vptr, dvo);                                                                 \
                if ((DV) && k == VIL0 + 5) dma_nom0<2048>(vptr, dvo);                                                                 \
                if ((DV) && k == VIL0 + 7) dma_nom0<3072>(vptr, dvo);                                                                 \
                if ((DK) && k == VIL0 + 8) set_m0(m0b + ks_cur);                                                                      \
                if ((DK) && k == VIL0 + 9) dma_nom0<0>(kptr, dvo);                                                                    \
                if ((DK) && k == VIL0 + 11) dma_nom0<1024>(kptr, dvo);                                                                \
                if ((DK) && k == VIL0 + 13) dma_nom0<2048>(kptr, dvo);                                                                \
                if ((DK) && k == VIL0 + 15) dma_nom0<3072>(kptr, dvo);                                                                \
            }                                                                                                                         \
        }                                                                                                                             \
        _Pragma("unroll") for (int k = 0; k < 4; ++k) pf[k] = __builtin_bit_cast(bf16x8, u32x4{c_[4 * k], c_[4 * k + 1], c_[4 * k + 2], c_[4 * k + 3]}); \
        ts_ = v_add(a_[0], a_[1]);                                                                                                    \
    } while (0)
#define REBASE(FIRST)                                                          \
    do {                                                                                                                              \
        float rm = S[0][0];                                                                                                           \
        _Pragma("unroll") for (int e = 1; e < 32; ++e) rm = __builtin_fmaxf(rm, S[e >> 4][e & 15]);                                   \
        rm = xh_max(rm);                                                                                                              \
        const float dl = (FIRST) ? (rm == -__builtin_inff() ? 0.f : rm) : __builtin_fmaxf(rm, 0.f);                                   \
        mref += dl;                                                                                                                   \
        if (!(FIRST)) { float f = __builtin_amdgcn_exp2f(-dl); asm volatile("s_nop 1" : "+v"(f)); lsum *= f;               \
            _Pragma("unroll") for (int i = 0; i < 4; ++i) _Pragma("unroll") for (int r = 0; r < 16; ++r) o[i][r] = mul_s(o[i][r], f); } \
        _Pragma("unroll") for (int r = 0; r < 16; ++r) { S[0][r] = sub_s(S[0][r], dl); S[1][r] = sub_s(S[1][r], dl); }                \
        const float nm = -mref;                                                                                                       \
        _Pragma("unroll") for (int r = 0; r < 16; ++r) negm[r] = nm;                                                                  \
    } while (0)
#define PHASE_V(t, WN, LASTV, VS, DK, DV)                                                                                             \
    do {                                                                                                                              \
        const bool swn = ((t) + 1 == nsw), sw0 = ((t) == nsw);                                                                        \
        const bool diag = 64 * tile + 63 > q0 || sw0, first_ = (t) == 0 || sw0;                                                       \
        if (!VIL) { if (DV) dma4(m0b + ((VS) ? vslot1 : vslot0), vptr, dvo); if (DK) dma4(m0b + ks_cur, kptr, dvo); }                 \
        NOP_SO(S, o);                                                                                                                 \
        float ts_;                                                                                                                    \
        if (__builtin_expect(diag || first_, 0)) {                                                                                    \
            if (sw0) {                                                                                \
                float il_ = __builtin_amdgcn_rcpf(xh_sum(lsum));                                                                      \
                asm volatile("s_nop 1" : "+v"(il_));                                                                                  \
                const char* cp_ = uptr(ws + WS_CH + ((size_t)(8 * LB + 2 * w4) * NKS_CH + 4 * head) * 1024);                          \
                const unsigned co_ = CTX_VO(flane());                                                                                 \
                asm volatile("s_nop 4");                                                                                              \
                st_wt_i<0>(cp_, co_, pack_o(o[0], 0, il_)); st_wt_i<512>(cp_, co_, pack_o(o[0], 1, il_));                             \
                st_wt_i<1024>(cp_, co_, pack_o(o[1], 0, il_)); st_wt_i<1536>(cp_, co_, pack_o(o[1], 1, il_));                         \
                st_wt_i<2048>(cp_, co_, pack_o(o[2], 0, il_)); st_wt_i<2560>(cp_, co_, pack_o(o[2], 1, il_));                         \
                st_wt_i<3072>(cp_, co_, pack_o(o[3], 0, il_)); st_wt_i<3584>(cp_, co_, pack_o(o[3], 1, il_));                         \
                _Pragma("unroll") for (int i = 0; i < 4; ++i) o[i] = f32x16{};                                                        \
                lsum = 0.f; mref = 0.f; q0 = 128 * HB + 32 * w4;                                                                      \
            }                                                                                                                         \
            if (64 * tile + 63 > q0) {         \
                const int dq = (int)(lane & 31) - 4 * (int)(lane >> 5);                                                               \
                _Pragma("unroll") for (int u = 0; u < 2; ++u) {                                                                       \
                    const int kb = 64 * tile + 32 * u;                                                                                \
                    if (kb > q0) { _Pragma("unroll") for (int r = 0; r < 16; ++r) S[u][r] = -__builtin_inff(); }                       \
                    else if (kb == q0) mask_diag(S[u], dq);                                                                           \
                }                                                                                                                     \
            }                                                                                                                         \
            if (first_) REBASE(true);                                                                                                 \
        }                                                                                                                             \
        EXP_PACK(DV, DK, VS);                                                                                                         \
        if (DV) vptr += 16384 + ((t) + 2 == nsw ? jump : 0);                                                                          \
        if (DK) kptr += 16384 + ((t) + 4 == nsw ? jump : 0);                                                                          \
        if (__builtin_expect(__any(ts_ > PLIM), 0)) { REBASE(false); EXP_PACK(false, false, 0); }     \
        lsum += ts_;                                                                                                                  \
        if (!(LASTV)) EARLY_K(ks_nxt);                                                                                                \
        if (__builtin_expect(swn, 0)) {                                                                                               \
            negm = f32x16{};                                                                                                          \
            asm volatile("s_waitcnt vmcnt(%9)" : "+v"(qf[0]), "+v"(qf[1]), "+v"(qf[2]), "+v"(qf[3]), "+v"(qf[4]), "+v"(qf[5]), "+v"(qf[6]), "+v"(qf[7]), "+v"(negm) : "i"(WN) : "memory"); \
            tile += 17 - nL;                                                                                                          \
        }                                                                                                                             \
        TICK(tB);                                                                                                                     \
        asm volatile("s_cmp_eq_u32 %7, 0\n\ts_cbranch_scc1 .Lv1%=\n\ts_waitcnt vmcnt(%5)\n\ts_branch .Lv2%=\n.Lv1%=:\n\ts_waitcnt vmcnt(%6)\n.Lv2%=:\n\ts_barrier"                \
                     : "+v"(pf[0]), "+v"(pf[1]), "+v"(pf[2]), "+v"(pf[3]), "+v"(negm) : "i"(WN), "i"((WN) + 8), "s"(nsw - (t)) : "scc", "memory");   \
        TICK(tO);                                                                                                                     \
        { const unsigned r_ = ks_cur; ks_cur = ks_nxt; ks_nxt = ks_nn; ks_nn = r_; }                                                  \
        tile += 1;                                                                                                                    \
    } while (0)
    constexpr float PLIM = 256.0f;
    PHASE_M(0, true, false, 1);
    PHASE_V(0, 8, false, 1, true, true);
    flag_set(w4, flb, FL_H + (myblk * 32 + head) * 8, tag);
    for (int t = 1; t < 13; t += 2) {
        PHASE_M(t, true, true, 0); PHASE_V(t, 8, false, 0, true, true);
        PHASE_M(t + 1, true, true, 1); PHASE_V(t + 1, 8, false, 1, true, true);
    }
    PHASE_M(13, true, true, 0); PHASE_V(13, 8, false, 0, true, true);
    PHASE_M(14, true, true, 1); PHASE_V(14, 4, false, 1, false, true);
    PHASE_M(15, true, true, 0);
    const int ks = xcd >> 1, qt = 8 * (xcd & 1) + (idx >> 2), mt = idx & 3;
    u32x2 fh;
    const unsigned fho = FL_H + (qt * 32 + (lane & 31)) * 8;
    flag_ld(fh, flb, fho);
    PHASE_V(15, 4, false, 0, false, true);
    PIN(fh);
    PHASE_M(16, true, true, 1); PHASE_V(16, 0, true, 1, false, false);
    PHASE_M(17, false, true, 0);
    flag_wait<false>(0, fh, flb, fho, tag);
    if (!grp) asm volatile("s_barrier" ::: "memory");
    NOP_SO(S, o);
    K3S k3 = k3_setup(lds0, wid, lane);
    k3_point(k3, ws, w2, wid, qt, mt, NKS_C + 14 * ks, 7 * ks);
    k3_stage(k3, 0, grp == 0); k3_stage(k3, K3_SLOT, grp == 0);
    STAMP(8);
    {
        const float ls = xh_sum(lsum);
        const float mm = ls > 0.f ? mref : -__builtin_inff();
        lds_ptr X = (lds_ptr)lds + 81920 + wid * 8192 + lane * 16;
        f32x16 keep[2];
        if (grp) {
#pragma unroll
            for (int i = 0; i < 2; ++i) {
                keep[i] = o[2 + i];
#pragma unroll
                for (int y = 0; y < 4; ++y) *(__attribute__((address_space(3))) f32x4*)(X + (4 * i + y) * 1024) = f32x4{o[i][4 * y], o[i][4 * y + 1], o[i][4 * y + 2], o[i][4 * y + 3]};
            }
        } else {
#pragma unroll
            for (int i = 0; i < 2; ++i) {
                keep[i] = o[i];
#pragma unroll
                for (int y = 0; y < 4; ++y) *(__attribute__((address_space(3))) f32x4*)(X + (4 * i + y) * 1024) = f32x4{o[2 + i][4 * y], o[2 + i][4 * y + 1], o[2 + i][4 * y + 2], o[2 + i][4 * y + 3]};
            }
        }
        lds_ptr Y = (lds_ptr)lds + 73728 + wid * 512 + lane * 8;
        *(__attribute__((address_space(3))) f32x2*)Y = f32x2{mm, ls};
        asm volatile("s_waitcnt lgkmcnt(0)\n\ts_barrier" ::: "memory");
        const unsigned ow = wid ^ 4;
        lds_cptr Xo = (lds_cptr)lds + 81920 + ow * 8192 + lane * 16;
        const f32x2 oml = *(const __attribute__((address_space(3))) f32x2*)((lds_cptr)lds + 73728 + ow * 512 + lane * 8);
        const float mx = __builtin_fmaxf(mm, oml[0]);
        const float e0 = __builtin_amdgcn_exp2f(mm - mx), e1 = __builtin_amdgcn_exp2f(oml[0] - mx);
        const float inv = __builtin_amdgcn_rcpf(ls * e0 + oml[1] * e1);
        const float fs = e0 * inv, fo = e1 * inv;
        const char* cp = uptr(ws + WS_CH + ((size_t)(8 * HB + 2 * w4) * NKS_CH + 4 * head + 2 * grp) * 1024);
        const unsigned co = CTX_VO(lane);
#pragma unroll
        for (int i = 0; i < 2; ++i) {
            f32x16 m;
#pragma unroll
            for (int y = 0; y < 4; ++y) {
                const f32x4 pv = *(const __attribute__((address_space(3))) f32x4*)(Xo + (4 * i + y) * 1024);
#pragma unroll
                for (int r = 0; r < 4; ++r) m[4 * y + r] = keep[i][4 * y + r] * fs + pv[r] * fo;
            }
            if (i == 0) { st_wt_i<0>(cp, co, pack_o(m, 0, 1.f)); st_wt_i<512>(cp, co, pack_o(m, 1, 1.f)); }
            else { st_wt_i<1024>(cp, co, pack_o(m, 0, 1.f)); st_wt_i<1536>(cp, co, pack_o(m, 1, 1.f)); }
        }
    }
    STAMP(9);
#undef KOFF
#undef CTX_VO
#undef VOFF
#undef EARLY_K
#undef PHASE_M
#undef PHASE_V
#undef EXP_PACK
#undef REBASE
    if (grp == 0) k3_body<0>(k3, x, wo, ws, out, lds, wid, tag, ks, qt, mt, FL_C + (pi * 32 + head) * 8);
    else k3_body<1>(k3, x, wo, ws, out, lds, wid, tag, ks, qt, mt, FL_C + (pi * 32 + head) * 8);
}

extern "C" void kernel_launch(void* const* d_in, const int* in_sizes, int n_in, void* d_out, int out_size, void* d_ws, size_t ws_size,
                              hipStream_t stream) {
    const char* x = (const char*)d_in[0];
    const char* ckv = (const char*)d_in[1];
    const char* wuk = (const char*)d_in[3];
    const char* wuv = (const char*)d_in[4];
    char* ws = (char*)d_ws;
    (void)hipFuncSetAttribute((const void*)fused, hipFuncAttributeMaxDynamicSharedMemorySize, 163840);
    fused<<<256, 512, 163840, stream>>>(x, ckv, wuk, wuv, (const char*)d_in[2], (const char*)d_in[7], (const char*)d_in[8], ws, (const char*)d_in[5], (const char*)d_in[9], (char*)d_out);
}
```

```cpp
#include <hip/hip_runtime.h>
#include <hip/hip_bf16.h>
#include <stdint.h>

typedef __bf16 bf16x8 __attribute__((ext_vector_type(8)));
typedef __bf16 bf16x2 __attribute__((ext_vector_type(2)));
typedef short s16x4 __attribute__((ext_vector_type(4)));
typedef short s16x8 __attribute__((ext_vector_type(8)));
typedef short v4i16_t __attribute__((ext_vector_type(4)));
typedef float f32x16 __attribute__((ext_vector_type(16)));
typedef float f32x4 __attribute__((ext_vector_type(4)));
typedef float f32x2 __attribute__((ext_vector_type(2)));
typedef unsigned u32x4 __attribute__((ext_vector_type(4)));
typedef unsigned u32x2 __attribute__((ext_vector_type(2)));
typedef __attribute__((address_space(3))) const char* lds_cptr;
typedef __attribute__((address_space(3))) char* lds_ptr;

#define DI __device__ __forceinline__
#define SBAR() __builtin_amdgcn_sched_barrier(0)
#define MFMA(a, b, c) __builtin_amdgcn_mfma_f32_16x16x32_bf16((a), (b), (c), 0, 0, 0)
#define PIN(x) asm volatile("" : "+v"(x))
#define PINR(x) asm volatile("" ::"v"(x))

constexpr int S_ = 2048, NH = 32, HD = 128, DM = 640, DC = 512, FF = 1728;
constexpr int NKS_X = DM / 32;
constexpr int NKS_C = (NH * HD) / 32;
constexpr int NKS_H = FF / 32;
constexpr int NKS_CH = NKS_C + NKS_H;
constexpr size_t WS_XF = 0;
constexpr size_t WS_KF = WS_XF + (size_t)128 * NKS_X * 1024;
constexpr size_t WS_VF = WS_KF + (size_t)NH * 128 * 4 * 1024;
constexpr size_t WS_CH = WS_VF + (size_t)NH * 64 * 8 * 1024;
constexpr size_t WS_QH = WS_CH + (size_t)128 * NKS_CH * 1024;
constexpr size_t WS_PP = WS_QH + (size_t)256 * 32768;
constexpr size_t PP_BYTES = (size_t)64 * 8 * 8 * 5 * 1024;
constexpr size_t WS_FL = WS_PP + PP_BYTES;
constexpr size_t FL_EPOCH = 0;
constexpr size_t FL_X = 256;
constexpr size_t FL_KV = FL_X + 2048;
constexpr size_t FL_H = FL_KV + 2048;
constexpr size_t FL_C = FL_H + 4096;
constexpr size_t FL_S = 16384;
constexpr size_t WS_END = WS_FL + FL_S + (size_t)256 * 128;

DI unsigned rfl(unsigned x) { return __builtin_amdgcn_readfirstlane(x); }
DI unsigned flane() { unsigned r; asm volatile("v_mbcnt_lo_u32_b32 %0, -1, 0\n\tv_mbcnt_hi_u32_b32 %0, -1, %0" : "=v"(r)); return r; }
#define STAMP(i)
#define TICK(acc)
#define PJSTAMP(i)
DI const char* uptr(const char* p) { const unsigned long long v = (unsigned long long)p; const unsigned lo = rfl((unsigned)v), hi = rfl((unsigned)(v >> 32)); return (const char*)(((unsigned long long)hi << 32) | lo); }
DI unsigned cvtpk(float lo, float hi) { f32x2 v = {lo, hi}; bf16x2 b = __builtin_convertvector(v, bf16x2); return __builtin_bit_cast(unsigned, b); }
DI bf16x8 lds_frag(lds_cptr p) { return *(const __attribute__((address_space(3))) bf16x8*)p; }
DI s16x4 lds_tr(lds_cptr p) { return __builtin_bit_cast(s16x4, __builtin_amdgcn_ds_read_tr16_b64_v4i16((__attribute__((address_space(3))) v4i16_t*)p)); }
DI bf16x8 tr_frag(lds_cptr p0, lds_cptr p1) { s16x4 lo = lds_tr(p0), hi = lds_tr(p1); s16x8 v = __builtin_shufflevector(lo, hi, 0, 1, 2, 3, 4, 5, 6, 7); return __builtin_bit_cast(bf16x8, v); }
template <int OFF> DI void dma1(unsigned m0v, const void* sbase, unsigned voff) {
    asm volatile("s_mov_b32 m0, %0\n\ts_nop 0\n\tglobal_load_lds_dwordx4 %1, %2 offset:%3" ::"s"(m0v), "v"(voff), "s"(sbase), "i"(OFF) : "memory");
}
DI u32x4 pack44(const f32x4& a, const f32x4& b) { u32x4 w; w[0] = cvtpk(a[0], a[1]); w[1] = cvtpk(a[2], a[3]); w[2] = cvtpk(b[0], b[1]); w[3] = cvtpk(b[2], b[3]); return w; }
DI u32x4 pack44s(const f32x4& a, const f32x4& b, float sc) { u32x4 w; w[0] = cvtpk(a[0] * sc, a[1] * sc); w[1] = cvtpk(a[2] * sc, a[3] * sc); w[2] = cvtpk(b[0] * sc, b[1] * sc); w[3] = cvtpk(b[2] * sc, b[3] * sc); return w; }
DI u32x4 pack8(const f32x16& a, int b, float sc) {
    u32x4 w; w[0] = cvtpk(a[b + 0] * sc, a[b + 1] * sc); w[1] = cvtpk(a[b + 2] * sc, a[b + 3] * sc);
    w[2] = cvtpk(a[b + 4] * sc, a[b + 5] * sc); w[3] = cvtpk(a[b + 6] * sc, a[b + 7] * sc); return w;
}
DI u32x4 pack8n(const f32x16& a, int b) {
    u32x4 w; w[0] = cvtpk(a[b + 0], a[b + 1]); w[1] = cvtpk(a[b + 2], a[b + 3]); w[2] = cvtpk(a[b + 4], a[b + 5]); w[3] = cvtpk(a[b + 6], a[b + 7]); return w;
}
DI float bf2f(unsigned short u) { return __uint_as_float((unsigned)u << 16); }
DI float bflo(unsigned w) { return __uint_as_float(w << 16); }
DI float bfhi(unsigned w) { return __uint_as_float(w & 0xffff0000u); }
DI float xhalf_sum(float v) { auto r = __builtin_amdgcn_permlane32_swap(__float_as_uint(v), __float_as_uint(v), false, false); return __uint_as_float(r[0]) + __uint_as_float(r[1]); }
DI float xhalf_max(float v) { auto r = __builtin_amdgcn_permlane32_swap(__float_as_uint(v), __float_as_uint(v), false, false); return __builtin_fmaxf(__uint_as_float(r[0]), __uint_as_float(r[1])); }

DI void warm_load(unsigned& d, const char* wa, const char* wb, unsigned id) {
    const unsigned j = id - 1280u;
    const char* p = uptr(id < 1280u ? wa + (size_t)id * 4096 : wb + (size_t)(j < 539u ? j : 539u) * 4096);
    const unsigned voff = flane() * 64;
    asm volatile("s_nop 4\n\tglobal_load_dword %0, %1, %2" : "=&v"(d) : "v"(voff), "s"(p) : "memory");
}
DI void warm_lds(unsigned m0v, const char* wa, const char* wb, unsigned id) {
    const unsigned j = id - 1280u;
    const char* p = uptr(id < 1280u ? wa + (size_t)id * 4096 : wb + (size_t)(j < 539u ? j : 539u) * 4096);
    const unsigned voff = flane() * 64;
    asm volatile("s_mov_b32 m0, %0\n\ts_nop 4\n\tglobal_load_lds_dword %1, %2" ::"s"(m0v), "v"(voff), "s"(p) : "memory");
}

DI void mfma_acc(f32x4& acc, const bf16x8& a, const bf16x8& b) { asm volatile("v_mfma_f32_16x16x32_bf16 %0, %1, %2, %0" : "+v"(acc) : "v"(a), "v"(b)); }
DI void mfma_acc_s(f32x4& acc, const bf16x8& a, const bf16x8& b) { asm volatile("s_nop 1\n\tv_mfma_f32_16x16x32_bf16 %0, %1, %2, %0" : "+v"(acc) : "v"(a), "v"(b)); }
DI void mfma32_acc(f32x16& acc, const bf16x8& a, const bf16x8& b) { asm volatile("v_mfma_f32_32x32x16_bf16 %0, %1, %2, %0" : "+v"(acc) : "v"(a), "v"(b)); }
DI void mfma32_ini(f32x16& d, const bf16x8& a, const bf16x8& b, const f32x16& c) { asm volatile("v_mfma_f32_32x32x16_bf16 %0, %1, %2, %3" : "=&v"(d) : "v"(a), "v"(b), "v"(c)); }
DI float v_exp(float x) { float r; asm volatile("v_exp_f32 %0, %1" : "=v"(r) : "v"(x)); return r; }
DI float v_add(float a, float b) { float r; asm volatile("v_add_f32 %0, %1, %2" : "=v"(r) : "v"(a), "v"(b)); return r; }
DI unsigned v_cvt(float lo, float hi) { unsigned r; asm volatile("v_cvt_pk_bf16_f32 %0, %1, %2" : "=v"(r) : "v"(lo), "v"(hi)); return r; }
DI void set_m0(unsigned v) { asm volatile("s_mov_b32 m0, %0" ::"s"(v) : "memory"); }
template <int OFF> DI void dma_nom0(const void* sbase, unsigned voff) { asm volatile("global_load_lds_dwordx4 %0, %1 offset:%2" ::"v"(voff), "s"(sbase), "i"(OFF) : "memory"); }
template <int OFF> DI void dma_sc1(const void* sbase, unsigned voff) { asm volatile("global_load_lds_dwordx4 %0, %1 offset:%2 sc1" ::"v"(voff), "s"(sbase), "i"(OFF) : "memory"); }
template <int OFF> DI void dma1_sc1(unsigned m0v, const void* sbase, unsigned voff) {
    asm volatile("s_mov_b32 m0, %0\n\ts_nop 0\n\tglobal_load_lds_dwordx4 %1, %2 offset:%3 sc1" ::"s"(m0v), "v"(voff), "s"(sbase), "i"(OFF) : "memory");
}
DI void dma4(unsigned m0v, const void* sbase, unsigned voff) {
    asm volatile("s_mov_b32 m0, %0\n\ts_nop 0\n\tglobal_load_lds_dwordx4 %1, %2 sc1\n\tglobal_load_lds_dwordx4 %1, %2 offset:1024 sc1\n\tglobal_load_lds_dwordx4 %1, %2 offset:2048 sc1\n\tglobal_load_lds_dwordx4 %1, %2 offset:3072 sc1"
                 ::"s"(m0v), "v"(voff), "s"(sbase) : "memory");
}

typedef unsigned long long u64;
DI void st_wt(char* p, const u32x4& v) { asm volatile("global_store_dwordx4 %0, %1, off sc1\n\ts_nop 1" ::"v"(p), "v"(v) : "memory"); }
DI void st_wt_o(const char* sbase, unsigned voff, const u32x4& v) { asm volatile("global_store_dwordx4 %0, %1, %2 sc1\n\ts_nop 1" ::"v"(voff), "v"(v), "s"(sbase) : "memory"); }
template <int OFF> DI void st_wt_i(const char* sbase, unsigned voff, const u32x4& v) {
    asm volatile("global_store_dwordx4 %0, %1, %2 offset:%3 sc1\n\ts_nop 1" ::"v"(voff), "v"(v), "s"(sbase), "i"(OFF) : "memory");
}
DI void ld_wt(u32x4& d, const char* sbase, unsigned voff) { asm volatile("global_load_dwordx4 %0, %1, %2 sc1" : "=&v"(d) : "v"(voff), "s"(sbase) : "memory"); }
DI void flag_set(unsigned wid, const char* sbase, unsigned off, u64 tag) {
    const u32x2 v = u32x2{(unsigned)tag, (unsigned)(tag >> 32)};
    u64 ex;
    asm volatile("s_cmp_lg_u32 %1, 0\n\ts_cbranch_scc1 .Lfs%=\n\ts_mov_b64 %0, exec\n\ts_mov_b64 exec, 1\n\t"
                 "global_store_dwordx2 %2, %3, %4 sc1\n\ts_mov_b64 exec, %0\n.Lfs%=:\n\ts_nop 1"
                 : "=&s"(ex) : "s"(wid), "v"(off), "v"(v), "s"(sbase) : "scc", "memory");
}
DI void flag_ld(u32x2& f, const char* sbase, unsigned voff) { asm volatile("global_load_dwordx2 %0, %1, %2 sc1" : "=&v"(f) : "v"(voff), "s"(sbase) : "memory"); }
DI void flag_ld0(unsigned skip, u32x2& f, const char* sbase, unsigned voff) {
    asm volatile("s_cmp_lg_u32 %1, 0\n\ts_cbranch_scc1 .Lfz%=\n\tglobal_load_dwordx2 %0, %2, %3 sc1\n.Lfz%=:" : "+v"(f) : "s"(skip), "v"(voff), "s"(sbase) : "scc", "memory");
}
template <bool FLY>
DI void flag_wait(unsigned skip, u32x2& f, const char* sbase, unsigned voff, u64 tag) {
    unsigned cnt;
    asm volatile("s_cmp_lg_u32 %2, 0\n\ts_cbranch_scc1 .Lfe%=\n\t.if %6\n\ts_waitcnt vmcnt(0)\n\t.endif\n\t"
                 "v_cmp_ne_u64 vcc, %3, %0\n\ts_cbranch_vccz .Lfd%=\n\ts_mov_b32 %1, 0x40000\n"
                 ".Lfl%=:\n\tglobal_load_dwordx2 %0, %4, %5 sc1\n\ts_waitcnt vmcnt(0)\n\tv_cmp_ne_u64 vcc, %3, %0\n\ts_cbranch_vccz .Lfd%=\n\t"
                 "s_sleep 2\n\ts_sub_u32 %1, %1, 1\n\ts_cbranch_scc0 .Lfl%=\n"
                 ".Lfd%=:\n"
                 ".Lfe%=:"
                 : "+v"(f), "=&s"(cnt) : "s"(skip), "s"(tag), "v"(voff), "s"(sbase), "i"(FLY ? 1 : 0) : "vcc", "scc", "memory");
}

constexpr int WQ_PIECE = 1040, W1_PIECE = 1056, W1_OFF = 16 * WQ_PIECE, PSLOT = W1_OFF + 8 * W1_PIECE;
struct PJ { unsigned wqvo, w1vo, xv0, xv1; const char *wqs, *w1s, *xs; };
DI PJ pj_setup(const char* wq, const char* w1, const char* ws, unsigned wid, int head, int f0, bool nft4, int q16_0) {
    const unsigned lane = flane();
    PJ j;
    j.wqvo = ((lane >> 2) & 3) * 8192 + (lane >> 4) * 64 + (lane & 3) * 16;
    j.w1vo = ((lane >> 2) & 7) * 3456 + (nft4 ? (lane >> 5) * 64 : 0) + (lane & 3) * 16;
    j.wqs = uptr(wq + (size_t)wid * 8 * 8192 + head * 256);
    j.w1s = uptr(w1 + (size_t)wid * 8 * 3456 + f0 * 2);
    j.xs = uptr(ws + WS_XF + (size_t)q16_0 * NKS_X * 1024);
    j.xv0 = lane * 16; j.xv1 = lane * 16 + NKS_X * 1024;
    return j;
}
DI void pj_dma(const PJ& j, unsigned lds0, unsigned wid, int c, int slot) {
    dma1<0>(lds0 + slot + (2 * wid) * WQ_PIECE, j.wqs + (size_t)c * 64 * 8192, j.wqvo);
    dma1<0>(lds0 + slot + (2 * wid + 1) * WQ_PIECE, j.wqs + (size_t)c * 64 * 8192 + 4 * 8192, j.wqvo);
    dma1<0>(lds0 + slot + W1_OFF + wid * W1_PIECE, j.w1s + (size_t)c * 64 * 3456, j.w1vo);
}
DI void pj_dma1(const PJ& j, unsigned lds0, unsigned wid, int c, int slot, int i) {
    if (i < 2) dma1<0>(lds0 + slot + (2 * wid + i) * WQ_PIECE, j.wqs + (size_t)c * 64 * 8192 + i * 4 * 8192, j.wqvo);
    else dma1<0>(lds0 + slot + W1_OFF + wid * W1_PIECE, j.w1s + (size_t)c * 64 * 3456, j.w1vo);
}
DI void pj_xload1(const PJ& j, u32x4& X, int c, int i) {
    const char* p_ = j.xs + c * 2048 + (i >> 1) * 1024;
    asm volatile("global_load_dwordx4 %0, %1, %2 sc1" : "=&v"(X) : "v"((i & 1) ? j.xv1 : j.xv0), "s"(p_) : "memory");
}
struct KVO { const char* base; unsigned lo[2]; };
DI void kv_store(const KVO& o, const u32x4& v, int i) {
    if (i & 2) st_wt_i<2048>(o.base + (i >> 2) * 8192, o.lo[i & 1], v); else st_wt_i<0>(o.base + (i >> 2) * 8192, o.lo[i & 1], v);
}
DI void pj_xload(const PJ& j, u32x4 (&X)[4], int c) {
    const char* p_ = j.xs + c * 2048;
    asm volatile("global_load_dwordx4 %0, %4, %6 sc1\n\tglobal_load_dwordx4 %1, %5, %6 sc1\n\tglobal_load_dwordx4 %2, %4, %6 offset:1024 sc1\n\tglobal_load_dwordx4 %3, %5, %6 offset:1024 sc1"
                 : "=&v"(X[0]), "=&v"(X[1]), "=&v"(X[2]), "=&v"(X[3]) : "v"(j.xv0), "v"(j.xv1), "s"(p_) : "memory");
}

template <bool VT, class F>
DI void k1_mma(f32x4 (&acc)[8][4], int half, const bf16x8 (&c)[4], const bf16x8 (&w)[4], F&& filler) {
#pragma unroll
    for (int rt = 0; rt < 4; ++rt)
#pragma unroll
        for (int nt = 0; nt < 4; ++nt) {
            if (half == 0) acc[rt][nt] = VT ? MFMA(c[rt], w[nt], acc[rt][nt]) : MFMA(w[nt], c[rt], acc[rt][nt]);
            else acc[4 + rt][nt] = VT ? MFMA(c[rt], w[nt], acc[4 + rt][nt]) : MFMA(w[nt], c[rt], acc[4 + rt][nt]);
            filler(rt * 4 + nt);
        }
}

template <bool VT>
DI void k1_tile(const char* __restrict__ Abase, const char* __restrict__ Wbase, char* __restrict__ outp, char* lds, const unsigned (&avo)[4], const unsigned (&wvo)[4],
                unsigned wid, int hp, int rb, char* __restrict__ ws, const char* __restrict__ wq, const char* __restrict__ w1, const char* __restrict__ b1, u64 tag,
                int head2, int f0, bool nft4, int q16_0, u32x4 (&xa)[4], u32x2 (&bb)[4], u32x4 (&kv)[16], KVO& kvo) {
    char* stamp_base = ws; (void)stamp_base;
    const char* flb = uptr(ws + WS_FL);
    const unsigned wm = wid >> 2, wn = wid & 3;
    const unsigned lds0 = (unsigned)(uintptr_t)lds;
    unsigned aaddr[2], baddr[4];
    {
        const unsigned lane = flane(), c = lane & 15, g = lane >> 4;
        const unsigned x = (c >> 1) & 7;
#pragma unroll
        for (int s = 0; s < 2; ++s) aaddr[s] = (128 * wm + c) * 128 + (((4 * s + g) ^ x) << 4);
        const unsigned q = (lane & 15) >> 2, p = lane & 3, f = q | ((g & 1) << 2);
#pragma unroll
        for (int nt = 0; nt < 4; ++nt) baddr[nt] = 32768 + (8 * g + q) * 512 + (((8 * wn + 2 * nt + (p >> 1)) ^ (f << 1)) << 4) + ((p & 1) << 3);
    }
    f32x4 acc[8][4];
#pragma unroll
    for (int i = 0; i < 8; ++i)
#pragma unroll
        for (int j = 0; j < 4; ++j) acc[i][j] = f32x4{};
    bf16x8 cf[2][4], wf[2][4];
    lds_cptr L = (lds_cptr)lds;
#define K1_RC(buf, stg, s, half) do { _Pragma("unroll") for (int rt = 0; rt < 4; ++rt) cf[buf][rt] = lds_frag(L + (stg) + aaddr[s] + ((half) * 4 + rt) * 2048); } while (0)
#define K1_RW(buf, stg, s) do { _Pragma("unroll") for (int nt = 0; nt < 4; ++nt) wf[buf][nt] = tr_frag(L + (stg) + baddr[nt] + (s) * 16384, L + (stg) + baddr[nt] + (s) * 16384 + 2048); } while (0)
    const char* Aw = Abase + (size_t)wid * 32 * 1024;
    const char* Ww = Wbase + (size_t)wid * 8 * 8192;
    const unsigned m0a = lds0 + wid * 4096, m0w = lds0 + 32768 + wid * 4096;
#define K1_DMA(kt, stg)                                                                   \
    do {                                                                                  \
        const char* a_ = Aw + (kt) * 128; const char* w_ = Ww + (size_t)(kt) * 64 * 8192; \
        dma1<0>(m0a + (stg), a_, avo[0]); dma1<1024>(m0a + (stg), a_, avo[1]); dma1<2048>(m0a + (stg), a_, avo[2]); dma1<3072>(m0a + (stg), a_, avo[3]); \
        dma1<0>(m0w + (stg), w_, wvo[0]); dma1<1024>(m0w + (stg), w_, wvo[1]); dma1<2048>(m0w + (stg), w_, wvo[2]); dma1<3072>(m0w + (stg), w_, wvo[3]); \
    } while (0)
    unsigned pfd = 0;
    u32x2 fx = u32x2{};
#define K1_FXO() (FL_X + (2 * q16_0 + (flane() & 3)) * 8)
    STAMP(1);
    asm volatile("s_waitcnt vmcnt(10)\n\ts_barrier" ::: "memory");
    STAMP(2);
    K1_RW(0, 0, 0); K1_RC(0, 0, 0, 0);
#define K1_RC1(buf, stg, s, half, rt) cf[buf][rt] = lds_frag(L + (stg) + aaddr[s] + ((half) * 4 + (rt)) * 2048)
#define K1_RW1(buf, stg, s, nt) wf[buf][nt] = tr_frag(L + (stg) + baddr[nt] + (s) * 16384, L + (stg) + baddr[nt] + (s) * 16384 + 2048)
#pragma unroll
    for (int kt = 0; kt < 8; ++kt) {
        const unsigned stg = (kt & 1) * 65536, nstg = stg ^ 65536;
        SBAR();
        k1_mma<VT>(acc, 0, cf[0], wf[0], [&](int m) { if (m < 8 && (m & 1) == 0) { K1_RC1(1, stg, 0, 1, m >> 1); SBAR(); } });
        SBAR();
        k1_mma<VT>(acc, 1, cf[1], wf[0], [&](int m) { if (m < 4) { K1_RC1(0, stg, 1, 0, m); SBAR(); } else if (m < 12 && (m & 1) == 0) { K1_RW1(1, stg, 1, (m - 4) >> 1); SBAR(); } });
        SBAR();
        k1_mma<VT>(acc, 0, cf[0], wf[1], [&](int m) { if (m < 8 && (m & 1) == 0) { K1_RC1(1, stg, 1, 1, m >> 1); SBAR(); } });
        SBAR();
        if (kt == 7) asm volatile("s_waitcnt vmcnt(0) lgkmcnt(0)\n\ts_barrier" : "+v"(pfd)::"memory");
        else asm volatile("s_waitcnt vmcnt(0) lgkmcnt(0)\n\ts_barrier" : "+v"(fx)::"memory");
        if (kt == 0) flag_set(wid, flb, FL_X + blockIdx.x * 8, tag);
        if (kt == 6) flag_wait<false>(0, fx, flb, K1_FXO(), tag);
        const char* a_ = Aw + (kt + 2) * 128; const char* w_ = Ww + (size_t)(kt + 2) * 64 * 8192;
        k1_mma<VT>(acc, 1, cf[1], wf[1], [&](int m) {
            if (kt + 1 < 8) { if (m < 4) { K1_RW1(0, nstg, 0, m); SBAR(); } else if (m < 8) { K1_RC1(0, nstg, 0, 0, m - 4); SBAR(); } }
            if (kt + 2 < 8) {
                if (m == 1) { dma1<0>(m0a + stg, a_, avo[0]); SBAR(); } if (m == 3) { dma1<1024>(m0a + stg, a_, avo[1]); SBAR(); }
                if (m == 5) { dma1<2048>(m0a + stg, a_, avo[2]); SBAR(); } if (m == 7) { dma1<3072>(m0a + stg, a_, avo[3]); SBAR(); }
                if (m == 9) { dma1<0>(m0w + stg, w_, wvo[0]); SBAR(); } if (m == 11) { dma1<1024>(m0w + stg, w_, wvo[1]); SBAR(); }
                if (m == 13) { dma1<2048>(m0w + stg, w_, wvo[2]); SBAR(); } if (m == 15) { dma1<3072>(m0w + stg, w_, wvo[3]); SBAR(); }
            }
            if (kt == 5 && m == 15) {
                warm_load(pfd, wq, w1, blockIdx.x * 8 + wid);
                SBAR();
            }
            if (kt == 4 && m == 14) { flag_ld(fx, flb, K1_FXO()); SBAR(); }
            if (kt == 6 && m == 2) {
                const PJ j = pj_setup(wq, w1, ws, wid, head2, f0, nft4, q16_0); pj_dma(j, lds0, wid, 0, 0); pj_dma(j, lds0, wid, 1, PSLOT); SBAR();
            }
            if (kt == 7 && m == 0) { const PJ j = pj_setup(wq, w1, ws, wid, head2, f0, nft4, q16_0); asm volatile("s_nop 4"); pj_xload(j, xa, 0); SBAR(); }
            if (kt == 7 && m == 2) {
                const char* bp = uptr(b1 + f0 * 2);
                const unsigned bo = (flane() >> 4) * 8, b2 = bo + (nft4 ? 64 : 0);
                asm volatile("s_nop 4\n\tglobal_load_dwordx2 %0, %4, %6\n\tglobal_load_dwordx2 %1, %4, %6 offset:32\n\tglobal_load_dwordx2 %2, %5, %6\n\tglobal_load_dwordx2 %3, %5, %6 offset:32"
                             : "=&v"(bb[0]), "=&v"(bb[1]), "=&v"(bb[2]), "=&v"(bb[3]) : "v"(bo), "v"(b2), "s"(bp) : "memory");
                SBAR();
            }
        });
        SBAR();
    }
    STAMP(3);
    const int head = 2 * hp + (wn >> 1), d0 = (wn & 1) * 64;
    const int key0 = 256 * rb + 128 * wm;
    const unsigned lane = flane(), c = lane & 15, g = lane >> 4;
    kvo.base = uptr(outp + ((size_t)(head * 64 + (key0 >> 5)) * 4 + (d0 >> 5)) * 2048);
    kvo.lo[0] = (g >> 1) * 1024 + ((VT ? c : (c & 7) + 16 * (c >> 3)) + 32 * (g & 1)) * 16;
    kvo.lo[1] = kvo.lo[0] + (VT ? 256 : 128);
#pragma unroll
    for (int i = 0; i < 16; ++i) {
        const int u = i >> 2, a = (i >> 1) & 1, b2 = i & 1;
        kv[i] = VT ? pack44(acc[2 * u][2 * a + b2], acc[2 * u + 1][2 * a + b2]) : pack44(acc[2 * u + b2][2 * a], acc[2 * u + b2][2 * a + 1]);
    }
    asm volatile("s_waitcnt vmcnt(0)" : "+v"(xa[0]), "+v"(xa[1]), "+v"(xa[2]), "+v"(xa[3]), "+v"(bb[0]), "+v"(bb[1]), "+v"(bb[2]), "+v"(bb[3])::"memory");
    STAMP(5);
#undef K1_RC
#undef K1_RW
#undef K1_DMA
#undef K1_FXO
}

constexpr float QSCALE = 0.08838834764831845f * 1.4426950408889634f;
constexpr float THR = 8.0f;
DI float x4_max(float x) {
    auto s = __builtin_amdgcn_permlane16_swap(__float_as_uint(x), __float_as_uint(x), false, false);
    x = __builtin_fmaxf(__uint_as_float(s[0]), __uint_as_float(s[1]));
    auto t = __builtin_amdgcn_permlane32_swap(__float_as_uint(x), __float_as_uint(x), false, false);
    return __builtin_fmaxf(__uint_as_float(t[0]), __uint_as_float(t[1]));
}
DI float x4_sum(float x) {
    auto s = __builtin_amdgcn_permlane16_swap(__float_as_uint(x), __float_as_uint(x), false, false);
    x = __uint_as_float(s[0]) + __uint_as_float(s[1]);
    auto t = __builtin_amdgcn_permlane32_swap(__float_as_uint(x), __float_as_uint(x), false, false);
    return __uint_as_float(t[0]) + __uint_as_float(t[1]);
}
DI float mish_f(float h) {
    const float e = __builtin_amdgcn_exp2f(__builtin_fminf(h, 30.0f) * 1.4426950408889634f);
    const float n = e * (e + 2.0f);
    return h * n * __builtin_amdgcn_rcpf(n + 2.0f);
}

#define SPC 4
#define KVP0 7
#define KVPS 2
template <int NFT>
DI void proj_phase(const char* __restrict__ wq, const char* __restrict__ w1, char* __restrict__ ws, char* lds, int head, int f0, int q16_0, bf16x8 (&qf)[2][4],
                   const char* __restrict__ wo, const char* __restrict__ w2, unsigned m0v0, unsigned wid, u32x4 (&xa)[4], u32x4 (&xb)[4], const u32x2 (&bb)[4], u64 tag,
                   unsigned kvflag, int hp2, unsigned m0k0, unsigned m0k1, const char* kptr0, const char* kptr1, const u32x4 (&kv)[16], const KVO& kvo) {
    const unsigned lane = flane(), g = lane >> 4;
    char* stamp_base = ws; (void)stamp_base;
    const unsigned lds0 = (unsigned)(uintptr_t)lds;
    lds_cptr L = (lds_cptr)lds;
    const PJ pj = pj_setup(wq, w1, ws, wid, head, f0, NFT == 4, q16_0);
    const char* flb = uptr(ws + WS_FL);
    u32x2 fkv = u32x2{};
    const unsigned fkvo = FL_KV + (hp2 * 16 + (lane & 15)) * 8;
    const unsigned q = (lane & 15) >> 2, p = lane & 3;
    const unsigned wqb = 2 * g * WQ_PIECE + q * 64 + p * 8;
    const unsigned w1b = W1_OFF + g * W1_PIECE + q * 64 + p * 8;
    f32x4 aq[8][2], ah[NFT][2];
#pragma unroll
    for (int i = 0; i < 8; ++i) { aq[i][0] = f32x4{}; aq[i][1] = f32x4{}; }
#pragma unroll
    for (int i = 0; i < NFT; ++i) { ah[i][0] = f32x4{}; ah[i][1] = f32x4{}; }
    constexpr int NT = 8 + NFT;
    bf16x8 wf[4];
#define PJ_RD(slot, s, n)                                                                                       \
    (((n) < 8) ? tr_frag(L + (slot) + wqb + (8 * (s)) * WQ_PIECE + ((n) >> 1) * 256 + ((n) & 1) * 32, L + (slot) + wqb + (8 * (s) + 1) * WQ_PIECE + ((n) >> 1) * 256 + ((n) & 1) * 32) \
               : tr_frag(L + (slot) + w1b + (4 * (s)) * W1_PIECE + (((n) - 8) >> 1) * 512 + ((n) & 1) * 32, L + (slot) + w1b + (4 * (s)) * W1_PIECE + 256 + (((n) - 8) >> 1) * 512 + ((n) & 1) * 32))
#pragma unroll
    for (int n = 0; n < 3; ++n) wf[n] = PJ_RD(0, 0, n);
#define PJ_CHUNK(c, XC, XN)                                                                                     \
    do {                                                                                                        \
        constexpr int slot_ = ((c) % 3) * PSLOT, nslot_ = (((c) + 1) % 3) * PSLOT, dslot_ = (((c) + 2) % 3) * PSLOT; \
          \
        if ((c) == 9) flag_wait<false>(0, fkv, flb, fkvo, tag);                                                 \
        SBAR();                                                                                                 \
        constexpr int nst_ = 16 - SPC * (c) < 0 ? 0 : 16 - SPC * (c) < SPC ? 16 - SPC * (c) : SPC;        \
        _Pragma("unroll") for (int m = 0; m < 2 * NT; ++m) {                                                    \
            const int s = m / NT, n = m % NT;                                                                   \
            if (m == 2 * NT - 3) {       \
                if ((c) + 2 < 10) asm volatile("s_waitcnt vmcnt(%4) lgkmcnt(0)\n\ts_barrier" : "+v"(XN[0]), "+v"(XN[1]), "+v"(XN[2]), "+v"(XN[3]) : "i"(3 + nst_) : "memory"); \
                else if ((c) + 1 < 10) asm volatile("s_waitcnt vmcnt(0) lgkmcnt(0)\n\ts_barrier" : "+v"(XN[0]), "+v"(XN[1]), "+v"(XN[2]), "+v"(XN[3]), "+v"(fkv)::"memory"); \
                if (nst_ == 0 && 16 - SPC * ((c) - 1) > 0) flag_set(wid, flb, kvflag, tag);        \
            }                                                                                                   \
            const int m3 = m + 3;                                                                               \
            if (m3 < 2 * NT) wf[m3 & 3] = PJ_RD(slot_, m3 / NT, m3 % NT);                                       \
            else if ((c) + 1 < 10) wf[m3 & 3] = PJ_RD(nslot_, 0, m3 - 2 * NT);                                  \
            const bf16x8 x0 = __builtin_bit_cast(bf16x8, XC[2 * s]), x1 = __builtin_bit_cast(bf16x8, XC[2 * s + 1]); \
            if (n < 8) { aq[n & 7][0] = MFMA(wf[m & 3], x0, aq[n & 7][0]); aq[n & 7][1] = MFMA(wf[m & 3], x1, aq[n & 7][1]); } \
            else { ah[(n - 8) % NFT][0] = MFMA(wf[m & 3], x0, ah[(n - 8) % NFT][0]); ah[(n - 8) % NFT][1] = MFMA(wf[m & 3], x1, ah[(n - 8) % NFT][1]); } \
            SBAR();                                                                                             \
                       \
            if ((c) + 1 < 10 && m < 4) pj_xload1(pj, XN[m & 3], (c) + 1, m);                                    \
            if ((c) + 2 < 10 && m >= 4 && m < 7) pj_dma1(pj, lds0, wid, (c) + 2, dslot_, m - 4);                \
            if (m >= KVP0 && (m - KVP0) % KVPS == 0 && (m - KVP0) / KVPS < nst_) { const int i_ = (SPC * (c) + (m - KVP0) / KVPS) & 15; kv_store(kvo, kv[i_], i_); } \
            if ((c) == 8 && m == NT) flag_ld(fkv, flb, fkvo);                                                   \
            if ((c) == 9 && m < 16 && !(m & 1)) {                           \
                const int i_ = m >> 1;                                                                          \
                if ((i_ & 3) == 0) dma1_sc1<0>(i_ < 4 ? m0k0 : m0k1, i_ < 4 ? kptr0 : kptr1, lane * 16);            \
                if ((i_ & 3) == 1) dma1_sc1<1024>(i_ < 4 ? m0k0 : m0k1, i_ < 4 ? kptr0 : kptr1, lane * 16);         \
                if ((i_ & 3) == 2) dma1_sc1<2048>(i_ < 4 ? m0k0 : m0k1, i_ < 4 ? kptr0 : kptr1, lane * 16);         \
                if ((i_ & 3) == 3) dma1_sc1<3072>(i_ < 4 ? m0k0 : m0k1, i_ < 4 ? kptr0 : kptr1, lane * 16);         \
            }                                                                                                   \
            if ((c) == 9 && m == 16) warm_lds(m0v0, wo, w2, blockIdx.x * 8 + wid);        \
            SBAR();                                                                                             \
        }                                                                                                       \
        PJSTAMP(20 + (c));                                                                                      \
    } while (0)
    static_assert((2 * NT) % 4 == 0, "fragment window");
    PJ_CHUNK(0, xa, xb); PJ_CHUNK(1, xb, xa); PJ_CHUNK(2, xa, xb); PJ_CHUNK(3, xb, xa); PJ_CHUNK(4, xa, xb);
    PJ_CHUNK(5, xb, xa); PJ_CHUNK(6, xa, xb); PJ_CHUNK(7, xb, xa); PJ_CHUNK(8, xa, xb); PJ_CHUNK(9, xb, xa);
#undef PJ_CHUNK
#undef PJ_RD
    asm volatile("s_waitcnt vmcnt(1)" ::: "memory");
#pragma unroll
    for (int a = 0; a < 2; ++a)
#pragma unroll
        for (int v = 0; v < 4; ++v) qf[a][v] = __builtin_bit_cast(bf16x8, pack44s(aq[2 * v][a], aq[2 * v + 1][a], QSCALE));
#pragma unroll
    for (int v = 0; v < NFT / 2; ++v) {
        float bias[8];
#pragma unroll
        for (int t = 0; t < 2; ++t) {
            const u32x2 q2 = bb[2 * v + t];
            bias[4 * t + 0] = bflo(q2[0]); bias[4 * t + 1] = bfhi(q2[0]); bias[4 * t + 2] = bflo(q2[1]); bias[4 * t + 3] = bfhi(q2[1]);
        }
#pragma unroll
        for (int a = 0; a < 2; ++a) {
            f32x4 h0, h1;
#pragma unroll
            for (int i = 0; i < 4; ++i) { h0[i] = mish_f(ah[2 * v][a][i] + bias[i]); h1[i] = mish_f(ah[2 * v + 1][a][i] + bias[4 + i]); }
            st_wt_o(uptr(ws + WS_CH + ((size_t)(q16_0 + a) * NKS_CH + NKS_C + (f0 >> 5) + v) * 1024), lane * 16, pack44(h0, h1));
        }
    }
}

#define NOP_SO(S, o) asm volatile("s_nop 7\n\ts_nop 3" : "+v"(S[0]), "+v"(S[1]), "+v"(o[0]), "+v"(o[1]), "+v"(o[2]), "+v"(o[3]))
#define NOP_A(A) asm volatile("s_nop 7\n\ts_nop 1" : "+v"(A[0][0]), "+v"(A[0][1]), "+v"(A[0][2]), "+v"(A[0][3]), "+v"(A[1][0]), "+v"(A[1][1]), "+v"(A[1][2]), "+v"(A[1][3]), \
    "+v"(A[2][0]), "+v"(A[2][1]), "+v"(A[2][2]), "+v"(A[2][3]), "+v"(A[3][0]), "+v"(A[3][1]), "+v"(A[3][2]), "+v"(A[3][3]), "+v"(A[4][0]), "+v"(A[4][1]), "+v"(A[4][2]), "+v"(A[4][3]))
DI float xh_sum(float v) { auto r = __builtin_amdgcn_permlane32_swap(__float_as_uint(v), __float_as_uint(v), false, false); return __uint_as_float(r[0]) + __uint_as_float(r[1]); }
DI float xh_max(float v) { auto r = __builtin_amdgcn_permlane32_swap(__float_as_uint(v), __float_as_uint(v), false, false); return __builtin_fmaxf(__uint_as_float(r[0]), __uint_as_float(r[1])); }
DI float mul_s(float a, float b) { float r; asm("v_mul_f32 %0, %1, %2" : "=v"(r) : "v"(a), "v"(b)); return r; }
DI float sub_s(float a, float b) { float r; asm("v_sub_f32 %0, %1, %2" : "=v"(r) : "v"(a), "v"(b)); return r; }
DI u32x4 pack_o(const f32x16& a, int y, float sc) {
    u32x4 w; w[0] = cvtpk(mul_s(a[4 * y + 0], sc), mul_s(a[4 * y + 1], sc)); w[1] = cvtpk(mul_s(a[4 * y + 2], sc), mul_s(a[4 * y + 3], sc));
    w[2] = cvtpk(mul_s(a[8 + 4 * y + 0], sc), mul_s(a[8 + 4 * y + 1], sc)); w[3] = cvtpk(mul_s(a[8 + 4 * y + 2], sc), mul_s(a[8 + 4 * y + 3], sc)); return w;
}
DI void mask_diag(f32x16& s, int dq) {
    const float ninf = -__builtin_inff();
#pragma unroll
    for (int r0 = 0; r0 < 16; r0 += 4) {
        unsigned long long m0, m1, m2, m3;
        const int kc = 16 * ((r0 >> 2) & 1) + 8 * (r0 >> 3);
        float a = s[r0], b = s[r0 + 1], c = s[r0 + 2], d = s[r0 + 3];
        asm("v_cmp_gt_i32_e64 %4, %9, %8\n\tv_cmp_gt_i32_e64 %5, %10, %8\n\tv_cmp_gt_i32_e64 %6, %11, %8\n\tv_cmp_gt_i32_e64 %7, %12, %8\n\t"
            "v_cndmask_b32_e64 %0, %0, %13, %4\n\tv_cndmask_b32_e64 %1, %1, %13, %5\n\tv_cndmask_b32_e64 %2, %2, %13, %6\n\tv_cndmask_b32_e64 %3, %3, %13, %7"
            : "+v"(a), "+v"(b), "+v"(c), "+v"(d), "=&s"(m0), "=&s"(m1), "=&s"(m2), "=&s"(m3)
            : "v"(dq), "i"(kc), "i"(kc + 1), "i"(kc + 2), "i"(kc + 3), "v"(ninf));
        s[r0] = a; s[r0 + 1] = b; s[r0 + 2] = c; s[r0 + 3] = d;
    }
}
#define VIL 1
#define VIL0 0
#define FRW 8
#define FRA 6

#define K3DPOS {2, 3, 6, 7, 10, 14, 17}
constexpr int K3D[7] = K3DPOS;
constexpr int K3_SLOT = 36864;
constexpr int PP_SET = 8 * 5 * 1024;
DI float silu_f(float v) { return v * __builtin_amdgcn_rcpf(1.0f + __builtin_amdgcn_exp2f(-1.4426950408889634f * v)); }
DI f32x4 unpk_lo(const u32x4& w) { return f32x4{bflo(w[0]), bfhi(w[0]), bflo(w[1]), bfhi(w[1])}; }
DI f32x4 unpk_hi(const u32x4& w) { return f32x4{bflo(w[2]), bfhi(w[2]), bflo(w[3]), bfhi(w[3])}; }

struct K3S { const char *aptr, *wptr; unsigned avo, wvo[3], m0a, m0w; };
DI K3S k3_setup(unsigned lds0, unsigned wid, unsigned lane) {
    K3S k;
    k.aptr = nullptr; k.wptr = nullptr;
    k.avo = lane * 16;
    const unsigned p0 = wid >= 4 ? 12 + 2 * (wid & 3) : 3 * wid;
#pragma unroll
    for (int i = 0; i < 3; ++i) {
        const unsigned o = (p0 + i) * 1024 + lane * 16, row = o / 320, c16 = (o % 320) >> 4;
        k.wvo[i] = row * 1280 + ((c16 ^ (((row >> 2) & 1) << 1)) << 4) - 1024 * i;
    }
    k.m0a = lds0 + wid * 2048; k.m0w = lds0 + 16384 + p0 * 1024;
    return k;
}
DI void k3_point(K3S& k, const char* ws, const char* w, unsigned wid, int qt, int mt, int kstep, int stage) {
    k.aptr = uptr(ws + WS_CH + ((size_t)(8 * qt + wid) * NKS_CH + kstep) * 1024);
    k.wptr = uptr(w + (size_t)stage * 64 * 1280 + mt * 320);
    asm volatile("s_nop 4" : "+s"(k.aptr), "+s"(k.wptr));
}
DI void k3_stage(K3S& k, unsigned slot, bool three) {
    set_m0(k.m0a + slot); asm volatile("s_nop 0"); dma_sc1<0>(k.aptr, k.avo); dma_sc1<1024>(k.aptr, k.avo);
    set_m0(k.m0w + slot); asm volatile("s_nop 0"); dma_nom0<0>(k.wptr, k.wvo[0]); dma_nom0<1024>(k.wptr, k.wvo[1]);
    if (three) dma_nom0<2048>(k.wptr, k.wvo[2]);
    k.aptr += 2048; k.wptr += 81920;
}
DI void k3_publish(const f32x4 (&res)[10], f32x4 (&own)[2][2], const char* pset, unsigned vo, unsigned r) {
    own[1][0] = f32x4{}; own[1][1] = f32x4{};
#pragma unroll
    for (int j = 0; j < 5; ++j) {
        if ((j & 3) == r) { own[j >> 2][0] = res[2 * j]; own[j >> 2][1] = res[2 * j + 1]; }
        else st_wt_o(pset + j * 1024, vo, pack44(res[2 * j], res[2 * j + 1]));
    }
}

template <int GRP>
DI void k3_body(K3S& k, const char* __restrict__ x, const char* __restrict__ wo, char* __restrict__ ws, char* __restrict__ out, char* lds, unsigned wid, u64 tag,
                int ks, int qt, int mt, unsigned cflag) {
    constexpr int P = GRP ? 4 : 5;
    const unsigned lane = flane(), c = lane & 15, g = lane >> 4;
    const unsigned w4 = wid & 3, qh = w4 >> 1, mh = w4 & 1;
    char* stamp_base = ws; (void)stamp_base;
    const char* flb = uptr(ws + WS_FL);
    lds_cptr L = (lds_cptr)lds;
    const unsigned ard = (8 * qh + GRP) * 1024 + lane * 16;
    const unsigned q = (lane & 15) >> 2, p = lane & 3;
    const unsigned wrow = 16384 + (32 * GRP + 4 * g + q) * 320 + 160 * mh + 8 * p;
    const unsigned sgn = ((g & 1) ? 32u : 0u);
    const unsigned wrdE = (mh & 1) ? wrow - sgn : wrow + sgn;
    const unsigned wrdO = (mh & 1) ? wrow + sgn : wrow - sgn;
    f32x4 acc[5][4], accB[5][4];
    bf16x8 af[2][4], wf[4], w4f[2];
    unsigned cur = 0;
#define K3_RW(slot, n) tr_frag(L + (slot) + (((n) & 1) ? wrdO : wrdE) + 32 * (n), L + (slot) + (((n) & 1) ? wrdO : wrdE) + 32 * (n) + 16 * 320)
#define K3_RA(slot, a) lds_frag(L + (slot) + ard + (a) * 2048)
#define K3_ZERO(acc) do { _Pragma("unroll") for (int n = 0; n < 5; ++n) _Pragma("unroll") for (int a = 0; a < 4; ++a) acc[n][a] = f32x4{}; NOP_A(acc); } while (0)
#define K3_ITER(acc, PAR, WN, RD, DM, SAFE, HOOK)                                                                       \
    do {                                                                                                                \
        asm volatile("s_waitcnt vmcnt(%0) lgkmcnt(0)\n\ts_barrier" ::"i"(WN) : "memory");                               \
        HOOK;                                                                                                           \
        const unsigned nxt_ = cur == 3 * K3_SLOT ? 0u : cur + K3_SLOT;                                                  \
        _Pragma("unroll") for (int n = 0; n < 5; ++n) {                                                                 \
            _Pragma("unroll") for (int a = 0; a < 4; ++a) {                                                             \
                if (SAFE) mfma_acc_s(acc[n][a], n < 4 ? wf[n & 3] : w4f[PAR], af[PAR][a]);                              \
                else mfma_acc(acc[n][a], n < 4 ? wf[n & 3] : w4f[PAR], af[PAR][a]);                                     \
                SBAR();                                                                                                 \
                if (RD) {                                                                                               \
                    if (n == 0 && a == 0) w4f[1 - (PAR)] = K3_RW(nxt_, 4);                                              \
                    if (n < 4 && a == 1) af[1 - (PAR)][n & 3] = K3_RA(nxt_, n & 3);                                     \
                    if (n >= 1 && a == 0) wf[(n - 1) & 3] = K3_RW(nxt_, n - 1);                                         \
                }                                                                                                       \
                if (DM) {                                                                                               \
                    const int z_ = 4 * n + a;                                                                           \
                    if (z_ == K3D[0]) set_m0(k.m0a + cur);                                                              \
                    if (z_ == K3D[1]) dma_sc1<0>(k.aptr, k.avo);                                                        \
                    if (z_ == K3D[2]) dma_sc1<1024>(k.aptr, k.avo);                                                     \
                    if (z_ == K3D[3]) set_m0(k.m0w + cur);                                                              \
                    if (z_ == K3D[4]) dma_nom0<0>(k.wptr, k.wvo[0]);                                                    \
                    if (z_ == K3D[5]) dma_nom0<1024>(k.wptr, k.wvo[1]);                                                 \
                    if (z_ == K3D[6] && !GRP) dma_nom0<2048>(k.wptr, k.wvo[2]);                                         \
                }                                                                                                       \
                SBAR();                                                                                                 \
            }                                                                                                           \
        }                                                                                                               \
        if (DM) { k.aptr += 2048; k.wptr += 81920; }                                                                    \
        cur = nxt_;                                                                                                     \
    } while (0)
#define K3_FIRST(PAR) do { _Pragma("unroll") for (int a = 0; a < 4; ++a) af[PAR][a] = K3_RA(0, a);                      \
        _Pragma("unroll") for (int n = 0; n < 4; ++n) wf[n] = K3_RW(0, n);                                              \
        w4f[PAR] = K3_RW(0, 4); } while (0)
#define K3_REDUCE()                                                                                                     \
    do {                                                                                                                \
        NOP_A(acc);                                                                                                     \
        lds_ptr X_ = (lds_ptr)lds + wid * 10240 + lane * 16;                                                            \
        lds_cptr Y_ = (lds_cptr)lds + (wid ^ 4) * 10240 + lane * 16;                                                    \
        _Pragma("unroll") for (int a = 0; a < 2; ++a) _Pragma("unroll") for (int n = 0; n < 5; ++n)                     \
            *(__attribute__((address_space(3))) f32x4*)(X_ + (a * 5 + n) * 1024) = acc[n][GRP ? a : 2 + a];             \
        asm volatile("s_waitcnt lgkmcnt(0)\n\ts_barrier" ::: "memory");                                                 \
        _Pragma("unroll") for (int a = 0; a < 2; ++a) _Pragma("unroll") for (int n = 0; n < 5; ++n)                     \
            res[a * 5 + n] = acc[n][GRP ? 2 + a : a] + *(const __attribute__((address_space(3))) f32x4*)(Y_ + (a * 5 + n) * 1024); \
    } while (0)
#define K3_BPUT(j0, j1)                                                                                                 \
    do {                                                                                                                \
        _Pragma("unroll") for (int j = j0; j < j1; ++j)                                                                 \
            *(__attribute__((address_space(3))) u32x4*)((lds_ptr)lds + 4 * K3_SLOT + wid * 2048 + lane * 16 + (j - (j0)) * 1024) = \
                pack44(accB[(2 * j) % 5][(GRP ? 0 : 2) + (2 * j) / 5], accB[(2 * j + 1) % 5][(GRP ? 0 : 2) + (2 * j + 1) / 5]); \
        SBAR();                                                                                                         \
    } while (0)
#define K3_BGET(j0, j1)                                             \
    do {                                                                                                                \
        _Pragma("unroll") for (int j = j0; j < j1; ++j)                                                                 \
            yb[j - (j0)] = *(const __attribute__((address_space(3))) u32x4*)((lds_cptr)lds + 4 * K3_SLOT + (wid ^ 4) * 2048 + lane * 16 + (j - (j0)) * 1024); \
        SBAR();                                                                                                         \
    } while (0)
#define K3_BADD(j0, j1)                                                                                                 \
    do {                                                                                                                \
        _Pragma("unroll") for (int j = j0; j < j1; ++j) {                                                               \
            resB[2 * j] = accB[(2 * j) % 5][(GRP ? 2 : 0) + (2 * j) / 5] + unpk_lo(yb[j - (j0)]);                       \
            resB[2 * j + 1] = accB[(2 * j + 1) % 5][(GRP ? 2 : 0) + (2 * j + 1) / 5] + unpk_hi(yb[j - (j0)]);           \
            PIN(resB[2 * j]); PIN(resB[2 * j + 1]);                                                                     \
        }                                                                                                               \
        SBAR();                                                                                                         \
    } while (0)
#define K3_BOUT(s)                                                                                                      \
    do {                                                                                                                \
        const bool lo_ = r != 0 && (s) < r;                                                                             \
        const unsigned hi_ = (s) == 3 ? 4 : (s) + 1;                                                                    \
        const u32x4 a_ = pack44(resB[2 * (s)], resB[2 * (s) + 1]), b_ = pack44(resB[2 * hi_], resB[2 * hi_ + 1]);       \
        const bool ua_ = lo_ || ((s) == 3 && r == 0);                                                                   \
        u32x4 v_;                                                                                                       \
        _Pragma("unroll") for (int e = 0; e < 4; ++e) v_[e] = ua_ ? a_[e] : b_[e];                                      \
        st_wt_o(pbase + (4 + ks) * PP_SET + (ua_ ? (s) : hi_) * 1024, vo, v_);                                          \
        SBAR();                                                                                                         \
    } while (0)
#define NOHOOK (void)0
    const int grp64 = qt * 4 + mt;
    const char* pbase = uptr(ws + WS_PP + (size_t)grp64 * (8 * PP_SET) + wid * 5120);
    const unsigned vo = lane * 16;
    const unsigned r = (ks - wid) & 3;
    f32x4 res[10], resB[10], ownB[2][2], ownS[2][2];
    asm volatile("s_waitcnt vmcnt(%0) lgkmcnt(0)\n\ts_barrier" ::"i"(P + 4) : "memory");
    STAMP(10);
    k3_stage(k, 2 * K3_SLOT, !GRP); k3_stage(k, 3 * K3_SLOT, !GRP);
    K3_ZERO(accB);
    if (ks != 3) { K3_FIRST(1); K3_ITER(accB, 1, 2 * P + 4, true, true, true, NOHOOK); }
    else K3_FIRST(0);
    K3_ITER(accB, 0, 2 * P, true, true, true, flag_set(wid, flb, cflag, tag));
    K3_ITER(accB, 1, 2 * P, true, true, false, NOHOOK);
    u32x2 fc = u32x2{~(unsigned)tag, 0u};
    const unsigned fco = FL_C + ((qt < 8 ? qt : 15 - qt) * 32 + 8 * ks + (lane & 7)) * 8;
    K3_ITER(accB, 0, 2 * P, true, false, false, flag_ld0(wid, fc, flb, fco));
    STAMP(11);
    flag_wait<true>(wid, fc, flb, fco, tag);
    STAMP(18);
    k3_point(k, ws, wo, wid, qt, mt, 32 * ks, 16 * ks);
    K3_ITER(accB, 1, P, true, true, false, k3_stage(k, cur == 0 ? 3 * K3_SLOT : cur - K3_SLOT, !GRP));
    K3_ITER(accB, 0, 2 * P, true, true, false, NOHOOK);
    K3_ITER(accB, 1, 2 * P, true, true, false, NOHOOK);
    STAMP(19);
    K3_ZERO(acc);
    NOP_A(accB);
    u32x4 yb[2];
#define K3_H2(A, B) do { A; B; } while (0)
    K3_ITER(acc, 0, 2 * P, true, true, true, K3_BPUT(0, 2)); K3_ITER(acc, 1, 2 * P, true, true, false, K3_BGET(0, 2));
    K3_ITER(acc, 0, 2 * P, true, true, false, K3_H2(K3_BADD(0, 2), K3_BPUT(2, 4))); K3_ITER(acc, 1, 2 * P, true, true, false, K3_BGET(2, 4));
    K3_ITER(acc, 0, 2 * P, true, true, false, K3_H2(K3_BADD(2, 4), K3_BPUT(4, 5))); K3_ITER(acc, 1, 2 * P, true, true, false, K3_BGET(4, 5));
    K3_ITER(acc, 0, 2 * P, true, true, false, K3_H2(K3_BADD(4, 5), K3_BOUT(0)));
    K3_ITER(acc, 1, 2 * P + 1, true, true, false, K3_BOUT(1)); K3_ITER(acc, 0, 2 * P + 2, true, true, false, K3_BOUT(2));
    K3_ITER(acc, 1, 2 * P + 2, true, true, false, K3_BOUT(3));
#pragma unroll
    for (int t = 0; t < 2; ++t)
#pragma unroll
        for (int e = 0; e < 4; ++e) {
            ownB[0][t][e] = r == 0 ? resB[t][e] : r == 1 ? resB[2 + t][e] : r == 2 ? resB[4 + t][e] : resB[6 + t][e];
            ownB[1][t][e] = resB[8 + t][e];
        }
    K3_ITER(acc, 0, 2 * P + 2, true, true, false, NOHOOK); K3_ITER(acc, 1, 2 * P + 1, true, true, false, NOHOOK);
#undef K3_H2
    u32x2 xv[2][2];
    unsigned oo[2][2];
#pragma unroll
    for (int sl = 0; sl < 2; ++sl)
#pragma unroll
        for (int t = 0; t < 2; ++t) {
            const unsigned i = 2 * (sl ? 4 : r) + t, a = i >= 5 ? 1 : 0, n = i - 5 * a;
            oo[sl][t] = (16 * (8 * qt + 4 * qh + 2 * GRP + a) + c) * (DM * 2) + (16 * (10 * mt + 5 * mh + n) + 4 * g) * 2;
        }
#define K3_XLD() asm volatile("global_load_dwordx2 %0, %4, %8\n\tglobal_load_dwordx2 %1, %5, %8\n\tglobal_load_dwordx2 %2, %6, %8\n\tglobal_load_dwordx2 %3, %7, %8" \
        : "=&v"(xv[0][0]), "=&v"(xv[0][1]), "=&v"(xv[1][0]), "=&v"(xv[1][1]) : "v"(oo[0][0]), "v"(oo[0][1]), "v"(oo[1][0]), "v"(oo[1][1]), "s"(x) : "memory")
    K3_ITER(acc, 0, 2 * P, true, false, false, NOHOOK); K3_ITER(acc, 1, P, true, false, false, NOHOOK);
    K3_ITER(acc, 0, 0, true, false, false, K3_XLD()); K3_ITER(acc, 1, 0, false, false, false, NOHOOK);
    STAMP(12);
    K3_REDUCE();
    STAMP(17);
    k3_publish(res, ownS, pbase + ks * PP_SET, vo, r);
    u32x2 fe[4];
    const bool bump = blockIdx.x == 0 && wid == 0;
    if (bump) {
#pragma unroll
        for (int i = 0; i < 4; ++i) flag_ld(fe[i], flb, FL_X + (64 * i + lane) * 8);
    }
    STAMP(14);
    asm volatile("s_waitcnt vmcnt(0)\n\ts_barrier" : "+v"(xv[0][0]), "+v"(xv[0][1]), "+v"(xv[1][0]), "+v"(xv[1][1]), "+v"(fe[0]), "+v"(fe[1]), "+v"(fe[2]), "+v"(fe[3])::"memory");
    STAMP(15);
    flag_set(wid, flb, FL_S + (grp64 * 4 + ks) * 128, tag);
    {
        u32x2 f = u32x2{~(unsigned)tag, 0u};
        const unsigned o3 = (lane & 3) == (unsigned)ks ? (ks + 1) & 3 : lane & 3;
        flag_wait<false>(0, f, flb, FL_S + (grp64 * 4 + o3) * 128, tag);
    }
    STAMP(16);
    const int nown = r == 0 ? 2 : 1;
    u32x4 pp[2][6];
#pragma unroll
    for (int sl = 0; sl < 2; ++sl)
        if (sl < nown) {
            const char* pj = pbase + (sl ? 4 : r) * 1024;
#pragma unroll
            for (int i = 0; i < 3; ++i) { const unsigned st = (ks + 1 + i) & 3; ld_wt(pp[sl][i], pj + st * PP_SET, vo); ld_wt(pp[sl][3 + i], pj + (4 + st) * PP_SET, vo); }
        }
    asm volatile("s_waitcnt vmcnt(0)" : "+v"(pp[0][0]), "+v"(pp[0][1]), "+v"(pp[0][2]), "+v"(pp[0][3]), "+v"(pp[0][4]), "+v"(pp[0][5]),
                 "+v"(pp[1][0]), "+v"(pp[1][1]), "+v"(pp[1][2]), "+v"(pp[1][3]), "+v"(pp[1][4]), "+v"(pp[1][5])::"memory");
#pragma unroll
    for (int sl = 0; sl < 2; ++sl)
        if (sl < nown) {
#pragma unroll
            for (int t = 0; t < 2; ++t) {
                f32x4 sv = ownS[sl][t], bv = ownB[sl][t];
#pragma unroll
                for (int i = 0; i < 3; ++i) { sv += t ? unpk_hi(pp[sl][i]) : unpk_lo(pp[sl][i]); bv += t ? unpk_hi(pp[sl][3 + i]) : unpk_lo(pp[sl][3 + i]); }
                const float o0 = bflo(xv[sl][t][0]) + sv[0] * silu_f(bv[0]), o1 = bfhi(xv[sl][t][0]) + sv[1] * silu_f(bv[1]);
                const float o2 = bflo(xv[sl][t][1]) + sv[2] * silu_f(bv[2]), o3 = bfhi(xv[sl][t][1]) + sv[3] * silu_f(bv[3]);
                u32x2 w; w[0] = cvtpk(o0, o1); w[1] = cvtpk(o2, o3);
                *(u32x2*)(out + oo[sl][t]) = w;
            }
        }
    if (bump) {
#pragma unroll
        for (int i = 0; i < 4; ++i) flag_wait<false>(0, fe[i], flb, FL_X + (64 * i + lane) * 8, tag);
        flag_set(0, flb, FL_EPOCH, tag);
    }
    STAMP(13);
#undef K3_RW
#undef K3_RA
#undef K3_ZERO
#undef K3_ITER
#undef K3_FIRST
#undef K3_REDUCE
#undef K3_BPUT
#undef K3_BGET
#undef K3_BADD
#undef K3_BOUT
#undef K3_XLD
#undef NOHOOK
}

__global__ __launch_bounds__(512, 2) void fused(const char* __restrict__ x, const char* __restrict__ ckv, const char* __restrict__ wuk, const char* __restrict__ wuv,
                                              const char* __restrict__ wq, const char* __restrict__ w1, const char* __restrict__ b1, char* __restrict__ ws,
                                              const char* __restrict__ wo, const char* __restrict__ w2, char* __restrict__ out) {
    extern __shared__ __attribute__((aligned(16))) char lds[];
    const unsigned wid = rfl(threadIdx.x >> 6), grp = wid >> 2, w4 = wid & 3;
    char* stamp_base = ws; (void)stamp_base;
    STAMP(0);
    const int b = blockIdx.x, xcd = b & 7, idx = b >> 3;
    const int vt = idx & 1, hp = 2 * xcd + ((idx >> 1) & 1), rb = idx >> 2;
    const int head = 4 * xcd + (idx & 3), pi = idx >> 2;
    const int HB = 15 - pi, LB = pi;
    const int myblk = grp ? LB : HB;
    const int q16_0 = 8 * myblk + 2 * w4;
    const bool nft4 = head < 22;
    const int f0 = nft4 ? 64 * head : 1408 + 32 * (head - 22);
    const char* Abase = ckv + (size_t)rb * 256 * 1024;
    const char* Wbase = (vt ? wuv : wuk) + hp * 512;
    const unsigned lane0 = flane();
#define lane lane0
    const int row = 8 * b + wid;
    const unsigned l40 = lane < 40 ? lane : 39;
    u32x4 x0, x1;
    {
        const char* xr = uptr(x + (size_t)row * 1280);
        const unsigned vo = l40 * 32;
        asm volatile("global_load_dwordx4 %0, %2, %3\n\tglobal_load_dwordx4 %1, %2, %3 offset:16" : "=&v"(x0), "=&v"(x1) : "v"(vo), "s"(xr) : "memory");
    }
    unsigned avo[4], wvo[4];
    {
        const unsigned lds0 = (unsigned)(uintptr_t)lds;
        const char* Aw = Abase + (size_t)wid * 32 * 1024;
        const char* Ww = Wbase + (size_t)wid * 8 * 8192;
        const unsigned m0a = lds0 + wid * 4096, m0w = lds0 + 32768 + wid * 4096;
#pragma unroll
        for (int p = 0; p < 4; ++p) {
            const unsigned rw = 8 * p + (lane >> 3);
            avo[p] = rw * 1024 + (((lane & 7) ^ ((rw >> 1) & 7)) << 4) - 1024 * p;
            const unsigned k = 2 * p + (lane >> 5);
            const unsigned f = (k & 3) | ((wid & 1) << 2);
            wvo[p] = k * 8192 + (((lane & 31) ^ (f << 1)) << 4) - 1024 * p;
        }
#pragma unroll
        for (int kt = 0; kt < 2; ++kt) {
            const char* a_ = Aw + kt * 128; const char* w_ = Ww + (size_t)kt * 64 * 8192; const unsigned stg = kt * 65536;
            dma1<0>(m0a + stg, a_, avo[0]); dma1<1024>(m0a + stg, a_, avo[1]); dma1<2048>(m0a + stg, a_, avo[2]); dma1<3072>(m0a + stg, a_, avo[3]);
            dma1<0>(m0w + stg, w_, wvo[0]); dma1<1024>(m0w + stg, w_, wvo[1]); dma1<2048>(m0w + stg, w_, wvo[2]); dma1<3072>(m0w + stg, w_, wvo[3]);
        }
    }
    u64 tag;
    asm volatile("s_load_dwordx2 %0, %1, 0x0\n\ts_waitcnt lgkmcnt(0)" : "=s"(tag) : "s"(uptr(ws + WS_FL + FL_EPOCH)) : "memory");
    tag += 1;
    {
        asm volatile("s_waitcnt vmcnt(16)" : "+v"(x0), "+v"(x1)::"memory");
        float v[16];
#pragma unroll
        for (int i = 0; i < 4; ++i) { v[2 * i] = bflo(x0[i]); v[2 * i + 1] = bfhi(x0[i]); v[8 + 2 * i] = bflo(x1[i]); v[8 + 2 * i + 1] = bfhi(x1[i]); }
        float s = 0.f;
#pragma unroll
        for (int i = 0; i < 16; ++i) s += v[i];
        if (lane >= 40) s = 0.f;
#pragma unroll
        for (int o = 32; o >= 1; o >>= 1) s += __shfl_xor(s, o);
        const float mean = s * (1.0f / DM);
        float q = 0.f;
#pragma unroll
        for (int i = 0; i < 16; ++i) { v[i] -= mean; q += v[i] * v[i]; }
        if (lane >= 40) q = 0.f;
#pragma unroll
        for (int o = 32; o >= 1; o >>= 1) q += __shfl_xor(q, o);
        const float rstd = 1.0f / sqrtf(q * (1.0f / DM) + 1e-5f);
        u32x4 o0, o1;
#pragma unroll
        for (int i = 0; i < 4; ++i) { o0[i] = cvtpk(v[2 * i] * rstd, v[2 * i + 1] * rstd); o1[i] = cvtpk(v[8 + 2 * i] * rstd, v[8 + 2 * i + 1] * rstd); }
        char* xf = ws + WS_XF + ((size_t)(row >> 4) * NKS_X + (l40 >> 1)) * 1024 + ((row & 15) + 32 * (l40 & 1)) * 16;
        if (lane < 40) { st_wt(xf, o0); st_wt(xf + 256, o1); }
    }
#undef lane
    u32x4 xa[4], xb[4], kv[16];
    u32x2 bb[4];
    KVO kvo;
    if (vt) k1_tile<true>(Abase, Wbase, ws + WS_VF, lds, avo, wvo, wid, hp, rb, ws, wq, w1, b1, tag, head, f0, nft4, q16_0, xa, bb, kv, kvo);
    else    k1_tile<false>(Abase, Wbase, ws + WS_KF, lds, avo, wvo, wid, hp, rb, ws, wq, w1, b1, tag, head, f0, nft4, q16_0, xa, bb, kv, kvo);
    STAMP(6);
    const unsigned lds0 = (unsigned)(uintptr_t)lds;
    const unsigned kslot0 = grp ? 114688 : 81920, kslot1 = kslot0 + 16384, kslot2 = grp ? 16384 : 0;
    const unsigned vslot0 = grp ? 147456 : 32768, vslot1 = grp ? 65536 : 49152;
    const int nL = 2 * pi + 2;
    const int nsw = grp ? nL : 1000;
    const size_t jump = (size_t)(17 - nL) * 16384;
    const char* kptr = uptr(ws + WS_KF + (size_t)head * 524288 + w4 * 4096);
    const char* vptr = uptr(ws + WS_VF + (size_t)head * 524288 + w4 * 4096);
    bf16x8 qf[8];
    {
        bf16x8 q16[2][4];
        const unsigned kvflag = FL_KV + (hp * 16 + vt * 8 + rb) * 8;
        if (nft4) proj_phase<4>(wq, w1, ws, lds, head, f0, q16_0, q16, wo, w2, lds0 + vslot0 + w4 * 4096, wid, xa, xb, bb, tag, kvflag, head >> 1, lds0 + kslot0 + w4 * 4096, lds0 + kslot1 + w4 * 4096, kptr, kptr + 16384, kv, kvo);
        else      proj_phase<2>(wq, w1, ws, lds, head, f0, q16_0, q16, wo, w2, lds0 + vslot0 + w4 * 4096, wid, xa, xb, bb, tag, kvflag, head >> 1, lds0 + kslot0 + w4 * 4096, lds0 + kslot1 + w4 * 4096, kptr, kptr + 16384, kv, kvo);
        kptr += 2 * 16384; if (nsw == 2) kptr += jump;
#pragma unroll
        for (int v = 0; v < 4; ++v) {
            const u32x4 r0 = __builtin_bit_cast(u32x4, q16[0][v]), r1 = __builtin_bit_cast(u32x4, q16[1][v]);
            u32x4 z0, z1;
#pragma unroll
            for (int w = 0; w < 4; ++w) {
                const auto s1 = __builtin_amdgcn_permlane16_swap(r0[w], r1[w], false, false);
                const auto s2 = __builtin_amdgcn_permlane32_swap(s1[0], s1[1], false, false);
                z0[w] = s2[0]; z1[w] = s2[1];
            }
            qf[2 * v] = __builtin_bit_cast(bf16x8, z0); qf[2 * v + 1] = __builtin_bit_cast(bf16x8, z1);
        }
    }
    STAMP(7);
    const unsigned lane = flane();
    if (grp == 0) {
        char* qh = ws + WS_QH + (size_t)b * 32768 + w4 * 8192 + lane * 16;
#pragma unroll
        for (int s = 0; s < 8; ++s) *(bf16x8*)(qh + s * 1024) = qf[s];
    }
    const char* flb = uptr(ws + WS_FL);
    lds_cptr Lb = (lds_cptr)lds + lane * 16;
    const unsigned m0b = lds0 + w4 * 4096;
    const unsigned dvo = lane * 16;
    int q0 = 128 * myblk + 32 * w4;
    int tile = 0;
    asm volatile("s_waitcnt lgkmcnt(0)\n\ts_barrier" ::: "memory");
    dma4(m0b + vslot0, vptr, dvo); vptr += 16384;
    dma4(m0b + kslot2, kptr, dvo); kptr += 16384;
    f32x16 o[4], S[2], negm;
    float mref = 0.f, lsum = 0.f, lfin = 1.f;
    bf16x8 pf[4], fr[FRW];
#pragma unroll
    for (int i = 0; i < 4; ++i) o[i] = f32x16{};
    negm = f32x16{}; S[0] = f32x16{}; S[1] = f32x16{};
    asm volatile("s_nop 1" : "+v"(negm));
    NOP_SO(S, o);
    unsigned ks_cur = kslot0, ks_nxt = kslot1, ks_nn = kslot2;
#define CTX_VO(l) ((((l) >> 4) & 1) * (NKS_CH * 1024) + (((l) & 15) + 16 * ((l) >> 5)) * 16)
#define KOFF(i) (((i) & 1) * 8192 + ((i) >> 1) * 1024)
#define VOFF(j) (((j) >> 3) * 8192 + ((j) & 3) * 2048 + (((j) >> 2) & 1) * 1024)
#define EARLY_K(slot) do { _Pragma("unroll") for (int i = 0; i < FRA; ++i) fr[i] = lds_frag(Lb + (slot) + KOFF(i)); } while (0)
    EARLY_K(kslot0);
    if (grp) asm volatile("s_barrier" ::: "memory");
#define PHASE_M(t, QK, PV, VS)                                                                              \
    do {                                                                                                                              \
        __builtin_amdgcn_s_setprio(0);                                                                                                \
        lds_cptr kp_ = Lb + ks_cur; lds_cptr vp_ = Lb + ((VS) ? vslot1 : vslot0);                                                     \
        if (!(QK)) { _Pragma("unroll") for (int i = 16; i < 16 + FRA; ++i) fr[i % FRW] = lds_frag(vp_ + VOFF(i - 16)); }               \
        _Pragma("unroll") for (int i = (QK) ? 0 : 16; i < ((PV) ? 32 : 16); ++i) {                                                    \
            const int n = i + FRA;                                                                                                    \
            if (n < 16) fr[n % FRW] = lds_frag(kp_ + KOFF(n)); else if ((PV) && n < 32) fr[n % FRW] = lds_frag(vp_ + VOFF(n - 16));    \
            SBAR();                                                                                                                   \
            if (i < 2) mfma32_ini(S[i & 1], fr[i % FRW], qf[i >> 1], negm);                                                        \
            else if (i < 16) mfma32_acc(S[i & 1], fr[i % FRW], qf[i >> 1]);                                                             \
            else mfma32_acc(o[i & 3], fr[i % FRW], pf[(i - 16) >> 2]);                                                                  \
            SBAR();                                                                                                                   \
            if ((QK) && i == 16 && (t) + 1 == nsw) {        \
                const char* qs_ = uptr(ws + WS_QH + (size_t)b * 32768 + w4 * 8192);                                                   \
                asm volatile("s_nop 4\n\tglobal_load_dwordx4 %0, %8, %9\n\tglobal_load_dwordx4 %1, %8, %9 offset:1024\n\tglobal_load_dwordx4 %2, %8, %9 offset:2048\n\tglobal_load_dwordx4 %3, %8, %9 offset:3072" \
                             : "+v"(qf[0]), "+v"(qf[1]), "+v"(qf[2]), "+v"(qf[3]), "+v"(qf[4]), "+v"(qf[5]), "+v"(qf[6]), "+v"(qf[7]) : "v"(dvo), "s"(qs_) : "memory"); \
                asm volatile("global_load_dwordx4 %4, %8, %9\n\tglobal_load_dwordx4 %5, %8, %9 offset:1024\n\tglobal_load_dwordx4 %6, %8, %9 offset:2048\n\tglobal_load_dwordx4 %7, %8, %9 offset:3072" \
                             : "+v"(qf[0]), "+v"(qf[1]), "+v"(qf[2]), "+v"(qf[3]), "+v"(qf[4]), "+v"(qf[5]), "+v"(qf[6]), "+v"(qf[7]) : "v"(dvo), "s"(qs_ + 4096) : "memory"); \
            }                                                                                                                         \
            SBAR();                                                                                                                   \
        }                                                                                                                             \
        __builtin_amdgcn_s_setprio(3);                                                                                                \
        TICK(tA);                                                                                                                     \
        asm volatile("s_barrier" ::: "memory");                                                                                       \
        TICK(tX);                                                                                                                     \
    } while (0)
#define EXP_PACK(DV, DK, VS)         \
    do {                                                                                                                              \
        float e_[32], a_[2];                                                                                                          \
        unsigned c_[16];                                                                                                              \
        _Pragma("unroll") for (int k = 0; k < 18; ++k) {                                                                              \
            if (k < 16) { e_[2 * k] = v_exp(S[k >> 3][2 * (k & 7)]); e_[2 * k + 1] = v_exp(S[k >> 3][2 * (k & 7) + 1]); }             \
            if (k == 1) { a_[0] = e_[0]; a_[1] = e_[1]; }                                                                             \
            if (k >= 2 && k < 17) { a_[0] = v_add(a_[0], e_[2 * k - 2]); a_[1] = v_add(a_[1], e_[2 * k - 1]); }                        \
            if (k >= 2) c_[k - 2] = v_cvt(e_[2 * k - 4], e_[2 * k - 3]);                                                              \
            if (VIL) {                                                   \
                if ((DV) && k == VIL0) set_m0(m0b + ((VS) ? vslot1 : vslot0));                                                        \
                if ((DV) && k == VIL0 + 1) dma_sc1<0>(vptr, dvo);                                                                    \
                if ((DV) && k == VIL0 + 3) dma_sc1<1024>(vptr, dvo);                                                                 \
                if ((DV) && k == VIL0 + 5) dma_sc1<2048>(vptr, dvo);                                                                 \
                if ((DV) && k == VIL0 + 7) dma_sc1<3072>(vptr, dvo);                                                                 \
                if ((DK) && k == VIL0 + 8) set_m0(m0b + ks_cur);                                                                      \
                if ((DK) && k == VIL0 + 9) dma_sc1<0>(kptr, dvo);                                                                    \
                if ((DK) && k == VIL0 + 11) dma_sc1<1024>(kptr, dvo);                                                                \
                if ((DK) && k == VIL0 + 13) dma_sc1<2048>(kptr, dvo);                                                                \
                if ((DK) && k == VIL0 + 15) dma_sc1<3072>(kptr, dvo);                                                                \
            }                                                                                                                         \
        }                                                                                                                             \
        _Pragma("unroll") for (int k = 0; k < 4; ++k) pf[k] = __builtin_bit_cast(bf16x8, u32x4{c_[4 * k], c_[4 * k + 1], c_[4 * k + 2], c_[4 * k + 3]}); \
        ts_ = v_add(a_[0], a_[1]);                                                                                                    \
    } while (0)
#define REBASE(FIRST)                                                          \
    do {                                                                                                                              \
        float rm = S[0][0];                                                                                                           \
        _Pragma("unroll") for (int e = 1; e < 32; ++e) rm = __builtin_fmaxf(rm, S[e >> 4][e & 15]);                                   \
        rm = xh_max(rm);                                                                                                              \
        const float dl = (FIRST) ? (rm == -__builtin_inff() ? 0.f : rm) : __builtin_fmaxf(rm, 0.f);                                   \
        mref += dl;                                                                                                                   \
        if (!(FIRST)) { float f = __builtin_amdgcn_exp2f(-dl); asm volatile("s_nop 1" : "+v"(f)); lsum *= f;               \
            _Pragma("unroll") for (int i = 0; i < 4; ++i) _Pragma("unroll") for (int r = 0; r < 16; ++r) o[i][r] = mul_s(o[i][r], f); } \
        _Pragma("unroll") for (int r = 0; r < 16; ++r) { S[0][r] = sub_s(S[0][r], dl); S[1][r] = sub_s(S[1][r], dl); }                \
        const float nm = -mref;                                                                                                       \
        _Pragma("unroll") for (int r = 0; r < 16; ++r) negm[r] = nm;                                                                  \
    } while (0)
#define PHASE_V(t, WN, LASTV, VS, DK, DV)                                                                                             \
    do {                                                                                                                              \
        const bool swn = ((t) + 1 == nsw), sw0 = ((t) == nsw);                                                                        \
        const bool diag = 64 * tile + 63 > q0 || sw0, first_ = (t) == 0 || sw0;                                                       \
        if (!VIL) { if (DV) dma4(m0b + ((VS) ? vslot1 : vslot0), vptr, dvo); if (DK) dma4(m0b + ks_cur, kptr, dvo); }                 \
        NOP_SO(S, o);                                                                                                                 \
        float ts_;                                                                                                                    \
        if (__builtin_expect(diag || first_, 0)) {                                                                                    \
            if (sw0) {                                                                                \
                float il_ = __builtin_amdgcn_rcpf(xh_sum(lsum));                                                                      \
                asm volatile("s_nop 1" : "+v"(il_));                                                                                  \
                const char* cp_ = uptr(ws + WS_CH + ((size_t)(8 * LB + 2 * w4) * NKS_CH + 4 * head) * 1024);                          \
                const unsigned co_ = CTX_VO(flane());                                                                                 \
                asm volatile("s_nop 4");                                                                                              \
                st_wt_i<0>(cp_, co_, pack_o(o[0], 0, il_)); st_wt_i<512>(cp_, co_, pack_o(o[0], 1, il_));                             \
                st_wt_i<1024>(cp_, co_, pack_o(o[1], 0, il_)); st_wt_i<1536>(cp_, co_, pack_o(o[1], 1, il_));                         \
                st_wt_i<2048>(cp_, co_, pack_o(o[2], 0, il_)); st_wt_i<2560>(cp_, co_, pack_o(o[2], 1, il_));                         \
                st_wt_i<3072>(cp_, co_, pack_o(o[3], 0, il_)); st_wt_i<3584>(cp_, co_, pack_o(o[3], 1, il_));                         \
                _Pragma("unroll") for (int i = 0; i < 4; ++i) o[i] = f32x16{};                                                        \
                lsum = 0.f; mref = 0.f; q0 = 128 * HB + 32 * w4;                                                                      \
            }                                                                                                                         \
            if (64 * tile + 63 > q0) {         \
                const int dq = (int)(lane & 31) - 4 * (int)(lane >> 5);                                                               \
                _Pragma("unroll") for (int u = 0; u < 2; ++u) {                                                                       \
                    const int kb = 64 * tile + 32 * u;                                                                                \
                    if (kb > q0) { _Pragma("unroll") for (int r = 0; r < 16; ++r) S[u][r] = -__builtin_inff(); }                       \
                    else if (kb == q0) mask_diag(S[u], dq);                                                                           \
                }                                                                                                                     \
            }                                                                                                                         \
            if (first_) REBASE(true);                                                                                                 \
        }                                                                                                                             \
        EXP_PACK(DV, DK, VS);                                                                                                         \
        if (DV) vptr += 16384 + ((t) + 2 == nsw ? jump : 0);                                                                          \
        if (DK) kptr += 16384 + ((t) + 4 == nsw ? jump : 0);                                                                          \
        if (__builtin_expect(__any(ts_ > PLIM), 0)) { REBASE(false); EXP_PACK(false, false, 0); }     \
        lsum += ts_;                                                                                                                  \
        if (!(LASTV)) EARLY_K(ks_nxt);                                                                                                \
        if (__builtin_expect(swn, 0)) {                                                                                               \
            negm = f32x16{};                                                                                                          \
            asm volatile("s_waitcnt vmcnt(%9)" : "+v"(qf[0]), "+v"(qf[1]), "+v"(qf[2]), "+v"(qf[3]), "+v"(qf[4]), "+v"(qf[5]), "+v"(qf[6]), "+v"(qf[7]), "+v"(negm) : "i"(WN) : "memory"); \
            tile += 17 - nL;                                                                                                          \
        }                                                                                                                             \
        TICK(tB);                                                                                                                     \
        asm volatile("s_cmp_eq_u32 %7, 0\n\ts_cbranch_scc1 .Lv1%=\n\ts_waitcnt vmcnt(%5)\n\ts_branch .Lv2%=\n.Lv1%=:\n\ts_waitcnt vmcnt(%6)\n.Lv2%=:\n\ts_barrier"                \
                     : "+v"(pf[0]), "+v"(pf[1]), "+v"(pf[2]), "+v"(pf[3]), "+v"(negm) : "i"(WN), "i"((WN) + 8), "s"(nsw - (t)) : "scc", "memory");   \
        __builtin_amdgcn_s_waitcnt(0xC07F);        \
        TICK(tO);                                                                                                                     \
        { const unsigned r_ = ks_cur; ks_cur = ks_nxt; ks_nxt = ks_nn; ks_nn = r_; }                                                  \
        tile += 1;                                                                                                                    \
    } while (0)
    constexpr float PLIM = 256.0f;
    PHASE_M(0, true, false, 1);
    PHASE_V(0, 8, false, 1, true, true);
    flag_set(w4, flb, FL_H + (myblk * 32 + head) * 8, tag);
    for (int t = 1; t < 13; t += 2) {
        PHASE_M(t, true, true, 0); PHASE_V(t, 8, false, 0, true, true);
        PHASE_M(t + 1, true, true, 1); PHASE_V(t + 1, 8, false, 1, true, true);
    }
    PHASE_M(13, true, true, 0); PHASE_V(13, 8, false, 0, true, true);
    PHASE_M(14, true, true, 1); PHASE_V(14, 4, false, 1, false, true);
    PHASE_M(15, true, true, 0);
    const int ks = xcd >> 1, qt = 8 * (xcd & 1) + (idx >> 2), mt = idx & 3;
    u32x2 fh;
    const unsigned fho = FL_H + (qt * 32 + (lane & 31)) * 8;
    flag_ld(fh, flb, fho);
    PHASE_V(15, 4, false, 0, false, true);
    PIN(fh);
    PHASE_M(16, true, true, 1); PHASE_V(16, 0, true, 1, false, false);
    PHASE_M(17, false, true, 0);
    flag_wait<false>(0, fh, flb, fho, tag);
    if (!grp) asm volatile("s_barrier" ::: "memory");
    NOP_SO(S, o);
    K3S k3 = k3_setup(lds0, wid, lane);
    k3_point(k3, ws, w2, wid, qt, mt, NKS_C + 14 * ks, 7 * ks);
    k3_stage(k3, 0, grp == 0); k3_stage(k3, K3_SLOT, grp == 0);
    STAMP(8);
    {
        const float ls = xh_sum(lsum);
        const float mm = ls > 0.f ? mref : -__builtin_inff();
        lds_ptr X = (lds_ptr)lds + 81920 + wid * 8192 + lane * 16;
        f32x16 keep[2];
        if (grp) {
#pragma unroll
            for (int i = 0; i < 2; ++i) {
                keep[i] = o[2 + i];
#pragma unroll
                for (int y = 0; y < 4; ++y) *(__attribute__((address_space(3))) f32x4*)(X + (4 * i + y) * 1024) = f32x4{o[i][4 * y], o[i][4 * y + 1], o[i][4 * y + 2], o[i][4 * y + 3]};
            }
        } else {
#pragma unroll
            for (int i = 0; i < 2; ++i) {
                keep[i] = o[i];
#pragma unroll
                for (int y = 0; y < 4; ++y) *(__attribute__((address_space(3))) f32x4*)(X + (4 * i + y) * 1024) = f32x4{o[2 + i][4 * y], o[2 + i][4 * y + 1], o[2 + i][4 * y + 2], o[2 + i][4 * y + 3]};
            }
        }
        lds_ptr Y = (lds_ptr)lds + 73728 + wid * 512 + lane * 8;
        *(__attribute__((address_space(3))) f32x2*)Y = f32x2{mm, ls};
        asm volatile("s_waitcnt lgkmcnt(0)\n\ts_barrier" ::: "memory");
        const unsigned ow = wid ^ 4;
        lds_cptr Xo = (lds_cptr)lds + 81920 + ow * 8192 + lane * 16;
        const f32x2 oml = *(const __attribute__((address_space(3))) f32x2*)((lds_cptr)lds + 73728 + ow * 512 + lane * 8);
        const float mx = __builtin_fmaxf(mm, oml[0]);
        const float e0 = __builtin_amdgcn_exp2f(mm - mx), e1 = __builtin_amdgcn_exp2f(oml[0] - mx);
        const float inv = __builtin_amdgcn_rcpf(ls * e0 + oml[1] * e1);
        const float fs = e0 * inv, fo = e1 * inv;
        const char* cp = uptr(ws + WS_CH + ((size_t)(8 * HB + 2 * w4) * NKS_CH + 4 * head + 2 * grp) * 1024);
        const unsigned co = CTX_VO(lane);
#pragma unroll
        for (int i = 0; i < 2; ++i) {
            f32x16 m;
#pragma unroll
            for (int y = 0; y < 4; ++y) {
                const f32x4 pv = *(const __attribute__((address_space(3))) f32x4*)(Xo + (4 * i + y) * 1024);
#pragma unroll
                for (int r = 0; r < 4; ++r) m[4 * y + r] = keep[i][4 * y + r] * fs + pv[r] * fo;
            }
            if (i == 0) { st_wt_i<0>(cp, co, pack_o(m, 0, 1.f)); st_wt_i<512>(cp, co, pack_o(m, 1, 1.f)); }
            else { st_wt_i<1024>(cp, co, pack_o(m, 0, 1.f)); st_wt_i<1536>(cp, co, pack_o(m, 1, 1.f)); }
        }
    }
    STAMP(9);
#undef KOFF
#undef CTX_VO
#undef VOFF
#undef EARLY_K
#undef PHASE_M
#undef PHASE_V
#undef EXP_PACK
#undef REBASE
    if (grp == 0) k3_body<0>(k3, x, wo, ws, out, lds, wid, tag, ks, qt, mt, FL_C + (pi * 32 + head) * 8);
    else k3_body<1>(k3, x, wo, ws, out, lds, wid, tag, ks, qt, mt, FL_C + (pi * 32 + head) * 8);
}

extern "C" void kernel_launch(void* const* d_in, const int* in_sizes, int n_in, void* d_out, int out_size, void* d_ws, size_t ws_size,
                              hipStream_t stream) {
    const char* x = (const char*)d_in[0];
    const char* ckv = (const char*)d_in[1];
    const char* wuk = (const char*)d_in[3];
    const char* wuv = (const char*)d_in[4];
    char* ws = (char*)d_ws;
    (void)hipFuncSetAttribute((const void*)fused, hipFuncAttributeMaxDynamicSharedMemorySize, 163840);
    fused<<<256, 512, 163840, stream>>>(x, ckv, wuk, wuv, (const char*)d_in[2], (const char*)d_in[7], (const char*)d_in[8], ws, (const char*)d_in[5], (const char*)d_in[9], (char*)d_out);
}
```

```cpp
#include <hip/hip_runtime.h>
#include <hip/hip_bf16.h>
#include <stdint.h>

typedef __bf16 bf16x8 __attribute__((ext_vector_type(8)));
typedef __bf16 bf16x2 __attribute__((ext_vector_type(2)));
typedef short s16x4 __attribute__((ext_vector_type(4)));
typedef short s16x8 __attribute__((ext_vector_type(8)));
typedef short v4i16_t __attribute__((ext_vector_type(4)));
typedef float f32x16 __attribute__((ext_vector_type(16)));
typedef float f32x4 __attribute__((ext_vector_type(4)));
typedef float f32x2 __attribute__((ext_vector_type(2)));
typedef unsigned u32x4 __attribute__((ext_vector_type(4)));
typedef unsigned u32x2 __attribute__((ext_vector_type(2)));
typedef __attribute__((address_space(3))) const char* lds_cptr;
typedef __attribute__((address_space(3))) char* lds_ptr;

#define DI __device__ __forceinline__
#define SBAR() __builtin_amdgcn_sched_barrier(0)
#define MFMA(a, b, c) __builtin_amdgcn_mfma_f32_16x16x32_bf16((a), (b), (c), 0, 0, 0)
#define PIN(x) asm volatile("" : "+v"(x))
#define PINR(x) asm volatile("" ::"v"(x))

constexpr int S_ = 2048, NH = 32, HD = 128, DM = 640, DC = 512, FF = 1728;
constexpr int NKS_X = DM / 32;
constexpr int NKS_C = (NH * HD) / 32;
constexpr int NKS_H = FF / 32;
constexpr int NKS_CH = NKS_C + NKS_H;
constexpr size_t WS_XF = 0;
constexpr size_t WS_KF = WS_XF + (size_t)128 * NKS_X * 1024;
constexpr size_t WS_VF = WS_KF + (size_t)NH * 128 * 4 * 1024;
constexpr size_t WS_CH = WS_VF + (size_t)NH * 64 * 8 * 1024;
constexpr size_t WS_QH = WS_CH + (size_t)128 * NKS_CH * 1024;
constexpr size_t WS_PP = WS_QH + (size_t)256 * 32768;
constexpr size_t PP_BYTES = (size_t)64 * 8 * 8 * 5 * 1024;
constexpr size_t WS_FL = WS_PP + PP_BYTES;
constexpr size_t FL_EPOCH = 0;
constexpr size_t FL_X = 256;
constexpr size_t FL_KV = FL_X + 2048;
constexpr size_t FL_H = FL_KV + 2048;
constexpr size_t FL_C = FL_H + 4096;
constexpr size_t FL_S = 16384;
constexpr size_t FL_B = FL_S + 32768;
constexpr size_t WS_END = WS_FL + FL_B + 32768;

DI unsigned rfl(unsigned x) { return __builtin_amdgcn_readfirstlane(x); }
DI unsigned flane() { unsigned r; asm volatile("v_mbcnt_lo_u32_b32 %0, -1, 0\n\tv_mbcnt_hi_u32_b32 %0, -1, %0" : "=v"(r)); return r; }
#define STAMP(i)
#define TICK(acc)
#define PJSTAMP(i)
DI const char* uptr(const char* p) { const unsigned long long v = (unsigned long long)p; const unsigned lo = rfl((unsigned)v), hi = rfl((unsigned)(v >> 32)); return (const char*)(((unsigned long long)hi << 32) | lo); }
DI unsigned cvtpk(float lo, float hi) { f32x2 v = {lo, hi}; bf16x2 b = __builtin_convertvector(v, bf16x2); return __builtin_bit_cast(unsigned, b); }
DI bf16x8 lds_frag(lds_cptr p) { return *(const __attribute__((address_space(3))) bf16x8*)p; }
DI s16x4 lds_tr(lds_cptr p) { return __builtin_bit_cast(s16x4, __builtin_amdgcn_ds_read_tr16_b64_v4i16((__attribute__((address_space(3))) v4i16_t*)p)); }
DI bf16x8 tr_frag(lds_cptr p0, lds_cptr p1) { s16x4 lo = lds_tr(p0), hi = lds_tr(p1); s16x8 v = __builtin_shufflevector(lo, hi, 0, 1, 2, 3, 4, 5, 6, 7); return __builtin_bit_cast(bf16x8, v); }
template <int OFF> DI void dma1(unsigned m0v, const void* sbase, unsigned voff) {
    asm volatile("s_mov_b32 m0, %0\n\ts_nop 0\n\tglobal_load_lds_dwordx4 %1, %2 offset:%3" ::"s"(m0v), "v"(voff), "s"(sbase), "i"(OFF) : "memory");
}
DI u32x4 pack44(const f32x4& a, const f32x4& b) { u32x4 w; w[0] = cvtpk(a[0], a[1]); w[1] = cvtpk(a[2], a[3]); w[2] = cvtpk(b[0], b[1]); w[3] = cvtpk(b[2], b[3]); return w; }
DI u32x4 pack44s(const f32x4& a, const f32x4& b, float sc) { u32x4 w; w[0] = cvtpk(a[0] * sc, a[1] * sc); w[1] = cvtpk(a[2] * sc, a[3] * sc); w[2] = cvtpk(b[0] * sc, b[1] * sc); w[3] = cvtpk(b[2] * sc, b[3] * sc); return w; }
DI u32x4 pack8(const f32x16& a, int b, float sc) {
    u32x4 w; w[0] = cvtpk(a[b + 0] * sc, a[b + 1] * sc); w[1] = cvtpk(a[b + 2] * sc, a[b + 3] * sc);
    w[2] = cvtpk(a[b + 4] * sc, a[b + 5] * sc); w[3] = cvtpk(a[b + 6] * sc, a[b + 7] * sc); return w;
}
DI u32x4 pack8n(const f32x16& a, int b) {
    u32x4 w; w[0] = cvtpk(a[b + 0], a[b + 1]); w[1] = cvtpk(a[b + 2], a[b + 3]); w[2] = cvtpk(a[b + 4], a[b + 5]); w[3] = cvtpk(a[b + 6], a[b + 7]); return w;
}
DI float bf2f(unsigned short u) { return __uint_as_float((unsigned)u << 16); }
DI float bflo(unsigned w) { return __uint_as_float(w << 16); }
DI float bfhi(unsigned w) { return __uint_as_float(w & 0xffff0000u); }
DI float xhalf_sum(float v) { auto r = __builtin_amdgcn_permlane32_swap(__float_as_uint(v), __float_as_uint(v), false, false); return __uint_as_float(r[0]) + __uint_as_float(r[1]); }
DI float xhalf_max(float v) { auto r = __builtin_amdgcn_permlane32_swap(__float_as_uint(v), __float_as_uint(v), false, false); return __builtin_fmaxf(__uint_as_float(r[0]), __uint_as_float(r[1])); }

DI void warm_load(unsigned& d, const char* wa, const char* wb, unsigned id) {
    const unsigned j = id - 1280u;
    const char* p = uptr(id < 1280u ? wa + (size_t)id * 4096 : wb + (size_t)(j < 539u ? j : 539u) * 4096);
    const unsigned voff = flane() * 64;
    asm volatile("s_nop 4\n\tglobal_load_dword %0, %1, %2" : "=&v"(d) : "v"(voff), "s"(p) : "memory");
}
DI void warm_lds(unsigned m0v, const char* wa, const char* wb, unsigned id) {
    const unsigned j = id - 1280u;
    const char* p = uptr(id < 1280u ? wa + (size_t)id * 4096 : wb + (size_t)(j < 539u ? j : 539u) * 4096);
    const unsigned voff = flane() * 64;
    asm volatile("s_mov_b32 m0, %0\n\ts_nop 4\n\tglobal_load_lds_dword %1, %2" ::"s"(m0v), "v"(voff), "s"(p) : "memory");
}

DI void mfma_acc(f32x4& acc, const bf16x8& a, const bf16x8& b) { asm volatile("v_mfma_f32_16x16x32_bf16 %0, %1, %2, %0" : "+v"(acc) : "v"(a), "v"(b)); }
DI void mfma_acc_s(f32x4& acc, const bf16x8& a, const bf16x8& b) { asm volatile("s_nop 1\n\tv_mfma_f32_16x16x32_bf16 %0, %1, %2, %0" : "+v"(acc) : "v"(a), "v"(b)); }
DI void mfma32_acc(f32x16& acc, const bf16x8& a, const bf16x8& b) { asm volatile("v_mfma_f32_32x32x16_bf16 %0, %1, %2, %0" : "+v"(acc) : "v"(a), "v"(b)); }
DI void mfma32_ini(f32x16& d, const bf16x8& a, const bf16x8& b, const f32x16& c) { asm volatile("v_mfma_f32_32x32x16_bf16 %0, %1, %2, %3" : "=&v"(d) : "v"(a), "v"(b), "v"(c)); }
DI float v_exp(float x) { float r; asm volatile("v_exp_f32 %0, %1" : "=v"(r) : "v"(x)); return r; }
DI float v_add(float a, float b) { float r; asm volatile("v_add_f32 %0, %1, %2" : "=v"(r) : "v"(a), "v"(b)); return r; }
DI unsigned v_cvt(float lo, float hi) { unsigned r; asm volatile("v_cvt_pk_bf16_f32 %0, %1, %2" : "=v"(r) : "v"(lo), "v"(hi)); return r; }
DI void set_m0(unsigned v) { asm volatile("s_mov_b32 m0, %0" ::"s"(v) : "memory"); }
template <int OFF> DI void dma_nom0(const void* sbase, unsigned voff) { asm volatile("global_load_lds_dwordx4 %0, %1 offset:%2" ::"v"(voff), "s"(sbase), "i"(OFF) : "memory"); }
template <int OFF> DI void dma_sc1(const void* sbase, unsigned voff) { asm volatile("global_load_lds_dwordx4 %0, %1 offset:%2 sc1" ::"v"(voff), "s"(sbase), "i"(OFF) : "memory"); }
template <int OFF> DI void dma1_sc1(unsigned m0v, const void* sbase, unsigned voff) {
    asm volatile("s_mov_b32 m0, %0\n\ts_nop 0\n\tglobal_load_lds_dwordx4 %1, %2 offset:%3 sc1" ::"s"(m0v), "v"(voff), "s"(sbase), "i"(OFF) : "memory");
}
DI void dma4(unsigned m0v, const void* sbase, unsigned voff) {
    asm volatile("s_mov_b32 m0, %0\n\ts_nop 0\n\tglobal_load_lds_dwordx4 %1, %2 sc1\n\tglobal_load_lds_dwordx4 %1, %2 offset:1024 sc1\n\tglobal_load_lds_dwordx4 %1, %2 offset:2048 sc1\n\tglobal_load_lds_dwordx4 %1, %2 offset:3072 sc1"
                 ::"s"(m0v), "v"(voff), "s"(sbase) : "memory");
}

typedef unsigned long long u64;
DI void st_wt(char* p, const u32x4& v) { asm volatile("global_store_dwordx4 %0, %1, off sc1\n\ts_nop 1" ::"v"(p), "v"(v) : "memory"); }
DI void st_wt_o(const char* sbase, unsigned voff, const u32x4& v) { asm volatile("global_store_dwordx4 %0, %1, %2 sc1\n\ts_nop 1" ::"v"(voff), "v"(v), "s"(sbase) : "memory"); }
template <int OFF> DI void st_wt_i(const char* sbase, unsigned voff, const u32x4& v) {
    asm volatile("global_store_dwordx4 %0, %1, %2 offset:%3 sc1\n\ts_nop 1" ::"v"(voff), "v"(v), "s"(sbase), "i"(OFF) : "memory");
}
DI void ld_wt(u32x4& d, const char* sbase, unsigned voff) { asm volatile("global_load_dwordx4 %0, %1, %2 sc1" : "=&v"(d) : "v"(voff), "s"(sbase) : "memory"); }
DI void ld_wt3(unsigned skip, u32x4& a, u32x4& b, u32x4& c, const char* p0, const char* p1, const char* p2, unsigned voff) {
    asm volatile("s_cmp_lg_u32 %3, 0\n\ts_cbranch_scc1 .Lw%=\n\tglobal_load_dwordx4 %0, %4, %5 sc1\n\tglobal_load_dwordx4 %1, %4, %6 sc1\n\tglobal_load_dwordx4 %2, %4, %7 sc1\n.Lw%=:"
                 : "+v"(a), "+v"(b), "+v"(c) : "s"(skip), "v"(voff), "s"(p0), "s"(p1), "s"(p2) : "scc", "memory");
}
DI void flag_set(unsigned wid, const char* sbase, unsigned off, u64 tag) {
    const u32x2 v = u32x2{(unsigned)tag, (unsigned)(tag >> 32)};
    u64 ex;
    asm volatile("s_cmp_lg_u32 %1, 0\n\ts_cbranch_scc1 .Lfs%=\n\ts_mov_b64 %0, exec\n\ts_mov_b64 exec, 1\n\t"
                 "global_store_dwordx2 %2, %3, %4 sc1\n\ts_mov_b64 exec, %0\n.Lfs%=:\n\ts_nop 1"
                 : "=&s"(ex) : "s"(wid), "v"(off), "v"(v), "s"(sbase) : "scc", "memory");
}
DI void flag_ld(u32x2& f, const char* sbase, unsigned voff) { asm volatile("global_load_dwordx2 %0, %1, %2 sc1" : "=&v"(f) : "v"(voff), "s"(sbase) : "memory"); }
DI void flag_ld0(unsigned skip, u32x2& f, const char* sbase, unsigned voff) {
    asm volatile("s_cmp_lg_u32 %1, 0\n\ts_cbranch_scc1 .Lfz%=\n\tglobal_load_dwordx2 %0, %2, %3 sc1\n.Lfz%=:" : "+v"(f) : "s"(skip), "v"(voff), "s"(sbase) : "scc", "memory");
}
template <bool FLY>
DI void flag_wait(unsigned skip, u32x2& f, const char* sbase, unsigned voff, u64 tag) {
    unsigned cnt;
    asm volatile("s_cmp_lg_u32 %2, 0\n\ts_cbranch_scc1 .Lfe%=\n\t.if %6\n\ts_waitcnt vmcnt(0)\n\t.endif\n\t"
                 "v_cmp_ne_u64 vcc, %3, %0\n\ts_cbranch_vccz .Lfd%=\n\ts_mov_b32 %1, 0x40000\n"
                 ".Lfl%=:\n\tglobal_load_dwordx2 %0, %4, %5 sc1\n\ts_waitcnt vmcnt(0)\n\tv_cmp_ne_u64 vcc, %3, %0\n\ts_cbranch_vccz .Lfd%=\n\t"
                 "s_sleep 2\n\ts_sub_u32 %1, %1, 1\n\ts_cbranch_scc0 .Lfl%=\n"
                 ".Lfd%=:\n"
                 ".Lfe%=:"
                 : "+v"(f), "=&s"(cnt) : "s"(skip), "s"(tag), "v"(voff), "s"(sbase), "i"(FLY ? 1 : 0) : "vcc", "scc", "memory");
}

constexpr int WQ_PIECE = 1040, W1_PIECE = 1056, W1_OFF = 16 * WQ_PIECE, PSLOT = W1_OFF + 8 * W1_PIECE;
struct PJ { unsigned wqvo, w1vo, xv0, xv1; const char *wqs, *w1s, *xs; };
DI PJ pj_setup(const char* wq, const char* w1, const char* ws, unsigned wid, int head, int f0, bool nft4, int q16_0) {
    const unsigned lane = flane();
    PJ j;
    j.wqvo = ((lane >> 2) & 3) * 8192 + (lane >> 4) * 64 + (lane & 3) * 16;
    j.w1vo = ((lane >> 2) & 7) * 3456 + (nft4 ? (lane >> 5) * 64 : 0) + (lane & 3) * 16;
    j.wqs = uptr(wq + (size_t)wid * 8 * 8192 + head * 256);
    j.w1s = uptr(w1 + (size_t)wid * 8 * 3456 + f0 * 2);
    j.xs = uptr(ws + WS_XF + (size_t)q16_0 * NKS_X * 1024);
    j.xv0 = lane * 16; j.xv1 = lane * 16 + NKS_X * 1024;
    return j;
}
DI void pj_dma(const PJ& j, unsigned lds0, unsigned wid, int c, int slot) {
    dma1<0>(lds0 + slot + (2 * wid) * WQ_PIECE, j.wqs + (size_t)c * 64 * 8192, j.wqvo);
    dma1<0>(lds0 + slot + (2 * wid + 1) * WQ_PIECE, j.wqs + (size_t)c * 64 * 8192 + 4 * 8192, j.wqvo);
    dma1<0>(lds0 + slot + W1_OFF + wid * W1_PIECE, j.w1s + (size_t)c * 64 * 3456, j.w1vo);
}
DI void pj_dma1(const PJ& j, unsigned lds0, unsigned wid, int c, int slot, int i) {
    if (i < 2) dma1<0>(lds0 + slot + (2 * wid + i) * WQ_PIECE, j.wqs + (size_t)c * 64 * 8192 + i * 4 * 8192, j.wqvo);
    else dma1<0>(lds0 + slot + W1_OFF + wid * W1_PIECE, j.w1s + (size_t)c * 64 * 3456, j.w1vo);
}
DI void pj_xload1(const PJ& j, u32x4& X, int c, int i) {
    const char* p_ = j.xs + c * 2048 + (i >> 1) * 1024;
    asm volatile("global_load_dwordx4 %0, %1, %2 sc1" : "=&v"(X) : "v"((i & 1) ? j.xv1 : j.xv0), "s"(p_) : "memory");
}
struct KVO { const char* base; unsigned lo[2]; };
DI void kv_store(const KVO& o, const u32x4& v, int i) {
    if (i & 2) st_wt_i<2048>(o.base + (i >> 2) * 8192, o.lo[i & 1], v); else st_wt_i<0>(o.base + (i >> 2) * 8192, o.lo[i & 1], v);
}
DI void pj_xload(const PJ& j, u32x4 (&X)[4], int c) {
    const char* p_ = j.xs + c * 2048;
    asm volatile("global_load_dwordx4 %0, %4, %6 sc1\n\tglobal_load_dwordx4 %1, %5, %6 sc1\n\tglobal_load_dwordx4 %2, %4, %6 offset:1024 sc1\n\tglobal_load_dwordx4 %3, %5, %6 offset:1024 sc1"
                 : "=&v"(X[0]), "=&v"(X[1]), "=&v"(X[2]), "=&v"(X[3]) : "v"(j.xv0), "v"(j.xv1), "s"(p_) : "memory");
}

template <bool VT, class F>
DI void k1_mma(f32x4 (&acc)[8][4], int half, const bf16x8 (&c)[4], const bf16x8 (&w)[4], F&& filler) {
#pragma unroll
    for (int rt = 0; rt < 4; ++rt)
#pragma unroll
        for (int nt = 0; nt < 4; ++nt) {
            if (half == 0) acc[rt][nt] = VT ? MFMA(c[rt], w[nt], acc[rt][nt]) : MFMA(w[nt], c[rt], acc[rt][nt]);
            else acc[4 + rt][nt] = VT ? MFMA(c[rt], w[nt], acc[4 + rt][nt]) : MFMA(w[nt], c[rt], acc[4 + rt][nt]);
            filler(rt * 4 + nt);
        }
}

template <bool VT>
DI void k1_tile(const char* __restrict__ Abase, const char* __restrict__ Wbase, char* __restrict__ outp, char* lds, const unsigned (&avo)[4], const unsigned (&wvo)[4],
                unsigned wid, int hp, int rb, char* __restrict__ ws, const char* __restrict__ wq, const char* __restrict__ w1, const char* __restrict__ b1, u64 tag,
                int head2, int f0, bool nft4, int q16_0, u32x4 (&xa)[4], u32x2 (&bb)[4], u32x4 (&kv)[16], KVO& kvo) {
    char* stamp_base = ws; (void)stamp_base;
    const char* flb = uptr(ws + WS_FL);
    const unsigned wm = wid >> 2, wn = wid & 3;
    const unsigned lds0 = (unsigned)(uintptr_t)lds;
    unsigned aaddr[2], baddr[4];
    {
        const unsigned lane = flane(), c = lane & 15, g = lane >> 4;
        const unsigned x = (c >> 1) & 7;
#pragma unroll
        for (int s = 0; s < 2; ++s) aaddr[s] = (128 * wm + c) * 128 + (((4 * s + g) ^ x) << 4);
        const unsigned q = (lane & 15) >> 2, p = lane & 3, f = q | ((g & 1) << 2);
#pragma unroll
        for (int nt = 0; nt < 4; ++nt) baddr[nt] = 32768 + (8 * g + q) * 512 + (((8 * wn + 2 * nt + (p >> 1)) ^ (f << 1)) << 4) + ((p & 1) << 3);
    }
    f32x4 acc[8][4];
#pragma unroll
    for (int i = 0; i < 8; ++i)
#pragma unroll
        for (int j = 0; j < 4; ++j) acc[i][j] = f32x4{};
    bf16x8 cf[2][4], wf[2][4];
    lds_cptr L = (lds_cptr)lds;
#define K1_RC(buf, stg, s, half) do { _Pragma("unroll") for (int rt = 0; rt < 4; ++rt) cf[buf][rt] = lds_frag(L + (stg) + aaddr[s] + ((half) * 4 + rt) * 2048); } while (0)
#define K1_RW(buf, stg, s) do { _Pragma("unroll") for (int nt = 0; nt < 4; ++nt) wf[buf][nt] = tr_frag(L + (stg) + baddr[nt] + (s) * 16384, L + (stg) + baddr[nt] + (s) * 16384 + 2048); } while (0)
    const char* Aw = Abase + (size_t)wid * 32 * 1024;
    const char* Ww = Wbase + (size_t)wid * 8 * 8192;
    const unsigned m0a = lds0 + wid * 4096, m0w = lds0 + 32768 + wid * 4096;
#define K1_DMA(kt, stg)                                                                   \
    do {                                                                                  \
        const char* a_ = Aw + (kt) * 128; const char* w_ = Ww + (size_t)(kt) * 64 * 8192; \
        dma1<0>(m0a + (stg), a_, avo[0]); dma1<1024>(m0a + (stg), a_, avo[1]); dma1<2048>(m0a + (stg), a_, avo[2]); dma1<3072>(m0a + (stg), a_, avo[3]); \
        dma1<0>(m0w + (stg), w_, wvo[0]); dma1<1024>(m0w + (stg), w_, wvo[1]); dma1<2048>(m0w + (stg), w_, wvo[2]); dma1<3072>(m0w + (stg), w_, wvo[3]); \
    } while (0)
    unsigned pfd = 0;
    u32x2 fx = u32x2{};
#define K1_FXO() (FL_X + (2 * q16_0 + (flane() & 3)) * 8)
    STAMP(1);
    asm volatile("s_waitcnt vmcnt(10)\n\ts_barrier" ::: "memory");
    STAMP(2);
    K1_RW(0, 0, 0); K1_RC(0, 0, 0, 0);
#define K1_RC1(buf, stg, s, half, rt) cf[buf][rt] = lds_frag(L + (stg) + aaddr[s] + ((half) * 4 + (rt)) * 2048)
#define K1_RW1(buf, stg, s, nt) wf[buf][nt] = tr_frag(L + (stg) + baddr[nt] + (s) * 16384, L + (stg) + baddr[nt] + (s) * 16384 + 2048)
#pragma unroll
    for (int kt = 0; kt < 8; ++kt) {
        const unsigned stg = (kt & 1) * 65536, nstg = stg ^ 65536;
        SBAR();
        k1_mma<VT>(acc, 0, cf[0], wf[0], [&](int m) { if (m < 8 && (m & 1) == 0) { K1_RC1(1, stg, 0, 1, m >> 1); SBAR(); } });
        SBAR();
        k1_mma<VT>(acc, 1, cf[1], wf[0], [&](int m) { if (m < 4) { K1_RC1(0, stg, 1, 0, m); SBAR(); } else if (m < 12 && (m & 1) == 0) { K1_RW1(1, stg, 1, (m - 4) >> 1); SBAR(); } });
        SBAR();
        k1_mma<VT>(acc, 0, cf[0], wf[1], [&](int m) { if (m < 8 && (m & 1) == 0) { K1_RC1(1, stg, 1, 1, m >> 1); SBAR(); } });
        SBAR();
        if (kt == 7) asm volatile("s_waitcnt vmcnt(0) lgkmcnt(0)\n\ts_barrier" : "+v"(pfd)::"memory");
        else asm volatile("s_waitcnt vmcnt(0) lgkmcnt(0)\n\ts_barrier" : "+v"(fx)::"memory");
        if (kt == 0) flag_set(wid, flb, FL_X + blockIdx.x * 8, tag);
        if (kt == 6) flag_wait<false>(0, fx, flb, K1_FXO(), tag);
        const char* a_ = Aw + (kt + 2) * 128; const char* w_ = Ww + (size_t)(kt + 2) * 64 * 8192;
        k1_mma<VT>(acc, 1, cf[1], wf[1], [&](int m) {
            if (kt + 1 < 8) { if (m < 4) { K1_RW1(0, nstg, 0, m); SBAR(); } else if (m < 8) { K1_RC1(0, nstg, 0, 0, m - 4); SBAR(); } }
            if (kt + 2 < 8) {
                if (m == 1) { dma1<0>(m0a + stg, a_, avo[0]); SBAR(); } if (m == 3) { dma1<1024>(m0a + stg, a_, avo[1]); SBAR(); }
                if (m == 5) { dma1<2048>(m0a + stg, a_, avo[2]); SBAR(); } if (m == 7) { dma1<3072>(m0a + stg, a_, avo[3]); SBAR(); }
                if (m == 9) { dma1<0>(m0w + stg, w_, wvo[0]); SBAR(); } if (m == 11) { dma1<1024>(m0w + stg, w_, wvo[1]); SBAR(); }
                if (m == 13) { dma1<2048>(m0w + stg, w_, wvo[2]); SBAR(); } if (m == 15) { dma1<3072>(m0w + stg, w_, wvo[3]); SBAR(); }
            }
            if (kt == 5 && m == 15) {
                warm_load(pfd, wq, w1, blockIdx.x * 8 + wid);
                SBAR();
            }
            if (kt == 4 && m == 14) { flag_ld(fx, flb, K1_FXO()); SBAR(); }
            if (kt == 6 && m == 2) {
                const PJ j = pj_setup(wq, w1, ws, wid, head2, f0, nft4, q16_0); pj_dma(j, lds0, wid, 0, 0); pj_dma(j, lds0, wid, 1, PSLOT); SBAR();
            }
            if (kt == 7 && m == 0) { const PJ j = pj_setup(wq, w1, ws, wid, head2, f0, nft4, q16_0); asm volatile("s_nop 4"); pj_xload(j, xa, 0); SBAR(); }
            if (kt == 7 && m == 2) {
                const char* bp = uptr(b1 + f0 * 2);
                const unsigned bo = (flane() >> 4) * 8, b2 = bo + (nft4 ? 64 : 0);
                asm volatile("s_nop 4\n\tglobal_load_dwordx2 %0, %4, %6\n\tglobal_load_dwordx2 %1, %4, %6 offset:32\n\tglobal_load_dwordx2 %2, %5, %6\n\tglobal_load_dwordx2 %3, %5, %6 offset:32"
                             : "=&v"(bb[0]), "=&v"(bb[1]), "=&v"(bb[2]), "=&v"(bb[3]) : "v"(bo), "v"(b2), "s"(bp) : "memory");
                SBAR();
            }
        });
        SBAR();
    }
    STAMP(3);
    const int head = 2 * hp + (wn >> 1), d0 = (wn & 1) * 64;
    const int key0 = 256 * rb + 128 * wm;
    const unsigned lane = flane(), c = lane & 15, g = lane >> 4;
    kvo.base = uptr(outp + ((size_t)(head * 64 + (key0 >> 5)) * 4 + (d0 >> 5)) * 2048);
    kvo.lo[0] = (g >> 1) * 1024 + ((VT ? c : (c & 7) + 16 * (c >> 3)) + 32 * (g & 1)) * 16;
    kvo.lo[1] = kvo.lo[0] + (VT ? 256 : 128);
#pragma unroll
    for (int i = 0; i < 16; ++i) {
        const int u = i >> 2, a = (i >> 1) & 1, b2 = i & 1;
        kv[i] = VT ? pack44(acc[2 * u][2 * a + b2], acc[2 * u + 1][2 * a + b2]) : pack44(acc[2 * u + b2][2 * a], acc[2 * u + b2][2 * a + 1]);
    }
    asm volatile("s_waitcnt vmcnt(0)" : "+v"(xa[0]), "+v"(xa[1]), "+v"(xa[2]), "+v"(xa[3]), "+v"(bb[0]), "+v"(bb[1]), "+v"(bb[2]), "+v"(bb[3])::"memory");
    STAMP(5);
#undef K1_RC
#undef K1_RW
#undef K1_DMA
#undef K1_FXO
}

constexpr float QSCALE = 0.08838834764831845f * 1.4426950408889634f;
constexpr float THR = 8.0f;
DI float x4_max(float x) {
    auto s = __builtin_amdgcn_permlane16_swap(__float_as_uint(x), __float_as_uint(x), false, false);
    x = __builtin_fmaxf(__uint_as_float(s[0]), __uint_as_float(s[1]));
    auto t = __builtin_amdgcn_permlane32_swap(__float_as_uint(x), __float_as_uint(x), false, false);
    return __builtin_fmaxf(__uint_as_float(t[0]), __uint_as_float(t[1]));
}
DI float x4_sum(float x) {
    auto s = __builtin_amdgcn_permlane16_swap(__float_as_uint(x), __float_as_uint(x), false, false);
    x = __uint_as_float(s[0]) + __uint_as_float(s[1]);
    auto t = __builtin_amdgcn_permlane32_swap(__float_as_uint(x), __float_as_uint(x), false, false);
    return __uint_as_float(t[0]) + __uint_as_float(t[1]);
}
DI float mish_f(float h) {
    const float e = __builtin_amdgcn_exp2f(__builtin_fminf(h, 30.0f) * 1.4426950408889634f);
    const float n = e * (e + 2.0f);
    return h * n * __builtin_amdgcn_rcpf(n + 2.0f);
}

#define SPC 4
#define KVP0 7
#define KVPS 2
template <int NFT>
DI void proj_phase(const char* __restrict__ wq, const char* __restrict__ w1, char* __restrict__ ws, char* lds, int head, int f0, int q16_0, bf16x8 (&qf)[2][4],
                   const char* __restrict__ wo, const char* __restrict__ w2, unsigned m0v0, unsigned wid, u32x4 (&xa)[4], u32x4 (&xb)[4], const u32x2 (&bb)[4], u64 tag,
                   unsigned kvflag, int hp2, unsigned m0k0, unsigned m0k1, const char* kptr0, const char* kptr1, const u32x4 (&kv)[16], const KVO& kvo) {
    const unsigned lane = flane(), g = lane >> 4;
    char* stamp_base = ws; (void)stamp_base;
    const unsigned lds0 = (unsigned)(uintptr_t)lds;
    lds_cptr L = (lds_cptr)lds;
    const PJ pj = pj_setup(wq, w1, ws, wid, head, f0, NFT == 4, q16_0);
    const char* flb = uptr(ws + WS_FL);
    u32x2 fkv = u32x2{};
    const unsigned fkvo = FL_KV + (hp2 * 16 + (lane & 15)) * 8;
    const unsigned q = (lane & 15) >> 2, p = lane & 3;
    const unsigned wqb = 2 * g * WQ_PIECE + q * 64 + p * 8;
    const unsigned w1b = W1_OFF + g * W1_PIECE + q * 64 + p * 8;
    f32x4 aq[8][2], ah[NFT][2];
#pragma unroll
    for (int i = 0; i < 8; ++i) { aq[i][0] = f32x4{}; aq[i][1] = f32x4{}; }
#pragma unroll
    for (int i = 0; i < NFT; ++i) { ah[i][0] = f32x4{}; ah[i][1] = f32x4{}; }
    constexpr int NT = 8 + NFT;
    bf16x8 wf[4];
#define PJ_RD(slot, s, n)                                                                                       \
    (((n) < 8) ? tr_frag(L + (slot) + wqb + (8 * (s)) * WQ_PIECE + ((n) >> 1) * 256 + ((n) & 1) * 32, L + (slot) + wqb + (8 * (s) + 1) * WQ_PIECE + ((n) >> 1) * 256 + ((n) & 1) * 32) \
               : tr_frag(L + (slot) + w1b + (4 * (s)) * W1_PIECE + (((n) - 8) >> 1) * 512 + ((n) & 1) * 32, L + (slot) + w1b + (4 * (s)) * W1_PIECE + 256 + (((n) - 8) >> 1) * 512 + ((n) & 1) * 32))
#pragma unroll
    for (int n = 0; n < 3; ++n) wf[n] = PJ_RD(0, 0, n);
#define PJ_CHUNK(c, XC, XN)                                                                                     \
    do {                                                                                                        \
        constexpr int slot_ = ((c) % 3) * PSLOT, nslot_ = (((c) + 1) % 3) * PSLOT, dslot_ = (((c) + 2) % 3) * PSLOT; \
          \
        if ((c) == 9) flag_wait<false>(0, fkv, flb, fkvo, tag);                                                 \
        SBAR();                                                                                                 \
        constexpr int nst_ = 16 - SPC * (c) < 0 ? 0 : 16 - SPC * (c) < SPC ? 16 - SPC * (c) : SPC;        \
        _Pragma("unroll") for (int m = 0; m < 2 * NT; ++m) {                                                    \
            const int s = m / NT, n = m % NT;                                                                   \
            if (m == 2 * NT - 3) {       \
                if ((c) + 2 < 10) asm volatile("s_waitcnt vmcnt(%4) lgkmcnt(0)\n\ts_barrier" : "+v"(XN[0]), "+v"(XN[1]), "+v"(XN[2]), "+v"(XN[3]) : "i"(3 + nst_) : "memory"); \
                else if ((c) + 1 < 10) asm volatile("s_waitcnt vmcnt(0) lgkmcnt(0)\n\ts_barrier" : "+v"(XN[0]), "+v"(XN[1]), "+v"(XN[2]), "+v"(XN[3]), "+v"(fkv)::"memory"); \
                if (nst_ == 0 && 16 - SPC * ((c) - 1) > 0) flag_set(wid, flb, kvflag, tag);        \
            }                                                                                                   \
            const int m3 = m + 3;                                                                               \
            if (m3 < 2 * NT) wf[m3 & 3] = PJ_RD(slot_, m3 / NT, m3 % NT);                                       \
            else if ((c) + 1 < 10) wf[m3 & 3] = PJ_RD(nslot_, 0, m3 - 2 * NT);                                  \
            const bf16x8 x0 = __builtin_bit_cast(bf16x8, XC[2 * s]), x1 = __builtin_bit_cast(bf16x8, XC[2 * s + 1]); \
            if (n < 8) { aq[n & 7][0] = MFMA(wf[m & 3], x0, aq[n & 7][0]); aq[n & 7][1] = MFMA(wf[m & 3], x1, aq[n & 7][1]); } \
            else { ah[(n - 8) % NFT][0] = MFMA(wf[m & 3], x0, ah[(n - 8) % NFT][0]); ah[(n - 8) % NFT][1] = MFMA(wf[m & 3], x1, ah[(n - 8) % NFT][1]); } \
            SBAR();                                                                                             \
                       \
            if ((c) + 1 < 10 && m < 4) pj_xload1(pj, XN[m & 3], (c) + 1, m);                                    \
            if ((c) + 2 < 10 && m >= 4 && m < 7) pj_dma1(pj, lds0, wid, (c) + 2, dslot_, m - 4);                \
            if (m >= KVP0 && (m - KVP0) % KVPS == 0 && (m - KVP0) / KVPS < nst_) { const int i_ = (SPC * (c) + (m - KVP0) / KVPS) & 15; kv_store(kvo, kv[i_], i_); } \
            if ((c) == 8 && m == NT) flag_ld(fkv, flb, fkvo);                                                   \
            if ((c) == 9 && m < 16 && !(m & 1)) {                           \
                const int i_ = m >> 1;                                                                          \
                if ((i_ & 3) == 0) dma1_sc1<0>(i_ < 4 ? m0k0 : m0k1, i_ < 4 ? kptr0 : kptr1, lane * 16);            \
                if ((i_ & 3) == 1) dma1_sc1<1024>(i_ < 4 ? m0k0 : m0k1, i_ < 4 ? kptr0 : kptr1, lane * 16);         \
                if ((i_ & 3) == 2) dma1_sc1<2048>(i_ < 4 ? m0k0 : m0k1, i_ < 4 ? kptr0 : kptr1, lane * 16);         \
                if ((i_ & 3) == 3) dma1_sc1<3072>(i_ < 4 ? m0k0 : m0k1, i_ < 4 ? kptr0 : kptr1, lane * 16);         \
            }                                                                                                   \
            if ((c) == 9 && m == 16) warm_lds(m0v0, wo, w2, blockIdx.x * 8 + wid);        \
            SBAR();                                                                                             \
        }                                                                                                       \
        PJSTAMP(20 + (c));                                                                                      \
    } while (0)
    static_assert((2 * NT) % 4 == 0, "fragment window");
    PJ_CHUNK(0, xa, xb); PJ_CHUNK(1, xb, xa); PJ_CHUNK(2, xa, xb); PJ_CHUNK(3, xb, xa); PJ_CHUNK(4, xa, xb);
    PJ_CHUNK(5, xb, xa); PJ_CHUNK(6, xa, xb); PJ_CHUNK(7, xb, xa); PJ_CHUNK(8, xa, xb); PJ_CHUNK(9, xb, xa);
#undef PJ_CHUNK
#undef PJ_RD
    asm volatile("s_waitcnt vmcnt(1)" ::: "memory");
#pragma unroll
    for (int a = 0; a < 2; ++a)
#pragma unroll
        for (int v = 0; v < 4; ++v) qf[a][v] = __builtin_bit_cast(bf16x8, pack44s(aq[2 * v][a], aq[2 * v + 1][a], QSCALE));
#pragma unroll
    for (int v = 0; v < NFT / 2; ++v) {
        float bias[8];
#pragma unroll
        for (int t = 0; t < 2; ++t) {
            const u32x2 q2 = bb[2 * v + t];
            bias[4 * t + 0] = bflo(q2[0]); bias[4 * t + 1] = bfhi(q2[0]); bias[4 * t + 2] = bflo(q2[1]); bias[4 * t + 3] = bfhi(q2[1]);
        }
#pragma unroll
        for (int a = 0; a < 2; ++a) {
            f32x4 h0, h1;
#pragma unroll
            for (int i = 0; i < 4; ++i) { h0[i] = mish_f(ah[2 * v][a][i] + bias[i]); h1[i] = mish_f(ah[2 * v + 1][a][i] + bias[4 + i]); }
            st_wt_o(uptr(ws + WS_CH + ((size_t)(q16_0 + a) * NKS_CH + NKS_C + (f0 >> 5) + v) * 1024), lane * 16, pack44(h0, h1));
        }
    }
}

#define NOP_SO(S, o) asm volatile("s_nop 7\n\ts_nop 3" : "+v"(S[0]), "+v"(S[1]), "+v"(o[0]), "+v"(o[1]), "+v"(o[2]), "+v"(o[3]))
#define NOP_A(A) asm volatile("s_nop 7\n\ts_nop 1" : "+v"(A[0][0]), "+v"(A[0][1]), "+v"(A[0][2]), "+v"(A[0][3]), "+v"(A[1][0]), "+v"(A[1][1]), "+v"(A[1][2]), "+v"(A[1][3]), \
    "+v"(A[2][0]), "+v"(A[2][1]), "+v"(A[2][2]), "+v"(A[2][3]), "+v"(A[3][0]), "+v"(A[3][1]), "+v"(A[3][2]), "+v"(A[3][3]), "+v"(A[4][0]), "+v"(A[4][1]), "+v"(A[4][2]), "+v"(A[4][3]))
DI float xh_sum(float v) { auto r = __builtin_amdgcn_permlane32_swap(__float_as_uint(v), __float_as_uint(v), false, false); return __uint_as_float(r[0]) + __uint_as_float(r[1]); }
DI float xh_max(float v) { auto r = __builtin_amdgcn_permlane32_swap(__float_as_uint(v), __float_as_uint(v), false, false); return __builtin_fmaxf(__uint_as_float(r[0]), __uint_as_float(r[1])); }
DI float mul_s(float a, float b) { float r; asm("v_mul_f32 %0, %1, %2" : "=v"(r) : "v"(a), "v"(b)); return r; }
DI float sub_s(float a, float b) { float r; asm("v_sub_f32 %0, %1, %2" : "=v"(r) : "v"(a), "v"(b)); return r; }
DI u32x4 pack_o(const f32x16& a, int y, float sc) {
    u32x4 w; w[0] = cvtpk(mul_s(a[4 * y + 0], sc), mul_s(a[4 * y + 1], sc)); w[1] = cvtpk(mul_s(a[4 * y + 2], sc), mul_s(a[4 * y + 3], sc));
    w[2] = cvtpk(mul_s(a[8 + 4 * y + 0], sc), mul_s(a[8 + 4 * y + 1], sc)); w[3] = cvtpk(mul_s(a[8 + 4 * y + 2], sc), mul_s(a[8 + 4 * y + 3], sc)); return w;
}
DI void mask_diag(f32x16& s, int dq) {
    const float ninf = -__builtin_inff();
#pragma unroll
    for (int r0 = 0; r0 < 16; r0 += 4) {
        unsigned long long m0, m1, m2, m3;
        const int kc = 16 * ((r0 >> 2) & 1) + 8 * (r0 >> 3);
        float a = s[r0], b = s[r0 + 1], c = s[r0 + 2], d = s[r0 + 3];
        asm("v_cmp_gt_i32_e64 %4, %9, %8\n\tv_cmp_gt_i32_e64 %5, %10, %8\n\tv_cmp_gt_i32_e64 %6, %11, %8\n\tv_cmp_gt_i32_e64 %7, %12, %8\n\t"
            "v_cndmask_b32_e64 %0, %0, %13, %4\n\tv_cndmask_b32_e64 %1, %1, %13, %5\n\tv_cndmask_b32_e64 %2, %2, %13, %6\n\tv_cndmask_b32_e64 %3, %3, %13, %7"
            : "+v"(a), "+v"(b), "+v"(c), "+v"(d), "=&s"(m0), "=&s"(m1), "=&s"(m2), "=&s"(m3)
            : "v"(dq), "i"(kc), "i"(kc + 1), "i"(kc + 2), "i"(kc + 3), "v"(ninf));
        s[r0] = a; s[r0 + 1] = b; s[r0 + 2] = c; s[r0 + 3] = d;
    }
}
#define VIL 1
#define VIL0 0
#define FRW 8
#define FRA 6

#define K3DPOS {2, 3, 6, 7, 10, 14, 17}
constexpr int K3D[7] = K3DPOS;
constexpr int K3_SLOT = 36864;
constexpr int PP_SET = 8 * 5 * 1024;
DI float silu_f(float v) { return v * __builtin_amdgcn_rcpf(1.0f + __builtin_amdgcn_exp2f(-1.4426950408889634f * v)); }
DI f32x4 unpk_lo(const u32x4& w) { return f32x4{bflo(w[0]), bfhi(w[0]), bflo(w[1]), bfhi(w[1])}; }
DI f32x4 unpk_hi(const u32x4& w) { return f32x4{bflo(w[2]), bfhi(w[2]), bflo(w[3]), bfhi(w[3])}; }

struct K3S { const char *aptr, *wptr; unsigned avo, wvo[3], m0a, m0w; };
DI K3S k3_setup(unsigned lds0, unsigned wid, unsigned lane) {
    K3S k;
    k.aptr = nullptr; k.wptr = nullptr;
    k.avo = lane * 16;
    const unsigned p0 = wid >= 4 ? 12 + 2 * (wid & 3) : 3 * wid;
#pragma unroll
    for (int i = 0; i < 3; ++i) {
        const unsigned o = (p0 + i) * 1024 + lane * 16, row = o / 320, c16 = (o % 320) >> 4;
        k.wvo[i] = row * 1280 + ((c16 ^ (((row >> 2) & 1) << 1)) << 4) - 1024 * i;
    }
    k.m0a = lds0 + wid * 2048; k.m0w = lds0 + 16384 + p0 * 1024;
    return k;
}
DI void k3_point(K3S& k, const char* ws, const char* w, unsigned wid, int qt, int mt, int kstep, int stage) {
    k.aptr = uptr(ws + WS_CH + ((size_t)(8 * qt + wid) * NKS_CH + kstep) * 1024);
    k.wptr = uptr(w + (size_t)stage * 64 * 1280 + mt * 320);
    asm volatile("s_nop 4" : "+s"(k.aptr), "+s"(k.wptr));
}
DI void k3_stage(K3S& k, unsigned slot, bool three) {
    set_m0(k.m0a + slot); asm volatile("s_nop 0"); dma_sc1<0>(k.aptr, k.avo); dma_sc1<1024>(k.aptr, k.avo);
    set_m0(k.m0w + slot); asm volatile("s_nop 0"); dma_nom0<0>(k.wptr, k.wvo[0]); dma_nom0<1024>(k.wptr, k.wvo[1]);
    if (three) dma_nom0<2048>(k.wptr, k.wvo[2]);
    k.aptr += 2048; k.wptr += 81920;
}
DI void k3_publish(const f32x4 (&res)[10], f32x4 (&own)[2][2], const char* pset, unsigned vo, unsigned r) {
    own[1][0] = f32x4{}; own[1][1] = f32x4{};
#pragma unroll
    for (int j = 0; j < 5; ++j) {
        if ((j & 3) == r) { own[j >> 2][0] = res[2 * j]; own[j >> 2][1] = res[2 * j + 1]; }
        else st_wt_o(pset + j * 1024, vo, pack44(res[2 * j], res[2 * j + 1]));
    }
}

template <int GRP>
DI void k3_body(K3S& k, const char* __restrict__ x, const char* __restrict__ wo, char* __restrict__ ws, char* __restrict__ out, char* lds, unsigned wid, u64 tag,
                int ks, int qt, int mt, unsigned cflag) {
    constexpr int P = GRP ? 4 : 5;
    const unsigned lane = flane(), c = lane & 15, g = lane >> 4;
    const unsigned w4 = wid & 3, qh = w4 >> 1, mh = w4 & 1;
    char* stamp_base = ws; (void)stamp_base;
    const char* flb = uptr(ws + WS_FL);
    lds_cptr L = (lds_cptr)lds;
    const unsigned ard = (8 * qh + GRP) * 1024 + lane * 16;
    const unsigned q = (lane & 15) >> 2, p = lane & 3;
    const unsigned wrow = 16384 + (32 * GRP + 4 * g + q) * 320 + 160 * mh + 8 * p;
    const unsigned sgn = ((g & 1) ? 32u : 0u);
    const unsigned wrdE = (mh & 1) ? wrow - sgn : wrow + sgn;
    const unsigned wrdO = (mh & 1) ? wrow + sgn : wrow - sgn;
    f32x4 acc[5][4], accB[5][4];
    bf16x8 af[2][4], wf[4], w4f[2];
    unsigned cur = 0;
#define K3_RW(slot, n) tr_frag(L + (slot) + (((n) & 1) ? wrdO : wrdE) + 32 * (n), L + (slot) + (((n) & 1) ? wrdO : wrdE) + 32 * (n) + 16 * 320)
#define K3_RA(slot, a) lds_frag(L + (slot) + ard + (a) * 2048)
#define K3_ZERO(acc) do { _Pragma("unroll") for (int n = 0; n < 5; ++n) _Pragma("unroll") for (int a = 0; a < 4; ++a) acc[n][a] = f32x4{}; NOP_A(acc); } while (0)
#define K3_ITER(acc, PAR, WN, RD, DM, SAFE, HOOK)                                                                       \
    do {                                                                                                                \
        if ((WN) >= 0) asm volatile("s_waitcnt vmcnt(%0) lgkmcnt(0)\n\ts_barrier" ::"i"((WN) >= 0 ? (WN) : 0) : "memory");   \
        else asm volatile("s_cmp_eq_u32 %0, 0\n\ts_cbranch_scc1 .Lq%=\n\ts_waitcnt vmcnt(0)\n.Lq%=:\n\ts_waitcnt vmcnt(1) lgkmcnt(0)\n\ts_barrier" \
                          ::"s"(wid) : "scc", "memory");                             \
        HOOK;                                                                                                           \
        const unsigned nxt_ = cur == 3 * K3_SLOT ? 0u : cur + K3_SLOT;                                                  \
        _Pragma("unroll") for (int n = 0; n < 5; ++n) {                                                                 \
            _Pragma("unroll") for (int a = 0; a < 4; ++a) {                                                             \
                if (SAFE) mfma_acc_s(acc[n][a], n < 4 ? wf[n & 3] : w4f[PAR], af[PAR][a]);                              \
                else mfma_acc(acc[n][a], n < 4 ? wf[n & 3] : w4f[PAR], af[PAR][a]);                                     \
                SBAR();                                                                                                 \
                if (RD) {                                                                                               \
                    if (n == 0 && a == 0) w4f[1 - (PAR)] = K3_RW(nxt_, 4);                                              \
                    if (n < 4 && a == 1) af[1 - (PAR)][n & 3] = K3_RA(nxt_, n & 3);                                     \
                    if (n >= 1 && a == 0) wf[(n - 1) & 3] = K3_RW(nxt_, n - 1);                                         \
                }                                                                                                       \
                if (DM) {                                                                                               \
                    const int z_ = 4 * n + a;                                                                           \
                    if (z_ == K3D[0]) set_m0(k.m0a + cur);                                                              \
                    if (z_ == K3D[1]) dma_sc1<0>(k.aptr, k.avo);                                                        \
                    if (z_ == K3D[2]) dma_sc1<1024>(k.aptr, k.avo);                                                     \
                    if (z_ == K3D[3]) set_m0(k.m0w + cur);                                                              \
                    if (z_ == K3D[4]) dma_nom0<0>(k.wptr, k.wvo[0]);                                                    \
                    if (z_ == K3D[5]) dma_nom0<1024>(k.wptr, k.wvo[1]);                                                 \
                    if (z_ == K3D[6] && !GRP) dma_nom0<2048>(k.wptr, k.wvo[2]);                                         \
                }                                                                                                       \
                SBAR();                                                                                                 \
            }                                                                                                           \
        }                                                                                                               \
        if (DM) { k.aptr += 2048; k.wptr += 81920; }                                                                    \
        cur = nxt_;                                                                                                     \
    } while (0)
#define K3_FIRST(PAR) do { _Pragma("unroll") for (int a = 0; a < 4; ++a) af[PAR][a] = K3_RA(0, a);                      \
        _Pragma("unroll") for (int n = 0; n < 4; ++n) wf[n] = K3_RW(0, n);                                              \
        w4f[PAR] = K3_RW(0, 4); } while (0)
#define K3_REDUCE()                                                                                                     \
    do {                                                                                                                \
        NOP_A(acc);                                                                                                     \
        lds_ptr X_ = (lds_ptr)lds + wid * 10240 + lane * 16;                                                            \
        lds_cptr Y_ = (lds_cptr)lds + (wid ^ 4) * 10240 + lane * 16;                                                    \
        _Pragma("unroll") for (int a = 0; a < 2; ++a) _Pragma("unroll") for (int n = 0; n < 5; ++n)                     \
            *(__attribute__((address_space(3))) f32x4*)(X_ + (a * 5 + n) * 1024) = acc[n][GRP ? a : 2 + a];             \
        asm volatile("s_waitcnt lgkmcnt(0)\n\ts_barrier" ::: "memory");                                                 \
        _Pragma("unroll") for (int a = 0; a < 2; ++a) _Pragma("unroll") for (int n = 0; n < 5; ++n)                     \
            res[a * 5 + n] = acc[n][GRP ? 2 + a : a] + *(const __attribute__((address_space(3))) f32x4*)(Y_ + (a * 5 + n) * 1024); \
    } while (0)
#define K3_BPUT(j0, j1)                                                                                                 \
    do {                                                                                                                \
        _Pragma("unroll") for (int j = j0; j < j1; ++j)                                                                 \
            *(__attribute__((address_space(3))) u32x4*)((lds_ptr)lds + 4 * K3_SLOT + wid * 2048 + lane * 16 + (j - (j0)) * 1024) = \
                pack44(accB[(2 * j) % 5][(GRP ? 0 : 2) + (2 * j) / 5], accB[(2 * j + 1) % 5][(GRP ? 0 : 2) + (2 * j + 1) / 5]); \
        SBAR();                                                                                                         \
    } while (0)
#define K3_BGET(j0, j1)                                             \
    do {                                                                                                                \
        _Pragma("unroll") for (int j = j0; j < j1; ++j)                                                                 \
            yb[j - (j0)] = *(const __attribute__((address_space(3))) u32x4*)((lds_cptr)lds + 4 * K3_SLOT + (wid ^ 4) * 2048 + lane * 16 + (j - (j0)) * 1024); \
        SBAR();                                                                                                         \
    } while (0)
#define K3_BADD(j0, j1)                                                                                                 \
    do {                                                                                                                \
        _Pragma("unroll") for (int j = j0; j < j1; ++j) {                                                               \
            resB[2 * j] = accB[(2 * j) % 5][(GRP ? 2 : 0) + (2 * j) / 5] + unpk_lo(yb[j - (j0)]);                       \
            resB[2 * j + 1] = accB[(2 * j + 1) % 5][(GRP ? 2 : 0) + (2 * j + 1) / 5] + unpk_hi(yb[j - (j0)]);           \
            PIN(resB[2 * j]); PIN(resB[2 * j + 1]);                                                                     \
        }                                                                                                               \
        SBAR();                                                                                                         \
    } while (0)
#define K3_BOUT(s)                                                                                                      \
    do {                                                                                                                \
        const bool lo_ = r != 0 && (s) < r;                                                                             \
        const unsigned hi_ = (s) == 3 ? 4 : (s) + 1;                                                                    \
        const u32x4 a_ = pack44(resB[2 * (s)], resB[2 * (s) + 1]), b_ = pack44(resB[2 * hi_], resB[2 * hi_ + 1]);       \
        const bool ua_ = lo_ || ((s) == 3 && r == 0);                                                                   \
        u32x4 v_;                                                                                                       \
        _Pragma("unroll") for (int e = 0; e < 4; ++e) v_[e] = ua_ ? a_[e] : b_[e];                                      \
        st_wt_o(pbase + (4 + ks) * PP_SET + (ua_ ? (s) : hi_) * 1024, vo, v_);                                          \
        SBAR();                                                                                                         \
    } while (0)
#define NOHOOK (void)0
    const int grp64 = qt * 4 + mt;
    const char* pbase = uptr(ws + WS_PP + (size_t)grp64 * (8 * PP_SET) + wid * 5120);
    const unsigned vo = lane * 16;
    const unsigned r = (ks - wid) & 3;
    f32x4 res[10], resB[10], ownB[2][2], ownS[2][2];
    asm volatile("s_waitcnt vmcnt(%0) lgkmcnt(0)\n\ts_barrier" ::"i"(P + 4) : "memory");
    STAMP(10);
    k3_stage(k, 2 * K3_SLOT, !GRP); k3_stage(k, 3 * K3_SLOT, !GRP);
    K3_ZERO(accB);
    if (ks != 3) { K3_FIRST(1); K3_ITER(accB, 1, 2 * P + 4, true, true, true, NOHOOK); }
    else K3_FIRST(0);
    K3_ITER(accB, 0, 2 * P, true, true, true, flag_set(wid, flb, cflag, tag));
    K3_ITER(accB, 1, 2 * P, true, true, false, NOHOOK);
    u32x2 fc = u32x2{~(unsigned)tag, 0u};
    const unsigned fco = FL_C + ((qt < 8 ? qt : 15 - qt) * 32 + 8 * ks + (lane & 7)) * 8;
    K3_ITER(accB, 0, 2 * P, true, false, false, flag_ld0(wid, fc, flb, fco));
    STAMP(11);
    flag_wait<true>(wid, fc, flb, fco, tag);
    STAMP(18);
    k3_point(k, ws, wo, wid, qt, mt, 32 * ks, 16 * ks);
    K3_ITER(accB, 1, P, true, true, false, k3_stage(k, cur == 0 ? 3 * K3_SLOT : cur - K3_SLOT, !GRP));
    K3_ITER(accB, 0, 2 * P, true, true, false, NOHOOK);
    K3_ITER(accB, 1, 2 * P, true, true, false, NOHOOK);
    STAMP(19);
    K3_ZERO(acc);
    NOP_A(accB);
    u32x4 yb[2];
#define K3_H2(A, B) do { A; B; } while (0)
    K3_ITER(acc, 0, 2 * P, true, true, true, K3_BPUT(0, 2)); K3_ITER(acc, 1, 2 * P, true, true, false, K3_BGET(0, 2));
    K3_ITER(acc, 0, 2 * P, true, true, false, K3_H2(K3_BADD(0, 2), K3_BPUT(2, 4))); K3_ITER(acc, 1, 2 * P, true, true, false, K3_BGET(2, 4));
    K3_ITER(acc, 0, 2 * P, true, true, false, K3_H2(K3_BADD(2, 4), K3_BPUT(4, 5))); K3_ITER(acc, 1, 2 * P, true, true, false, K3_BGET(4, 5));
    K3_ITER(acc, 0, 2 * P, true, true, false, K3_H2(K3_BADD(4, 5), K3_BOUT(0)));
    K3_ITER(acc, 1, 2 * P + 1, true, true, false, K3_BOUT(1)); K3_ITER(acc, 0, 2 * P + 2, true, true, false, K3_BOUT(2));
    K3_ITER(acc, 1, 2 * P + 2, true, true, false, K3_BOUT(3));
#pragma unroll
    for (int t = 0; t < 2; ++t)
#pragma unroll
        for (int e = 0; e < 4; ++e) {
            ownB[0][t][e] = r == 0 ? resB[t][e] : r == 1 ? resB[2 + t][e] : r == 2 ? resB[4 + t][e] : resB[6 + t][e];
            ownB[1][t][e] = resB[8 + t][e];
        }
    K3_ITER(acc, 0, 2 * P + 2, true, true, false, NOHOOK); K3_ITER(acc, 1, 2 * P + 1, true, true, false, NOHOOK);
#undef K3_H2
    u32x2 xv[2][2];
    unsigned oo[2][2];
#pragma unroll
    for (int sl = 0; sl < 2; ++sl)
#pragma unroll
        for (int t = 0; t < 2; ++t) {
            const unsigned i = 2 * (sl ? 4 : r) + t, a = i >= 5 ? 1 : 0, n = i - 5 * a;
            oo[sl][t] = (16 * (8 * qt + 4 * qh + 2 * GRP + a) + c) * (DM * 2) + (16 * (10 * mt + 5 * mh + n) + 4 * g) * 2;
        }
#define K3_XLD() asm volatile("global_load_dwordx2 %0, %4, %8\n\tglobal_load_dwordx2 %1, %5, %8\n\tglobal_load_dwordx2 %2, %6, %8\n\tglobal_load_dwordx2 %3, %7, %8" \
        : "=&v"(xv[0][0]), "=&v"(xv[0][1]), "=&v"(xv[1][0]), "=&v"(xv[1][1]) : "v"(oo[0][0]), "v"(oo[0][1]), "v"(oo[1][0]), "v"(oo[1][1]), "s"(x) : "memory")
    const unsigned fown = (grp64 * 4 + ks) * 128, fpeer = (grp64 * 4 + ((lane & 3) == (unsigned)ks ? (ks + 1) & 3 : lane & 3)) * 128;
    u32x2 fb;
    K3_ITER(acc, 0, 2 * P, true, false, false, flag_set(wid, flb, FL_B + fown, tag));
    K3_ITER(acc, 1, P, true, false, false, NOHOOK);
#define K3_H14() do { K3_XLD(); flag_ld(fb, flb, FL_B + fpeer); } while (0)
    u32x4 pp[2][6];
#pragma unroll
    for (int i = 0; i < 6; ++i) pp[1][i] = u32x4{};
    const unsigned s1 = ((ks + 1) & 3) * PP_SET, s2 = ((ks + 2) & 3) * PP_SET, s3 = ((ks + 3) & 3) * PP_SET;
    const char* pj0 = pbase + r * 1024;
    const char* pj1 = pbase + 4 * 1024;
#define K3_H15()                                                                                                        \
    do {                                                                                                                \
        PIN(xv[0][0]); PIN(xv[0][1]); PIN(xv[1][0]); PIN(xv[1][1]);                                                     \
        flag_wait<false>(0, fb, flb, FL_B + fpeer, tag);                                                                \
        ld_wt(pp[0][3], pj0 + 4 * PP_SET + s1, vo); ld_wt(pp[0][4], pj0 + 4 * PP_SET + s2, vo); ld_wt(pp[0][5], pj0 + 4 * PP_SET + s3, vo); \
        ld_wt3(r, pp[1][3], pp[1][4], pp[1][5], pj1 + 4 * PP_SET + s1, pj1 + 4 * PP_SET + s2, pj1 + 4 * PP_SET + s3, vo); \
        SBAR();                                                                                                         \
    } while (0)
    K3_ITER(acc, 0, -1, true, false, false, K3_H14()); K3_ITER(acc, 1, 0, false, false, false, K3_H15());
#undef K3_H14
#undef K3_H15
    STAMP(12);
    K3_REDUCE();
    STAMP(17);
    k3_publish(res, ownS, pbase + ks * PP_SET, vo, r);
    u32x2 fe[4];
    const bool bump = blockIdx.x == 0 && wid == 0;
    if (bump) {
#pragma unroll
        for (int i = 0; i < 4; ++i) flag_ld(fe[i], flb, FL_X + (64 * i + lane) * 8);
    }
    const int nown = r == 0 ? 2 : 1;
    STAMP(14);
    asm volatile("s_waitcnt vmcnt(0)\n\ts_barrier" : "+v"(fe[0]), "+v"(fe[1]), "+v"(fe[2]), "+v"(fe[3]), "+v"(pp[0][3]), "+v"(pp[0][4]), "+v"(pp[0][5]),
                 "+v"(pp[1][3]), "+v"(pp[1][4]), "+v"(pp[1][5])::"memory");
    STAMP(15);
    flag_set(wid, flb, FL_S + fown, tag);
    {
        u32x2 f = u32x2{~(unsigned)tag, 0u};
        flag_wait<false>(0, f, flb, FL_S + fpeer, tag);
    }
    STAMP(16);
    ld_wt(pp[0][0], pj0 + s1, vo); ld_wt(pp[0][1], pj0 + s2, vo); ld_wt(pp[0][2], pj0 + s3, vo);
    ld_wt3(r, pp[1][0], pp[1][1], pp[1][2], pj1 + s1, pj1 + s2, pj1 + s3, vo);
    asm volatile("s_waitcnt vmcnt(0)" : "+v"(pp[0][0]), "+v"(pp[0][1]), "+v"(pp[0][2]), "+v"(pp[0][3]), "+v"(pp[0][4]), "+v"(pp[0][5]),
                 "+v"(pp[1][0]), "+v"(pp[1][1]), "+v"(pp[1][2]), "+v"(pp[1][3]), "+v"(pp[1][4]), "+v"(pp[1][5])::"memory");
#pragma unroll
    for (int sl = 0; sl < 2; ++sl)
        if (sl < nown) {
#pragma unroll
            for (int t = 0; t < 2; ++t) {
                f32x4 sv = ownS[sl][t], bv = ownB[sl][t];
#pragma unroll
                for (int i = 0; i < 3; ++i) { sv += t ? unpk_hi(pp[sl][i]) : unpk_lo(pp[sl][i]); bv += t ? unpk_hi(pp[sl][3 + i]) : unpk_lo(pp[sl][3 + i]); }
                const float o0 = bflo(xv[sl][t][0]) + sv[0] * silu_f(bv[0]), o1 = bfhi(xv[sl][t][0]) + sv[1] * silu_f(bv[1]);
                const float o2 = bflo(xv[sl][t][1]) + sv[2] * silu_f(bv[2]), o3 = bfhi(xv[sl][t][1]) + sv[3] * silu_f(bv[3]);
                u32x2 w; w[0] = cvtpk(o0, o1); w[1] = cvtpk(o2, o3);
                *(u32x2*)(out + oo[sl][t]) = w;
            }
        }
    if (bump) {
#pragma unroll
        for (int i = 0; i < 4; ++i) flag_wait<false>(0, fe[i], flb, FL_X + (64 * i + lane) * 8, tag);
        flag_set(0, flb, FL_EPOCH, tag);
    }
    STAMP(13);
#undef K3_RW
#undef K3_RA
#undef K3_ZERO
#undef K3_ITER
#undef K3_FIRST
#undef K3_REDUCE
#undef K3_BPUT
#undef K3_BGET
#undef K3_BADD
#undef K3_BOUT
#undef K3_XLD
#undef NOHOOK
}

__global__ __launch_bounds__(512, 2) void fused(const char* __restrict__ x, const char* __restrict__ ckv, const char* __restrict__ wuk, const char* __restrict__ wuv,
                                              const char* __restrict__ wq, const char* __restrict__ w1, const char* __restrict__ b1, char* __restrict__ ws,
                                              const char* __restrict__ wo, const char* __restrict__ w2, char* __restrict__ out) {
    extern __shared__ __attribute__((aligned(16))) char lds[];
    const unsigned wid = rfl(threadIdx.x >> 6), grp = wid >> 2, w4 = wid & 3;
    char* stamp_base = ws; (void)stamp_base;
    STAMP(0);
    const int b = blockIdx.x, xcd = b & 7, idx = b >> 3;
    const int vt = idx & 1, hp = 2 * xcd + ((idx >> 1) & 1), rb = idx >> 2;
    const int head = 4 * xcd + (idx & 3), pi = idx >> 2;
    const int HB = 15 - pi, LB = pi;
    const int myblk = grp ? LB : HB;
    const int q16_0 = 8 * myblk + 2 * w4;
    const bool nft4 = head < 22;
    const int f0 = nft4 ? 64 * head : 1408 + 32 * (head - 22);
    const char* Abase = ckv + (size_t)rb * 256 * 1024;
    const char* Wbase = (vt ? wuv : wuk) + hp * 512;
    const unsigned lane0 = flane();
#define lane lane0
    const int row = 8 * b + wid;
    const unsigned l40 = lane < 40 ? lane : 39;
    u32x4 x0, x1;
    {
        const char* xr = uptr(x + (size_t)row * 1280);
        const unsigned vo = l40 * 32;
        asm volatile("global_load_dwordx4 %0, %2, %3\n\tglobal_load_dwordx4 %1, %2, %3 offset:16" : "=&v"(x0), "=&v"(x1) : "v"(vo), "s"(xr) : "memory");
    }
    unsigned avo[4], wvo[4];
    {
        const unsigned lds0 = (unsigned)(uintptr_t)lds;
        const char* Aw = Abase + (size_t)wid * 32 * 1024;
        const char* Ww = Wbase + (size_t)wid * 8 * 8192;
        const unsigned m0a = lds0 + wid * 4096, m0w = lds0 + 32768 + wid * 4096;
#pragma unroll
        for (int p = 0; p < 4; ++p) {
            const unsigned rw = 8 * p + (lane >> 3);
            avo[p] = rw * 1024 + (((lane & 7) ^ ((rw >> 1) & 7)) << 4) - 1024 * p;
            const unsigned k = 2 * p + (lane >> 5);
            const unsigned f = (k & 3) | ((wid & 1) << 2);
            wvo[p] = k * 8192 + (((lane & 31) ^ (f << 1)) << 4) - 1024 * p;
        }
#pragma unroll
        for (int kt = 0; kt < 2; ++kt) {
            const char* a_ = Aw + kt * 128; const char* w_ = Ww + (size_t)kt * 64 * 8192; const unsigned stg = kt * 65536;
            dma1<0>(m0a + stg, a_, avo[0]); dma1<1024>(m0a + stg, a_, avo[1]); dma1<2048>(m0a + stg, a_, avo[2]); dma1<3072>(m0a + stg, a_, avo[3]);
            dma1<0>(m0w + stg, w_, wvo[0]); dma1<1024>(m0w + stg, w_, wvo[1]); dma1<2048>(m0w + stg, w_, wvo[2]); dma1<3072>(m0w + stg, w_, wvo[3]);
        }
    }
    u64 tag;
    asm volatile("s_load_dwordx2 %0, %1, 0x0\n\ts_waitcnt lgkmcnt(0)" : "=s"(tag) : "s"(uptr(ws + WS_FL + FL_EPOCH)) : "memory");
    tag += 1;
    {
        asm volatile("s_waitcnt vmcnt(16)" : "+v"(x0), "+v"(x1)::"memory");
        float v[16];
#pragma unroll
        for (int i = 0; i < 4; ++i) { v[2 * i] = bflo(x0[i]); v[2 * i + 1] = bfhi(x0[i]); v[8 + 2 * i] = bflo(x1[i]); v[8 + 2 * i + 1] = bfhi(x1[i]); }
        float s = 0.f;
#pragma unroll
        for (int i = 0; i < 16; ++i) s += v[i];
        if (lane >= 40) s = 0.f;
#pragma unroll
        for (int o = 32; o >= 1; o >>= 1) s += __shfl_xor(s, o);
        const float mean = s * (1.0f / DM);
        float q = 0.f;
#pragma unroll
        for (int i = 0; i < 16; ++i) { v[i] -= mean; q += v[i] * v[i]; }
        if (lane >= 40) q = 0.f;
#pragma unroll
        for (int o = 32; o >= 1; o >>= 1) q += __shfl_xor(q, o);
        const float rstd = 1.0f / sqrtf(q * (1.0f / DM) + 1e-5f);
        u32x4 o0, o1;
#pragma unroll
        for (int i = 0; i < 4; ++i) { o0[i] = cvtpk(v[2 * i] * rstd, v[2 * i + 1] * rstd); o1[i] = cvtpk(v[8 + 2 * i] * rstd, v[8 + 2 * i + 1] * rstd); }
        char* xf = ws + WS_XF + ((size_t)(row >> 4) * NKS_X + (l40 >> 1)) * 1024 + ((row & 15) + 32 * (l40 & 1)) * 16;
        if (lane < 40) { st_wt(xf, o0); st_wt(xf + 256, o1); }
    }
#undef lane
    u32x4 xa[4], xb[4], kv[16];
    u32x2 bb[4];
    KVO kvo;
    if (vt) k1_tile<true>(Abase, Wbase, ws + WS_VF, lds, avo, wvo, wid, hp, rb, ws, wq, w1, b1, tag, head, f0, nft4, q16_0, xa, bb, kv, kvo);
    else    k1_tile<false>(Abase, Wbase, ws + WS_KF, lds, avo, wvo, wid, hp, rb, ws, wq, w1, b1, tag, head, f0, nft4, q16_0, xa, bb, kv, kvo);
    STAMP(6);
    const unsigned lds0 = (unsigned)(uintptr_t)lds;
    const unsigned kslot0 = grp ? 114688 : 81920, kslot1 = kslot0 + 16384, kslot2 = grp ? 16384 : 0;
    const unsigned vslot0 = grp ? 147456 : 32768, vslot1 = grp ? 65536 : 49152;
    const int nL = 2 * pi + 2;
    const int nsw = grp ? nL : 1000;
    const size_t jump = (size_t)(17 - nL) * 16384;
    const char* kptr = uptr(ws + WS_KF + (size_t)head * 524288 + w4 * 4096);
    const char* vptr = uptr(ws + WS_VF + (size_t)head * 524288 + w4 * 4096);
    bf16x8 qf[8];
    {
        bf16x8 q16[2][4];
        const unsigned kvflag = FL_KV + (hp * 16 + vt * 8 + rb) * 8;
        if (nft4) proj_phase<4>(wq, w1, ws, lds, head, f0, q16_0, q16, wo, w2, lds0 + vslot0 + w4 * 4096, wid, xa, xb, bb, tag, kvflag, head >> 1, lds0 + kslot0 + w4 * 4096, lds0 + kslot1 + w4 * 4096, kptr, kptr + 16384, kv, kvo);
        else      proj_phase<2>(wq, w1, ws, lds, head, f0, q16_0, q16, wo, w2, lds0 + vslot0 + w4 * 4096, wid, xa, xb, bb, tag, kvflag, head >> 1, lds0 + kslot0 + w4 * 4096, lds0 + kslot1 + w4 * 4096, kptr, kptr + 16384, kv, kvo);
        kptr += 2 * 16384; if (nsw == 2) kptr += jump;
#pragma unroll
        for (int v = 0; v < 4; ++v) {
            const u32x4 r0 = __builtin_bit_cast(u32x4, q16[0][v]), r1 = __builtin_bit_cast(u32x4, q16[1][v]);
            u32x4 z0, z1;
#pragma unroll
            for (int w = 0; w < 4; ++w) {
                const auto s1 = __builtin_amdgcn_permlane16_swap(r0[w], r1[w], false, false);
                const auto s2 = __builtin_amdgcn_permlane32_swap(s1[0], s1[1], false, false);
                z0[w] = s2[0]; z1[w] = s2[1];
            }
            qf[2 * v] = __builtin_bit_cast(bf16x8, z0); qf[2 * v + 1] = __builtin_bit_cast(bf16x8, z1);
        }
    }
    STAMP(7);
    const unsigned lane = flane();
    if (grp == 0) {
        char* qh = ws + WS_QH + (size_t)b * 32768 + w4 * 8192 + lane * 16;
#pragma unroll
        for (int s = 0; s < 8; ++s) *(bf16x8*)(qh + s * 1024) = qf[s];
    }
    const char* flb = uptr(ws + WS_FL);
    lds_cptr Lb = (lds_cptr)lds + lane * 16;
    const unsigned m0b = lds0 + w4 * 4096;
    const unsigned dvo = lane * 16;
    int q0 = 128 * myblk + 32 * w4;
    int tile = 0;
    asm volatile("s_waitcnt lgkmcnt(0)\n\ts_barrier" ::: "memory");
    dma4(m0b + vslot0, vptr, dvo); vptr += 16384;
    dma4(m0b + kslot2, kptr, dvo); kptr += 16384;
    f32x16 o[4], S[2], negm;
    float mref = 0.f, lsum = 0.f, lfin = 1.f;
    bf16x8 pf[4], fr[FRW];
#pragma unroll
    for (int i = 0; i < 4; ++i) o[i] = f32x16{};
    negm = f32x16{}; S[0] = f32x16{}; S[1] = f32x16{};
    asm volatile("s_nop 1" : "+v"(negm));
    NOP_SO(S, o);
    unsigned ks_cur = kslot0, ks_nxt = kslot1, ks_nn = kslot2;
#define CTX_VO(l) ((((l) >> 4) & 1) * (NKS_CH * 1024) + (((l) & 15) + 16 * ((l) >> 5)) * 16)
#define KOFF(i) (((i) & 1) * 8192 + ((i) >> 1) * 1024)
#define VOFF(j) (((j) >> 3) * 8192 + ((j) & 3) * 2048 + (((j) >> 2) & 1) * 1024)
#define EARLY_K(slot) do { _Pragma("unroll") for (int i = 0; i < FRA; ++i) fr[i] = lds_frag(Lb + (slot) + KOFF(i)); } while (0)
    EARLY_K(kslot0);
    if (grp) asm volatile("s_barrier" ::: "memory");
#define PHASE_M(t, QK, PV, VS)                                                                              \
    do {                                                                                                                              \
        __builtin_amdgcn_s_setprio(0);                                                                                                \
        lds_cptr kp_ = Lb + ks_cur; lds_cptr vp_ = Lb + ((VS) ? vslot1 : vslot0);                                                     \
        if (!(QK)) { _Pragma("unroll") for (int i = 16; i < 16 + FRA; ++i) fr[i % FRW] = lds_frag(vp_ + VOFF(i - 16)); }               \
        _Pragma("unroll") for (int i = (QK) ? 0 : 16; i < ((PV) ? 32 : 16); ++i) {                                                    \
            const int n = i + FRA;                                                                                                    \
            if (n < 16) fr[n % FRW] = lds_frag(kp_ + KOFF(n)); else if ((PV) && n < 32) fr[n % FRW] = lds_frag(vp_ + VOFF(n - 16));    \
            SBAR();                                                                                                                   \
            if (i < 2) mfma32_ini(S[i & 1], fr[i % FRW], qf[i >> 1], negm);                                                        \
            else if (i < 16) mfma32_acc(S[i & 1], fr[i % FRW], qf[i >> 1]);                                                             \
            else mfma32_acc(o[i & 3], fr[i % FRW], pf[(i - 16) >> 2]);                                                                  \
            SBAR();                                                                                                                   \
            if ((QK) && i == 16 && (t) + 1 == nsw) {        \
                const char* qs_ = uptr(ws + WS_QH + (size_t)b * 32768 + w4 * 8192);                                                   \
                asm volatile("s_nop 4\n\tglobal_load_dwordx4 %0, %8, %9\n\tglobal_load_dwordx4 %1, %8, %9 offset:1024\n\tglobal_load_dwordx4 %2, %8, %9 offset:2048\n\tglobal_load_dwordx4 %3, %8, %9 offset:3072" \
                             : "+v"(qf[0]), "+v"(qf[1]), "+v"(qf[2]), "+v"(qf[3]), "+v"(qf[4]), "+v"(qf[5]), "+v"(qf[6]), "+v"(qf[7]) : "v"(dvo), "s"(qs_) : "memory"); \
                asm volatile("global_load_dwordx4 %4, %8, %9\n\tglobal_load_dwordx4 %5, %8, %9 offset:1024\n\tglobal_load_dwordx4 %6, %8, %9 offset:2048\n\tglobal_load_dwordx4 %7, %8, %9 offset:3072" \
                             : "+v"(qf[0]), "+v"(qf[1]), "+v"(qf[2]), "+v"(qf[3]), "+v"(qf[4]), "+v"(qf[5]), "+v"(qf[6]), "+v"(qf[7]) : "v"(dvo), "s"(qs_ + 4096) : "memory"); \
            }                                                                                                                         \
            SBAR();                                                                                                                   \
        }                                                                                                                             \
        __builtin_amdgcn_s_setprio(3);                                                                                                \
        TICK(tA);                                                                                                                     \
        asm volatile("s_barrier" ::: "memory");                                                                                       \
        TICK(tX);                                                                                                                     \
    } while (0)
#define EXP_PACK(DV, DK, VS)         \
    do {                                                                                                                              \
        float e_[32], a_[2];                                                                                                          \
        unsigned c_[16];                                                                                                              \
        _Pragma("unroll") for (int k = 0; k < 18; ++k) {                                                                              \
            if (k < 16) { e_[2 * k] = v_exp(S[k >> 3][2 * (k & 7)]); e_[2 * k + 1] = v_exp(S[k >> 3][2 * (k & 7) + 1]); }             \
            if (k == 1) { a_[0] = e_[0]; a_[1] = e_[1]; }                                                                             \
            if (k >= 2 && k < 17) { a_[0] = v_add(a_[0], e_[2 * k - 2]); a_[1] = v_add(a_[1], e_[2 * k - 1]); }                        \
            if (k >= 2) c_[k - 2] = v_cvt(e_[2 * k - 4], e_[2 * k - 3]);                                                              \
            if (VIL) {                                                   \
                if ((DV) && k == VIL0) set_m0(m0b + ((VS) ? vslot1 : vslot0));                                                        \
                if ((DV) && k == VIL0 + 1) dma_sc1<0>(vptr, dvo);                                                                    \
                if ((DV) && k == VIL0 + 3) dma_sc1<1024>(vptr, dvo);                                                                 \
                if ((DV) && k == VIL0 + 5) dma_sc1<2048>(vptr, dvo);                                                                 \
                if ((DV) && k == VIL0 + 7) dma_sc1<3072>(vptr, dvo);                                                                 \
                if ((DK) && k == VIL0 + 8) set_m0(m0b + ks_cur);                                                                      \
                if ((DK) && k == VIL0 + 9) dma_sc1<0>(kptr, dvo);                                                                    \
                if ((DK) && k == VIL0 + 11) dma_sc1<1024>(kptr, dvo);                                                                \
                if ((DK) && k == VIL0 + 13) dma_sc1<2048>(kptr, dvo);                                                                \
                if ((DK) && k == VIL0 + 15) dma_sc1<3072>(kptr, dvo);                                                                \
            }                                                                                                                         \
        }                                                                                                                             \
        _Pragma("unroll") for (int k = 0; k < 4; ++k) pf[k] = __builtin_bit_cast(bf16x8, u32x4{c_[4 * k], c_[4 * k + 1], c_[4 * k + 2], c_[4 * k + 3]}); \
        ts_ = v_add(a_[0], a_[1]);                                                                                                    \
    } while (0)
#define REBASE(FIRST)                                                          \
    do {                                                                                                                              \
        float rm = S[0][0];                                                                                                           \
        _Pragma("unroll") for (int e = 1; e < 32; ++e) rm = __builtin_fmaxf(rm, S[e >> 4][e & 15]);                                   \
        rm = xh_max(rm);                                                                                                              \
        const float dl = (FIRST) ? (rm == -__builtin_inff() ? 0.f : rm) : __builtin_fmaxf(rm, 0.f);                                   \
        mref += dl;                                                                                                                   \
        if (!(FIRST)) { float f = __builtin_amdgcn_exp2f(-dl); asm volatile("s_nop 1" : "+v"(f)); lsum *= f;               \
            _Pragma("unroll") for (int i = 0; i < 4; ++i) _Pragma("unroll") for (int r = 0; r < 16; ++r) o[i][r] = mul_s(o[i][r], f); } \
        _Pragma("unroll") for (int r = 0; r < 16; ++r) { S[0][r] = sub_s(S[0][r], dl); S[1][r] = sub_s(S[1][r], dl); }                \
        const float nm = -mref;                                                                                                       \
        _Pragma("unroll") for (int r = 0; r < 16; ++r) negm[r] = nm;                                                                  \
    } while (0)
#define PHASE_V(t, WN, LASTV, VS, DK, DV)                                                                                             \
    do {                                                                                                                              \
        const bool swn = ((t) + 1 == nsw), sw0 = ((t) == nsw);                                                                        \
        const bool diag = 64 * tile + 63 > q0 || sw0, first_ = (t) == 0 || sw0;                                                       \
        if (!VIL) { if (DV) dma4(m0b + ((VS) ? vslot1 : vslot0), vptr, dvo); if (DK) dma4(m0b + ks_cur, kptr, dvo); }                 \
        NOP_SO(S, o);                                                                                                                 \
        float ts_;                                                                                                                    \
        if (__builtin_expect(diag || first_, 0)) {                                                                                    \
            if (sw0) {                                                                                \
                float il_ = __builtin_amdgcn_rcpf(xh_sum(lsum));                                                                      \
                asm volatile("s_nop 1" : "+v"(il_));                                                                                  \
                const char* cp_ = uptr(ws + WS_CH + ((size_t)(8 * LB + 2 * w4) * NKS_CH + 4 * head) * 1024);                          \
                const unsigned co_ = CTX_VO(flane());                                                                                 \
                asm volatile("s_nop 4");                                                                                              \
                st_wt_i<0>(cp_, co_, pack_o(o[0], 0, il_)); st_wt_i<512>(cp_, co_, pack_o(o[0], 1, il_));                             \
                st_wt_i<1024>(cp_, co_, pack_o(o[1], 0, il_)); st_wt_i<1536>(cp_, co_, pack_o(o[1], 1, il_));                         \
                st_wt_i<2048>(cp_, co_, pack_o(o[2], 0, il_)); st_wt_i<2560>(cp_, co_, pack_o(o[2], 1, il_));                         \
                st_wt_i<3072>(cp_, co_, pack_o(o[3], 0, il_)); st_wt_i<3584>(cp_, co_, pack_o(o[3], 1, il_));                         \
                _Pragma("unroll") for (int i = 0; i < 4; ++i) o[i] = f32x16{};                                                        \
                lsum = 0.f; mref = 0.f; q0 = 128 * HB + 32 * w4;                                                                      \
            }                                                                                                                         \
            if (64 * tile + 63 > q0) {         \
                const int dq = (int)(lane & 31) - 4 * (int)(lane >> 5);                                                               \
                _Pragma("unroll") for (int u = 0; u < 2; ++u) {                                                                       \
                    const int kb = 64 * tile + 32 * u;                                                                                \
                    if (kb > q0) { _Pragma("unroll") for (int r = 0; r < 16; ++r) S[u][r] = -__builtin_inff(); }                       \
                    else if (kb == q0) mask_diag(S[u], dq);                                                                           \
                }                                                                                                                     \
            }                                                                                                                         \
            if (first_) REBASE(true);                                                                                                 \
        }                                                                                                                             \
        EXP_PACK(DV, DK, VS);                                                                                                         \
        if (DV) vptr += 16384 + ((t) + 2 == nsw ? jump : 0);                                                                          \
        if (DK) kptr += 16384 + ((t) + 4 == nsw ? jump : 0);                                                                          \
        if (__builtin_expect(__any(ts_ > PLIM), 0)) { REBASE(false); EXP_PACK(false, false, 0); }     \
        lsum += ts_;                                                                                                                  \
        if (!(LASTV)) EARLY_K(ks_nxt);                                                                                                \
        if (__builtin_expect(swn, 0)) {                                                                                               \
            negm = f32x16{};                                                                                                          \
            asm volatile("s_waitcnt vmcnt(%9)" : "+v"(qf[0]), "+v"(qf[1]), "+v"(qf[2]), "+v"(qf[3]), "+v"(qf[4]), "+v"(qf[5]), "+v"(qf[6]), "+v"(qf[7]), "+v"(negm) : "i"(WN) : "memory"); \
            tile += 17 - nL;                                                                                                          \
        }                                                                                                                             \
        TICK(tB);                                                                                                                     \
        asm volatile("s_cmp_eq_u32 %7, 0\n\ts_cbranch_scc1 .Lv1%=\n\ts_waitcnt vmcnt(%5)\n\ts_branch .Lv2%=\n.Lv1%=:\n\ts_waitcnt vmcnt(%6)\n.Lv2%=:\n\ts_barrier"                \
                     : "+v"(pf[0]), "+v"(pf[1]), "+v"(pf[2]), "+v"(pf[3]), "+v"(negm) : "i"(WN), "i"((WN) + 8), "s"(nsw - (t)) : "scc", "memory");   \
        __builtin_amdgcn_s_waitcnt(0xC07F);        \
        TICK(tO);                                                                                                                     \
        { const unsigned r_ = ks_cur; ks_cur = ks_nxt; ks_nxt = ks_nn; ks_nn = r_; }                                                  \
        tile += 1;                                                                                                                    \
    } while (0)
    constexpr float PLIM = 256.0f;
    PHASE_M(0, true, false, 1);
    PHASE_V(0, 8, false, 1, true, true);
    flag_set(w4, flb, FL_H + (myblk * 32 + head) * 8, tag);
    for (int t = 1; t < 13; t += 2) {
        PHASE_M(t, true, true, 0); PHASE_V(t, 8, false, 0, true, true);
        PHASE_M(t + 1, true, true, 1); PHASE_V(t + 1, 8, false, 1, true, true);
    }
    PHASE_M(13, true, true, 0); PHASE_V(13, 8, false, 0, true, true);
    PHASE_M(14, true, true, 1); PHASE_V(14, 4, false, 1, false, true);
    PHASE_M(15, true, true, 0);
    const int ks = xcd >> 1, qt = 8 * (xcd & 1) + (idx >> 2), mt = idx & 3;
    u32x2 fh;
    const unsigned fho = FL_H + (qt * 32 + (lane & 31)) * 8;
    flag_ld(fh, flb, fho);
    PHASE_V(15, 4, false, 0, false, true);
    PIN(fh);
    PHASE_M(16, true, true, 1); PHASE_V(16, 0, true, 1, false, false);
    PHASE_M(17, false, true, 0);
    flag_wait<false>(0, fh, flb, fho, tag);
    if (!grp) asm volatile("s_barrier" ::: "memory");
    NOP_SO(S, o);
    K3S k3 = k3_setup(lds0, wid, lane);
    k3_point(k3, ws, w2, wid, qt, mt, NKS_C + 14 * ks, 7 * ks);
    k3_stage(k3, 0, grp == 0); k3_stage(k3, K3_SLOT, grp == 0);
    STAMP(8);
    {
        const float ls = xh_sum(lsum);
        const float mm = ls > 0.f ? mref : -__builtin_inff();
        lds_ptr X = (lds_ptr)lds + 81920 + wid * 8192 + lane * 16;
        f32x16 keep[2];
        if (grp) {
#pragma unroll
            for (int i = 0; i < 2; ++i) {
                keep[i] = o[2 + i];
#pragma unroll
                for (int y = 0; y < 4; ++y) *(__attribute__((address_space(3))) f32x4*)(X + (4 * i + y) * 1024) = f32x4{o[i][4 * y], o[i][4 * y + 1], o[i][4 * y + 2], o[i][4 * y + 3]};
            }
        } else {
#pragma unroll
            for (int i = 0; i < 2; ++i) {
                keep[i] = o[i];
#pragma unroll
                for (int y = 0; y < 4; ++y) *(__attribute__((address_space(3))) f32x4*)(X + (4 * i + y) * 1024) = f32x4{o[2 + i][4 * y], o[2 + i][4 * y + 1], o[2 + i][4 * y + 2], o[2 + i][4 * y + 3]};
            }
        }
        lds_ptr Y = (lds_ptr)lds + 73728 + wid * 512 + lane * 8;
        *(__attribute__((address_space(3))) f32x2*)Y = f32x2{mm, ls};
        asm volatile("s_waitcnt lgkmcnt(0)\n\ts_barrier" ::: "memory");
        const unsigned ow = wid ^ 4;
        lds_cptr Xo = (lds_cptr)lds + 81920 + ow * 8192 + lane * 16;
        const f32x2 oml = *(const __attribute__((address_space(3))) f32x2*)((lds_cptr)lds + 73728 + ow * 512 + lane * 8);
        const float mx = __builtin_fmaxf(mm, oml[0]);
        const float e0 = __builtin_amdgcn_exp2f(mm - mx), e1 = __builtin_amdgcn_exp2f(oml[0] - mx);
        const float inv = __builtin_amdgcn_rcpf(ls * e0 + oml[1] * e1);
        const float fs = e0 * inv, fo = e1 * inv;
        const char* cp = uptr(ws + WS_CH + ((size_t)(8 * HB + 2 * w4) * NKS_CH + 4 * head + 2 * grp) * 1024);
        const unsigned co = CTX_VO(lane);
#pragma unroll
        for (int i = 0; i < 2; ++i) {
            f32x16 m;
#pragma unroll
            for (int y = 0; y < 4; ++y) {
                const f32x4 pv = *(const __attribute__((address_space(3))) f32x4*)(Xo + (4 * i + y) * 1024);
#pragma unroll
                for (int r = 0; r < 4; ++r) m[4 * y + r] = keep[i][4 * y + r] * fs + pv[r] * fo;
            }
            if (i == 0) { st_wt_i<0>(cp, co, pack_o(m, 0, 1.f)); st_wt_i<512>(cp, co, pack_o(m, 1, 1.f)); }
            else { st_wt_i<1024>(cp, co, pack_o(m, 0, 1.f)); st_wt_i<1536>(cp, co, pack_o(m, 1, 1.f)); }
        }
    }
    STAMP(9);
#undef KOFF
#undef CTX_VO
#undef VOFF
#undef EARLY_K
#undef PHASE_M
#undef PHASE_V
#undef EXP_PACK
#undef REBASE
    if (grp == 0) k3_body<0>(k3, x, wo, ws, out, lds, wid, tag, ks, qt, mt, FL_C + (pi * 32 + head) * 8);
    else k3_body<1>(k3, x, wo, ws, out, lds, wid, tag, ks, qt, mt, FL_C + (pi * 32 + head) * 8);
}

extern "C" void kernel_launch(void* const* d_in, const int* in_sizes, int n_in, void* d_out, int out_size, void* d_ws, size_t ws_size,
                              hipStream_t stream) {
    const char* x = (const char*)d_in[0];
    const char* ckv = (const char*)d_in[1];
    const char* wuk = (const char*)d_in[3];
    const char* wuv = (const char*)d_in[4];
    char* ws = (char*)d_ws;
    (void)hipFuncSetAttribute((const void*)fused, hipFuncAttributeMaxDynamicSharedMemorySize, 163840);
    fused<<<256, 512, 163840, stream>>>(x, ckv, wuk, wuv, (const char*)d_in[2], (const char*)d_in[7], (const char*)d_in[8], ws, (const char*)d_in[5], (const char*)d_in[9], (char*)d_out);
}
```

```cpp
#include <hip/hip_runtime.h>
#include <hip/hip_bf16.h>
#include <stdint.h>

typedef __bf16 bf16x8 __attribute__((ext_vector_type(8)));
typedef __bf16 bf16x2 __attribute__((ext_vector_type(2)));
typedef short s16x4 __attribute__((ext_vector_type(4)));
typedef short s16x8 __attribute__((ext_vector_type(8)));
typedef short v4i16_t __attribute__((ext_vector_type(4)));
typedef float f32x16 __attribute__((ext_vector_type(16)));
typedef float f32x4 __attribute__((ext_vector_type(4)));
typedef float f32x2 __attribute__((ext_vector_type(2)));
typedef unsigned u32x4 __attribute__((ext_vector_type(4)));
typedef unsigned u32x2 __attribute__((ext_vector_type(2)));
typedef __attribute__((address_space(3))) const char* lds_cptr;
typedef __attribute__((address_space(3))) char* lds_ptr;

#define DI __device__ __forceinline__
#define SBAR() __builtin_amdgcn_sched_barrier(0)
#define MFMA(a, b, c) __builtin_amdgcn_mfma_f32_16x16x32_bf16((a), (b), (c), 0, 0, 0)
#define PIN(x) asm volatile("" : "+v"(x))
#define PINR(x) asm volatile("" ::"v"(x))

constexpr int S_ = 2048, NH = 32, HD = 128, DM = 640, DC = 512, FF = 1728;
constexpr int NKS_X = DM / 32;
constexpr int NKS_C = (NH * HD) / 32;
constexpr int NKS_H = FF / 32;
constexpr int NKS_CH = NKS_C + NKS_H;
constexpr size_t WS_XF = 0;
constexpr size_t WS_KF = WS_XF + (size_t)128 * NKS_X * 1024;
constexpr size_t WS_VF = WS_KF + (size_t)NH * 128 * 4 * 1024;
constexpr size_t WS_CH = WS_VF + (size_t)NH * 64 * 8 * 1024;
constexpr size_t WS_QH = WS_CH + (size_t)128 * NKS_CH * 1024;
constexpr size_t WS_PP = WS_QH + (size_t)256 * 32768;
constexpr size_t PP_BYTES = (size_t)64 * 8 * 8 * 5 * 1024;
constexpr size_t WS_FL = WS_PP + PP_BYTES;
constexpr size_t FL_EPOCH = 0;
constexpr size_t FL_X = 256;
constexpr size_t FL_KV = FL_X + 2048;
constexpr size_t FL_H = FL_KV + 2048;
constexpr size_t FL_C = FL_H + 4096;
constexpr size_t FL_S = 16384;
constexpr size_t FL_B = FL_S + 32768;
constexpr size_t WS_END = WS_FL + FL_B + 32768;

DI unsigned rfl(unsigned x) { return __builtin_amdgcn_readfirstlane(x); }
DI unsigned flane() { unsigned r; asm volatile("v_mbcnt_lo_u32_b32 %0, -1, 0\n\tv_mbcnt_hi_u32_b32 %0, -1, %0" : "=v"(r)); return r; }
#define STAMP(i)
#define TICK(acc)
#define PJSTAMP(i)
DI const char* uptr(const char* p) { const unsigned long long v = (unsigned long long)p; const unsigned lo = rfl((unsigned)v), hi = rfl((unsigned)(v >> 32)); return (const char*)(((unsigned long long)hi << 32) | lo); }
DI unsigned cvtpk(float lo, float hi) { f32x2 v = {lo, hi}; bf16x2 b = __builtin_convertvector(v, bf16x2); return __builtin_bit_cast(unsigned, b); }
DI bf16x8 lds_frag(lds_cptr p) { return *(const __attribute__((address_space(3))) bf16x8*)p; }
DI s16x4 lds_tr(lds_cptr p) { return __builtin_bit_cast(s16x4, __builtin_amdgcn_ds_read_tr16_b64_v4i16((__attribute__((address_space(3))) v4i16_t*)p)); }
DI bf16x8 tr_frag(lds_cptr p0, lds_cptr p1) { s16x4 lo = lds_tr(p0), hi = lds_tr(p1); s16x8 v = __builtin_shufflevector(lo, hi, 0, 1, 2, 3, 4, 5, 6, 7); return __builtin_bit_cast(bf16x8, v); }
template <int OFF> DI void dma1(unsigned m0v, const void* sbase, unsigned voff) {
    asm volatile("s_mov_b32 m0, %0\n\ts_nop 0\n\tglobal_load_lds_dwordx4 %1, %2 offset:%3" ::"s"(m0v), "v"(voff), "s"(sbase), "i"(OFF) : "memory");
}
DI u32x4 pack44(const f32x4& a, const f32x4& b) { u32x4 w; w[0] = cvtpk(a[0], a[1]); w[1] = cvtpk(a[2], a[3]); w[2] = cvtpk(b[0], b[1]); w[3] = cvtpk(b[2], b[3]); return w; }
DI u32x4 pack44s(const f32x4& a, const f32x4& b, float sc) { u32x4 w; w[0] = cvtpk(a[0] * sc, a[1] * sc); w[1] = cvtpk(a[2] * sc, a[3] * sc); w[2] = cvtpk(b[0] * sc, b[1] * sc); w[3] = cvtpk(b[2] * sc, b[3] * sc); return w; }
DI u32x4 pack8(const f32x16& a, int b, float sc) {
    u32x4 w; w[0] = cvtpk(a[b + 0] * sc, a[b + 1] * sc); w[1] = cvtpk(a[b + 2] * sc, a[b + 3] * sc);
    w[2] = cvtpk(a[b + 4] * sc, a[b + 5] * sc); w[3] = cvtpk(a[b + 6] * sc, a[b + 7] * sc); return w;
}
DI u32x4 pack8n(const f32x16& a, int b) {
    u32x4 w; w[0] = cvtpk(a[b + 0], a[b + 1]); w[1] = cvtpk(a[b + 2], a[b + 3]); w[2] = cvtpk(a[b + 4], a[b + 5]); w[3] = cvtpk(a[b + 6], a[b + 7]); return w;
}
DI float bf2f(unsigned short u) { return __uint_as_float((unsigned)u << 16); }
DI float bflo(unsigned w) { return __uint_as_float(w << 16); }
DI float bfhi(unsigned w) { return __uint_as_float(w & 0xffff0000u); }
DI float xhalf_sum(float v) { auto r = __builtin_amdgcn_permlane32_swap(__float_as_uint(v), __float_as_uint(v), false, false); return __uint_as_float(r[0]) + __uint_as_float(r[1]); }
DI float xhalf_max(float v) { auto r = __builtin_amdgcn_permlane32_swap(__float_as_uint(v), __float_as_uint(v), false, false); return __builtin_fmaxf(__uint_as_float(r[0]), __uint_as_float(r[1])); }

DI void warm_load(unsigned& d, const char* wa, const char* wb, unsigned id) {
    const unsigned j = id - 1280u;
    const char* p = uptr(id < 1280u ? wa + (size_t)id * 4096 : wb + (size_t)(j < 539u ? j : 539u) * 4096);
    const unsigned voff = flane() * 64;
    asm volatile("s_nop 4\n\tglobal_load_dword %0, %1, %2" : "=&v"(d) : "v"(voff), "s"(p) : "memory");
}
DI void warm_lds(unsigned m0v, const char* wa, const char* wb, unsigned id) {
    const unsigned j = id - 1280u;
    const char* p = uptr(id < 1280u ? wa + (size_t)id * 4096 : wb + (size_t)(j < 539u ? j : 539u) * 4096);
    const unsigned voff = flane() * 64;
    asm volatile("s_mov_b32 m0, %0\n\ts_nop 4\n\tglobal_load_lds_dword %1, %2" ::"s"(m0v), "v"(voff), "s"(p) : "memory");
}

DI void mfma_acc(f32x4& acc, const bf16x8& a, const bf16x8& b) { asm volatile("v_mfma_f32_16x16x32_bf16 %0, %1, %2, %0" : "+v"(acc) : "v"(a), "v"(b)); }
DI void mfma_acc_s(f32x4& acc, const bf16x8& a, const bf16x8& b) { asm volatile("s_nop 1\n\tv_mfma_f32_16x16x32_bf16 %0, %1, %2, %0" : "+v"(acc) : "v"(a), "v"(b)); }
DI void mfma32_acc(f32x16& acc, const bf16x8& a, const bf16x8& b) { asm volatile("v_mfma_f32_32x32x16_bf16 %0, %1, %2, %0" : "+v"(acc) : "v"(a), "v"(b)); }
DI void mfma32_ini(f32x16& d, const bf16x8& a, const bf16x8& b, const f32x16& c) { asm volatile("v_mfma_f32_32x32x16_bf16 %0, %1, %2, %3" : "=&v"(d) : "v"(a), "v"(b), "v"(c)); }
DI float v_exp(float x) { float r; asm volatile("v_exp_f32 %0, %1" : "=v"(r) : "v"(x)); return r; }
DI float v_add(float a, float b) { float r; asm volatile("v_add_f32 %0, %1, %2" : "=v"(r) : "v"(a), "v"(b)); return r; }
DI unsigned v_cvt(float lo, float hi) { unsigned r; asm volatile("v_cvt_pk_bf16_f32 %0, %1, %2" : "=v"(r) : "v"(lo), "v"(hi)); return r; }
DI void set_m0(unsigned v) { asm volatile("s_mov_b32 m0, %0" ::"s"(v) : "memory"); }
template <int OFF> DI void dma_nom0(const void* sbase, unsigned voff) { asm volatile("global_load_lds_dwordx4 %0, %1 offset:%2" ::"v"(voff), "s"(sbase), "i"(OFF) : "memory"); }
template <int OFF> DI void dma_sc1(const void* sbase, unsigned voff) { asm volatile("global_load_lds_dwordx4 %0, %1 offset:%2 sc1" ::"v"(voff), "s"(sbase), "i"(OFF) : "memory"); }
template <int OFF> DI void dma1_sc1(unsigned m0v, const void* sbase, unsigned voff) {
    asm volatile("s_mov_b32 m0, %0\n\ts_nop 0\n\tglobal_load_lds_dwordx4 %1, %2 offset:%3 sc1" ::"s"(m0v), "v"(voff), "s"(sbase), "i"(OFF) : "memory");
}
DI void dma4(unsigned m0v, const void* sbase, unsigned voff) {
    asm volatile("s_mov_b32 m0, %0\n\ts_nop 0\n\tglobal_load_lds_dwordx4 %1, %2 sc1\n\tglobal_load_lds_dwordx4 %1, %2 offset:1024 sc1\n\tglobal_load_lds_dwordx4 %1, %2 offset:2048 sc1\n\tglobal_load_lds_dwordx4 %1, %2 offset:3072 sc1"
                 ::"s"(m0v), "v"(voff), "s"(sbase) : "memory");
}

typedef unsigned long long u64;
DI void st_wt(char* p, const u32x4& v) { asm volatile("global_store_dwordx4 %0, %1, off sc1\n\ts_nop 1" ::"v"(p), "v"(v) : "memory"); }
DI void st_wt_o(const char* sbase, unsigned voff, const u32x4& v) { asm volatile("global_store_dwordx4 %0, %1, %2 sc1\n\ts_nop 1" ::"v"(voff), "v"(v), "s"(sbase) : "memory"); }
template <int OFF> DI void st_wt_i(const char* sbase, unsigned voff, const u32x4& v) {
    asm volatile("global_store_dwordx4 %0, %1, %2 offset:%3 sc1\n\ts_nop 1" ::"v"(voff), "v"(v), "s"(sbase), "i"(OFF) : "memory");
}
DI void ld_wt(u32x4& d, const char* sbase, unsigned voff) { asm volatile("global_load_dwordx4 %0, %1, %2 sc1" : "=&v"(d) : "v"(voff), "s"(sbase) : "memory"); }
DI void ld_wt3(unsigned skip, u32x4& a, u32x4& b, u32x4& c, const char* p0, const char* p1, const char* p2, unsigned voff) {
    asm volatile("s_cmp_lg_u32 %3, 0\n\ts_cbranch_scc1 .Lw%=\n\tglobal_load_dwordx4 %0, %4, %5 sc1\n\tglobal_load_dwordx4 %1, %4, %6 sc1\n\tglobal_load_dwordx4 %2, %4, %7 sc1\n.Lw%=:"
                 : "+v"(a), "+v"(b), "+v"(c) : "s"(skip), "v"(voff), "s"(p0), "s"(p1), "s"(p2) : "scc", "memory");
}
DI void flag_set(unsigned wid, const char* sbase, unsigned off, u64 tag) {
    const u32x2 v = u32x2{(unsigned)tag, (unsigned)(tag >> 32)};
    u64 ex;
    asm volatile("s_cmp_lg_u32 %1, 0\n\ts_cbranch_scc1 .Lfs%=\n\ts_mov_b64 %0, exec\n\ts_mov_b64 exec, 1\n\t"
                 "global_store_dwordx2 %2, %3, %4 sc1\n\ts_mov_b64 exec, %0\n.Lfs%=:\n\ts_nop 1"
                 : "=&s"(ex) : "s"(wid), "v"(off), "v"(v), "s"(sbase) : "scc", "memory");
}
DI void flag_ld(u32x2& f, const char* sbase, unsigned voff) { asm volatile("global_load_dwordx2 %0, %1, %2 sc1" : "=&v"(f) : "v"(voff), "s"(sbase) : "memory"); }
DI void flag_ld0(unsigned skip, u32x2& f, const char* sbase, unsigned voff) {
    asm volatile("s_cmp_lg_u32 %1, 0\n\ts_cbranch_scc1 .Lfz%=\n\tglobal_load_dwordx2 %0, %2, %3 sc1\n.Lfz%=:" : "+v"(f) : "s"(skip), "v"(voff), "s"(sbase) : "scc", "memory");
}
template <bool FLY>
DI void flag_wait(unsigned skip, u32x2& f, const char* sbase, unsigned voff, u64 tag) {
    unsigned cnt;
    asm volatile("s_cmp_lg_u32 %2, 0\n\ts_cbranch_scc1 .Lfe%=\n\t.if %6\n\ts_waitcnt vmcnt(0)\n\t.endif\n\t"
                 "v_cmp_ne_u64 vcc, %3, %0\n\ts_cbranch_vccz .Lfd%=\n\ts_mov_b32 %1, 0x40000\n"
                 ".Lfl%=:\n\tglobal_load_dwordx2 %0, %4, %5 sc1\n\ts_waitcnt vmcnt(0)\n\tv_cmp_ne_u64 vcc, %3, %0\n\ts_cbranch_vccz .Lfd%=\n\t"
                 "s_sleep 2\n\ts_sub_u32 %1, %1, 1\n\ts_cbranch_scc0 .Lfl%=\n"
                 ".Lfd%=:\n"
                 ".Lfe%=:"
                 : "+v"(f), "=&s"(cnt) : "s"(skip), "s"(tag), "v"(voff), "s"(sbase), "i"(FLY ? 1 : 0) : "vcc", "scc", "memory");
}

constexpr int WQ_PIECE = 1040, W1_PIECE = 1056, W1_OFF = 16 * WQ_PIECE, PSLOT = W1_OFF + 8 * W1_PIECE;
struct PJ { unsigned wqvo, w1vo, xv0, xv1; const char *wqs, *w1s, *xs; };
DI PJ pj_setup(const char* wq, const char* w1, const char* ws, unsigned wid, int head, int f0, bool nft4, int q16_0) {
    const unsigned lane = flane();
    PJ j;
    j.wqvo = ((lane >> 2) & 3) * 8192 + (lane >> 4) * 64 + (lane & 3) * 16;
    j.w1vo = ((lane >> 2) & 7) * 3456 + (nft4 ? (lane >> 5) * 64 : 0) + (lane & 3) * 16;
    j.wqs = uptr(wq + (size_t)wid * 8 * 8192 + head * 256);
    j.w1s = uptr(w1 + (size_t)wid * 8 * 3456 + f0 * 2);
    j.xs = uptr(ws + WS_XF + (size_t)q16_0 * NKS_X * 1024);
    j.xv0 = lane * 16; j.xv1 = lane * 16 + NKS_X * 1024;
    return j;
}
DI void pj_dma(const PJ& j, unsigned lds0, unsigned wid, int c, int slot) {
    dma1<0>(lds0 + slot + (2 * wid) * WQ_PIECE, j.wqs + (size_t)c * 64 * 8192, j.wqvo);
    dma1<0>(lds0 + slot + (2 * wid + 1) * WQ_PIECE, j.wqs + (size_t)c * 64 * 8192 + 4 * 8192, j.wqvo);
    dma1<0>(lds0 + slot + W1_OFF + wid * W1_PIECE, j.w1s + (size_t)c * 64 * 3456, j.w1vo);
}
DI void pj_dma1(const PJ& j, unsigned lds0, unsigned wid, int c, int slot, int i) {
    if (i < 2) dma1<0>(lds0 + slot + (2 * wid + i) * WQ_PIECE, j.wqs + (size_t)c * 64 * 8192 + i * 4 * 8192, j.wqvo);
    else dma1<0>(lds0 + slot + W1_OFF + wid * W1_PIECE, j.w1s + (size_t)c * 64 * 3456, j.w1vo);
}
DI void pj_xload1(const PJ& j, u32x4& X, int c, int i) {
    const char* p_ = j.xs + c * 2048 + (i >> 1) * 1024;
    asm volatile("global_load_dwordx4 %0, %1, %2 sc1" : "=&v"(X) : "v"((i & 1) ? j.xv1 : j.xv0), "s"(p_) : "memory");
}
struct KVO { const char* base; unsigned lo[2]; };
DI void kv_store(const KVO& o, const u32x4& v, int i) {
    if (i & 2) st_wt_i<2048>(o.base + (i >> 2) * 8192, o.lo[i & 1], v); else st_wt_i<0>(o.base + (i >> 2) * 8192, o.lo[i & 1], v);
}
DI void pj_xload(const PJ& j, u32x4 (&X)[4], int c) {
    const char* p_ = j.xs + c * 2048;
    asm volatile("global_load_dwordx4 %0, %4, %6 sc1\n\tglobal_load_dwordx4 %1, %5, %6 sc1\n\tglobal_load_dwordx4 %2, %4, %6 offset:1024 sc1\n\tglobal_load_dwordx4 %3, %5, %6 offset:1024 sc1"
                 : "=&v"(X[0]), "=&v"(X[1]), "=&v"(X[2]), "=&v"(X[3]) : "v"(j.xv0), "v"(j.xv1), "s"(p_) : "memory");
}

template <bool VT, class F>
DI void k1_mma(f32x4 (&acc)[8][4], int half, const bf16x8 (&c)[4], const bf16x8 (&w)[4], F&& filler) {
#pragma unroll
    for (int rt = 0; rt < 4; ++rt)
#pragma unroll
        for (int nt = 0; nt < 4; ++nt) {
            if (half == 0) acc[rt][nt] = VT ? MFMA(c[rt], w[nt], acc[rt][nt]) : MFMA(w[nt], c[rt], acc[rt][nt]);
            else acc[4 + rt][nt] = VT ? MFMA(c[rt], w[nt], acc[4 + rt][nt]) : MFMA(w[nt], c[rt], acc[4 + rt][nt]);
            filler(rt * 4 + nt);
        }
}

template <bool VT>
DI void k1_tile(const char* __restrict__ Abase, const char* __restrict__ Wbase, char* __restrict__ outp, char* lds, const unsigned (&avo)[4], const unsigned (&wvo)[4],
                unsigned wid, int hp, int rb, char* __restrict__ ws, const char* __restrict__ wq, const char* __restrict__ w1, const char* __restrict__ b1, u64 tag,
                int head2, int f0, bool nft4, int q16_0, u32x4 (&xa)[4], u32x2 (&bb)[4], u32x4 (&kv)[16], KVO& kvo) {
    char* stamp_base = ws; (void)stamp_base;
    const char* flb = uptr(ws + WS_FL);
    const unsigned wm = wid >> 2, wn = wid & 3;
    const unsigned lds0 = (unsigned)(uintptr_t)lds;
    unsigned aaddr[2], baddr[4];
    {
        const unsigned lane = flane(), c = lane & 15, g = lane >> 4;
        const unsigned x = (c >> 1) & 7;
#pragma unroll
        for (int s = 0; s < 2; ++s) aaddr[s] = (128 * wm + c) * 128 + (((4 * s + g) ^ x) << 4);
        const unsigned q = (lane & 15) >> 2, p = lane & 3, f = q | ((g & 1) << 2);
#pragma unroll
        for (int nt = 0; nt < 4; ++nt) baddr[nt] = 32768 + (8 * g + q) * 512 + (((8 * wn + 2 * nt + (p >> 1)) ^ (f << 1)) << 4) + ((p & 1) << 3);
    }
    f32x4 acc[8][4];
#pragma unroll
    for (int i = 0; i < 8; ++i)
#pragma unroll
        for (int j = 0; j < 4; ++j) acc[i][j] = f32x4{};
    bf16x8 cf[2][4], wf[2][4];
    lds_cptr L = (lds_cptr)lds;
#define K1_RC(buf, stg, s, half) do { _Pragma("unroll") for (int rt = 0; rt < 4; ++rt) cf[buf][rt] = lds_frag(L + (stg) + aaddr[s] + ((half) * 4 + rt) * 2048); } while (0)
#define K1_RW(buf, stg, s) do { _Pragma("unroll") for (int nt = 0; nt < 4; ++nt) wf[buf][nt] = tr_frag(L + (stg) + baddr[nt] + (s) * 16384, L + (stg) + baddr[nt] + (s) * 16384 + 2048); } while (0)
    const char* Aw = Abase + (size_t)wid * 32 * 1024;
    const char* Ww = Wbase + (size_t)wid * 8 * 8192;
    const unsigned m0a = lds0 + wid * 4096, m0w = lds0 + 32768 + wid * 4096;
#define K1_DMA(kt, stg)                                                                   \
    do {                                                                                  \
        const char* a_ = Aw + (kt) * 128; const char* w_ = Ww + (size_t)(kt) * 64 * 8192; \
        dma1<0>(m0a + (stg), a_, avo[0]); dma1<1024>(m0a + (stg), a_, avo[1]); dma1<2048>(m0a + (stg), a_, avo[2]); dma1<3072>(m0a + (stg), a_, avo[3]); \
        dma1<0>(m0w + (stg), w_, wvo[0]); dma1<1024>(m0w + (stg), w_, wvo[1]); dma1<2048>(m0w + (stg), w_, wvo[2]); dma1<3072>(m0w + (stg), w_, wvo[3]); \
    } while (0)
    unsigned pfd = 0;
    u32x2 fx = u32x2{};
#define K1_FXO() (FL_X + (2 * q16_0 + (flane() & 3)) * 8)
    STAMP(1);
    asm volatile("s_waitcnt vmcnt(10)\n\ts_barrier" ::: "memory");
    STAMP(2);
    K1_RW(0, 0, 0); K1_RC(0, 0, 0, 0);
#define K1_RC1(buf, stg, s, half, rt) cf[buf][rt] = lds_frag(L + (stg) + aaddr[s] + ((half) * 4 + (rt)) * 2048)
#define K1_RW1(buf, stg, s, nt) wf[buf][nt] = tr_frag(L + (stg) + baddr[nt] + (s) * 16384, L + (stg) + baddr[nt] + (s) * 16384 + 2048)
#pragma unroll
    for (int kt = 0; kt < 8; ++kt) {
        const unsigned stg = (kt & 1) * 65536, nstg = stg ^ 65536;
        SBAR();
        k1_mma<VT>(acc, 0, cf[0], wf[0], [&](int m) { if (m < 8 && (m & 1) == 0) { K1_RC1(1, stg, 0, 1, m >> 1); SBAR(); } });
        SBAR();
        k1_mma<VT>(acc, 1, cf[1], wf[0], [&](int m) { if (m < 4) { K1_RC1(0, stg, 1, 0, m); SBAR(); } else if (m < 12 && (m & 1) == 0) { K1_RW1(1, stg, 1, (m - 4) >> 1); SBAR(); } });
        SBAR();
        k1_mma<VT>(acc, 0, cf[0], wf[1], [&](int m) { if (m < 8 && (m & 1) == 0) { K1_RC1(1, stg, 1, 1, m >> 1); SBAR(); } });
        SBAR();
        if (kt == 7) asm volatile("s_waitcnt vmcnt(0) lgkmcnt(0)\n\ts_barrier" : "+v"(pfd)::"memory");
        else asm volatile("s_waitcnt vmcnt(0) lgkmcnt(0)\n\ts_barrier" : "+v"(fx)::"memory");
        if (kt == 0) flag_set(wid, flb, FL_X + blockIdx.x * 8, tag);
        if (kt == 6) flag_wait<false>(0, fx, flb, K1_FXO(), tag);
        const char* a_ = Aw + (kt + 2) * 128; const char* w_ = Ww + (size_t)(kt + 2) * 64 * 8192;
        k1_mma<VT>(acc, 1, cf[1], wf[1], [&](int m) {
            if (kt + 1 < 8) { if (m < 4) { K1_RW1(0, nstg, 0, m); SBAR(); } else if (m < 8) { K1_RC1(0, nstg, 0, 0, m - 4); SBAR(); } }
            if (kt + 2 < 8) {
                if (m == 1) { dma1<0>(m0a + stg, a_, avo[0]); SBAR(); } if (m == 3) { dma1<1024>(m0a + stg, a_, avo[1]); SBAR(); }
                if (m == 5) { dma1<2048>(m0a + stg, a_, avo[2]); SBAR(); } if (m == 7) { dma1<3072>(m0a + stg, a_, avo[3]); SBAR(); }
                if (m == 9) { dma1<0>(m0w + stg, w_, wvo[0]); SBAR(); } if (m == 11) { dma1<1024>(m0w + stg, w_, wvo[1]); SBAR(); }
                if (m == 13) { dma1<2048>(m0w + stg, w_, wvo[2]); SBAR(); } if (m == 15) { dma1<3072>(m0w + stg, w_, wvo[3]); SBAR(); }
            }
            if (kt == 5 && m == 15) {
                warm_load(pfd, wq, w1, blockIdx.x * 8 + wid);
                SBAR();
            }
            if (kt == 4 && m == 14) { flag_ld(fx, flb, K1_FXO()); SBAR(); }
            if (kt == 6 && m == 2) {
                const PJ j = pj_setup(wq, w1, ws, wid, head2, f0, nft4, q16_0); pj_dma(j, lds0, wid, 0, 0); pj_dma(j, lds0, wid, 1, PSLOT); SBAR();
            }
            if (kt == 7 && m == 0) { const PJ j = pj_setup(wq, w1, ws, wid, head2, f0, nft4, q16_0); asm volatile("s_nop 4"); pj_xload(j, xa, 0); SBAR(); }
            if (kt == 7 && m == 2) {
                const char* bp = uptr(b1 + f0 * 2);
                const unsigned bo = (flane() >> 4) * 8, b2 = bo + (nft4 ? 64 : 0);
                asm volatile("s_nop 4\n\tglobal_load_dwordx2 %0, %4, %6\n\tglobal_load_dwordx2 %1, %4, %6 offset:32\n\tglobal_load_dwordx2 %2, %5, %6\n\tglobal_load_dwordx2 %3, %5, %6 offset:32"
                             : "=&v"(bb[0]), "=&v"(bb[1]), "=&v"(bb[2]), "=&v"(bb[3]) : "v"(bo), "v"(b2), "s"(bp) : "memory");
                SBAR();
            }
        });
        SBAR();
    }
    STAMP(3);
    const int head = 2 * hp + (wn >> 1), d0 = (wn & 1) * 64;
    const int key0 = 256 * rb + 128 * wm;
    const unsigned lane = flane(), c = lane & 15, g = lane >> 4;
    kvo.base = uptr(outp + ((size_t)(head * 64 + (key0 >> 5)) * 4 + (d0 >> 5)) * 2048);
    kvo.lo[0] = (g >> 1) * 1024 + ((VT ? c : (c & 7) + 16 * (c >> 3)) + 32 * (g & 1)) * 16;
    kvo.lo[1] = kvo.lo[0] + (VT ? 256 : 128);
#pragma unroll
    for (int i = 0; i < 16; ++i) {
        const int u = i >> 2, a = (i >> 1) & 1, b2 = i & 1;
        kv[i] = VT ? pack44(acc[2 * u][2 * a + b2], acc[2 * u + 1][2 * a + b2]) : pack44(acc[2 * u + b2][2 * a], acc[2 * u + b2][2 * a + 1]);
    }
    asm volatile("s_waitcnt vmcnt(0)" : "+v"(xa[0]), "+v"(xa[1]), "+v"(xa[2]), "+v"(xa[3]), "+v"(bb[0]), "+v"(bb[1]), "+v"(bb[2]), "+v"(bb[3])::"memory");
    STAMP(5);
#undef K1_RC
#undef K1_RW
#undef K1_DMA
#undef K1_FXO
}

constexpr float QSCALE = 0.08838834764831845f * 1.4426950408889634f;
constexpr float THR = 8.0f;
DI float x4_max(float x) {
    auto s = __builtin_amdgcn_permlane16_swap(__float_as_uint(x), __float_as_uint(x), false, false);
    x = __builtin_fmaxf(__uint_as_float(s[0]), __uint_as_float(s[1]));
    auto t = __builtin_amdgcn_permlane32_swap(__float_as_uint(x), __float_as_uint(x), false, false);
    return __builtin_fmaxf(__uint_as_float(t[0]), __uint_as_float(t[1]));
}
DI float x4_sum(float x) {
    auto s = __builtin_amdgcn_permlane16_swap(__float_as_uint(x), __float_as_uint(x), false, false);
    x = __uint_as_float(s[0]) + __uint_as_float(s[1]);
    auto t = __builtin_amdgcn_permlane32_swap(__float_as_uint(x), __float_as_uint(x), false, false);
    return __uint_as_float(t[0]) + __uint_as_float(t[1]);
}
DI float mish_f(float h) {
    const float e = __builtin_amdgcn_exp2f(__builtin_fminf(h, 30.0f) * 1.4426950408889634f);
    const float n = e * (e + 2.0f);
    return h * n * __builtin_amdgcn_rcpf(n + 2.0f);
}

#define SPC 4
#define KVP0 7
#define KVPS 2
template <int NFT>
DI void proj_phase(const char* __restrict__ wq, const char* __restrict__ w1, char* __restrict__ ws, char* lds, int head, int f0, int q16_0, bf16x8 (&qf)[2][4],
                   const char* __restrict__ wo, const char* __restrict__ w2, unsigned m0v0, unsigned wid, u32x4 (&xa)[4], u32x4 (&xb)[4], const u32x2 (&bb)[4], u64 tag,
                   unsigned kvflag, int hp2, unsigned m0k0, unsigned m0k1, const char* kptr0, const char* kptr1, const u32x4 (&kv)[16], const KVO& kvo) {
    const unsigned lane = flane(), g = lane >> 4;
    char* stamp_base = ws; (void)stamp_base;
    const unsigned lds0 = (unsigned)(uintptr_t)lds;
    lds_cptr L = (lds_cptr)lds;
    const PJ pj = pj_setup(wq, w1, ws, wid, head, f0, NFT == 4, q16_0);
    const char* flb = uptr(ws + WS_FL);
    u32x2 fkv = u32x2{};
    const unsigned fkvo = FL_KV + (hp2 * 16 + (lane & 15)) * 8;
    const unsigned q = (lane & 15) >> 2, p = lane & 3;
    const unsigned wqb = 2 * g * WQ_PIECE + q * 64 + p * 8;
    const unsigned w1b = W1_OFF + g * W1_PIECE + q * 64 + p * 8;
    f32x4 aq[8][2], ah[NFT][2];
#pragma unroll
    for (int i = 0; i < 8; ++i) { aq[i][0] = f32x4{}; aq[i][1] = f32x4{}; }
#pragma unroll
    for (int i = 0; i < NFT; ++i) { ah[i][0] = f32x4{}; ah[i][1] = f32x4{}; }
    constexpr int NT = 8 + NFT;
    bf16x8 wf[4];
#define PJ_RD(slot, s, n)                                                                                       \
    (((n) < 8) ? tr_frag(L + (slot) + wqb + (8 * (s)) * WQ_PIECE + ((n) >> 1) * 256 + ((n) & 1) * 32, L + (slot) + wqb + (8 * (s) + 1) * WQ_PIECE + ((n) >> 1) * 256 + ((n) & 1) * 32) \
               : tr_frag(L + (slot) + w1b + (4 * (s)) * W1_PIECE + (((n) - 8) >> 1) * 512 + ((n) & 1) * 32, L + (slot) + w1b + (4 * (s)) * W1_PIECE + 256 + (((n) - 8) >> 1) * 512 + ((n) & 1) * 32))
#pragma unroll
    for (int n = 0; n < 3; ++n) wf[n] = PJ_RD(0, 0, n);
#define PJ_CHUNK(c, XC, XN)                                                                                     \
    do {                                                                                                        \
        constexpr int slot_ = ((c) % 3) * PSLOT, nslot_ = (((c) + 1) % 3) * PSLOT, dslot_ = (((c) + 2) % 3) * PSLOT; \
          \
        if ((c) == 9) flag_wait<false>(0, fkv, flb, fkvo, tag);                                                 \
        SBAR();                                                                                                 \
        constexpr int nst_ = 16 - SPC * (c) < 0 ? 0 : 16 - SPC * (c) < SPC ? 16 - SPC * (c) : SPC;        \
        _Pragma("unroll") for (int m = 0; m < 2 * NT; ++m) {                                                    \
            const int s = m / NT, n = m % NT;                                                                   \
            if (m == 2 * NT - 3) {       \
                if ((c) + 2 < 10) asm volatile("s_waitcnt vmcnt(%4) lgkmcnt(0)\n\ts_barrier" : "+v"(XN[0]), "+v"(XN[1]), "+v"(XN[2]), "+v"(XN[3]) : "i"(3 + nst_) : "memory"); \
                else if ((c) + 1 < 10) asm volatile("s_waitcnt vmcnt(0) lgkmcnt(0)\n\ts_barrier" : "+v"(XN[0]), "+v"(XN[1]), "+v"(XN[2]), "+v"(XN[3]), "+v"(fkv)::"memory"); \
                if (nst_ == 0 && 16 - SPC * ((c) - 1) > 0) flag_set(wid, flb, kvflag, tag);        \
            }                                                                                                   \
            const int m3 = m + 3;                                                                               \
            if (m3 < 2 * NT) wf[m3 & 3] = PJ_RD(slot_, m3 / NT, m3 % NT);                                       \
            else if ((c) + 1 < 10) wf[m3 & 3] = PJ_RD(nslot_, 0, m3 - 2 * NT);                                  \
            const bf16x8 x0 = __builtin_bit_cast(bf16x8, XC[2 * s]), x1 = __builtin_bit_cast(bf16x8, XC[2 * s + 1]); \
            if (n < 8) { aq[n & 7][0] = MFMA(wf[m & 3], x0, aq[n & 7][0]); aq[n & 7][1] = MFMA(wf[m & 3], x1, aq[n & 7][1]); } \
            else { ah[(n - 8) % NFT][0] = MFMA(wf[m & 3], x0, ah[(n - 8) % NFT][0]); ah[(n - 8) % NFT][1] = MFMA(wf[m & 3], x1, ah[(n - 8) % NFT][1]); } \
            SBAR();                                                                                             \
                       \
            if ((c) + 1 < 10 && m < 4) pj_xload1(pj, XN[m & 3], (c) + 1, m);                                    \
            if ((c) + 2 < 10 && m >= 4 && m < 7) pj_dma1(pj, lds0, wid, (c) + 2, dslot_, m - 4);                \
            if (m >= KVP0 && (m - KVP0) % KVPS == 0 && (m - KVP0) / KVPS < nst_) { const int i_ = (SPC * (c) + (m - KVP0) / KVPS) & 15; kv_store(kvo, kv[i_], i_); } \
            if ((c) == 8 && m == NT) flag_ld(fkv, flb, fkvo);                                                   \
            if ((c) == 9 && m < 16 && !(m & 1)) {                           \
                const int i_ = m >> 1;                                                                          \
                if ((i_ & 3) == 0) dma1_sc1<0>(i_ < 4 ? m0k0 : m0k1, i_ < 4 ? kptr0 : kptr1, lane * 16);            \
                if ((i_ & 3) == 1) dma1_sc1<1024>(i_ < 4 ? m0k0 : m0k1, i_ < 4 ? kptr0 : kptr1, lane * 16);         \
                if ((i_ & 3) == 2) dma1_sc1<2048>(i_ < 4 ? m0k0 : m0k1, i_ < 4 ? kptr0 : kptr1, lane * 16);         \
                if ((i_ & 3) == 3) dma1_sc1<3072>(i_ < 4 ? m0k0 : m0k1, i_ < 4 ? kptr0 : kptr1, lane * 16);         \
            }                                                                                                   \
            if ((c) == 9 && m == 16) warm_lds(m0v0, wo, w2, blockIdx.x * 8 + wid);        \
            SBAR();                                                                                             \
        }                                                                                                       \
        PJSTAMP(20 + (c));                                                                                      \
    } while (0)
    static_assert((2 * NT) % 4 == 0, "fragment window");
    PJ_CHUNK(0, xa, xb); PJ_CHUNK(1, xb, xa); PJ_CHUNK(2, xa, xb); PJ_CHUNK(3, xb, xa); PJ_CHUNK(4, xa, xb);
    PJ_CHUNK(5, xb, xa); PJ_CHUNK(6, xa, xb); PJ_CHUNK(7, xb, xa); PJ_CHUNK(8, xa, xb); PJ_CHUNK(9, xb, xa);
#undef PJ_CHUNK
#undef PJ_RD
    asm volatile("s_waitcnt vmcnt(1)" ::: "memory");
#pragma unroll
    for (int a = 0; a < 2; ++a)
#pragma unroll
        for (int v = 0; v < 4; ++v) qf[a][v] = __builtin_bit_cast(bf16x8, pack44s(aq[2 * v][a], aq[2 * v + 1][a], QSCALE));
#pragma unroll
    for (int v = 0; v < NFT / 2; ++v) {
        float bias[8];
#pragma unroll
        for (int t = 0; t < 2; ++t) {
            const u32x2 q2 = bb[2 * v + t];
            bias[4 * t + 0] = bflo(q2[0]); bias[4 * t + 1] = bfhi(q2[0]); bias[4 * t + 2] = bflo(q2[1]); bias[4 * t + 3] = bfhi(q2[1]);
        }
#pragma unroll
        for (int a = 0; a < 2; ++a) {
            f32x4 h0, h1;
#pragma unroll
            for (int i = 0; i < 4; ++i) { h0[i] = mish_f(ah[2 * v][a][i] + bias[i]); h1[i] = mish_f(ah[2 * v + 1][a][i] + bias[4 + i]); }
            st_wt_o(uptr(ws + WS_CH + ((size_t)(q16_0 + a) * NKS_CH + NKS_C + (f0 >> 5) + v) * 1024), lane * 16, pack44(h0, h1));
        }
    }
}

#define NOP_SO(S, o) asm volatile("s_nop 7\n\ts_nop 3" : "+v"(S[0]), "+v"(S[1]), "+v"(o[0]), "+v"(o[1]), "+v"(o[2]), "+v"(o[3]))
#define NOP_A(A) asm volatile("s_nop 7\n\ts_nop 1" : "+v"(A[0][0]), "+v"(A[0][1]), "+v"(A[0][2]), "+v"(A[0][3]), "+v"(A[1][0]), "+v"(A[1][1]), "+v"(A[1][2]), "+v"(A[1][3]), \
    "+v"(A[2][0]), "+v"(A[2][1]), "+v"(A[2][2]), "+v"(A[2][3]), "+v"(A[3][0]), "+v"(A[3][1]), "+v"(A[3][2]), "+v"(A[3][3]), "+v"(A[4][0]), "+v"(A[4][1]), "+v"(A[4][2]), "+v"(A[4][3]))
DI float xh_sum(float v) { auto r = __builtin_amdgcn_permlane32_swap(__float_as_uint(v), __float_as_uint(v), false, false); return __uint_as_float(r[0]) + __uint_as_float(r[1]); }
DI float xh_max(float v) { auto r = __builtin_amdgcn_permlane32_swap(__float_as_uint(v), __float_as_uint(v), false, false); return __builtin_fmaxf(__uint_as_float(r[0]), __uint_as_float(r[1])); }
DI float mul_s(float a, float b) { float r; asm("v_mul_f32 %0, %1, %2" : "=v"(r) : "v"(a), "v"(b)); return r; }
DI float sub_s(float a, float b) { float r; asm("v_sub_f32 %0, %1, %2" : "=v"(r) : "v"(a), "v"(b)); return r; }
DI u32x4 pack_o(const f32x16& a, int y, float sc) {
    u32x4 w; w[0] = cvtpk(mul_s(a[4 * y + 0], sc), mul_s(a[4 * y + 1], sc)); w[1] = cvtpk(mul_s(a[4 * y + 2], sc), mul_s(a[4 * y + 3], sc));
    w[2] = cvtpk(mul_s(a[8 + 4 * y + 0], sc), mul_s(a[8 + 4 * y + 1], sc)); w[3] = cvtpk(mul_s(a[8 + 4 * y + 2], sc), mul_s(a[8 + 4 * y + 3], sc)); return w;
}
DI u32x4 pack_o1(const f32x16& a, int y) {
    u32x4 w; w[0] = cvtpk(a[4 * y + 0], a[4 * y + 1]); w[1] = cvtpk(a[4 * y + 2], a[4 * y + 3]);
    w[2] = cvtpk(a[8 + 4 * y + 0], a[8 + 4 * y + 1]); w[3] = cvtpk(a[8 + 4 * y + 2], a[8 + 4 * y + 3]); return w;
}
DI void mask_diag(f32x16& s, int dq) {
    const float ninf = -__builtin_inff();
#pragma unroll
    for (int r0 = 0; r0 < 16; r0 += 4) {
        unsigned long long m0, m1, m2, m3;
        const int kc = 16 * ((r0 >> 2) & 1) + 8 * (r0 >> 3);
        float a = s[r0], b = s[r0 + 1], c = s[r0 + 2], d = s[r0 + 3];
        asm("v_cmp_gt_i32_e64 %4, %9, %8\n\tv_cmp_gt_i32_e64 %5, %10, %8\n\tv_cmp_gt_i32_e64 %6, %11, %8\n\tv_cmp_gt_i32_e64 %7, %12, %8\n\t"
            "v_cndmask_b32_e64 %0, %0, %13, %4\n\tv_cndmask_b32_e64 %1, %1, %13, %5\n\tv_cndmask_b32_e64 %2, %2, %13, %6\n\tv_cndmask_b32_e64 %3, %3, %13, %7"
            : "+v"(a), "+v"(b), "+v"(c), "+v"(d), "=&s"(m0), "=&s"(m1), "=&s"(m2), "=&s"(m3)
            : "v"(dq), "i"(kc), "i"(kc + 1), "i"(kc + 2), "i"(kc + 3), "v"(ninf));
        s[r0] = a; s[r0 + 1] = b; s[r0 + 2] = c; s[r0 + 3] = d;
    }
}
#define VIL 1
#define VIL0 0
#define FRW 8
#define FRA 6

#define K3DPOS {2, 3, 6, 7, 10, 14, 17}
constexpr int K3D[7] = K3DPOS;
constexpr int K3_SLOT = 36864;
constexpr int PP_SET = 8 * 5 * 1024;
DI float silu_f(float v) { return v * __builtin_amdgcn_rcpf(1.0f + __builtin_amdgcn_exp2f(-1.4426950408889634f * v)); }
DI f32x4 unpk_lo(const u32x4& w) { return f32x4{bflo(w[0]), bfhi(w[0]), bflo(w[1]), bfhi(w[1])}; }
DI f32x4 unpk_hi(const u32x4& w) { return f32x4{bflo(w[2]), bfhi(w[2]), bflo(w[3]), bfhi(w[3])}; }

struct K3S { const char *aptr, *wptr; unsigned avo, wvo[3], m0a, m0w; };
DI K3S k3_setup(unsigned lds0, unsigned wid, unsigned lane) {
    K3S k;
    k.aptr = nullptr; k.wptr = nullptr;
    k.avo = lane * 16;
    const unsigned p0 = wid >= 4 ? 12 + 2 * (wid & 3) : 3 * wid;
#pragma unroll
    for (int i = 0; i < 3; ++i) {
        const unsigned o = (p0 + i) * 1024 + lane * 16, row = o / 320, c16 = (o % 320) >> 4;
        k.wvo[i] = row * 1280 + ((c16 ^ (((row >> 2) & 1) << 1)) << 4) - 1024 * i;
    }
    k.m0a = lds0 + wid * 2048; k.m0w = lds0 + 16384 + p0 * 1024;
    return k;
}
DI void k3_point(K3S& k, const char* ws, const char* w, unsigned wid, int qt, int mt, int kstep, int stage) {
    k.aptr = uptr(ws + WS_CH + ((size_t)(8 * qt + wid) * NKS_CH + kstep) * 1024);
    k.wptr = uptr(w + (size_t)stage * 64 * 1280 + mt * 320);
    asm volatile("s_nop 4" : "+s"(k.aptr), "+s"(k.wptr));
}
DI void k3_stage(K3S& k, unsigned slot, bool three) {
    set_m0(k.m0a + slot); asm volatile("s_nop 0"); dma_sc1<0>(k.aptr, k.avo); dma_sc1<1024>(k.aptr, k.avo);
    set_m0(k.m0w + slot); asm volatile("s_nop 0"); dma_nom0<0>(k.wptr, k.wvo[0]); dma_nom0<1024>(k.wptr, k.wvo[1]);
    if (three) dma_nom0<2048>(k.wptr, k.wvo[2]);
    k.aptr += 2048; k.wptr += 81920;
}
DI void k3_publish(const f32x4 (&res)[10], f32x4 (&own)[2][2], const char* pset, unsigned vo, unsigned r) {
    own[1][0] = f32x4{}; own[1][1] = f32x4{};
#pragma unroll
    for (int j = 0; j < 5; ++j) {
        if ((j & 3) == r) { own[j >> 2][0] = res[2 * j]; own[j >> 2][1] = res[2 * j + 1]; }
        else st_wt_o(pset + j * 1024, vo, pack44(res[2 * j], res[2 * j + 1]));
    }
}

template <int GRP>
DI void k3_body(K3S& k, const char* __restrict__ x, const char* __restrict__ wo, char* __restrict__ ws, char* __restrict__ out, char* lds, unsigned wid, u64 tag,
                int ks, int qt, int mt, unsigned cflag) {
    constexpr int P = GRP ? 4 : 5;
    const unsigned lane = flane(), c = lane & 15, g = lane >> 4;
    const unsigned w4 = wid & 3, qh = w4 >> 1, mh = w4 & 1;
    char* stamp_base = ws; (void)stamp_base;
    const char* flb = uptr(ws + WS_FL);
    lds_cptr L = (lds_cptr)lds;
    const unsigned ard = (8 * qh + GRP) * 1024 + lane * 16;
    const unsigned q = (lane & 15) >> 2, p = lane & 3;
    const unsigned wrow = 16384 + (32 * GRP + 4 * g + q) * 320 + 160 * mh + 8 * p;
    const unsigned sgn = ((g & 1) ? 32u : 0u);
    const unsigned wrdE = (mh & 1) ? wrow - sgn : wrow + sgn;
    const unsigned wrdO = (mh & 1) ? wrow + sgn : wrow - sgn;
    f32x4 acc[5][4], accB[5][4];
    bf16x8 af[2][4], wf[4], w4f[2];
    unsigned cur = 0;
#define K3_RW(slot, n) tr_frag(L + (slot) + (((n) & 1) ? wrdO : wrdE) + 32 * (n), L + (slot) + (((n) & 1) ? wrdO : wrdE) + 32 * (n) + 16 * 320)
#define K3_RA(slot, a) lds_frag(L + (slot) + ard + (a) * 2048)
#define K3_ZERO(acc) do { _Pragma("unroll") for (int n = 0; n < 5; ++n) _Pragma("unroll") for (int a = 0; a < 4; ++a) acc[n][a] = f32x4{}; NOP_A(acc); } while (0)
#define K3_ITER(acc, PAR, WN, RD, DM, SAFE, HOOK)                                                                       \
    do {                                                                                                                \
        if ((WN) >= 0) asm volatile("s_waitcnt vmcnt(%0) lgkmcnt(0)\n\ts_barrier" ::"i"((WN) >= 0 ? (WN) : 0) : "memory");   \
        else asm volatile("s_cmp_eq_u32 %0, 0\n\ts_cbranch_scc1 .Lq%=\n\ts_waitcnt vmcnt(0)\n.Lq%=:\n\ts_waitcnt vmcnt(1) lgkmcnt(0)\n\ts_barrier" \
                          ::"s"(wid) : "scc", "memory");                             \
        HOOK;                                                                                                           \
        const unsigned nxt_ = cur == 3 * K3_SLOT ? 0u : cur + K3_SLOT;                                                  \
        _Pragma("unroll") for (int n = 0; n < 5; ++n) {                                                                 \
            _Pragma("unroll") for (int a = 0; a < 4; ++a) {                                                             \
                if (SAFE) mfma_acc_s(acc[n][a], n < 4 ? wf[n & 3] : w4f[PAR], af[PAR][a]);                         \
                else mfma_acc(acc[n][a], n < 4 ? wf[n & 3] : w4f[PAR], af[PAR][a]);                                     \
                SBAR();                                                                                                 \
                if (RD) {                                                                                               \
                    if (n == 0 && a == 0) w4f[1 - (PAR)] = K3_RW(nxt_, 4);                                              \
                    if (n < 4 && a == 1) af[1 - (PAR)][n & 3] = K3_RA(nxt_, n & 3);                                     \
                    if (n >= 1 && a == 0) wf[(n - 1) & 3] = K3_RW(nxt_, n - 1);                                         \
                }                                                                                                       \
                if (DM) {                                                                                               \
                    const int z_ = 4 * n + a;                                                                           \
                    if (z_ == K3D[0]) set_m0(k.m0a + cur);                                                              \
                    if (z_ == K3D[1]) dma_sc1<0>(k.aptr, k.avo);                                                        \
                    if (z_ == K3D[2]) dma_sc1<1024>(k.aptr, k.avo);                                                     \
                    if (z_ == K3D[3]) set_m0(k.m0w + cur);                                                              \
                    if (z_ == K3D[4]) dma_nom0<0>(k.wptr, k.wvo[0]);                                                    \
                    if (z_ == K3D[5]) dma_nom0<1024>(k.wptr, k.wvo[1]);                                                 \
                    if (z_ == K3D[6] && !GRP) dma_nom0<2048>(k.wptr, k.wvo[2]);                                         \
                }                                                                                                       \
                SBAR();                                                                                                 \
            }                                                                                                           \
        }                                                                                                               \
        if (DM) { k.aptr += 2048; k.wptr += 81920; }                                                                    \
        cur = nxt_;                                                                                                     \
    } while (0)
#define K3_FIRST(PAR) do { _Pragma("unroll") for (int a = 0; a < 4; ++a) af[PAR][a] = K3_RA(0, a);                      \
        _Pragma("unroll") for (int n = 0; n < 4; ++n) wf[n] = K3_RW(0, n);                                              \
        w4f[PAR] = K3_RW(0, 4); } while (0)
#define K3_REDUCE()                                                                                                     \
    do {                                                                                                                \
        NOP_A(acc);                                                                                                     \
        lds_ptr X_ = (lds_ptr)lds + wid * 10240 + lane * 16;                                                            \
        lds_cptr Y_ = (lds_cptr)lds + (wid ^ 4) * 10240 + lane * 16;                                                    \
        _Pragma("unroll") for (int a = 0; a < 2; ++a) _Pragma("unroll") for (int n = 0; n < 5; ++n)                     \
            *(__attribute__((address_space(3))) f32x4*)(X_ + (a * 5 + n) * 1024) = acc[n][GRP ? a : 2 + a];             \
        asm volatile("s_waitcnt lgkmcnt(0)\n\ts_barrier" ::: "memory");                                                 \
        _Pragma("unroll") for (int a = 0; a < 2; ++a) _Pragma("unroll") for (int n = 0; n < 5; ++n)                     \
            res[a * 5 + n] = acc[n][GRP ? 2 + a : a] + *(const __attribute__((address_space(3))) f32x4*)(Y_ + (a * 5 + n) * 1024); \
    } while (0)
#define K3_BPUT(j0, j1)                                                                                                 \
    do {                                                                                                                \
        _Pragma("unroll") for (int j = j0; j < j1; ++j)                                                                 \
            *(__attribute__((address_space(3))) u32x4*)((lds_ptr)lds + 4 * K3_SLOT + wid * 2048 + lane * 16 + (j - (j0)) * 1024) = \
                pack44(accB[(2 * j) % 5][(GRP ? 0 : 2) + (2 * j) / 5], accB[(2 * j + 1) % 5][(GRP ? 0 : 2) + (2 * j + 1) / 5]); \
        SBAR();                                                                                                         \
    } while (0)
#define K3_BGET(j0, j1)                                             \
    do {                                                                                                                \
        _Pragma("unroll") for (int j = j0; j < j1; ++j)                                                                 \
            yb[j - (j0)] = *(const __attribute__((address_space(3))) u32x4*)((lds_cptr)lds + 4 * K3_SLOT + (wid ^ 4) * 2048 + lane * 16 + (j - (j0)) * 1024); \
        SBAR();                                                                                                         \
    } while (0)
#define K3_BADD(j0, j1)                                                                                                 \
    do {                                                                                                                \
        _Pragma("unroll") for (int j = j0; j < j1; ++j) {                                                               \
            resB[2 * j] = accB[(2 * j) % 5][(GRP ? 2 : 0) + (2 * j) / 5] + unpk_lo(yb[j - (j0)]);                       \
            resB[2 * j + 1] = accB[(2 * j + 1) % 5][(GRP ? 2 : 0) + (2 * j + 1) / 5] + unpk_hi(yb[j - (j0)]);           \
            PIN(resB[2 * j]); PIN(resB[2 * j + 1]);                                                                     \
        }                                                                                                               \
        SBAR();                                                                                                         \
    } while (0)
#define K3_BOUT(s)                                                                                                      \
    do {                                                                                                                \
        const bool lo_ = r != 0 && (s) < r;                                                                             \
        const unsigned hi_ = (s) == 3 ? 4 : (s) + 1;                                                                    \
        const u32x4 a_ = pack44(resB[2 * (s)], resB[2 * (s) + 1]), b_ = pack44(resB[2 * hi_], resB[2 * hi_ + 1]);       \
        const bool ua_ = lo_ || ((s) == 3 && r == 0);                                                                   \
        u32x4 v_;                                                                                                       \
        _Pragma("unroll") for (int e = 0; e < 4; ++e) v_[e] = ua_ ? a_[e] : b_[e];                                      \
        st_wt_o(pbase + (4 + ks) * PP_SET + (ua_ ? (s) : hi_) * 1024, vo, v_);                                          \
        SBAR();                                                                                                         \
    } while (0)
#define NOHOOK (void)0
    const int grp64 = qt * 4 + mt;
    const char* pbase = uptr(ws + WS_PP + (size_t)grp64 * (8 * PP_SET) + wid * 5120);
    const unsigned vo = lane * 16;
    const unsigned r = (ks - wid) & 3;
    f32x4 res[10], resB[10], ownB[2][2], ownS[2][2];
    asm volatile("s_waitcnt vmcnt(%0) lgkmcnt(0)\n\ts_barrier" ::"i"(P + 4) : "memory");
    STAMP(10);
    k3_stage(k, 2 * K3_SLOT, !GRP); k3_stage(k, 3 * K3_SLOT, !GRP);
    K3_ZERO(accB);
    if (ks != 3) { K3_FIRST(1); K3_ITER(accB, 1, 2 * P + 4, true, true, true, NOHOOK); }
    else K3_FIRST(0);
    K3_ITER(accB, 0, 2 * P, true, true, true, flag_set(wid, flb, cflag, tag));
    K3_ITER(accB, 1, 2 * P, true, true, false, NOHOOK);
    u32x2 fc = u32x2{~(unsigned)tag, 0u};
    const unsigned fco = FL_C + ((qt < 8 ? qt : 15 - qt) * 32 + 8 * ks + (lane & 7)) * 8;
    K3_ITER(accB, 0, 2 * P, true, false, false, flag_ld0(wid, fc, flb, fco));
    STAMP(11);
    flag_wait<true>(wid, fc, flb, fco, tag);
    STAMP(18);
    k3_point(k, ws, wo, wid, qt, mt, 32 * ks, 16 * ks);
    K3_ITER(accB, 1, P, true, true, false, k3_stage(k, cur == 0 ? 3 * K3_SLOT : cur - K3_SLOT, !GRP));
    K3_ITER(accB, 0, 2 * P, true, true, false, NOHOOK);
    K3_ITER(accB, 1, 2 * P, true, true, false, NOHOOK);
    STAMP(19);
    K3_ZERO(acc);
    NOP_A(accB);
    u32x4 yb[2];
#define K3_H2(A, B) do { A; B; } while (0)
    K3_ITER(acc, 0, 2 * P, true, true, true, K3_BPUT(0, 2)); K3_ITER(acc, 1, 2 * P, true, true, false, K3_BGET(0, 2));
    K3_ITER(acc, 0, 2 * P, true, true, false, K3_H2(K3_BADD(0, 2), K3_BPUT(2, 4))); K3_ITER(acc, 1, 2 * P, true, true, false, K3_BGET(2, 4));
    K3_ITER(acc, 0, 2 * P, true, true, false, K3_H2(K3_BADD(2, 4), K3_BPUT(4, 5))); K3_ITER(acc, 1, 2 * P, true, true, false, K3_BGET(4, 5));
    K3_ITER(acc, 0, 2 * P, true, true, false, K3_H2(K3_BADD(4, 5), K3_BOUT(0)));
    K3_ITER(acc, 1, 2 * P + 1, true, true, false, K3_BOUT(1)); K3_ITER(acc, 0, 2 * P + 2, true, true, false, K3_BOUT(2));
    K3_ITER(acc, 1, 2 * P + 2, true, true, false, K3_BOUT(3));
#pragma unroll
    for (int t = 0; t < 2; ++t)
#pragma unroll
        for (int e = 0; e < 4; ++e) {
            ownB[0][t][e] = r == 0 ? resB[t][e] : r == 1 ? resB[2 + t][e] : r == 2 ? resB[4 + t][e] : resB[6 + t][e];
            ownB[1][t][e] = resB[8 + t][e];
        }
    K3_ITER(acc, 0, 2 * P + 2, true, true, false, NOHOOK); K3_ITER(acc, 1, 2 * P + 1, true, true, false, NOHOOK);
#undef K3_H2
    u32x2 xv[2][2];
    unsigned oo[2][2];
#pragma unroll
    for (int sl = 0; sl < 2; ++sl)
#pragma unroll
        for (int t = 0; t < 2; ++t) {
            const unsigned i = 2 * (sl ? 4 : r) + t, a = i >= 5 ? 1 : 0, n = i - 5 * a;
            oo[sl][t] = (16 * (8 * qt + 4 * qh + 2 * GRP + a) + c) * (DM * 2) + (16 * (10 * mt + 5 * mh + n) + 4 * g) * 2;
        }
#define K3_XLD() asm volatile("global_load_dwordx2 %0, %4, %8\n\tglobal_load_dwordx2 %1, %5, %8\n\tglobal_load_dwordx2 %2, %6, %8\n\tglobal_load_dwordx2 %3, %7, %8" \
        : "=&v"(xv[0][0]), "=&v"(xv[0][1]), "=&v"(xv[1][0]), "=&v"(xv[1][1]) : "v"(oo[0][0]), "v"(oo[0][1]), "v"(oo[1][0]), "v"(oo[1][1]), "s"(x) : "memory")
    const unsigned fown = (grp64 * 4 + ks) * 128, fpeer = (grp64 * 4 + ((lane & 3) == (unsigned)ks ? (ks + 1) & 3 : lane & 3)) * 128;
    u32x2 fb;
    K3_ITER(acc, 0, 2 * P, true, false, false, flag_set(wid, flb, FL_B + fown, tag));
    K3_ITER(acc, 1, P, true, false, false, NOHOOK);
#define K3_H14() do { K3_XLD(); flag_ld(fb, flb, FL_B + fpeer); } while (0)
    u32x4 pp[2][6];
#pragma unroll
    for (int i = 0; i < 6; ++i) pp[1][i] = u32x4{};
    const unsigned s1 = ((ks + 1) & 3) * PP_SET, s2 = ((ks + 2) & 3) * PP_SET, s3 = ((ks + 3) & 3) * PP_SET;
    const char* pj0 = pbase + r * 1024;
    const char* pj1 = pbase + 4 * 1024;
#define K3_H15()                                                                                                        \
    do {                                                                                                                \
        PIN(xv[0][0]); PIN(xv[0][1]); PIN(xv[1][0]); PIN(xv[1][1]);                                                     \
        flag_wait<false>(0, fb, flb, FL_B + fpeer, tag);                                                                \
        ld_wt(pp[0][3], pj0 + 4 * PP_SET + s1, vo); ld_wt(pp[0][4], pj0 + 4 * PP_SET + s2, vo); ld_wt(pp[0][5], pj0 + 4 * PP_SET + s3, vo); \
        ld_wt3(r, pp[1][3], pp[1][4], pp[1][5], pj1 + 4 * PP_SET + s1, pj1 + 4 * PP_SET + s2, pj1 + 4 * PP_SET + s3, vo); \
        SBAR();                                                                                                         \
    } while (0)
    K3_ITER(acc, 0, -1, true, false, false, K3_H14()); K3_ITER(acc, 1, 0, false, false, false, K3_H15());
#undef K3_H14
#undef K3_H15
    STAMP(12);
    K3_REDUCE();
    STAMP(17);
    k3_publish(res, ownS, pbase + ks * PP_SET, vo, r);
    u32x2 fe[4];
    const bool bump = blockIdx.x == 0 && wid == 0;
    if (bump) {
#pragma unroll
        for (int i = 0; i < 4; ++i) flag_ld(fe[i], flb, FL_X + (64 * i + lane) * 8);
    }
    const int nown = r == 0 ? 2 : 1;
    STAMP(14);
    asm volatile("s_waitcnt vmcnt(0)\n\ts_barrier" : "+v"(fe[0]), "+v"(fe[1]), "+v"(fe[2]), "+v"(fe[3]), "+v"(pp[0][3]), "+v"(pp[0][4]), "+v"(pp[0][5]),
                 "+v"(pp[1][3]), "+v"(pp[1][4]), "+v"(pp[1][5])::"memory");
    STAMP(15);
    flag_set(wid, flb, FL_S + fown, tag);
    {
        u32x2 f = u32x2{~(unsigned)tag, 0u};
        flag_wait<false>(0, f, flb, FL_S + fpeer, tag);
    }
    STAMP(16);
    ld_wt(pp[0][0], pj0 + s1, vo); ld_wt(pp[0][1], pj0 + s2, vo); ld_wt(pp[0][2], pj0 + s3, vo);
    ld_wt3(r, pp[1][0], pp[1][1], pp[1][2], pj1 + s1, pj1 + s2, pj1 + s3, vo);
    asm volatile("s_waitcnt vmcnt(0)" : "+v"(pp[0][0]), "+v"(pp[0][1]), "+v"(pp[0][2]), "+v"(pp[0][3]), "+v"(pp[0][4]), "+v"(pp[0][5]),
                 "+v"(pp[1][0]), "+v"(pp[1][1]), "+v"(pp[1][2]), "+v"(pp[1][3]), "+v"(pp[1][4]), "+v"(pp[1][5])::"memory");
#pragma unroll
    for (int sl = 0; sl < 2; ++sl)
        if (sl < nown) {
#pragma unroll
            for (int t = 0; t < 2; ++t) {
                f32x4 sv = ownS[sl][t], bv = ownB[sl][t];
#pragma unroll
                for (int i = 0; i < 3; ++i) { sv += t ? unpk_hi(pp[sl][i]) : unpk_lo(pp[sl][i]); bv += t ? unpk_hi(pp[sl][3 + i]) : unpk_lo(pp[sl][3 + i]); }
                const float o0 = bflo(xv[sl][t][0]) + sv[0] * silu_f(bv[0]), o1 = bfhi(xv[sl][t][0]) + sv[1] * silu_f(bv[1]);
                const float o2 = bflo(xv[sl][t][1]) + sv[2] * silu_f(bv[2]), o3 = bfhi(xv[sl][t][1]) + sv[3] * silu_f(bv[3]);
                u32x2 w; w[0] = cvtpk(o0, o1); w[1] = cvtpk(o2, o3);
                *(u32x2*)(out + oo[sl][t]) = w;
            }
        }
    if (bump) {
#pragma unroll
        for (int i = 0; i < 4; ++i) flag_wait<false>(0, fe[i], flb, FL_X + (64 * i + lane) * 8, tag);
        flag_set(0, flb, FL_EPOCH, tag);
    }
    STAMP(13);
#undef K3_RW
#undef K3_RA
#undef K3_ZERO
#undef K3_ITER
#undef K3_FIRST
#undef K3_REDUCE
#undef K3_BPUT
#undef K3_BGET
#undef K3_BADD
#undef K3_BOUT
#undef K3_XLD
#undef NOHOOK
}

__global__ __launch_bounds__(512, 2) void fused(const char* __restrict__ x, const char* __restrict__ ckv, const char* __restrict__ wuk, const char* __restrict__ wuv,
                                              const char* __restrict__ wq, const char* __restrict__ w1, const char* __restrict__ b1, char* __restrict__ ws,
                                              const char* __restrict__ wo, const char* __restrict__ w2, char* __restrict__ out) {
    extern __shared__ __attribute__((aligned(16))) char lds[];
    const unsigned wid = rfl(threadIdx.x >> 6), grp = wid >> 2, w4 = wid & 3;
    char* stamp_base = ws; (void)stamp_base;
    STAMP(0);
    const int b = blockIdx.x, xcd = b & 7, idx = b >> 3;
    const int vt = idx & 1, hp = 2 * xcd + ((idx >> 1) & 1), rb = idx >> 2;
    const int head = 4 * xcd + (idx & 3), pi = idx >> 2;
    const int HB = 15 - pi, LB = pi;
    const int myblk = grp ? LB : HB;
    const int q16_0 = 8 * myblk + 2 * w4;
    const bool nft4 = head < 22;
    const int f0 = nft4 ? 64 * head : 1408 + 32 * (head - 22);
    const char* Abase = ckv + (size_t)rb * 256 * 1024;
    const char* Wbase = (vt ? wuv : wuk) + hp * 512;
    const unsigned lane0 = flane();
#define lane lane0
    const int row = 8 * b + wid;
    const unsigned l40 = lane < 40 ? lane : 39;
    u32x4 x0, x1;
    {
        const char* xr = uptr(x + (size_t)row * 1280);
        const unsigned vo = l40 * 32;
        asm volatile("global_load_dwordx4 %0, %2, %3\n\tglobal_load_dwordx4 %1, %2, %3 offset:16" : "=&v"(x0), "=&v"(x1) : "v"(vo), "s"(xr) : "memory");
    }
    unsigned avo[4], wvo[4];
    {
        const unsigned lds0 = (unsigned)(uintptr_t)lds;
        const char* Aw = Abase + (size_t)wid * 32 * 1024;
        const char* Ww = Wbase + (size_t)wid * 8 * 8192;
        const unsigned m0a = lds0 + wid * 4096, m0w = lds0 + 32768 + wid * 4096;
#pragma unroll
        for (int p = 0; p < 4; ++p) {
            const unsigned rw = 8 * p + (lane >> 3);
            avo[p] = rw * 1024 + (((lane & 7) ^ ((rw >> 1) & 7)) << 4) - 1024 * p;
            const unsigned k = 2 * p + (lane >> 5);
            const unsigned f = (k & 3) | ((wid & 1) << 2);
            wvo[p] = k * 8192 + (((lane & 31) ^ (f << 1)) << 4) - 1024 * p;
        }
#pragma unroll
        for (int kt = 0; kt < 2; ++kt) {
            const char* a_ = Aw + kt * 128; const char* w_ = Ww + (size_t)kt * 64 * 8192; const unsigned stg = kt * 65536;
            dma1<0>(m0a + stg, a_, avo[0]); dma1<1024>(m0a + stg, a_, avo[1]); dma1<2048>(m0a + stg, a_, avo[2]); dma1<3072>(m0a + stg, a_, avo[3]);
            dma1<0>(m0w + stg, w_, wvo[0]); dma1<1024>(m0w + stg, w_, wvo[1]); dma1<2048>(m0w + stg, w_, wvo[2]); dma1<3072>(m0w + stg, w_, wvo[3]);
        }
    }
    u64 tag;
    asm volatile("s_load_dwordx2 %0, %1, 0x0\n\ts_waitcnt lgkmcnt(0)" : "=s"(tag) : "s"(uptr(ws + WS_FL + FL_EPOCH)) : "memory");
    tag += 1;
    {
        asm volatile("s_waitcnt vmcnt(16)" : "+v"(x0), "+v"(x1)::"memory");
        float v[16];
#pragma unroll
        for (int i = 0; i < 4; ++i) { v[2 * i] = bflo(x0[i]); v[2 * i + 1] = bfhi(x0[i]); v[8 + 2 * i] = bflo(x1[i]); v[8 + 2 * i + 1] = bfhi(x1[i]); }
        float s = 0.f;
#pragma unroll
        for (int i = 0; i < 16; ++i) s += v[i];
        if (lane >= 40) s = 0.f;
#pragma unroll
        for (int o = 32; o >= 1; o >>= 1) s += __shfl_xor(s, o);
        const float mean = s * (1.0f / DM);
        float q = 0.f;
#pragma unroll
        for (int i = 0; i < 16; ++i) { v[i] -= mean; q += v[i] * v[i]; }
        if (lane >= 40) q = 0.f;
#pragma unroll
        for (int o = 32; o >= 1; o >>= 1) q += __shfl_xor(q, o);
        const float rstd = 1.0f / sqrtf(q * (1.0f / DM) + 1e-5f);
        u32x4 o0, o1;
#pragma unroll
        for (int i = 0; i < 4; ++i) { o0[i] = cvtpk(v[2 * i] * rstd, v[2 * i + 1] * rstd); o1[i] = cvtpk(v[8 + 2 * i] * rstd, v[8 + 2 * i + 1] * rstd); }
        char* xf = ws + WS_XF + ((size_t)(row >> 4) * NKS_X + (l40 >> 1)) * 1024 + ((row & 15) + 32 * (l40 & 1)) * 16;
        if (lane < 40) { st_wt(xf, o0); st_wt(xf + 256, o1); }
    }
#undef lane
    u32x4 xa[4], xb[4], kv[16];
    u32x2 bb[4];
    KVO kvo;
    if (vt) k1_tile<true>(Abase, Wbase, ws + WS_VF, lds, avo, wvo, wid, hp, rb, ws, wq, w1, b1, tag, head, f0, nft4, q16_0, xa, bb, kv, kvo);
    else    k1_tile<false>(Abase, Wbase, ws + WS_KF, lds, avo, wvo, wid, hp, rb, ws, wq, w1, b1, tag, head, f0, nft4, q16_0, xa, bb, kv, kvo);
    STAMP(6);
    const unsigned lds0 = (unsigned)(uintptr_t)lds;
    const unsigned kslot0 = grp ? 114688 : 81920, kslot1 = kslot0 + 16384, kslot2 = grp ? 16384 : 0;
    const unsigned vslot0 = grp ? 147456 : 32768, vslot1 = grp ? 65536 : 49152;
    const int nL = 2 * pi + 2;
    const int nsw = grp ? nL : 1000;
    const size_t jump = (size_t)(17 - nL) * 16384;
    const char* kptr = uptr(ws + WS_KF + (size_t)head * 524288 + w4 * 4096);
    const char* vptr = uptr(ws + WS_VF + (size_t)head * 524288 + w4 * 4096);
    bf16x8 qf[8];
    {
        bf16x8 q16[2][4];
        const unsigned kvflag = FL_KV + (hp * 16 + vt * 8 + rb) * 8;
        if (nft4) proj_phase<4>(wq, w1, ws, lds, head, f0, q16_0, q16, wo, w2, lds0 + vslot0 + w4 * 4096, wid, xa, xb, bb, tag, kvflag, head >> 1, lds0 + kslot0 + w4 * 4096, lds0 + kslot1 + w4 * 4096, kptr, kptr + 16384, kv, kvo);
        else      proj_phase<2>(wq, w1, ws, lds, head, f0, q16_0, q16, wo, w2, lds0 + vslot0 + w4 * 4096, wid, xa, xb, bb, tag, kvflag, head >> 1, lds0 + kslot0 + w4 * 4096, lds0 + kslot1 + w4 * 4096, kptr, kptr + 16384, kv, kvo);
        kptr += 2 * 16384; if (nsw == 2) kptr += jump;
#pragma unroll
        for (int v = 0; v < 4; ++v) {
            const u32x4 r0 = __builtin_bit_cast(u32x4, q16[0][v]), r1 = __builtin_bit_cast(u32x4, q16[1][v]);
            u32x4 z0, z1;
#pragma unroll
            for (int w = 0; w < 4; ++w) {
                const auto s1 = __builtin_amdgcn_permlane16_swap(r0[w], r1[w], false, false);
                const auto s2 = __builtin_amdgcn_permlane32_swap(s1[0], s1[1], false, false);
                z0[w] = s2[0]; z1[w] = s2[1];
            }
            qf[2 * v] = __builtin_bit_cast(bf16x8, z0); qf[2 * v + 1] = __builtin_bit_cast(bf16x8, z1);
        }
    }
    STAMP(7);
    const unsigned lane = flane();
    if (grp == 0) {
        char* qh = ws + WS_QH + (size_t)b * 32768 + w4 * 8192 + lane * 16;
#pragma unroll
        for (int s = 0; s < 8; ++s) *(bf16x8*)(qh + s * 1024) = qf[s];
    }
    const char* flb = uptr(ws + WS_FL);
    lds_cptr Lb = (lds_cptr)lds + lane * 16;
    const unsigned m0b = lds0 + w4 * 4096;
    const unsigned dvo = lane * 16;
    int q0 = 128 * myblk + 32 * w4;
    int tile = 0;
    asm volatile("s_waitcnt lgkmcnt(0)\n\ts_barrier" ::: "memory");
    dma4(m0b + vslot0, vptr, dvo); vptr += 16384;
    dma4(m0b + kslot2, kptr, dvo); kptr += 16384;
    f32x16 o[4], S[2], negm;
    float mref = 0.f, lsum = 0.f, lfin = 1.f;
    bf16x8 pf[4], fr[FRW];
#pragma unroll
    for (int i = 0; i < 4; ++i) o[i] = f32x16{};
    negm = f32x16{}; S[0] = f32x16{}; S[1] = f32x16{};
    asm volatile("s_nop 1" : "+v"(negm));
    NOP_SO(S, o);
    unsigned ks_cur = kslot0, ks_nxt = kslot1, ks_nn = kslot2;
#define CTX_VO(l) ((((l) >> 4) & 1) * (NKS_CH * 1024) + (((l) & 15) + 16 * ((l) >> 5)) * 16)
#define KOFF(i) (((i) & 1) * 8192 + ((i) >> 1) * 1024)
#define VOFF(j) (((j) >> 3) * 8192 + ((j) & 3) * 2048 + (((j) >> 2) & 1) * 1024)
#define EARLY_K(slot) do { _Pragma("unroll") for (int i = 0; i < FRA; ++i) fr[i] = lds_frag(Lb + (slot) + KOFF(i)); } while (0)
    EARLY_K(kslot0);
    if (grp) asm volatile("s_barrier" ::: "memory");
#define PHASE_M(t, QK, PV, VS)                                                                              \
    do {                                                                                                                              \
        __builtin_amdgcn_s_setprio(0);                                                                                                \
        lds_cptr kp_ = Lb + ks_cur; lds_cptr vp_ = Lb + ((VS) ? vslot1 : vslot0);                                                     \
        if (!(QK)) { _Pragma("unroll") for (int i = 16; i < 16 + FRA; ++i) fr[i % FRW] = lds_frag(vp_ + VOFF(i - 16)); }               \
        _Pragma("unroll") for (int i = (QK) ? 0 : 16; i < ((PV) ? 32 : 16); ++i) {                                                    \
            const int n = i + FRA;                                                                                                    \
            if (n < 16) fr[n % FRW] = lds_frag(kp_ + KOFF(n)); else if ((PV) && n < 32) fr[n % FRW] = lds_frag(vp_ + VOFF(n - 16));    \
            SBAR();                                                                                                                   \
            if (i < 2) mfma32_ini(S[i & 1], fr[i % FRW], qf[i >> 1], negm);                                                        \
            else if (i < 16) mfma32_acc(S[i & 1], fr[i % FRW], qf[i >> 1]);                                                             \
            else mfma32_acc(o[i & 3], fr[i % FRW], pf[(i - 16) >> 2]);                                                                  \
            SBAR();                                                                                                                   \
            if ((QK) && i == 16 && (t) + 1 == nsw) {        \
                const char* qs_ = uptr(ws + WS_QH + (size_t)b * 32768 + w4 * 8192);                                                   \
                asm volatile("s_nop 4\n\tglobal_load_dwordx4 %0, %8, %9\n\tglobal_load_dwordx4 %1, %8, %9 offset:1024\n\tglobal_load_dwordx4 %2, %8, %9 offset:2048\n\tglobal_load_dwordx4 %3, %8, %9 offset:3072" \
                             : "+v"(qf[0]), "+v"(qf[1]), "+v"(qf[2]), "+v"(qf[3]), "+v"(qf[4]), "+v"(qf[5]), "+v"(qf[6]), "+v"(qf[7]) : "v"(dvo), "s"(qs_) : "memory"); \
                asm volatile("global_load_dwordx4 %4, %8, %9\n\tglobal_load_dwordx4 %5, %8, %9 offset:1024\n\tglobal_load_dwordx4 %6, %8, %9 offset:2048\n\tglobal_load_dwordx4 %7, %8, %9 offset:3072" \
                             : "+v"(qf[0]), "+v"(qf[1]), "+v"(qf[2]), "+v"(qf[3]), "+v"(qf[4]), "+v"(qf[5]), "+v"(qf[6]), "+v"(qf[7]) : "v"(dvo), "s"(qs_ + 4096) : "memory"); \
            }                                                                                                                         \
            SBAR();                                                                                                                   \
        }                                                                                                                             \
        __builtin_amdgcn_s_setprio(3);                                                                                                \
        TICK(tA);                                                                                                                     \
        asm volatile("s_barrier" ::: "memory");                                                                                       \
        TICK(tX);                                                                                                                     \
    } while (0)
#define EXP_PACK(DV, DK, VS)         \
    do {                                                                                                                              \
        float e_[32], a_[2];                                                                                                          \
        unsigned c_[16];                                                                                                              \
        _Pragma("unroll") for (int k = 0; k < 18; ++k) {                                                                              \
            if (k < 16) { e_[2 * k] = v_exp(S[k >> 3][2 * (k & 7)]); e_[2 * k + 1] = v_exp(S[k >> 3][2 * (k & 7) + 1]); }             \
            if (k == 1) { a_[0] = e_[0]; a_[1] = e_[1]; }                                                                             \
            if (k >= 2 && k < 17) { a_[0] = v_add(a_[0], e_[2 * k - 2]); a_[1] = v_add(a_[1], e_[2 * k - 1]); }                        \
            if (k >= 2) c_[k - 2] = v_cvt(e_[2 * k - 4], e_[2 * k - 3]);                                                              \
            if (VIL) {                                                   \
                if ((DV) && k == VIL0) set_m0(m0b + ((VS) ? vslot1 : vslot0));                                                        \
                if ((DV) && k == VIL0 + 1) dma_sc1<0>(vptr, dvo);                                                                    \
                if ((DV) && k == VIL0 + 3) dma_sc1<1024>(vptr, dvo);                                                                 \
                if ((DV) && k == VIL0 + 5) dma_sc1<2048>(vptr, dvo);                                                                 \
                if ((DV) && k == VIL0 + 7) dma_sc1<3072>(vptr, dvo);                                                                 \
                if ((DK) && k == VIL0 + 8) set_m0(m0b + ks_cur);                                                                      \
                if ((DK) && k == VIL0 + 9) dma_sc1<0>(kptr, dvo);                                                                    \
                if ((DK) && k == VIL0 + 11) dma_sc1<1024>(kptr, dvo);                                                                \
                if ((DK) && k == VIL0 + 13) dma_sc1<2048>(kptr, dvo);                                                                \
                if ((DK) && k == VIL0 + 15) dma_sc1<3072>(kptr, dvo);                                                                \
            }                                                                                                                         \
        }                                                                                                                             \
        _Pragma("unroll") for (int k = 0; k < 4; ++k) pf[k] = __builtin_bit_cast(bf16x8, u32x4{c_[4 * k], c_[4 * k + 1], c_[4 * k + 2], c_[4 * k + 3]}); \
        ts_ = v_add(a_[0], a_[1]);                                                                                                    \
    } while (0)
#define REBASE(FIRST)                                                          \
    do {                                                                                                                              \
        float rm = S[0][0];                                                                                                           \
        _Pragma("unroll") for (int e = 1; e < 32; ++e) rm = __builtin_fmaxf(rm, S[e >> 4][e & 15]);                                   \
        rm = xh_max(rm);                                                                                                              \
        const float dl = (FIRST) ? (rm == -__builtin_inff() ? 0.f : rm) : __builtin_fmaxf(rm, 0.f);                                   \
        mref += dl;                                                                                                                   \
        if (!(FIRST)) { float f = __builtin_amdgcn_exp2f(-dl); asm volatile("s_nop 1" : "+v"(f)); lsum *= f;               \
            _Pragma("unroll") for (int i = 0; i < 4; ++i) _Pragma("unroll") for (int r = 0; r < 16; ++r) o[i][r] = mul_s(o[i][r], f); } \
        _Pragma("unroll") for (int r = 0; r < 16; ++r) { S[0][r] = sub_s(S[0][r], dl); S[1][r] = sub_s(S[1][r], dl); }                \
        const float nm = -mref;                                                                                                       \
        _Pragma("unroll") for (int r = 0; r < 16; ++r) negm[r] = nm;                                                                  \
    } while (0)
#define PHASE_V(t, WN, LASTV, VS, DK, DV)                                                                                             \
    do {                                                                                                                              \
        const bool swn = ((t) + 1 == nsw), sw0 = ((t) == nsw);                                                                        \
        const bool diag = 64 * tile + 63 > q0 || sw0, first_ = (t) == 0 || sw0;                                                       \
        if (!VIL) { if (DV) dma4(m0b + ((VS) ? vslot1 : vslot0), vptr, dvo); if (DK) dma4(m0b + ks_cur, kptr, dvo); }                 \
        NOP_SO(S, o);                                                                                                                 \
        float ts_;                                                                                                                    \
        if (__builtin_expect(diag || first_, 0)) {                                                                                    \
            if (sw0) {                                                                                \
                float il_ = __builtin_amdgcn_rcpf(xh_sum(lsum));                                                                      \
                asm volatile("s_nop 1" : "+v"(il_));                                                                                  \
                const char* cp_ = uptr(ws + WS_CH + ((size_t)(8 * LB + 2 * w4) * NKS_CH + 4 * head) * 1024);                          \
                const unsigned co_ = CTX_VO(flane());                                                                                 \
                asm volatile("s_nop 4");                                                                                              \
                st_wt_i<0>(cp_, co_, pack_o(o[0], 0, il_)); st_wt_i<512>(cp_, co_, pack_o(o[0], 1, il_));                             \
                st_wt_i<1024>(cp_, co_, pack_o(o[1], 0, il_)); st_wt_i<1536>(cp_, co_, pack_o(o[1], 1, il_));                         \
                st_wt_i<2048>(cp_, co_, pack_o(o[2], 0, il_)); st_wt_i<2560>(cp_, co_, pack_o(o[2], 1, il_));                         \
                st_wt_i<3072>(cp_, co_, pack_o(o[3], 0, il_)); st_wt_i<3584>(cp_, co_, pack_o(o[3], 1, il_));                         \
                _Pragma("unroll") for (int i = 0; i < 4; ++i) o[i] = f32x16{};                                                        \
                lsum = 0.f; mref = 0.f; q0 = 128 * HB + 32 * w4;                                                                      \
            }                                                                                                                         \
            if (64 * tile + 63 > q0) {         \
                const int dq = (int)(lane & 31) - 4 * (int)(lane >> 5);                                                               \
                _Pragma("unroll") for (int u = 0; u < 2; ++u) {                                                                       \
                    const int kb = 64 * tile + 32 * u;                                                                                \
                    if (kb > q0) { _Pragma("unroll") for (int r = 0; r < 16; ++r) S[u][r] = -__builtin_inff(); }                       \
                    else if (kb == q0) mask_diag(S[u], dq);                                                                           \
                }                                                                                                                     \
            }                                                                                                                         \
            if (first_) REBASE(true);                                                                                                 \
        }                                                                                                                             \
        EXP_PACK(DV, DK, VS);                                                                                                         \
        if (DV) vptr += 16384 + ((t) + 2 == nsw ? jump : 0);                                                                          \
        if (DK) kptr += 16384 + ((t) + 4 == nsw ? jump : 0);                                                                          \
        if (__builtin_expect(__any(ts_ > PLIM), 0)) { REBASE(false); EXP_PACK(false, false, 0); }     \
        lsum += ts_;                                                                                                                  \
        if (!(LASTV)) EARLY_K(ks_nxt);                                                                                                \
        if (__builtin_expect(swn, 0)) {                                                                                               \
            negm = f32x16{};                                                                                                          \
            asm volatile("s_waitcnt vmcnt(%9)" : "+v"(qf[0]), "+v"(qf[1]), "+v"(qf[2]), "+v"(qf[3]), "+v"(qf[4]), "+v"(qf[5]), "+v"(qf[6]), "+v"(qf[7]), "+v"(negm) : "i"(WN) : "memory"); \
            tile += 17 - nL;                                                                                                          \
        }                                                                                                                             \
        TICK(tB);                                                                                                                     \
        asm volatile("s_cmp_eq_u32 %7, 0\n\ts_cbranch_scc1 .Lv1%=\n\ts_waitcnt vmcnt(%5)\n\ts_branch .Lv2%=\n.Lv1%=:\n\ts_waitcnt vmcnt(%6)\n.Lv2%=:\n\ts_barrier"                \
                     : "+v"(pf[0]), "+v"(pf[1]), "+v"(pf[2]), "+v"(pf[3]), "+v"(negm) : "i"(WN), "i"((WN) + 8), "s"(nsw - (t)) : "scc", "memory");   \
        __builtin_amdgcn_s_waitcnt(0xC07F);        \
        TICK(tO);                                                                                                                     \
        { const unsigned r_ = ks_cur; ks_cur = ks_nxt; ks_nxt = ks_nn; ks_nn = r_; }                                                  \
        tile += 1;                                                                                                                    \
    } while (0)
    constexpr float PLIM = 256.0f;
    PHASE_M(0, true, false, 1);
    PHASE_V(0, 8, false, 1, true, true);
    flag_set(w4, flb, FL_H + (myblk * 32 + head) * 8, tag);
    for (int t = 1; t < 13; t += 2) {
        PHASE_M(t, true, true, 0); PHASE_V(t, 8, false, 0, true, true);
        PHASE_M(t + 1, true, true, 1); PHASE_V(t + 1, 8, false, 1, true, true);
    }
    PHASE_M(13, true, true, 0); PHASE_V(13, 8, false, 0, true, true);
    PHASE_M(14, true, true, 1); PHASE_V(14, 4, false, 1, false, true);
    PHASE_M(15, true, true, 0);
    const int ks = xcd >> 1, qt = 8 * (xcd & 1) + (idx >> 2), mt = idx & 3;
    u32x2 fh;
    const unsigned fho = FL_H + (qt * 32 + (lane & 31)) * 8;
    flag_ld(fh, flb, fho);
    PHASE_V(15, 4, false, 0, false, true);
    PIN(fh);
    PHASE_M(16, true, true, 1); PHASE_V(16, 0, true, 1, false, false);
    PHASE_M(17, false, true, 0);
    flag_wait<false>(0, fh, flb, fho, tag);
    if (!grp) asm volatile("s_barrier" ::: "memory");
    NOP_SO(S, o);
    K3S k3 = k3_setup(lds0, wid, lane);
    k3_point(k3, ws, w2, wid, qt, mt, NKS_C + 14 * ks, 7 * ks);
    k3_stage(k3, 0, grp == 0); k3_stage(k3, K3_SLOT, grp == 0);
    STAMP(8);
#define MERGE(G) \
    { \
        const float ls = xh_sum(lsum); \
        const float mm = ls > 0.f ? mref : -__builtin_inff(); \
        lds_ptr X = (lds_ptr)lds + 81920 + wid * 8192 + lane * 16; \
        _Pragma("unroll") for (int i = 0; i < 2; ++i) \
            _Pragma("unroll") for (int y = 0; y < 4; ++y) { \
                const int oi = (G) ? i : 2 + i; \
                *(__attribute__((address_space(3))) f32x4*)(X + (4 * i + y) * 1024) = f32x4{o[oi][4 * y], o[oi][4 * y + 1], o[oi][4 * y + 2], o[oi][4 * y + 3]}; \
            } \
        lds_ptr Y = (lds_ptr)lds + 73728 + wid * 512 + lane * 8; \
        *(__attribute__((address_space(3))) f32x2*)Y = f32x2{mm, ls}; \
        asm volatile("s_waitcnt lgkmcnt(0)\n\ts_barrier" ::: "memory"); \
        const unsigned ow = wid ^ 4; \
        lds_cptr Xo = (lds_cptr)lds + 81920 + ow * 8192 + lane * 16; \
        const f32x2 oml = *(const __attribute__((address_space(3))) f32x2*)((lds_cptr)lds + 73728 + ow * 512 + lane * 8); \
        const float mx = __builtin_fmaxf(mm, oml[0]); \
        const float e0 = __builtin_amdgcn_exp2f(mm - mx), e1 = __builtin_amdgcn_exp2f(oml[0] - mx); \
        const float inv = __builtin_amdgcn_rcpf(ls * e0 + oml[1] * e1); \
        const float fs = e0 * inv, fo = e1 * inv; \
        const char* cp = uptr(ws + WS_CH + ((size_t)(8 * HB + 2 * w4) * NKS_CH + 4 * head + 2 * (G)) * 1024); \
        const unsigned co = CTX_VO(lane); \
_Pragma("unroll") \
        for (int i = 0; i < 2; ++i) { \
            f32x16 m; \
_Pragma("unroll") \
            for (int y = 0; y < 4; ++y) { \
                const f32x4 pv = *(const __attribute__((address_space(3))) f32x4*)(Xo + (4 * i + y) * 1024); \
_Pragma("unroll") \
                for (int r = 0; r < 4; r += 2) { \
                    const f32x2 k2_ = f32x2{o[((G) ? 2 : 0) + i][4 * y + r], o[((G) ? 2 : 0) + i][4 * y + r + 1]}; \
                    const f32x2 r2_ = __builtin_elementwise_fma(k2_, f32x2{fs, fs}, f32x2{pv[r], pv[r + 1]} * f32x2{fo, fo}); \
                    m[4 * y + r] = r2_[0]; m[4 * y + r + 1] = r2_[1]; \
                } \
            } \
            if (i == 0) { st_wt_i<0>(cp, co, pack_o1(m, 0)); st_wt_i<512>(cp, co, pack_o1(m, 1)); } \
            else { st_wt_i<1024>(cp, co, pack_o1(m, 0)); st_wt_i<1536>(cp, co, pack_o1(m, 1)); } \
        } \
    }
    if (grp == 0) MERGE(0) else MERGE(1)
    STAMP(9);
    unsigned grp2 = grp;
    asm volatile("" : "+s"(grp2));
    if (grp2 == 0) k3_body<0>(k3, x, wo, ws, out, lds, wid, tag, ks, qt, mt, FL_C + (pi * 32 + head) * 8);
    else k3_body<1>(k3, x, wo, ws, out, lds, wid, tag, ks, qt, mt, FL_C + (pi * 32 + head) * 8);
#undef MERGE
#undef REBASE
#undef EXP_PACK
#undef PHASE_V
#undef PHASE_M
#undef EARLY_K
#undef VOFF
#undef CTX_VO
#undef KOFF
}

extern "C" void kernel_launch(void* const* d_in, const int* in_sizes, int n_in, void* d_out, int out_size, void* d_ws, size_t ws_size,
                              hipStream_t stream) {
    const char* x = (const char*)d_in[0];
    const char* ckv = (const char*)d_in[1];
    const char* wuk = (const char*)d_in[3];
    const char* wuv = (const char*)d_in[4];
    char* ws = (char*)d_ws;
    (void)hipFuncSetAttribute((const void*)fused, hipFuncAttributeMaxDynamicSharedMemorySize, 163840);
    fused<<<256, 512, 163840, stream>>>(x, ckv, wuk, wuv, (const char*)d_in[2], (const char*)d_in[7], (const char*)d_in[8], ws, (const char*)d_in[5], (const char*)d_in[9], (char*)d_out);
}
```

```cpp
#include <hip/hip_runtime.h>
#include <hip/hip_bf16.h>
#include <stdint.h>

typedef __bf16 bf16x8 __attribute__((ext_vector_type(8)));
typedef __bf16 bf16x2 __attribute__((ext_vector_type(2)));
typedef short s16x4 __attribute__((ext_vector_type(4)));
typedef short s16x8 __attribute__((ext_vector_type(8)));
typedef short v4i16_t __attribute__((ext_vector_type(4)));
typedef float f32x16 __attribute__((ext_vector_type(16)));
typedef float f32x4 __attribute__((ext_vector_type(4)));
typedef float f32x2 __attribute__((ext_vector_type(2)));
typedef unsigned u32x4 __attribute__((ext_vector_type(4)));
typedef unsigned u32x2 __attribute__((ext_vector_type(2)));
typedef __attribute__((address_space(3))) const char* lds_cptr;
typedef __attribute__((address_space(3))) char* lds_ptr;

#define DI __device__ __forceinline__
#define SBAR() __builtin_amdgcn_sched_barrier(0)
#define MFMA(a, b, c) __builtin_amdgcn_mfma_f32_16x16x32_bf16((a), (b), (c), 0, 0, 0)
#define PIN(x) asm volatile("" : "+v"(x))
#define PINR(x) asm volatile("" ::"v"(x))

constexpr int S_ = 2048, NH = 32, HD = 128, DM = 640, DC = 512, FF = 1728;
constexpr int NKS_X = DM / 32;
constexpr int NKS_C = (NH * HD) / 32;
constexpr int NKS_H = FF / 32;
constexpr int NKS_CH = NKS_C + NKS_H;
constexpr size_t WS_XF = 0;
constexpr size_t WS_KF = WS_XF + (size_t)128 * NKS_X * 1024;
constexpr size_t WS_VF = WS_KF + (size_t)NH * 128 * 4 * 1024;
constexpr size_t WS_CH = WS_VF + (size_t)NH * 64 * 8 * 1024;
constexpr size_t WS_QH = WS_CH + (size_t)128 * NKS_CH * 1024;
constexpr size_t WS_PP = WS_QH + (size_t)256 * 32768;
constexpr size_t PP_BYTES = (size_t)64 * 8 * 8 * 5 * 1024;
constexpr size_t WS_FL = WS_PP + PP_BYTES;
constexpr size_t FL_EPOCH = 0;
constexpr size_t FL_X = 256;
constexpr size_t FL_KV = FL_X + 2048;
constexpr size_t FL_H = FL_KV + 2048;
constexpr size_t FL_C = FL_H + 4096;
constexpr size_t FL_S = 16384;
constexpr size_t FL_B = FL_S + 32768;
constexpr size_t WS_END = WS_FL + FL_B + 32768;

DI unsigned rfl(unsigned x) { return __builtin_amdgcn_readfirstlane(x); }
DI unsigned flane() { unsigned r; asm volatile("v_mbcnt_lo_u32_b32 %0, -1, 0\n\tv_mbcnt_hi_u32_b32 %0, -1, %0" : "=v"(r)); return r; }
#define STAMP(i)
#define TICK(acc)
#define PJSTAMP(i)
DI const char* uptr(const char* p) { const unsigned long long v = (unsigned long long)p; const unsigned lo = rfl((unsigned)v), hi = rfl((unsigned)(v >> 32)); return (const char*)(((unsigned long long)hi << 32) | lo); }
DI unsigned cvtpk(float lo, float hi) { f32x2 v = {lo, hi}; bf16x2 b = __builtin_convertvector(v, bf16x2); return __builtin_bit_cast(unsigned, b); }
DI bf16x8 lds_frag(lds_cptr p) { return *(const __attribute__((address_space(3))) bf16x8*)p; }
DI s16x4 lds_tr(lds_cptr p) { return __builtin_bit_cast(s16x4, __builtin_amdgcn_ds_read_tr16_b64_v4i16((__attribute__((address_space(3))) v4i16_t*)p)); }
DI bf16x8 tr_frag(lds_cptr p0, lds_cptr p1) { s16x4 lo = lds_tr(p0), hi = lds_tr(p1); s16x8 v = __builtin_shufflevector(lo, hi, 0, 1, 2, 3, 4, 5, 6, 7); return __builtin_bit_cast(bf16x8, v); }
template <int OFF> DI void dma1(unsigned m0v, const void* sbase, unsigned voff) {
    asm volatile("s_mov_b32 m0, %0\n\ts_nop 0\n\tglobal_load_lds_dwordx4 %1, %2 offset:%3" ::"s"(m0v), "v"(voff), "s"(sbase), "i"(OFF) : "memory");
}
DI u32x4 pack44(const f32x4& a, const f32x4& b) { u32x4 w; w[0] = cvtpk(a[0], a[1]); w[1] = cvtpk(a[2], a[3]); w[2] = cvtpk(b[0], b[1]); w[3] = cvtpk(b[2], b[3]); return w; }
DI u32x4 pack44s(const f32x4& a, const f32x4& b, float sc) { u32x4 w; w[0] = cvtpk(a[0] * sc, a[1] * sc); w[1] = cvtpk(a[2] * sc, a[3] * sc); w[2] = cvtpk(b[0] * sc, b[1] * sc); w[3] = cvtpk(b[2] * sc, b[3] * sc); return w; }
DI u32x4 pack8(const f32x16& a, int b, float sc) {
    u32x4 w; w[0] = cvtpk(a[b + 0] * sc, a[b + 1] * sc); w[1] = cvtpk(a[b + 2] * sc, a[b + 3] * sc);
    w[2] = cvtpk(a[b + 4] * sc, a[b + 5] * sc); w[3] = cvtpk(a[b + 6] * sc, a[b + 7] * sc); return w;
}
DI u32x4 pack8n(const f32x16& a, int b) {
    u32x4 w; w[0] = cvtpk(a[b + 0], a[b + 1]); w[1] = cvtpk(a[b + 2], a[b + 3]); w[2] = cvtpk(a[b + 4], a[b + 5]); w[3] = cvtpk(a[b + 6], a[b + 7]); return w;
}
DI float bf2f(unsigned short u) { return __uint_as_float((unsigned)u << 16); }
DI float bflo(unsigned w) { return __uint_as_float(w << 16); }
DI float bfhi(unsigned w) { return __uint_as_float(w & 0xffff0000u); }
DI float xhalf_sum(float v) { auto r = __builtin_amdgcn_permlane32_swap(__float_as_uint(v), __float_as_uint(v), false, false); return __uint_as_float(r[0]) + __uint_as_float(r[1]); }
DI float xhalf_max(float v) { auto r = __builtin_amdgcn_permlane32_swap(__float_as_uint(v), __float_as_uint(v), false, false); return __builtin_fmaxf(__uint_as_float(r[0]), __uint_as_float(r[1])); }

DI void warm_load(unsigned& d, const char* wa, const char* wb, unsigned id) {
    const unsigned j = id - 1280u;
    const char* p = uptr(id < 1280u ? wa + (size_t)id * 4096 : wb + (size_t)(j < 539u ? j : 539u) * 4096);
    const unsigned voff = flane() * 64;
    asm volatile("s_nop 4\n\tglobal_load_dword %0, %1, %2" : "=&v"(d) : "v"(voff), "s"(p) : "memory");
}
DI void warm_lds(unsigned m0v, const char* wa, const char* wb, unsigned id) {
    const unsigned j = id - 1280u;
    const char* p = uptr(id < 1280u ? wa + (size_t)id * 4096 : wb + (size_t)(j < 539u ? j : 539u) * 4096);
    const unsigned voff = flane() * 64;
    asm volatile("s_mov_b32 m0, %0\n\ts_nop 4\n\tglobal_load_lds_dword %1, %2" ::"s"(m0v), "v"(voff), "s"(p) : "memory");
}

DI void mfma_acc(f32x4& acc, const bf16x8& a, const bf16x8& b) { asm volatile("v_mfma_f32_16x16x32_bf16 %0, %1, %2, %0" : "+v"(acc) : "v"(a), "v"(b)); }
DI void mfma_acc_s(f32x4& acc, const bf16x8& a, const bf16x8& b) { asm volatile("s_nop 1\n\tv_mfma_f32_16x16x32_bf16 %0, %1, %2, %0" : "+v"(acc) : "v"(a), "v"(b)); }
DI void mfma32_acc(f32x16& acc, const bf16x8& a, const bf16x8& b) { asm volatile("v_mfma_f32_32x32x16_bf16 %0, %1, %2, %0" : "+v"(acc) : "v"(a), "v"(b)); }
DI void mfma32_ini(f32x16& d, const bf16x8& a, const bf16x8& b, const f32x16& c) { asm volatile("v_mfma_f32_32x32x16_bf16 %0, %1, %2, %3" : "=&v"(d) : "v"(a), "v"(b), "v"(c)); }
DI float v_exp(float x) { float r; asm volatile("v_exp_f32 %0, %1" : "=v"(r) : "v"(x)); return r; }
DI float v_add(float a, float b) { float r; asm volatile("v_add_f32 %0, %1, %2" : "=v"(r) : "v"(a), "v"(b)); return r; }
DI unsigned v_cvt(float lo, float hi) { unsigned r; asm volatile("v_cvt_pk_bf16_f32 %0, %1, %2" : "=v"(r) : "v"(lo), "v"(hi)); return r; }
DI void set_m0(unsigned v) { asm volatile("s_mov_b32 m0, %0" ::"s"(v) : "memory"); }
template <int OFF> DI void dma_nom0(const void* sbase, unsigned voff) { asm volatile("global_load_lds_dwordx4 %0, %1 offset:%2" ::"v"(voff), "s"(sbase), "i"(OFF) : "memory"); }
template <int OFF> DI void dma_sc1(const void* sbase, unsigned voff) { asm volatile("global_load_lds_dwordx4 %0, %1 offset:%2 sc1" ::"v"(voff), "s"(sbase), "i"(OFF) : "memory"); }
template <int OFF> DI void dma1_sc1(unsigned m0v, const void* sbase, unsigned voff) {
    asm volatile("s_mov_b32 m0, %0\n\ts_nop 0\n\tglobal_load_lds_dwordx4 %1, %2 offset:%3 sc1" ::"s"(m0v), "v"(voff), "s"(sbase), "i"(OFF) : "memory");
}
DI void dma4(unsigned m0v, const void* sbase, unsigned voff) {
    asm volatile("s_mov_b32 m0, %0\n\ts_nop 0\n\tglobal_load_lds_dwordx4 %1, %2 sc1\n\tglobal_load_lds_dwordx4 %1, %2 offset:1024 sc1\n\tglobal_load_lds_dwordx4 %1, %2 offset:2048 sc1\n\tglobal_load_lds_dwordx4 %1, %2 offset:3072 sc1"
                 ::"s"(m0v), "v"(voff), "s"(sbase) : "memory");
}

typedef unsigned long long u64;
DI void st_wt(char* p, const u32x4& v) { asm volatile("global_store_dwordx4 %0, %1, off sc1\n\ts_nop 1" ::"v"(p), "v"(v) : "memory"); }
DI void st_wt_o(const char* sbase, unsigned voff, const u32x4& v) { asm volatile("global_store_dwordx4 %0, %1, %2 sc1\n\ts_nop 1" ::"v"(voff), "v"(v), "s"(sbase) : "memory"); }
template <int OFF> DI void st_wt_i(const char* sbase, unsigned voff, const u32x4& v) {
    asm volatile("global_store_dwordx4 %0, %1, %2 offset:%3 sc1\n\ts_nop 1" ::"v"(voff), "v"(v), "s"(sbase), "i"(OFF) : "memory");
}
DI void ld_wt(u32x4& d, const char* sbase, unsigned voff) { asm volatile("global_load_dwordx4 %0, %1, %2 sc1" : "=&v"(d) : "v"(voff), "s"(sbase) : "memory"); }
DI void ld_wt3(unsigned skip, u32x4& a, u32x4& b, u32x4& c, const char* p0, const char* p1, const char* p2, unsigned voff) {
    asm volatile("s_cmp_lg_u32 %3, 0\n\ts_cbranch_scc1 .Lw%=\n\tglobal_load_dwordx4 %0, %4, %5 sc1\n\tglobal_load_dwordx4 %1, %4, %6 sc1\n\tglobal_load_dwordx4 %2, %4, %7 sc1\n.Lw%=:"
                 : "+v"(a), "+v"(b), "+v"(c) : "s"(skip), "v"(voff), "s"(p0), "s"(p1), "s"(p2) : "scc", "memory");
}
DI void flag_set(unsigned wid, const char* sbase, unsigned off, u64 tag) {
    const u32x2 v = u32x2{(unsigned)tag, (unsigned)(tag >> 32)};
    u64 ex;
    asm volatile("s_cmp_lg_u32 %1, 0\n\ts_cbranch_scc1 .Lfs%=\n\ts_mov_b64 %0, exec\n\ts_mov_b64 exec, 1\n\t"
                 "global_store_dwordx2 %2, %3, %4 sc1\n\ts_mov_b64 exec, %0\n.Lfs%=:\n\ts_nop 1"
                 : "=&s"(ex) : "s"(wid), "v"(off), "v"(v), "s"(sbase) : "scc", "memory");
}
DI void flag_ld(u32x2& f, const char* sbase, unsigned voff) { asm volatile("global_load_dwordx2 %0, %1, %2 sc1" : "=&v"(f) : "v"(voff), "s"(sbase) : "memory"); }
DI void flag_ld0(unsigned skip, u32x2& f, const char* sbase, unsigned voff) {
    asm volatile("s_cmp_lg_u32 %1, 0\n\ts_cbranch_scc1 .Lfz%=\n\tglobal_load_dwordx2 %0, %2, %3 sc1\n.Lfz%=:" : "+v"(f) : "s"(skip), "v"(voff), "s"(sbase) : "scc", "memory");
}
template <bool FLY>
DI void flag_wait(unsigned skip, u32x2& f, const char* sbase, unsigned voff, u64 tag) {
    unsigned cnt;
    asm volatile("s_cmp_lg_u32 %2, 0\n\ts_cbranch_scc1 .Lfe%=\n\t.if %6\n\ts_waitcnt vmcnt(0)\n\t.endif\n\t"
                 "v_cmp_ne_u64 vcc, %3, %0\n\ts_cbranch_vccz .Lfd%=\n\ts_mov_b32 %1, 0x40000\n"
                 ".Lfl%=:\n\tglobal_load_dwordx2 %0, %4, %5 sc1\n\ts_waitcnt vmcnt(0)\n\tv_cmp_ne_u64 vcc, %3, %0\n\ts_cbranch_vccz .Lfd%=\n\t"
                 "s_sleep 2\n\ts_sub_u32 %1, %1, 1\n\ts_cbranch_scc0 .Lfl%=\n"
                 ".Lfd%=:\n"
                 ".Lfe%=:"
                 : "+v"(f), "=&s"(cnt) : "s"(skip), "s"(tag), "v"(voff), "s"(sbase), "i"(FLY ? 1 : 0) : "vcc", "scc", "memory");
}

constexpr int WQ_PIECE = 1040, W1_PIECE = 1056, W1_OFF = 16 * WQ_PIECE, PSLOT = W1_OFF + 8 * W1_PIECE;
struct PJ { unsigned wqvo, w1vo, xv0, xv1; const char *wqs, *w1s, *xs; };
DI PJ pj_setup(const char* wq, const char* w1, const char* ws, unsigned wid, int head, int f0, bool nft4, int q16_0) {
    const unsigned lane = flane();
    PJ j;
    j.wqvo = ((lane >> 2) & 3) * 8192 + (lane >> 4) * 64 + (lane & 3) * 16;
    j.w1vo = ((lane >> 2) & 7) * 3456 + (nft4 ? (lane >> 5) * 64 : 0) + (lane & 3) * 16;
    j.wqs = uptr(wq + (size_t)wid * 8 * 8192 + head * 256);
    j.w1s = uptr(w1 + (size_t)wid * 8 * 3456 + f0 * 2);
    j.xs = uptr(ws + WS_XF + (size_t)q16_0 * NKS_X * 1024);
    j.xv0 = lane * 16; j.xv1 = lane * 16 + NKS_X * 1024;
    return j;
}
DI void pj_dma(const PJ& j, unsigned lds0, unsigned wid, int c, int slot) {
    dma1<0>(lds0 + slot + (2 * wid) * WQ_PIECE, j.wqs + (size_t)c * 64 * 8192, j.wqvo);
    dma1<0>(lds0 + slot + (2 * wid + 1) * WQ_PIECE, j.wqs + (size_t)c * 64 * 8192 + 4 * 8192, j.wqvo);
    dma1<0>(lds0 + slot + W1_OFF + wid * W1_PIECE, j.w1s + (size_t)c * 64 * 3456, j.w1vo);
}
DI void pj_dma1(const PJ& j, unsigned lds0, unsigned wid, int c, int slot, int i) {
    if (i < 2) dma1<0>(lds0 + slot + (2 * wid + i) * WQ_PIECE, j.wqs + (size_t)c * 64 * 8192 + i * 4 * 8192, j.wqvo);
    else dma1<0>(lds0 + slot + W1_OFF + wid * W1_PIECE, j.w1s + (size_t)c * 64 * 3456, j.w1vo);
}
DI void pj_xload1(const PJ& j, u32x4& X, int c, int i) {
    const char* p_ = j.xs + c * 2048 + (i >> 1) * 1024;
    asm volatile("global_load_dwordx4 %0, %1, %2 sc1" : "=&v"(X) : "v"((i & 1) ? j.xv1 : j.xv0), "s"(p_) : "memory");
}
struct KVO { const char* base; unsigned lo[2]; };
DI void kv_store(const KVO& o, const u32x4& v, int i) {
    if (i & 2) st_wt_i<2048>(o.base + (i >> 2) * 8192, o.lo[i & 1], v); else st_wt_i<0>(o.base + (i >> 2) * 8192, o.lo[i & 1], v);
}
DI void pj_xload(const PJ& j, u32x4 (&X)[4], int c) {
    const char* p_ = j.xs + c * 2048;
    asm volatile("global_load_dwordx4 %0, %4, %6 sc1\n\tglobal_load_dwordx4 %1, %5, %6 sc1\n\tglobal_load_dwordx4 %2, %4, %6 offset:1024 sc1\n\tglobal_load_dwordx4 %3, %5, %6 offset:1024 sc1"
                 : "=&v"(X[0]), "=&v"(X[1]), "=&v"(X[2]), "=&v"(X[3]) : "v"(j.xv0), "v"(j.xv1), "s"(p_) : "memory");
}

template <bool VT, class F>
DI void k1_mma(f32x4 (&acc)[8][4], int half, const bf16x8 (&c)[4], const bf16x8 (&w)[4], F&& filler) {
#pragma unroll
    for (int rt = 0; rt < 4; ++rt)
#pragma unroll
        for (int nt = 0; nt < 4; ++nt) {
            if (half == 0) acc[rt][nt] = VT ? MFMA(c[rt], w[nt], acc[rt][nt]) : MFMA(w[nt], c[rt], acc[rt][nt]);
            else acc[4 + rt][nt] = VT ? MFMA(c[rt], w[nt], acc[4 + rt][nt]) : MFMA(w[nt], c[rt], acc[4 + rt][nt]);
            filler(rt * 4 + nt);
        }
}

template <bool VT>
DI void k1_tile(const char* __restrict__ Abase, const char* __restrict__ Wbase, char* __restrict__ outp, char* lds, const unsigned (&avo)[4], const unsigned (&wvo)[4],
                unsigned wid, int hp, int rb, char* __restrict__ ws, const char* __restrict__ wq, const char* __restrict__ w1, const char* __restrict__ b1, u64 tag,
                int head2, int f0, bool nft4, int q16_0, u32x4 (&xa)[4], u32x2 (&bb)[4], u32x4 (&kv)[16], KVO& kvo) {
    char* stamp_base = ws; (void)stamp_base;
    const char* flb = uptr(ws + WS_FL);
    const unsigned wm = wid >> 2, wn = wid & 3;
    const unsigned lds0 = (unsigned)(uintptr_t)lds;
    unsigned aaddr[2], baddr[4];
    {
        const unsigned lane = flane(), c = lane & 15, g = lane >> 4;
        const unsigned x = (c >> 1) & 7;
#pragma unroll
        for (int s = 0; s < 2; ++s) aaddr[s] = (128 * wm + c) * 128 + (((4 * s + g) ^ x) << 4);
        const unsigned q = (lane & 15) >> 2, p = lane & 3, f = q | ((g & 1) << 2);
#pragma unroll
        for (int nt = 0; nt < 4; ++nt) baddr[nt] = 32768 + (8 * g + q) * 512 + (((8 * wn + 2 * nt + (p >> 1)) ^ (f << 1)) << 4) + ((p & 1) << 3);
    }
    f32x4 acc[8][4];
#pragma unroll
    for (int i = 0; i < 8; ++i)
#pragma unroll
        for (int j = 0; j < 4; ++j) acc[i][j] = f32x4{};
    bf16x8 cf[2][4], wf[2][4];
    lds_cptr L = (lds_cptr)lds;
#define K1_RC(buf, stg, s, half) do { _Pragma("unroll") for (int rt = 0; rt < 4; ++rt) cf[buf][rt] = lds_frag(L + (stg) + aaddr[s] + ((half) * 4 + rt) * 2048); } while (0)
#define K1_RW(buf, stg, s) do { _Pragma("unroll") for (int nt = 0; nt < 4; ++nt) wf[buf][nt] = tr_frag(L + (stg) + baddr[nt] + (s) * 16384, L + (stg) + baddr[nt] + (s) * 16384 + 2048); } while (0)
    const char* Aw = Abase + (size_t)wid * 32 * 1024;
    const char* Ww = Wbase + (size_t)wid * 8 * 8192;
    const unsigned m0a = lds0 + wid * 4096, m0w = lds0 + 32768 + wid * 4096;
#define K1_DMA(kt, stg)                                                                   \
    do {                                                                                  \
        const char* a_ = Aw + (kt) * 128; const char* w_ = Ww + (size_t)(kt) * 64 * 8192; \
        dma1<0>(m0a + (stg), a_, avo[0]); dma1<1024>(m0a + (stg), a_, avo[1]); dma1<2048>(m0a + (stg), a_, avo[2]); dma1<3072>(m0a + (stg), a_, avo[3]); \
        dma1<0>(m0w + (stg), w_, wvo[0]); dma1<1024>(m0w + (stg), w_, wvo[1]); dma1<2048>(m0w + (stg), w_, wvo[2]); dma1<3072>(m0w + (stg), w_, wvo[3]); \
    } while (0)
    unsigned pfd = 0;
    u32x2 fx = u32x2{};
#define K1_FXO() (FL_X + (2 * q16_0 + (flane() & 3)) * 8)
    STAMP(1);
    asm volatile("s_waitcnt vmcnt(10)\n\ts_barrier" ::: "memory");
    STAMP(2);
    K1_RW(0, 0, 0); K1_RC(0, 0, 0, 0);
#define K1_RC1(buf, stg, s, half, rt) cf[buf][rt] = lds_frag(L + (stg) + aaddr[s] + ((half) * 4 + (rt)) * 2048)
#define K1_RW1(buf, stg, s, nt) wf[buf][nt] = tr_frag(L + (stg) + baddr[nt] + (s) * 16384, L + (stg) + baddr[nt] + (s) * 16384 + 2048)
#pragma unroll
    for (int kt = 0; kt < 8; ++kt) {
        const unsigned stg = (kt & 1) * 65536, nstg = stg ^ 65536;
        SBAR();
        k1_mma<VT>(acc, 0, cf[0], wf[0], [&](int m) { if (m < 8 && (m & 1) == 0) { K1_RC1(1, stg, 0, 1, m >> 1); SBAR(); } });
        SBAR();
        k1_mma<VT>(acc, 1, cf[1], wf[0], [&](int m) { if (m < 4) { K1_RC1(0, stg, 1, 0, m); SBAR(); } else if (m < 12 && (m & 1) == 0) { K1_RW1(1, stg, 1, (m - 4) >> 1); SBAR(); } });
        SBAR();
        k1_mma<VT>(acc, 0, cf[0], wf[1], [&](int m) { if (m < 8 && (m & 1) == 0) { K1_RC1(1, stg, 1, 1, m >> 1); SBAR(); } });
        SBAR();
        if (kt == 7) asm volatile("s_waitcnt vmcnt(0) lgkmcnt(0)\n\ts_barrier" : "+v"(pfd)::"memory");
        else asm volatile("s_waitcnt vmcnt(0) lgkmcnt(0)\n\ts_barrier" : "+v"(fx)::"memory");
        if (kt == 0) flag_set(wid, flb, FL_X + blockIdx.x * 8, tag);
        if (kt == 6) flag_wait<false>(0, fx, flb, K1_FXO(), tag);
        const char* a_ = Aw + (kt + 2) * 128; const char* w_ = Ww + (size_t)(kt + 2) * 64 * 8192;
        k1_mma<VT>(acc, 1, cf[1], wf[1], [&](int m) {
            if (kt + 1 < 8) { if (m < 4) { K1_RW1(0, nstg, 0, m); SBAR(); } else if (m < 8) { K1_RC1(0, nstg, 0, 0, m - 4); SBAR(); } }
            if (kt + 2 < 8) {
                if (m == 1) { dma1<0>(m0a + stg, a_, avo[0]); SBAR(); } if (m == 3) { dma1<1024>(m0a + stg, a_, avo[1]); SBAR(); }
                if (m == 5) { dma1<2048>(m0a + stg, a_, avo[2]); SBAR(); } if (m == 7) { dma1<3072>(m0a + stg, a_, avo[3]); SBAR(); }
                if (m == 9) { dma1<0>(m0w + stg, w_, wvo[0]); SBAR(); } if (m == 11) { dma1<1024>(m0w + stg, w_, wvo[1]); SBAR(); }
                if (m == 13) { dma1<2048>(m0w + stg, w_, wvo[2]); SBAR(); } if (m == 15) { dma1<3072>(m0w + stg, w_, wvo[3]); SBAR(); }
            }
            if (kt == 5 && m == 15) {
                warm_load(pfd, wq, w1, blockIdx.x * 8 + wid);
                SBAR();
            }
            if (kt == 4 && m == 14) { flag_ld(fx, flb, K1_FXO()); SBAR(); }
            if (kt == 6 && m == 2) {
                const PJ j = pj_setup(wq, w1, ws, wid, head2, f0, nft4, q16_0); pj_dma(j, lds0, wid, 0, 0); pj_dma(j, lds0, wid, 1, PSLOT); SBAR();
            }
            if (kt == 7 && m == 0) { const PJ j = pj_setup(wq, w1, ws, wid, head2, f0, nft4, q16_0); asm volatile("s_nop 4"); pj_xload(j, xa, 0); SBAR(); }
            if (kt == 7 && m == 2) {
                const char* bp = uptr(b1 + f0 * 2);
                const unsigned bo = (flane() >> 4) * 8, b2 = bo + (nft4 ? 64 : 0);
                asm volatile("s_nop 4\n\tglobal_load_dwordx2 %0, %4, %6\n\tglobal_load_dwordx2 %1, %4, %6 offset:32\n\tglobal_load_dwordx2 %2, %5, %6\n\tglobal_load_dwordx2 %3, %5, %6 offset:32"
                             : "=&v"(bb[0]), "=&v"(bb[1]), "=&v"(bb[2]), "=&v"(bb[3]) : "v"(bo), "v"(b2), "s"(bp) : "memory");
                SBAR();
            }
        });
        SBAR();
    }
    STAMP(3);
    const int head = 2 * hp + (wn >> 1), d0 = (wn & 1) * 64;
    const int key0 = 256 * rb + 128 * wm;
    const unsigned lane = flane(), c = lane & 15, g = lane >> 4;
    kvo.base = uptr(outp + ((size_t)(head * 64 + (key0 >> 5)) * 4 + (d0 >> 5)) * 2048);
    kvo.lo[0] = (g >> 1) * 1024 + ((VT ? c : (c & 7) + 16 * (c >> 3)) + 32 * (g & 1)) * 16;
    kvo.lo[1] = kvo.lo[0] + (VT ? 256 : 128);
#pragma unroll
    for (int i = 0; i < 16; ++i) {
        const int u = i >> 2, a = (i >> 1) & 1, b2 = i & 1;
        kv[i] = VT ? pack44(acc[2 * u][2 * a + b2], acc[2 * u + 1][2 * a + b2]) : pack44(acc[2 * u + b2][2 * a], acc[2 * u + b2][2 * a + 1]);
    }
    asm volatile("s_waitcnt vmcnt(0)" : "+v"(xa[0]), "+v"(xa[1]), "+v"(xa[2]), "+v"(xa[3]), "+v"(bb[0]), "+v"(bb[1]), "+v"(bb[2]), "+v"(bb[3])::"memory");
    STAMP(5);
#undef K1_RC
#undef K1_RW
#undef K1_DMA
#undef K1_FXO
}

constexpr float QSCALE = 0.08838834764831845f * 1.4426950408889634f;
constexpr float THR = 8.0f;
DI float x4_max(float x) {
    auto s = __builtin_amdgcn_permlane16_swap(__float_as_uint(x), __float_as_uint(x), false, false);
    x = __builtin_fmaxf(__uint_as_float(s[0]), __uint_as_float(s[1]));
    auto t = __builtin_amdgcn_permlane32_swap(__float_as_uint(x), __float_as_uint(x), false, false);
    return __builtin_fmaxf(__uint_as_float(t[0]), __uint_as_float(t[1]));
}
DI float x4_sum(float x) {
    auto s = __builtin_amdgcn_permlane16_swap(__float_as_uint(x), __float_as_uint(x), false, false);
    x = __uint_as_float(s[0]) + __uint_as_float(s[1]);
    auto t = __builtin_amdgcn_permlane32_swap(__float_as_uint(x), __float_as_uint(x), false, false);
    return __uint_as_float(t[0]) + __uint_as_float(t[1]);
}
DI float mish_f(float h) {
    const float e = __builtin_amdgcn_exp2f(__builtin_fminf(h, 30.0f) * 1.4426950408889634f);
    const float n = e * (e + 2.0f);
    return h * n * __builtin_amdgcn_rcpf(n + 2.0f);
}

#define SPC 4
#define KVP0 7
#define KVPS 2
template <int NFT>
DI void proj_phase(const char* __restrict__ wq, const char* __restrict__ w1, char* __restrict__ ws, char* lds, int head, int f0, int q16_0, bf16x8 (&qf)[2][4],
                   const char* __restrict__ wo, const char* __restrict__ w2, unsigned m0v0, unsigned wid, u32x4 (&xa)[4], u32x4 (&xb)[4], const u32x2 (&bb)[4], u64 tag,
                   unsigned kvflag, int hp2, unsigned m0k0, unsigned m0k1, const char* kptr0, const char* kptr1, const u32x4 (&kv)[16], const KVO& kvo) {
    const unsigned lane = flane(), g = lane >> 4;
    char* stamp_base = ws; (void)stamp_base;
    const unsigned lds0 = (unsigned)(uintptr_t)lds;
    lds_cptr L = (lds_cptr)lds;
    const PJ pj = pj_setup(wq, w1, ws, wid, head, f0, NFT == 4, q16_0);
    const char* flb = uptr(ws + WS_FL);
    u32x2 fkv = u32x2{};
    const unsigned fkvo = FL_KV + (hp2 * 16 + (lane & 15)) * 8;
    const unsigned q = (lane & 15) >> 2, p = lane & 3;
    const unsigned wqb = 2 * g * WQ_PIECE + q * 64 + p * 8;
    const unsigned w1b = W1_OFF + g * W1_PIECE + q * 64 + p * 8;
    f32x4 aq[8][2], ah[NFT][2];
#pragma unroll
    for (int i = 0; i < 8; ++i) { aq[i][0] = f32x4{}; aq[i][1] = f32x4{}; }
#pragma unroll
    for (int i = 0; i < NFT; ++i) { ah[i][0] = f32x4{}; ah[i][1] = f32x4{}; }
    constexpr int NT = 8 + NFT;
    bf16x8 wf[4];
#define PJ_RD(slot, s, n)                                                                                       \
    (((n) < 8) ? tr_frag(L + (slot) + wqb + (8 * (s)) * WQ_PIECE + ((n) >> 1) * 256 + ((n) & 1) * 32, L + (slot) + wqb + (8 * (s) + 1) * WQ_PIECE + ((n) >> 1) * 256 + ((n) & 1) * 32) \
               : tr_frag(L + (slot) + w1b + (4 * (s)) * W1_PIECE + (((n) - 8) >> 1) * 512 + ((n) & 1) * 32, L + (slot) + w1b + (4 * (s)) * W1_PIECE + 256 + (((n) - 8) >> 1) * 512 + ((n) & 1) * 32))
#pragma unroll
    for (int n = 0; n < 3; ++n) wf[n] = PJ_RD(0, 0, n);
#define PJ_CHUNK(c, XC, XN)                                                                                     \
    do {                                                                                                        \
        constexpr int slot_ = ((c) % 3) * PSLOT, nslot_ = (((c) + 1) % 3) * PSLOT, dslot_ = (((c) + 2) % 3) * PSLOT; \
          \
        if ((c) == 9) flag_wait<false>(0, fkv, flb, fkvo, tag);                                                 \
        SBAR();                                                                                                 \
        constexpr int nst_ = 16 - SPC * (c) < 0 ? 0 : 16 - SPC * (c) < SPC ? 16 - SPC * (c) : SPC;        \
        _Pragma("unroll") for (int m = 0; m < 2 * NT; ++m) {                                                    \
            const int s = m / NT, n = m % NT;                                                                   \
            if (m == 2 * NT - 3) {       \
                if ((c) + 2 < 10) asm volatile("s_waitcnt vmcnt(%4) lgkmcnt(0)\n\ts_barrier" : "+v"(XN[0]), "+v"(XN[1]), "+v"(XN[2]), "+v"(XN[3]) : "i"(3 + nst_) : "memory"); \
                else if ((c) + 1 < 10) asm volatile("s_waitcnt vmcnt(0) lgkmcnt(0)\n\ts_barrier" : "+v"(XN[0]), "+v"(XN[1]), "+v"(XN[2]), "+v"(XN[3]), "+v"(fkv)::"memory"); \
                if (nst_ == 0 && 16 - SPC * ((c) - 1) > 0) flag_set(wid, flb, kvflag, tag);        \
            }                                                                                                   \
            const int m3 = m + 3;                                                                               \
            if (m3 < 2 * NT) wf[m3 & 3] = PJ_RD(slot_, m3 / NT, m3 % NT);                                       \
            else if ((c) + 1 < 10) wf[m3 & 3] = PJ_RD(nslot_, 0, m3 - 2 * NT);                                  \
            const bf16x8 x0 = __builtin_bit_cast(bf16x8, XC[2 * s]), x1 = __builtin_bit_cast(bf16x8, XC[2 * s + 1]); \
            if (n < 8) { aq[n & 7][0] = MFMA(wf[m & 3], x0, aq[n & 7][0]); aq[n & 7][1] = MFMA(wf[m & 3], x1, aq[n & 7][1]); } \
            else { ah[(n - 8) % NFT][0] = MFMA(wf[m & 3], x0, ah[(n - 8) % NFT][0]); ah[(n - 8) % NFT][1] = MFMA(wf[m & 3], x1, ah[(n - 8) % NFT][1]); } \
            SBAR();                                                                                             \
                       \
            if ((c) + 1 < 10 && m < 4) pj_xload1(pj, XN[m & 3], (c) + 1, m);                                    \
            if ((c) + 2 < 10 && m >= 4 && m < 7) pj_dma1(pj, lds0, wid, (c) + 2, dslot_, m - 4);                \
            if (m >= KVP0 && (m - KVP0) % KVPS == 0 && (m - KVP0) / KVPS < nst_) { const int i_ = (SPC * (c) + (m - KVP0) / KVPS) & 15; kv_store(kvo, kv[i_], i_); } \
            if ((c) == 8 && m == NT) flag_ld(fkv, flb, fkvo);                                                   \
            if ((c) == 9 && m < 16 && !(m & 1)) {                           \
                const int i_ = m >> 1;                                                                          \
                if ((i_ & 3) == 0) dma1_sc1<0>(i_ < 4 ? m0k0 : m0k1, i_ < 4 ? kptr0 : kptr1, lane * 16);            \
                if ((i_ & 3) == 1) dma1_sc1<1024>(i_ < 4 ? m0k0 : m0k1, i_ < 4 ? kptr0 : kptr1, lane * 16);         \
                if ((i_ & 3) == 2) dma1_sc1<2048>(i_ < 4 ? m0k0 : m0k1, i_ < 4 ? kptr0 : kptr1, lane * 16);         \
                if ((i_ & 3) == 3) dma1_sc1<3072>(i_ < 4 ? m0k0 : m0k1, i_ < 4 ? kptr0 : kptr1, lane * 16);         \
            }                                                                                                   \
            if ((c) == 9 && m == 16) warm_lds(m0v0, wo, w2, blockIdx.x * 8 + wid);        \
            SBAR();                                                                                             \
        }                                                                                                       \
        PJSTAMP(20 + (c));                                                                                      \
    } while (0)
    PJ_CHUNK(0, xa, xb); PJ_CHUNK(1, xb, xa); PJ_CHUNK(2, xa, xb); PJ_CHUNK(3, xb, xa); PJ_CHUNK(4, xa, xb);
    PJ_CHUNK(5, xb, xa); PJ_CHUNK(6, xa, xb); PJ_CHUNK(7, xb, xa); PJ_CHUNK(8, xa, xb); PJ_CHUNK(9, xb, xa);
#undef PJ_CHUNK
#undef PJ_RD
    asm volatile("s_waitcnt vmcnt(1)" ::: "memory");
#pragma unroll
    for (int a = 0; a < 2; ++a)
#pragma unroll
        for (int v = 0; v < 4; ++v) qf[a][v] = __builtin_bit_cast(bf16x8, pack44s(aq[2 * v][a], aq[2 * v + 1][a], QSCALE));
#pragma unroll
    for (int v = 0; v < NFT / 2; ++v) {
        float bias[8];
#pragma unroll
        for (int t = 0; t < 2; ++t) {
            const u32x2 q2 = bb[2 * v + t];
            bias[4 * t + 0] = bflo(q2[0]); bias[4 * t + 1] = bfhi(q2[0]); bias[4 * t + 2] = bflo(q2[1]); bias[4 * t + 3] = bfhi(q2[1]);
        }
#pragma unroll
        for (int a = 0; a < 2; ++a) {
            f32x4 h0, h1;
#pragma unroll
            for (int i = 0; i < 4; ++i) { h0[i] = mish_f(ah[2 * v][a][i] + bias[i]); h1[i] = mish_f(ah[2 * v + 1][a][i] + bias[4 + i]); }
            st_wt_o(uptr(ws + WS_CH + ((size_t)(q16_0 + a) * NKS_CH + NKS_C + (f0 >> 5) + v) * 1024), lane * 16, pack44(h0, h1));
        }
    }
}

#define NOP_SO(S, o) asm volatile("s_nop 7\n\ts_nop 3" : "+v"(S[0]), "+v"(S[1]), "+v"(o[0]), "+v"(o[1]), "+v"(o[2]), "+v"(o[3]))
#define NOP_A(A) asm volatile("s_nop 7\n\ts_nop 1" : "+v"(A[0][0]), "+v"(A[0][1]), "+v"(A[0][2]), "+v"(A[0][3]), "+v"(A[1][0]), "+v"(A[1][1]), "+v"(A[1][2]), "+v"(A[1][3]), \
    "+v"(A[2][0]), "+v"(A[2][1]), "+v"(A[2][2]), "+v"(A[2][3]), "+v"(A[3][0]), "+v"(A[3][1]), "+v"(A[3][2]), "+v"(A[3][3]), "+v"(A[4][0]), "+v"(A[4][1]), "+v"(A[4][2]), "+v"(A[4][3]))
DI float xh_sum(float v) { auto r = __builtin_amdgcn_permlane32_swap(__float_as_uint(v), __float_as_uint(v), false, false); return __uint_as_float(r[0]) + __uint_as_float(r[1]); }
DI float xh_max(float v) { auto r = __builtin_amdgcn_permlane32_swap(__float_as_uint(v), __float_as_uint(v), false, false); return __builtin_fmaxf(__uint_as_float(r[0]), __uint_as_float(r[1])); }
DI float mul_s(float a, float b) { float r; asm("v_mul_f32 %0, %1, %2" : "=v"(r) : "v"(a), "v"(b)); return r; }
DI float sub_s(float a, float b) { float r; asm("v_sub_f32 %0, %1, %2" : "=v"(r) : "v"(a), "v"(b)); return r; }
DI u32x4 pack_o(const f32x16& a, int y, float sc) {
    u32x4 w; w[0] = cvtpk(mul_s(a[4 * y + 0], sc), mul_s(a[4 * y + 1], sc)); w[1] = cvtpk(mul_s(a[4 * y + 2], sc), mul_s(a[4 * y + 3], sc));
    w[2] = cvtpk(mul_s(a[8 + 4 * y + 0], sc), mul_s(a[8 + 4 * y + 1], sc)); w[3] = cvtpk(mul_s(a[8 + 4 * y + 2], sc), mul_s(a[8 + 4 * y + 3], sc)); return w;
}
DI u32x4 pack_o1(const f32x16& a, int y) {
    u32x4 w; w[0] = cvtpk(a[4 * y + 0], a[4 * y + 1]); w[1] = cvtpk(a[4 * y + 2], a[4 * y + 3]);
    w[2] = cvtpk(a[8 + 4 * y + 0], a[8 + 4 * y + 1]); w[3] = cvtpk(a[8 + 4 * y + 2], a[8 + 4 * y + 3]); return w;
}
DI void mask_diag(f32x16& s, int dq) {
    const float ninf = -__builtin_inff();
#pragma unroll
    for (int r0 = 0; r0 < 16; r0 += 4) {
        unsigned long long m0, m1, m2, m3;
        const int kc = 16 * ((r0 >> 2) & 1) + 8 * (r0 >> 3);
        float a = s[r0], b = s[r0 + 1], c = s[r0 + 2], d = s[r0 + 3];
        asm("v_cmp_gt_i32_e64 %4, %9, %8\n\tv_cmp_gt_i32_e64 %5, %10, %8\n\tv_cmp_gt_i32_e64 %6, %11, %8\n\tv_cmp_gt_i32_e64 %7, %12, %8\n\t"
            "v_cndmask_b32_e64 %0, %0, %13, %4\n\tv_cndmask_b32_e64 %1, %1, %13, %5\n\tv_cndmask_b32_e64 %2, %2, %13, %6\n\tv_cndmask_b32_e64 %3, %3, %13, %7"
            : "+v"(a), "+v"(b), "+v"(c), "+v"(d), "=&s"(m0), "=&s"(m1), "=&s"(m2), "=&s"(m3)
            : "v"(dq), "i"(kc), "i"(kc + 1), "i"(kc + 2), "i"(kc + 3), "v"(ninf));
        s[r0] = a; s[r0 + 1] = b; s[r0 + 2] = c; s[r0 + 3] = d;
    }
}
#define VIL 1
#define VIL0 0
#define FRW 8
#define FRA 6

#define K3DPOS {2, 3, 6, 7, 10, 14, 17}
constexpr int K3D[7] = K3DPOS;
constexpr int K3_SLOT = 36864;
constexpr int PP_SET = 8 * 5 * 1024;
DI float silu_f(float v) { return v * __builtin_amdgcn_rcpf(1.0f + __builtin_amdgcn_exp2f(-1.4426950408889634f * v)); }
DI f32x4 unpk_lo(const u32x4& w) { return f32x4{bflo(w[0]), bfhi(w[0]), bflo(w[1]), bfhi(w[1])}; }
DI f32x4 unpk_hi(const u32x4& w) { return f32x4{bflo(w[2]), bfhi(w[2]), bflo(w[3]), bfhi(w[3])}; }

struct K3S { const char *aptr, *wptr; unsigned avo, wvo[3], m0a, m0w; };
DI K3S k3_setup(unsigned lds0, unsigned wid, unsigned lane) {
    K3S k;
    k.aptr = nullptr; k.wptr = nullptr;
    k.avo = lane * 16;
    const unsigned p0 = wid >= 4 ? 12 + 2 * (wid & 3) : 3 * wid;
#pragma unroll
    for (int i = 0; i < 3; ++i) {
        const unsigned o = (p0 + i) * 1024 + lane * 16, row = o / 320, c16 = (o % 320) >> 4;
        k.wvo[i] = row * 1280 + ((c16 ^ (((row >> 2) & 1) << 1)) << 4) - 1024 * i;
    }
    k.m0a = lds0 + wid * 2048; k.m0w = lds0 + 16384 + p0 * 1024;
    return k;
}
DI void k3_point(K3S& k, const char* ws, const char* w, unsigned wid, int qt, int mt, int kstep, int stage) {
    k.aptr = uptr(ws + WS_CH + ((size_t)(8 * qt + wid) * NKS_CH + kstep) * 1024);
    k.wptr = uptr(w + (size_t)stage * 64 * 1280 + mt * 320);
    asm volatile("s_nop 4" : "+s"(k.aptr), "+s"(k.wptr));
}
DI void k3_stage(K3S& k, unsigned slot, bool three) {
    set_m0(k.m0a + slot); asm volatile("s_nop 0"); dma_sc1<0>(k.aptr, k.avo); dma_sc1<1024>(k.aptr, k.avo);
    set_m0(k.m0w + slot); asm volatile("s_nop 0"); dma_nom0<0>(k.wptr, k.wvo[0]); dma_nom0<1024>(k.wptr, k.wvo[1]);
    if (three) dma_nom0<2048>(k.wptr, k.wvo[2]);
    k.aptr += 2048; k.wptr += 81920;
}
template <int GRP>
DI void k3_body(K3S& k, const char* __restrict__ x, const char* __restrict__ wo, char* __restrict__ ws, char* __restrict__ out, char* lds, unsigned wid, u64 tag,
                int ks, int qt, int mt, unsigned cflag) {
    constexpr int P = GRP ? 4 : 5;
    const unsigned lane = flane(), c = lane & 15, g = lane >> 4;
    const unsigned w4 = wid & 3, qh = w4 >> 1, mh = w4 & 1;
    char* stamp_base = ws; (void)stamp_base;
    const char* flb = uptr(ws + WS_FL);
    lds_cptr L = (lds_cptr)lds;
    const unsigned ard = (8 * qh + GRP) * 1024 + lane * 16;
    const unsigned q = (lane & 15) >> 2, p = lane & 3;
    const unsigned wrow = 16384 + (32 * GRP + 4 * g + q) * 320 + 160 * mh + 8 * p;
    const unsigned sgn = ((g & 1) ? 32u : 0u);
    const unsigned wrdE = (mh & 1) ? wrow - sgn : wrow + sgn;
    const unsigned wrdO = (mh & 1) ? wrow + sgn : wrow - sgn;
    f32x4 acc[5][4], accB[5][4];
    bf16x8 af[2][4], wf[4], w4f[2];
    unsigned cur = 0;
#define K3_RW(slot, n) tr_frag(L + (slot) + (((n) & 1) ? wrdO : wrdE) + 32 * (n), L + (slot) + (((n) & 1) ? wrdO : wrdE) + 32 * (n) + 16 * 320)
#define K3_RA(slot, a) lds_frag(L + (slot) + ard + (a) * 2048)
#define K3_ZERO(acc) do { _Pragma("unroll") for (int n = 0; n < 5; ++n) _Pragma("unroll") for (int a = 0; a < 4; ++a) acc[n][a] = f32x4{}; NOP_A(acc); } while (0)
#define K3_ITER(acc, PAR, WN, RD, DM, SAFE, HOOK)                                                                       \
    do {                                                                                                                \
        if ((WN) >= 0) asm volatile("s_waitcnt vmcnt(%0) lgkmcnt(0)\n\ts_barrier" ::"i"((WN) >= 0 ? (WN) : 0) : "memory");   \
        else asm volatile("s_cmp_eq_u32 %0, 0\n\ts_cbranch_scc1 .Lq%=\n\ts_waitcnt vmcnt(0)\n.Lq%=:\n\ts_waitcnt vmcnt(1) lgkmcnt(0)\n\ts_barrier" \
                          ::"s"(wid) : "scc", "memory");                             \
        HOOK;                                                                                                           \
        const unsigned nxt_ = cur == 3 * K3_SLOT ? 0u : cur + K3_SLOT;                                                  \
        _Pragma("unroll") for (int n = 0; n < 5; ++n) {                                                                 \
            _Pragma("unroll") for (int a = 0; a < 4; ++a) {                                                             \
                if (SAFE) mfma_acc_s(acc[n][a], n < 4 ? wf[n & 3] : w4f[PAR], af[PAR][a]);                         \
                else mfma_acc(acc[n][a], n < 4 ? wf[n & 3] : w4f[PAR], af[PAR][a]);                                     \
                SBAR();                                                                                                 \
                if (RD) {                                                                                               \
                    if (n == 0 && a == 0) w4f[1 - (PAR)] = K3_RW(nxt_, 4);                                              \
                    if (n < 4 && a == 1) af[1 - (PAR)][n & 3] = K3_RA(nxt_, n & 3);                                     \
                    if (n >= 1 && a == 0) wf[(n - 1) & 3] = K3_RW(nxt_, n - 1);                                         \
                }                                                                                                       \
                if (DM) {                                                                                               \
                    const int z_ = 4 * n + a;                                                                           \
                    if (z_ == K3D[0]) set_m0(k.m0a + cur);                                                              \
                    if (z_ == K3D[1]) dma_sc1<0>(k.aptr, k.avo);                                                        \
                    if (z_ == K3D[2]) dma_sc1<1024>(k.aptr, k.avo);                                                     \
                    if (z_ == K3D[3]) set_m0(k.m0w + cur);                                                              \
                    if (z_ == K3D[4]) dma_nom0<0>(k.wptr, k.wvo[0]);                                                    \
                    if (z_ == K3D[5]) dma_nom0<1024>(k.wptr, k.wvo[1]);                                                 \
                    if (z_ == K3D[6] && !GRP) dma_nom0<2048>(k.wptr, k.wvo[2]);                                         \
                }                                                                                                       \
                SBAR();                                                                                                 \
            }                                                                                                           \
        }                                                                                                               \
        if (DM) { k.aptr += 2048; k.wptr += 81920; }                                                                    \
        cur = nxt_;                                                                                                     \
    } while (0)
#define K3_FIRST(PAR) do { _Pragma("unroll") for (int a = 0; a < 4; ++a) af[PAR][a] = K3_RA(0, a);                      \
        _Pragma("unroll") for (int n = 0; n < 4; ++n) wf[n] = K3_RW(0, n);                                              \
        w4f[PAR] = K3_RW(0, 4); } while (0)
#define K3_REDUCE()                                                                                                     \
    do {                                                                                                                \
        NOP_A(acc);                                                                                                     \
        lds_ptr X_ = (lds_ptr)lds + wid * 10240 + lane * 16;                                                            \
        lds_cptr Y_ = (lds_cptr)lds + (wid ^ 4) * 10240 + lane * 16;                                                    \
        _Pragma("unroll") for (int a = 0; a < 2; ++a) _Pragma("unroll") for (int n = 0; n < 5; ++n)                     \
            *(__attribute__((address_space(3))) f32x4*)(X_ + (a * 5 + n) * 1024) = acc[n][GRP ? a : 2 + a];             \
        asm volatile("s_waitcnt lgkmcnt(0)\n\ts_barrier" ::: "memory");                                                 \
        _Pragma("unroll") for (int a = 0; a < 2; ++a) _Pragma("unroll") for (int n = 0; n < 5; ++n)                     \
            res[a * 5 + n] = acc[n][GRP ? 2 + a : a] + *(const __attribute__((address_space(3))) f32x4*)(Y_ + (a * 5 + n) * 1024); \
    } while (0)
#define K3_BPUT(j0, j1)                                                                                                 \
    do {                                                                                                                \
        _Pragma("unroll") for (int j = j0; j < j1; ++j)                                                                 \
            *(__attribute__((address_space(3))) u32x4*)((lds_ptr)lds + 4 * K3_SLOT + wid * 2048 + lane * 16 + (j - (j0)) * 1024) = \
                pack44(accB[(2 * j) % 5][(GRP ? 0 : 2) + (2 * j) / 5], accB[(2 * j + 1) % 5][(GRP ? 0 : 2) + (2 * j + 1) / 5]); \
        SBAR();                                                                                                         \
    } while (0)
#define K3_BGET(j0, j1)                                             \
    do {                                                                                                                \
        _Pragma("unroll") for (int j = j0; j < j1; ++j)                                                                 \
            yb[j - (j0)] = *(const __attribute__((address_space(3))) u32x4*)((lds_cptr)lds + 4 * K3_SLOT + (wid ^ 4) * 2048 + lane * 16 + (j - (j0)) * 1024); \
        SBAR();                                                                                                         \
    } while (0)
#define K3_BADD(j0, j1)                                                                                                 \
    do {                                                                                                                \
        _Pragma("unroll") for (int j = j0; j < j1; ++j) {                                                               \
            resB[2 * j] = accB[(2 * j) % 5][(GRP ? 2 : 0) + (2 * j) / 5] + unpk_lo(yb[j - (j0)]);                       \
            resB[2 * j + 1] = accB[(2 * j + 1) % 5][(GRP ? 2 : 0) + (2 * j + 1) / 5] + unpk_hi(yb[j - (j0)]);           \
            PIN(resB[2 * j]); PIN(resB[2 * j + 1]);                                                                     \
        }                                                                                                               \
        SBAR();                                                                                                         \
    } while (0)
#define K3_BOUT(s)                                                                                                      \
    do {                                                                                                                \
        const bool lo_ = r != 0 && (s) < r;                                                                             \
        const unsigned hi_ = (s) == 3 ? 4 : (s) + 1;                                                                    \
        const u32x4 a_ = pack44(resB[2 * (s)], resB[2 * (s) + 1]), b_ = pack44(resB[2 * hi_], resB[2 * hi_ + 1]);       \
        const bool ua_ = lo_ || ((s) == 3 && r == 0);                                                                   \
        u32x4 v_;                                                                                                       \
        _Pragma("unroll") for (int e = 0; e < 4; ++e) v_[e] = ua_ ? a_[e] : b_[e];                                      \
        st_wt_o(pbase + (4 + ks) * PP_SET + (ua_ ? (s) : hi_) * 1024, vo, v_);                                          \
        SBAR();                                                                                                         \
    } while (0)
#define NOHOOK (void)0
    const int grp64 = qt * 4 + mt;
    const char* pbase = uptr(ws + WS_PP + (size_t)grp64 * (8 * PP_SET) + wid * 5120);
    const unsigned vo = lane * 16;
    const unsigned r = (ks - wid) & 3;
    f32x4 res[10], resB[10], ownB[2][2], ownS[2][2];
    K3_ZERO(accB);
    SBAR();
    asm volatile("s_waitcnt vmcnt(%0) lgkmcnt(0)\n\ts_barrier" ::"i"(P + 4) : "memory");
    STAMP(10);
    k3_stage(k, 2 * K3_SLOT, !GRP); k3_stage(k, 3 * K3_SLOT, !GRP);
    if (ks != 3) { K3_FIRST(1); K3_ITER(accB, 1, 2 * P + 4, true, true, true, NOHOOK); }
    else K3_FIRST(0);
    K3_ITER(accB, 0, 2 * P, true, true, true, flag_set(wid, flb, cflag, tag));
    K3_ITER(accB, 1, 2 * P, true, true, false, NOHOOK);
    u32x2 fc = u32x2{~(unsigned)tag, 0u};
    const unsigned fco = FL_C + ((qt < 8 ? qt : 15 - qt) * 32 + 8 * ks + (lane & 7)) * 8;
    K3_ITER(accB, 0, 2 * P, true, false, false, flag_ld0(wid, fc, flb, fco));
    STAMP(11);
    flag_wait<true>(wid, fc, flb, fco, tag);
    STAMP(18);
    k3_point(k, ws, wo, wid, qt, mt, 32 * ks, 16 * ks);
    K3_ITER(accB, 1, P, true, true, false, k3_stage(k, cur == 0 ? 3 * K3_SLOT : cur - K3_SLOT, !GRP));
    K3_ITER(accB, 0, 2 * P, true, true, false, NOHOOK);
    K3_ITER(accB, 1, 2 * P, true, true, false, NOHOOK);
    STAMP(19);
    K3_ZERO(acc);
    NOP_A(accB);
    u32x4 yb[2];
#define K3_H2(A, B) do { A; B; } while (0)
    K3_ITER(acc, 0, 2 * P, true, true, true, K3_BPUT(0, 2)); K3_ITER(acc, 1, 2 * P, true, true, false, K3_BGET(0, 2));
    K3_ITER(acc, 0, 2 * P, true, true, false, K3_H2(K3_BADD(0, 2), K3_BPUT(2, 4))); K3_ITER(acc, 1, 2 * P, true, true, false, K3_BGET(2, 4));
    K3_ITER(acc, 0, 2 * P, true, true, false, K3_H2(K3_BADD(2, 4), K3_BPUT(4, 5))); K3_ITER(acc, 1, 2 * P, true, true, false, K3_BGET(4, 5));
    K3_ITER(acc, 0, 2 * P, true, true, false, K3_H2(K3_BADD(4, 5), K3_BOUT(0)));
    K3_ITER(acc, 1, 2 * P + 1, true, true, false, K3_BOUT(1)); K3_ITER(acc, 0, 2 * P + 2, true, true, false, K3_BOUT(2));
    K3_ITER(acc, 1, 2 * P + 2, true, true, false, K3_BOUT(3));
    unsigned rv = r;
    asm volatile("" : "+v"(rv));
    {
        const bool r0 = rv == 0, r1 = rv == 1, r2 = rv == 2;
#pragma unroll
        for (int t = 0; t < 2; ++t)
#pragma unroll
            for (int e = 0; e < 4; ++e) {
                const float hi = r2 ? resB[4 + t][e] : resB[6 + t][e], lo = r0 ? resB[t][e] : resB[2 + t][e];
                ownB[0][t][e] = (r0 || r1) ? lo : hi;
                ownB[1][t][e] = resB[8 + t][e];
            }
        PIN(ownB[0][0]); PIN(ownB[0][1]);
    }
    SBAR();
    K3_ITER(acc, 0, 2 * P + 2, true, true, false, NOHOOK); K3_ITER(acc, 1, 2 * P + 1, true, true, false, NOHOOK);
#undef K3_H2
    u32x2 xv[2][2];
    unsigned oo[2][2];
#pragma unroll
    for (int sl = 0; sl < 2; ++sl)
#pragma unroll
        for (int t = 0; t < 2; ++t) {
            const unsigned i = 2 * (sl ? 4 : r) + t, a = i >= 5 ? 1 : 0, n = i - 5 * a;
            oo[sl][t] = (16 * (8 * qt + 4 * qh + 2 * GRP + a) + c) * (DM * 2) + (16 * (10 * mt + 5 * mh + n) + 4 * g) * 2;
        }
#define K3_XLD() asm volatile("global_load_dwordx2 %0, %4, %8\n\tglobal_load_dwordx2 %1, %5, %8\n\tglobal_load_dwordx2 %2, %6, %8\n\tglobal_load_dwordx2 %3, %7, %8" \
        : "=&v"(xv[0][0]), "=&v"(xv[0][1]), "=&v"(xv[1][0]), "=&v"(xv[1][1]) : "v"(oo[0][0]), "v"(oo[0][1]), "v"(oo[1][0]), "v"(oo[1][1]), "s"(x) : "memory")
    const unsigned fown = (grp64 * 4 + ks) * 128, fpeer = (grp64 * 4 + ((lane & 3) == (unsigned)ks ? (ks + 1) & 3 : lane & 3)) * 128;
    u32x2 fb;
    K3_ITER(acc, 0, 2 * P, true, false, false, flag_set(wid, flb, FL_B + fown, tag));
    K3_ITER(acc, 1, P, true, false, false, NOHOOK);
#define K3_H14() do { K3_XLD(); flag_ld(fb, flb, FL_B + fpeer); } while (0)
    u32x4 pp[2][6];
#pragma unroll
    for (int i = 0; i < 6; ++i) pp[1][i] = u32x4{};
    const unsigned s1 = ((ks + 1) & 3) * PP_SET, s2 = ((ks + 2) & 3) * PP_SET, s3 = ((ks + 3) & 3) * PP_SET;
    const char* pj0 = pbase + r * 1024;
    const char* pj1 = pbase + 4 * 1024;
#define K3_H15()                                                                                                        \
    do {                                                                                                                \
        PIN(xv[0][0]); PIN(xv[0][1]); PIN(xv[1][0]); PIN(xv[1][1]);                                                     \
        flag_wait<false>(0, fb, flb, FL_B + fpeer, tag);                                                                \
        ld_wt(pp[0][3], pj0 + 4 * PP_SET + s1, vo); ld_wt(pp[0][4], pj0 + 4 * PP_SET + s2, vo); ld_wt(pp[0][5], pj0 + 4 * PP_SET + s3, vo); \
        ld_wt3(r, pp[1][3], pp[1][4], pp[1][5], pj1 + 4 * PP_SET + s1, pj1 + 4 * PP_SET + s2, pj1 + 4 * PP_SET + s3, vo); \
        SBAR();                                                                                                         \
    } while (0)
    K3_ITER(acc, 0, -1, true, false, false, K3_H14()); K3_ITER(acc, 1, 0, false, false, false, K3_H15());
#undef K3_H14
#undef K3_H15
    STAMP(12);
    K3_REDUCE();
    STAMP(17);
    {
#pragma unroll
        for (int sl = 0; sl < 4; ++sl) {
            const bool lo_ = r != 0 && (unsigned)sl < r;
            const int hi_ = sl == 3 ? 4 : sl + 1;
            const u32x4 a_ = pack44(res[2 * sl], res[2 * sl + 1]), b_ = pack44(res[2 * hi_], res[2 * hi_ + 1]);
            const bool ua_ = lo_ || (sl == 3 && r == 0);
            u32x4 v_;
#pragma unroll
            for (int e = 0; e < 4; ++e) v_[e] = ua_ ? a_[e] : b_[e];
            st_wt_o(pbase + ks * PP_SET + (ua_ ? sl : hi_) * 1024, vo, v_);
        }
        const bool r0 = rv == 0, r1 = rv == 1, r2 = rv == 2;
#pragma unroll
        for (int t = 0; t < 2; ++t)
#pragma unroll
            for (int e = 0; e < 4; ++e) {
                const float hi = r2 ? res[4 + t][e] : res[6 + t][e], lo = r0 ? res[t][e] : res[2 + t][e];
                ownS[0][t][e] = (r0 || r1) ? lo : hi;
                ownS[1][t][e] = res[8 + t][e];
            }
    }
    u32x2 fe[4];
    const bool bump = blockIdx.x == 0 && wid == 0;
    if (bump) {
#pragma unroll
        for (int i = 0; i < 4; ++i) flag_ld(fe[i], flb, FL_X + (64 * i + lane) * 8);
    }
    const int nown = r == 0 ? 2 : 1;
    STAMP(14);
    asm volatile("s_waitcnt vmcnt(0)\n\ts_barrier" : "+v"(fe[0]), "+v"(fe[1]), "+v"(fe[2]), "+v"(fe[3]), "+v"(pp[0][3]), "+v"(pp[0][4]), "+v"(pp[0][5]),
                 "+v"(pp[1][3]), "+v"(pp[1][4]), "+v"(pp[1][5])::"memory");
    STAMP(15);
    flag_set(wid, flb, FL_S + fown, tag);
    {
        u32x2 f = u32x2{~(unsigned)tag, 0u};
        flag_wait<false>(0, f, flb, FL_S + fpeer, tag);
    }
    STAMP(16);
    ld_wt(pp[0][0], pj0 + s1, vo); ld_wt(pp[0][1], pj0 + s2, vo); ld_wt(pp[0][2], pj0 + s3, vo);
    ld_wt3(r, pp[1][0], pp[1][1], pp[1][2], pj1 + s1, pj1 + s2, pj1 + s3, vo);
    asm volatile("s_waitcnt vmcnt(0)" : "+v"(pp[0][0]), "+v"(pp[0][1]), "+v"(pp[0][2]), "+v"(pp[0][3]), "+v"(pp[0][4]), "+v"(pp[0][5]),
                 "+v"(pp[1][0]), "+v"(pp[1][1]), "+v"(pp[1][2]), "+v"(pp[1][3]), "+v"(pp[1][4]), "+v"(pp[1][5])::"memory");
#pragma unroll
    for (int sl = 0; sl < 2; ++sl)
        if (sl < nown) {
#pragma unroll
            for (int t = 0; t < 2; ++t) {
                f32x4 sv = ownS[sl][t], bv = ownB[sl][t];
#pragma unroll
                for (int i = 0; i < 3; ++i) { sv += t ? unpk_hi(pp[sl][i]) : unpk_lo(pp[sl][i]); bv += t ? unpk_hi(pp[sl][3 + i]) : unpk_lo(pp[sl][3 + i]); }
                const float o0 = bflo(xv[sl][t][0]) + sv[0] * silu_f(bv[0]), o1 = bfhi(xv[sl][t][0]) + sv[1] * silu_f(bv[1]);
                const float o2 = bflo(xv[sl][t][1]) + sv[2] * silu_f(bv[2]), o3 = bfhi(xv[sl][t][1]) + sv[3] * silu_f(bv[3]);
                u32x2 w; w[0] = cvtpk(o0, o1); w[1] = cvtpk(o2, o3);
                *(u32x2*)(out + oo[sl][t]) = w;
            }
        }
    if (bump) {
#pragma unroll
        for (int i = 0; i < 4; ++i) flag_wait<false>(0, fe[i], flb, FL_X + (64 * i + lane) * 8, tag);
        flag_set(0, flb, FL_EPOCH, tag);
    }
    STAMP(13);
#undef K3_RW
#undef K3_RA
#undef K3_ZERO
#undef K3_ITER
#undef K3_FIRST
#undef K3_REDUCE
#undef K3_BPUT
#undef K3_BGET
#undef K3_BADD
#undef K3_BOUT
#undef K3_XLD
#undef NOHOOK
}

__global__ __launch_bounds__(512, 2) void fused(const char* __restrict__ x, const char* __restrict__ ckv, const char* __restrict__ wuk, const char* __restrict__ wuv,
                                              const char* __restrict__ wq, const char* __restrict__ w1, const char* __restrict__ b1, char* __restrict__ ws,
                                              const char* __restrict__ wo, const char* __restrict__ w2, char* __restrict__ out) {
    extern __shared__ __attribute__((aligned(16))) char lds[];
    const unsigned wid = rfl(threadIdx.x >> 6), grp = wid >> 2, w4 = wid & 3;
    char* stamp_base = ws; (void)stamp_base;
    STAMP(0);
    const int b = blockIdx.x, xcd = b & 7, idx = b >> 3;
    const int vt = idx & 1, hp = 2 * xcd + ((idx >> 1) & 1), rb = idx >> 2;
    const int head = 4 * xcd + (idx & 3), pi = idx >> 2;
    const int HB = 15 - pi, LB = pi;
    const int myblk = grp ? LB : HB;
    const int q16_0 = 8 * myblk + 2 * w4;
    const bool nft4 = head < 22;
    const int f0 = nft4 ? 64 * head : 1408 + 32 * (head - 22);
    const char* Abase = ckv + (size_t)rb * 256 * 1024;
    const char* Wbase = (vt ? wuv : wuk) + hp * 512;
    const unsigned lane0 = flane();
#define lane lane0
    const int row = 8 * b + wid;
    const unsigned l40 = lane < 40 ? lane : 39;
    u32x4 x0, x1;
    {
        const char* xr = uptr(x + (size_t)row * 1280);
        const unsigned vo = l40 * 32;
        asm volatile("global_load_dwordx4 %0, %2, %3\n\tglobal_load_dwordx4 %1, %2, %3 offset:16" : "=&v"(x0), "=&v"(x1) : "v"(vo), "s"(xr) : "memory");
    }
    unsigned avo[4], wvo[4];
    {
        const unsigned lds0 = (unsigned)(uintptr_t)lds;
        const char* Aw = Abase + (size_t)wid * 32 * 1024;
        const char* Ww = Wbase + (size_t)wid * 8 * 8192;
        const unsigned m0a = lds0 + wid * 4096, m0w = lds0 + 32768 + wid * 4096;
#pragma unroll
        for (int p = 0; p < 4; ++p) {
            const unsigned rw = 8 * p + (lane >> 3);
            avo[p] = rw * 1024 + (((lane & 7) ^ ((rw >> 1) & 7)) << 4) - 1024 * p;
            const unsigned k = 2 * p + (lane >> 5);
            const unsigned f = (k & 3) | ((wid & 1) << 2);
            wvo[p] = k * 8192 + (((lane & 31) ^ (f << 1)) << 4) - 1024 * p;
        }
#pragma unroll
        for (int kt = 0; kt < 2; ++kt) {
            const char* a_ = Aw + kt * 128; const char* w_ = Ww + (size_t)kt * 64 * 8192; const unsigned stg = kt * 65536;
            dma1<0>(m0a + stg, a_, avo[0]); dma1<1024>(m0a + stg, a_, avo[1]); dma1<2048>(m0a + stg, a_, avo[2]); dma1<3072>(m0a + stg, a_, avo[3]);
            dma1<0>(m0w + stg, w_, wvo[0]); dma1<1024>(m0w + stg, w_, wvo[1]); dma1<2048>(m0w + stg, w_, wvo[2]); dma1<3072>(m0w + stg, w_, wvo[3]);
        }
    }
    u64 tag;
    asm volatile("s_load_dwordx2 %0, %1, 0x0\n\ts_waitcnt lgkmcnt(0)" : "=s"(tag) : "s"(uptr(ws + WS_FL + FL_EPOCH)) : "memory");
    tag += 1;
    {
        asm volatile("s_waitcnt vmcnt(16)" : "+v"(x0), "+v"(x1)::"memory");
        float v[16];
#pragma unroll
        for (int i = 0; i < 4; ++i) { v[2 * i] = bflo(x0[i]); v[2 * i + 1] = bfhi(x0[i]); v[8 + 2 * i] = bflo(x1[i]); v[8 + 2 * i + 1] = bfhi(x1[i]); }
        float s = 0.f;
#pragma unroll
        for (int i = 0; i < 16; ++i) s += v[i];
        if (lane >= 40) s = 0.f;
#pragma unroll
        for (int o = 32; o >= 1; o >>= 1) s += __shfl_xor(s, o);
        const float mean = s * (1.0f / DM);
        float q = 0.f;
#pragma unroll
        for (int i = 0; i < 16; ++i) { v[i] -= mean; q += v[i] * v[i]; }
        if (lane >= 40) q = 0.f;
#pragma unroll
        for (int o = 32; o >= 1; o >>= 1) q += __shfl_xor(q, o);
        const float rstd = 1.0f / sqrtf(q * (1.0f / DM) + 1e-5f);
        u32x4 o0, o1;
#pragma unroll
        for (int i = 0; i < 4; ++i) { o0[i] = cvtpk(v[2 * i] * rstd, v[2 * i + 1] * rstd); o1[i] = cvtpk(v[8 + 2 * i] * rstd, v[8 + 2 * i + 1] * rstd); }
        char* xf = ws + WS_XF + ((size_t)(row >> 4) * NKS_X + (l40 >> 1)) * 1024 + ((row & 15) + 32 * (l40 & 1)) * 16;
        if (lane < 40) { st_wt(xf, o0); st_wt(xf + 256, o1); }
    }
#undef lane
    u32x4 xa[4], xb[4], kv[16];
    u32x2 bb[4];
    KVO kvo;
    if (vt) k1_tile<true>(Abase, Wbase, ws + WS_VF, lds, avo, wvo, wid, hp, rb, ws, wq, w1, b1, tag, head, f0, nft4, q16_0, xa, bb, kv, kvo);
    else    k1_tile<false>(Abase, Wbase, ws + WS_KF, lds, avo, wvo, wid, hp, rb, ws, wq, w1, b1, tag, head, f0, nft4, q16_0, xa, bb, kv, kvo);
    STAMP(6);
    const unsigned lds0 = (unsigned)(uintptr_t)lds;
    const unsigned kslot0 = grp ? 114688 : 81920, kslot1 = kslot0 + 16384, kslot2 = grp ? 16384 : 0;
    const unsigned vslot0 = grp ? 147456 : 32768, vslot1 = grp ? 65536 : 49152;
    const int nL = 2 * pi + 2;
    const int nsw = grp ? nL : 1000;
    const size_t jump = (size_t)(17 - nL) * 16384;
    const char* kptr = uptr(ws + WS_KF + (size_t)head * 524288 + w4 * 4096);
    const char* vptr = uptr(ws + WS_VF + (size_t)head * 524288 + w4 * 4096);
    bf16x8 qf[8];
    {
        bf16x8 q16[2][4];
        const unsigned kvflag = FL_KV + (hp * 16 + vt * 8 + rb) * 8;
        if (nft4) proj_phase<4>(wq, w1, ws, lds, head, f0, q16_0, q16, wo, w2, lds0 + vslot0 + w4 * 4096, wid, xa, xb, bb, tag, kvflag, head >> 1, lds0 + kslot0 + w4 * 4096, lds0 + kslot1 + w4 * 4096, kptr, kptr + 16384, kv, kvo);
        else      proj_phase<2>(wq, w1, ws, lds, head, f0, q16_0, q16, wo, w2, lds0 + vslot0 + w4 * 4096, wid, xa, xb, bb, tag, kvflag, head >> 1, lds0 + kslot0 + w4 * 4096, lds0 + kslot1 + w4 * 4096, kptr, kptr + 16384, kv, kvo);
        kptr += 2 * 16384; if (nsw == 2) kptr += jump;
#pragma unroll
        for (int v = 0; v < 4; ++v) {
            const u32x4 r0 = __builtin_bit_cast(u32x4, q16[0][v]), r1 = __builtin_bit_cast(u32x4, q16[1][v]);
            u32x4 z0, z1;
#pragma unroll
            for (int w = 0; w < 4; ++w) {
                const auto s1 = __builtin_amdgcn_permlane16_swap(r0[w], r1[w], false, false);
                const auto s2 = __builtin_amdgcn_permlane32_swap(s1[0], s1[1], false, false);
                z0[w] = s2[0]; z1[w] = s2[1];
            }
            qf[2 * v] = __builtin_bit_cast(bf16x8, z0); qf[2 * v + 1] = __builtin_bit_cast(bf16x8, z1);
        }
    }
    STAMP(7);
    const unsigned lane = flane();
    if (grp == 0) {
        char* qh = ws + WS_QH + (size_t)b * 32768 + w4 * 8192 + lane * 16;
#pragma unroll
        for (int s = 0; s < 8; ++s) *(bf16x8*)(qh + s * 1024) = qf[s];
    }
    const char* flb = uptr(ws + WS_FL);
    lds_cptr Lb = (lds_cptr)lds + lane * 16;
    const unsigned m0b = lds0 + w4 * 4096;
    const unsigned dvo = lane * 16;
    int q0 = 128 * myblk + 32 * w4;
    int tile = 0;
    asm volatile("s_waitcnt lgkmcnt(0)\n\ts_barrier" ::: "memory");
    dma4(m0b + vslot0, vptr, dvo); vptr += 16384;
    dma4(m0b + kslot2, kptr, dvo); kptr += 16384;
    f32x16 o[4], S[2], negm;
    float mref = 0.f, lsum = 0.f, lfin = 1.f;
    bf16x8 pf[4], fr[FRW];
#pragma unroll
    for (int i = 0; i < 4; ++i) o[i] = f32x16{};
    negm = f32x16{}; S[0] = f32x16{}; S[1] = f32x16{};
    asm volatile("s_nop 1" : "+v"(negm));
    NOP_SO(S, o);
    unsigned ks_cur = kslot0, ks_nxt = kslot1, ks_nn = kslot2;
#define CTX_VO(l) ((((l) >> 4) & 1) * (NKS_CH * 1024) + (((l) & 15) + 16 * ((l) >> 5)) * 16)
#define KOFF(i) (((i) & 1) * 8192 + ((i) >> 1) * 1024)
#define VOFF(j) (((j) >> 3) * 8192 + ((j) & 3) * 2048 + (((j) >> 2) & 1) * 1024)
#define EARLY_K(slot) do { _Pragma("unroll") for (int i = 0; i < FRA; ++i) fr[i] = lds_frag(Lb + (slot) + KOFF(i)); } while (0)
    EARLY_K(kslot0);
    if (grp) asm volatile("s_barrier" ::: "memory");
#define PHASE_M(t, QK, PV, VS)                                                                              \
    do {                                                                                                                              \
        __builtin_amdgcn_s_setprio(0);                                                                                                \
        lds_cptr kp_ = Lb + ks_cur; lds_cptr vp_ = Lb + ((VS) ? vslot1 : vslot0);                                                     \
        if (!(QK)) { _Pragma("unroll") for (int i = 16; i < 16 + FRA; ++i) fr[i % FRW] = lds_frag(vp_ + VOFF(i - 16)); }               \
        _Pragma("unroll") for (int i = (QK) ? 0 : 16; i < ((PV) ? 32 : 16); ++i) {                                                    \
            const int n = i + FRA;                                                                                                    \
            if (n < 16) fr[n % FRW] = lds_frag(kp_ + KOFF(n)); else if ((PV) && n < 32) fr[n % FRW] = lds_frag(vp_ + VOFF(n - 16));    \
            SBAR();                                                                                                                   \
            if (i < 2) mfma32_ini(S[i & 1], fr[i % FRW], qf[i >> 1], negm);                                                        \
            else if (i < 16) mfma32_acc(S[i & 1], fr[i % FRW], qf[i >> 1]);                                                             \
            else mfma32_acc(o[i & 3], fr[i % FRW], pf[(i - 16) >> 2]);                                                                  \
            SBAR();                                                                                                                   \
            if ((QK) && i == 16 && (t) + 1 == nsw) {        \
                const char* qs_ = uptr(ws + WS_QH + (size_t)b * 32768 + w4 * 8192);                                                   \
                asm volatile("s_nop 4\n\tglobal_load_dwordx4 %0, %8, %9\n\tglobal_load_dwordx4 %1, %8, %9 offset:1024\n\tglobal_load_dwordx4 %2, %8, %9 offset:2048\n\tglobal_load_dwordx4 %3, %8, %9 offset:3072" \
                             : "+v"(qf[0]), "+v"(qf[1]), "+v"(qf[2]), "+v"(qf[3]), "+v"(qf[4]), "+v"(qf[5]), "+v"(qf[6]), "+v"(qf[7]) : "v"(dvo), "s"(qs_) : "memory"); \
                asm volatile("global_load_dwordx4 %4, %8, %9\n\tglobal_load_dwordx4 %5, %8, %9 offset:1024\n\tglobal_load_dwordx4 %6, %8, %9 offset:2048\n\tglobal_load_dwordx4 %7, %8, %9 offset:3072" \
                             : "+v"(qf[0]), "+v"(qf[1]), "+v"(qf[2]), "+v"(qf[3]), "+v"(qf[4]), "+v"(qf[5]), "+v"(qf[6]), "+v"(qf[7]) : "v"(dvo), "s"(qs_ + 4096) : "memory"); \
            }                                                                                                                         \
            SBAR();                                                                                                                   \
        }                                                                                                                             \
        __builtin_amdgcn_s_setprio(3);                                                                                                \
        TICK(tA);                                                                                                                     \
        asm volatile("s_barrier" ::: "memory");                                                                                       \
        TICK(tX);                                                                                                                     \
    } while (0)
#define EXP_PACK(DV, DK, VS)         \
    do {                                                                                                                              \
        float e_[32], a_[2];                                                                                                          \
        unsigned c_[16];                                                                                                              \
        _Pragma("unroll") for (int k = 0; k < 18; ++k) {                                                                              \
            if (k < 16) { e_[2 * k] = v_exp(S[k >> 3][2 * (k & 7)]); e_[2 * k + 1] = v_exp(S[k >> 3][2 * (k & 7) + 1]); }             \
            if (k == 1) { a_[0] = e_[0]; a_[1] = e_[1]; }                                                                             \
            if (k >= 2 && k < 17) { a_[0] = v_add(a_[0], e_[2 * k - 2]); a_[1] = v_add(a_[1], e_[2 * k - 1]); }                        \
            if (k >= 2) c_[k - 2] = v_cvt(e_[2 * k - 4], e_[2 * k - 3]);                                                              \
            if (VIL) {                                                   \
                if ((DV) && k == VIL0) set_m0(m0b + ((VS) ? vslot1 : vslot0));                                                        \
                if ((DV) && k == VIL0 + 1) dma_sc1<0>(vptr, dvo);                                                                    \
                if ((DV) && k == VIL0 + 3) dma_sc1<1024>(vptr, dvo);                                                                 \
                if ((DV) && k == VIL0 + 5) dma_sc1<2048>(vptr, dvo);                                                                 \
                if ((DV) && k == VIL0 + 7) dma_sc1<3072>(vptr, dvo);                                                                 \
                if ((DK) && k == VIL0 + 8) set_m0(m0b + ks_cur);                                                                      \
                if ((DK) && k == VIL0 + 9) dma_sc1<0>(kptr, dvo);                                                                    \
                if ((DK) && k == VIL0 + 11) dma_sc1<1024>(kptr, dvo);                                                                \
                if ((DK) && k == VIL0 + 13) dma_sc1<2048>(kptr, dvo);                                                                \
                if ((DK) && k == VIL0 + 15) dma_sc1<3072>(kptr, dvo);                                                                \
            }                                                                                                                         \
        }                                                                                                                             \
        _Pragma("unroll") for (int k = 0; k < 4; ++k) pf[k] = __builtin_bit_cast(bf16x8, u32x4{c_[4 * k], c_[4 * k + 1], c_[4 * k + 2], c_[4 * k + 3]}); \
        ts_ = v_add(a_[0], a_[1]);                                                                                                    \
    } while (0)
#define REBASE(FIRST)                                                          \
    do {                                                                                                                              \
        float rm = S[0][0];                                                                                                           \
        _Pragma("unroll") for (int e = 1; e < 32; ++e) rm = __builtin_fmaxf(rm, S[e >> 4][e & 15]);                                   \
        rm = xh_max(rm);                                                                                                              \
        const float dl = (FIRST) ? (rm == -__builtin_inff() ? 0.f : rm) : __builtin_fmaxf(rm, 0.f);                                   \
        mref += dl;                                                                                                                   \
        if (!(FIRST)) { float f = __builtin_amdgcn_exp2f(-dl); asm volatile("s_nop 1" : "+v"(f)); lsum *= f;               \
            _Pragma("unroll") for (int i = 0; i < 4; ++i) _Pragma("unroll") for (int r = 0; r < 16; ++r) o[i][r] = mul_s(o[i][r], f); } \
        _Pragma("unroll") for (int r = 0; r < 16; ++r) { S[0][r] = sub_s(S[0][r], dl); S[1][r] = sub_s(S[1][r], dl); }                \
        const float nm = -mref;                                                                                                       \
        _Pragma("unroll") for (int r = 0; r < 16; ++r) negm[r] = nm;                                                                  \
    } while (0)
#define PHASE_V(t, WN, LASTV, VS, DK, DV)                                                                                             \
    do {                                                                                                                              \
        const bool swn = ((t) + 1 == nsw), sw0 = ((t) == nsw);                                                                        \
        const bool diag = 64 * tile + 63 > q0 || sw0, first_ = (t) == 0 || sw0;                                                       \
        if (!VIL) { if (DV) dma4(m0b + ((VS) ? vslot1 : vslot0), vptr, dvo); if (DK) dma4(m0b + ks_cur, kptr, dvo); }                 \
        NOP_SO(S, o);                                                                                                                 \
        float ts_;                                                                                                                    \
        if (__builtin_expect(diag || first_, 0)) {                                                                                    \
            if (sw0) {                                                                                \
                float il_ = __builtin_amdgcn_rcpf(xh_sum(lsum));                                                                      \
                asm volatile("s_nop 1" : "+v"(il_));                                                                                  \
                const char* cp_ = uptr(ws + WS_CH + ((size_t)(8 * LB + 2 * w4) * NKS_CH + 4 * head) * 1024);                          \
                const unsigned co_ = CTX_VO(flane());                                                                                 \
                asm volatile("s_nop 4");                                                                                              \
                st_wt_i<0>(cp_, co_, pack_o(o[0], 0, il_)); st_wt_i<512>(cp_, co_, pack_o(o[0], 1, il_));                             \
                st_wt_i<1024>(cp_, co_, pack_o(o[1], 0, il_)); st_wt_i<1536>(cp_, co_, pack_o(o[1], 1, il_));                         \
                st_wt_i<2048>(cp_, co_, pack_o(o[2], 0, il_)); st_wt_i<2560>(cp_, co_, pack_o(o[2], 1, il_));                         \
                st_wt_i<3072>(cp_, co_, pack_o(o[3], 0, il_)); st_wt_i<3584>(cp_, co_, pack_o(o[3], 1, il_));                         \
                _Pragma("unroll") for (int i = 0; i < 4; ++i) o[i] = f32x16{};                                                        \
                lsum = 0.f; mref = 0.f; q0 = 128 * HB + 32 * w4;                                                                      \
            }                                                                                                                         \
            if (64 * tile + 63 > q0) {         \
                const int dq = (int)(lane & 31) - 4 * (int)(lane >> 5);                                                               \
                _Pragma("unroll") for (int u = 0; u < 2; ++u) {                                                                       \
                    const int kb = 64 * tile + 32 * u;                                                                                \
                    if (kb > q0) { _Pragma("unroll") for (int r = 0; r < 16; ++r) S[u][r] = -__builtin_inff(); }                       \
                    else if (kb == q0) mask_diag(S[u], dq);                                                                           \
                }                                                                                                                     \
            }                                                                                                                         \
            if (first_) REBASE(true);                                                                                                 \
        }                                                                                                                             \
        EXP_PACK(DV, DK, VS);                                                                                                         \
        if (DV) vptr += 16384 + ((t) + 2 == nsw ? jump : 0);                                                                          \
        if (DK) kptr += 16384 + ((t) + 4 == nsw ? jump : 0);                                                                          \
        if (__builtin_expect(__any(ts_ > PLIM), 0)) { REBASE(false); EXP_PACK(false, false, 0); }     \
        lsum += ts_;                                                                                                                  \
        if (!(LASTV)) EARLY_K(ks_nxt);                                                                                                \
        if (__builtin_expect(swn, 0)) {                                                                                               \
            negm = f32x16{};                                                                                                          \
            asm volatile("s_waitcnt vmcnt(%9)" : "+v"(qf[0]), "+v"(qf[1]), "+v"(qf[2]), "+v"(qf[3]), "+v"(qf[4]), "+v"(qf[5]), "+v"(qf[6]), "+v"(qf[7]), "+v"(negm) : "i"(WN) : "memory"); \
            tile += 17 - nL;                                                                                                          \
        }                                                                                                                             \
        TICK(tB);                                                                                                                     \
        asm volatile("s_cmp_eq_u32 %7, 0\n\ts_cbranch_scc1 .Lv1%=\n\ts_waitcnt vmcnt(%5)\n\ts_branch .Lv2%=\n.Lv1%=:\n\ts_waitcnt vmcnt(%6)\n.Lv2%=:\n\ts_barrier"                \
                     : "+v"(pf[0]), "+v"(pf[1]), "+v"(pf[2]), "+v"(pf[3]), "+v"(negm) : "i"(WN), "i"((WN) + 8), "s"(nsw - (t)) : "scc", "memory");   \
        __builtin_amdgcn_s_waitcnt(0xC07F);        \
        TICK(tO);                                                                                                                     \
        { const unsigned r_ = ks_cur; ks_cur = ks_nxt; ks_nxt = ks_nn; ks_nn = r_; }                                                  \
        tile += 1;                                                                                                                    \
    } while (0)
    constexpr float PLIM = 256.0f;
    PHASE_M(0, true, false, 1);
    PHASE_V(0, 8, false, 1, true, true);
    flag_set(w4, flb, FL_H + (myblk * 32 + head) * 8, tag);
    for (int t = 1; t < 13; t += 2) {
        PHASE_M(t, true, true, 0); PHASE_V(t, 8, false, 0, true, true);
        PHASE_M(t + 1, true, true, 1); PHASE_V(t + 1, 8, false, 1, true, true);
    }
    PHASE_M(13, true, true, 0); PHASE_V(13, 8, false, 0, true, true);
    PHASE_M(14, true, true, 1); PHASE_V(14, 4, false, 1, false, true);
    PHASE_M(15, true, true, 0);
    const int ks = xcd >> 1, qt = 8 * (xcd & 1) + (idx >> 2), mt = idx & 3;
    u32x2 fh;
    const unsigned fho = FL_H + (qt * 32 + (lane & 31)) * 8;
    flag_ld(fh, flb, fho);
    PHASE_V(15, 4, false, 0, false, true);
    PIN(fh);
    PHASE_M(16, true, true, 1); PHASE_V(16, 0, true, 1, false, false);
    PHASE_M(17, false, true, 0);
    flag_wait<false>(0, fh, flb, fho, tag);
    if (!grp) asm volatile("s_barrier" ::: "memory");
    NOP_SO(S, o);
    K3S k3 = k3_setup(lds0, wid, lane);
    k3_point(k3, ws, w2, wid, qt, mt, NKS_C + 14 * ks, 7 * ks);
    k3_stage(k3, 0, grp == 0); k3_stage(k3, K3_SLOT, grp == 0);
    STAMP(8);
#define MERGE(G) \
    { \
        const float ls = xh_sum(lsum); \
        const float mm = ls > 0.f ? mref : -__builtin_inff(); \
        lds_ptr X = (lds_ptr)lds + 81920 + wid * 8192 + lane * 16; \
        _Pragma("unroll") for (int i = 0; i < 2; ++i) \
            _Pragma("unroll") for (int y = 0; y < 4; ++y) { \
                const int oi = (G) ? i : 2 + i; \
                *(__attribute__((address_space(3))) f32x4*)(X + (4 * i + y) * 1024) = f32x4{o[oi][4 * y], o[oi][4 * y + 1], o[oi][4 * y + 2], o[oi][4 * y + 3]}; \
            } \
        lds_ptr Y = (lds_ptr)lds + 73728 + wid * 512 + lane * 8; \
        *(__attribute__((address_space(3))) f32x2*)Y = f32x2{mm, ls}; \
        asm volatile("s_waitcnt lgkmcnt(0)\n\ts_barrier" ::: "memory"); \
        const unsigned ow = wid ^ 4; \
        lds_cptr Xo = (lds_cptr)lds + 81920 + ow * 8192 + lane * 16; \
        const f32x2 oml = *(const __attribute__((address_space(3))) f32x2*)((lds_cptr)lds + 73728 + ow * 512 + lane * 8); \
        const float mx = __builtin_fmaxf(mm, oml[0]); \
        const float e0 = __builtin_amdgcn_exp2f(mm - mx), e1 = __builtin_amdgcn_exp2f(oml[0] - mx); \
        const float inv = __builtin_amdgcn_rcpf(ls * e0 + oml[1] * e1); \
        const float fs = e0 * inv, fo = e1 * inv; \
        const char* cp = uptr(ws + WS_CH + ((size_t)(8 * HB + 2 * w4) * NKS_CH + 4 * head + 2 * (G)) * 1024); \
        const unsigned co = CTX_VO(lane); \
_Pragma("unroll") \
        for (int i = 0; i < 2; ++i) { \
            f32x16 m; \
_Pragma("unroll") \
            for (int y = 0; y < 4; ++y) { \
                const f32x4 pv = *(const __attribute__((address_space(3))) f32x4*)(Xo + (4 * i + y) * 1024); \
_Pragma("unroll") \
                for (int r = 0; r < 4; r += 2) { \
                    const f32x2 k2_ = f32x2{o[((G) ? 2 : 0) + i][4 * y + r], o[((G) ? 2 : 0) + i][4 * y + r + 1]}; \
                    const f32x2 r2_ = __builtin_elementwise_fma(k2_, f32x2{fs, fs}, f32x2{pv[r], pv[r + 1]} * f32x2{fo, fo}); \
                    m[4 * y + r] = r2_[0]; m[4 * y + r + 1] = r2_[1]; \
                } \
            } \
            if (i == 0) { st_wt_i<0>(cp, co, pack_o1(m, 0)); st_wt_i<512>(cp, co, pack_o1(m, 1)); } \
            else { st_wt_i<1024>(cp, co, pack_o1(m, 0)); st_wt_i<1536>(cp, co, pack_o1(m, 1)); } \
        } \
    }
    if (grp == 0) MERGE(0) else MERGE(1)
    STAMP(9);
    unsigned grp2 = grp;
    asm volatile("" : "+s"(grp2));
    if (grp2 == 0) k3_body<0>(k3, x, wo, ws, out, lds, wid, tag, ks, qt, mt, FL_C + (pi * 32 + head) * 8);
    else k3_body<1>(k3, x, wo, ws, out, lds, wid, tag, ks, qt, mt, FL_C + (pi * 32 + head) * 8);
#undef MERGE
#undef REBASE
#undef EXP_PACK
#undef PHASE_V
#undef PHASE_M
#undef EARLY_K
#undef VOFF
#undef CTX_VO
#undef KOFF
}

extern "C" void kernel_launch(void* const* d_in, const int* in_sizes, int n_in, void* d_out, int out_size, void* d_ws, size_t ws_size,
                              hipStream_t stream) {
    const char* x = (const char*)d_in[0];
    const char* ckv = (const char*)d_in[1];
    const char* wuk = (const char*)d_in[3];
    const char* wuv = (const char*)d_in[4];
    char* ws = (char*)d_ws;
    (void)hipFuncSetAttribute((const void*)fused, hipFuncAttributeMaxDynamicSharedMemorySize, 163840);
    fused<<<256, 512, 163840, stream>>>(x, ckv, wuk, wuv, (const char*)d_in[2], (const char*)d_in[7], (const char*)d_in[8], ws, (const char*)d_in[5], (const char*)d_in[9], (char*)d_out);
}
```
